```python
import torch

def _softmax(x, axis=-1):
    return torch.softmax(x, dim=axis)
def _gelu(x):
    return torch.nn.functional.gelu(x, approximate="tanh")
def _silu(x):
    return torch.nn.functional.silu(x)
def _sigmoid(x):
    return torch.sigmoid(x)
def _layernorm(x, axis=-1, eps=1e-5):
    m = x.mean(dim=axis, keepdim=True)
    v = x.var(dim=axis, keepdim=True, unbiased=False)
    return (x - m) / torch.sqrt(v + eps)
def _scatter_add(tgt, idx, src, axis=0):
    return tgt.clone().index_add_(axis, idx, src)
def _segment_sum(src, seg, n):
    out = torch.zeros((n,) + src.shape[1:], dtype=src.dtype)
    return out.index_add_(0, seg, src)
def _pad(x, pw):
    flat = tuple(v for pair in reversed(pw) for v in pair)
    return torch.nn.functional.pad(x, flat)

kernel_name = 'sequential_preLN__sliding_block__glu_gated_bilinear__prompt'

def setup_inputs():

    _w = (lambda a: (lambda t: t.to(torch.bfloat16) if t.is_floating_point() else t)(torch.as_tensor(a)))
    def _A_setup():
        B, S, D = 1, 2048, 576
        return {
            "x": _w((lambda *s: torch.randn(*s, dtype=torch.bfloat16) * 0.05)(B, S, D)),
            "wqkv": _w((lambda *s: torch.randn(*s, dtype=torch.bfloat16) * 0.05)(D, 3 * D)),
            "wo": _w((lambda *s: torch.randn(*s, dtype=torch.bfloat16) * 0.05)(D, D)),
        }
    _A_in = _A_setup()

    def _B_setup():
        B, S, D, F = 1, 2048, 576, 4608
        return {
            "x": _w((lambda *s: torch.randn(*s, dtype=torch.bfloat16) * 0.05)(B, S, D)),
            "w_gate": _w((lambda *s: torch.randn(*s, dtype=torch.bfloat16) * 0.8)(D, F) * (D ** -0.5)),
            "w_up": _w((lambda *s: torch.randn(*s, dtype=torch.bfloat16) * 0.8)(D, F) * (D ** -0.5)),
            "w_down": _w((lambda *s: torch.randn(*s, dtype=torch.bfloat16) * 0.8)(F, D) * (F ** -0.5)),
        }
    _B_in = _B_setup()

    return {
        "A_x": _A_in["x"],
        "A_wqkv": _A_in["wqkv"],
        "A_wo": _A_in["wo"],
        "B_x": _B_in["x"],
        "B_w_gate": _B_in["w_gate"],
        "B_w_up": _B_in["w_up"],
        "B_w_down": _B_in["w_down"],
    }

def reference(A_x, A_wqkv, A_wo, B_x, B_w_gate, B_w_up, B_w_down):
    def _A_ref(x, wqkv, wo):
        B, S, D = x.shape
        H = 32; Dh = D // H; W = 256
        qkv = (lambda x, *d: x.permute(*d))((x @ wqkv).reshape(B, S, 3, H, Dh), 2, 0, 3, 1, 4)
        q, k, v = qkv[0], qkv[1], qkv[2]
        scores = torch.einsum("bhqd,bhkd->bhqk", q, k) * (Dh ** -0.5)
        idx = torch.arange(S)
        mask = (idx[:, None] - idx[None, :] >= 0) & (idx[:, None] - idx[None, :] < W)
        scores = torch.where(mask[None, None], scores, -1e9)
        p = _softmax(scores, axis=-1)
        o = torch.einsum("bhqk,bhkd->bhqd", p, v)
        return (lambda x, *d: x.permute(*d))(o, 0, 2, 1, 3).reshape(B, S, D) @ wo

    def _B_ref(x, w_gate, w_up, w_down):
        g = torch.einsum("bsd,df->bsf", x, w_gate)
        u = torch.einsum("bsd,df->bsf", x, w_up)
        h = g * u
        return torch.einsum("bsf,fd->bsd", h, w_down)

    x = A_x
    h = x + _A_ref(_layernorm(x, axis=-1), A_wqkv, A_wo)
    h2 = h + _B_ref(_layernorm(h, axis=-1), B_w_gate, B_w_up, B_w_down)
    return h2
```

```cpp
#include <hip/hip_runtime.h>
#include <hip/hip_bf16.h>
#include <stdint.h>
typedef unsigned short u16;
static __device__ __forceinline__ float b2f(u16 v){ return __uint_as_float(((unsigned)v)<<16); }
static __device__ __forceinline__ u16 f2b(float f){ unsigned u=__float_as_uint(f); u += 0x7fffu + ((u>>16)&1u); return (u16)(u>>16); }
constexpr int S=2048,D=576,H=32,DH=18,W=256,F=4608;

__global__ void ln_k(const u16* x, u16* y){
  int row=blockIdx.x; int t=threadIdx.x;
  float v[9]; float s=0;
  for(int i=0;i<9;i++){ v[i]=b2f(x[row*D+i*64+t]); s+=v[i]; }
  for(int o=32;o;o>>=1) s+=__shfl_xor(s,o);
  float m=s/D; float q=0;
  for(int i=0;i<9;i++){ float d=v[i]-m; q+=d*d; }
  for(int o=32;o;o>>=1) q+=__shfl_xor(q,o);
  float r=rsqrtf(q/D+1e-5f);
  for(int i=0;i<9;i++) y[row*D+i*64+t]=f2b((v[i]-m)*r);
}
__global__ void gemm_k(const u16* A,const u16* B,const u16* res,u16* C,int M,int N,int K){
  __shared__ float As[16][17], Bs[16][17];
  int tx=threadIdx.x&15, ty=threadIdx.x>>4;
  int row=blockIdx.y*16+ty, col=blockIdx.x*16+tx;
  float acc=0;
  for(int k0=0;k0<K;k0+=16){
    As[ty][tx]=b2f(A[row*K+k0+tx]); Bs[ty][tx]=b2f(B[(k0+ty)*N+col]);
    __syncthreads();
    for(int k=0;k<16;k++) acc+=As[ty][k]*Bs[k][tx];
    __syncthreads();
  }
  if(res) acc = b2f(f2b(acc)) + b2f(res[row*N+col]);
  C[row*N+col]=f2b(acc);
}
__global__ void attn_k(const u16* qkv,u16* o){
  int i=blockIdx.x*64+threadIdx.x; int h=blockIdx.y;
  float q[DH]; for(int d=0;d<DH;d++) q[d]=b2f(qkv[i*3*D+h*DH+d]);
  float m=-1e30f; int j0=max(0,i-W+1);
  for(int j=j0;j<=i;j++){ float s=0; for(int d=0;d<DH;d++) s+=q[d]*b2f(qkv[j*3*D+D+h*DH+d]); s*=0.23570226f; m=fmaxf(m,s);}
  float l=0; float acc[DH]; for(int d=0;d<DH;d++) acc[d]=0;
  for(int j=j0;j<=i;j++){ float s=0; for(int d=0;d<DH;d++) s+=q[d]*b2f(qkv[j*3*D+D+h*DH+d]); s*=0.23570226f; float p=__expf(s-m); l+=p; for(int d=0;d<DH;d++) acc[d]+=p*b2f(qkv[j*3*D+2*D+h*DH+d]);}
  for(int d=0;d<DH;d++) o[i*D+h*DH+d]=f2b(acc[d]/l);
}
__global__ void mul_k(const u16* g,const u16* u,u16* h,int n){ int i=blockIdx.x*256+threadIdx.x; if(i<n) h[i]=f2b(b2f(g[i])*b2f(u[i])); }

extern "C" void kernel_launch(void* const* d_in, const int* in_sizes, int n_in,
                              void* d_out, int out_size, void* d_ws, size_t ws_size,
                              hipStream_t stream) {
  const u16* x=(const u16*)d_in[0]; const u16* wqkv=(const u16*)d_in[1]; const u16* wo=(const u16*)d_in[2];
  const u16* wg=(const u16*)d_in[4]; const u16* wu=(const u16*)d_in[5]; const u16* wd=(const u16*)d_in[6];
  u16* ws=(u16*)d_ws;
  u16* ln1=ws; u16* qkv=ln1+S*D; u16* o=qkv+S*3*D; u16* h=o+S*D; u16* ln2=h+S*D; u16* g=ln2+S*D; u16* u=g+S*F; u16* hh=u+S*F;
  ln_k<<<S,64,0,stream>>>(x,ln1);
  gemm_k<<<dim3(3*D/16,S/16),256,0,stream>>>(ln1,wqkv,nullptr,qkv,S,3*D,D);
  attn_k<<<dim3(S/64,H),64,0,stream>>>(qkv,o);
  gemm_k<<<dim3(D/16,S/16),256,0,stream>>>(o,wo,x,h,S,D,D);
  ln_k<<<S,64,0,stream>>>(h,ln2);
  gemm_k<<<dim3(F/16,S/16),256,0,stream>>>(ln2,wg,nullptr,g,S,F,D);
  gemm_k<<<dim3(F/16,S/16),256,0,stream>>>(ln2,wu,nullptr,u,S,F,D);
  mul_k<<<S*F/256,256,0,stream>>>(g,u,hh,S*F);
  gemm_k<<<dim3(D/16,S/16),256,0,stream>>>(hh,wd,h,(u16*)d_out,S,D,F);
}
```

```cpp
#include <hip/hip_runtime.h>
#include <stdint.h>

typedef short bf16x8 __attribute__((ext_vector_type(8)));
typedef short bf16x4 __attribute__((ext_vector_type(4)));
typedef float f32x2 __attribute__((ext_vector_type(2)));
typedef float f32x4 __attribute__((ext_vector_type(4)));
typedef float f32x16 __attribute__((ext_vector_type(16)));
typedef unsigned u32x2 __attribute__((ext_vector_type(2)));
typedef unsigned u32x4 __attribute__((ext_vector_type(4)));
typedef __bf16 bf2_t __attribute__((ext_vector_type(2)));
typedef __amdgpu_buffer_rsrc_t rsrc_t;
#define LAS __attribute__((address_space(3)))
#define GAS __attribute__((address_space(1)))
#define DI __device__ __forceinline__

constexpr int S_ = 2048, D_ = 576, F_ = 4608;
constexpr float QSCALE = 0.23570226039551584f * 1.4426950408889634f;
constexpr unsigned ONES2 = 0x3f803f80u;

constexpr size_t QREC = 1152, KVREC = 2688;
constexpr size_t WS_QF = 0;
constexpr size_t WS_KV = WS_QF + 32 * 64 * QREC;
constexpr size_t WS_OF = WS_KV + 32 * 64 * KVREC;
constexpr size_t WS_HF = WS_OF + 128 * 18 * 1024;
constexpr size_t WS_ST = WS_HF + 128 * 18 * 1024;
constexpr size_t WS_CS = WS_ST + 9 * 2048 * 8;
constexpr size_t WS_CNT = WS_CS + 2 * 4608 * 4;
constexpr size_t WS_RS = WS_CNT + 512 * 64;
constexpr size_t WS_HH = WS_RS + 2048 * 4;
constexpr size_t PT_REC = 4608;
constexpr size_t WS_PT = WS_HH + (size_t)128 * 144 * 1024;
constexpr size_t WS_END = WS_PT + (size_t)64 * 8 * 4 * PT_REC;

DI unsigned rfl(unsigned v) { return __builtin_amdgcn_readfirstlane(v); }
DI rsrc_t mk_rsrc(const void* p, unsigned bytes) { return __builtin_amdgcn_make_buffer_rsrc((void*)p, (short)0, (int)bytes, 0x00020000); }
DI void glds16(unsigned lds, unsigned voff, rsrc_t r, unsigned soff) {
  asm volatile("s_mov_b32 m0, %0\n\ts_nop 0\n\tbuffer_load_dwordx4 %1, %2, %3 offen lds" ::"s"(lds), "v"(voff), "s"(r), "s"(soff) : "memory", "m0");
}
DI void bload16(bf16x8& d, unsigned voff, rsrc_t r, unsigned soff) {
  asm volatile("buffer_load_dwordx4 %0, %1, %2, %3 offen" : "=v"(d) : "v"(voff), "s"(r), "s"(soff) : "memory");
}
DI void bload8(u32x2& d, unsigned voff, rsrc_t r, unsigned soff) {
  asm volatile("buffer_load_dwordx2 %0, %1, %2, %3 offen" : "=v"(d) : "v"(voff), "s"(r), "s"(soff) : "memory");
}
DI void bload4(unsigned& d, unsigned voff, rsrc_t r, unsigned soff) {
  asm volatile("buffer_load_dword %0, %1, %2, %3 offen" : "=v"(d) : "v"(voff), "s"(r), "s"(soff) : "memory");
}
template <int N> DI void wait_vm() { asm volatile("s_waitcnt vmcnt(%0)" ::"i"(N) : "memory"); }
#define PIN(x) asm volatile("" : "+v"(x))
#define SBAR() __builtin_amdgcn_sched_barrier(0)
DI void bar() { SBAR(); asm volatile("s_barrier" ::: "memory"); SBAR(); }
DI void lds_bar() { SBAR(); asm volatile("s_waitcnt lgkmcnt(0)\n\ts_barrier" ::: "memory"); SBAR(); }
DI bf16x8 trfrag(unsigned a0, unsigned a1) {
  bf16x4 lo = __builtin_amdgcn_ds_read_tr16_b64_v4i16((LAS bf16x4*)(size_t)a0);
  bf16x4 hi = __builtin_amdgcn_ds_read_tr16_b64_v4i16((LAS bf16x4*)(size_t)a1);
  return __builtin_shufflevector(lo, hi, 0, 1, 2, 3, 4, 5, 6, 7);
}
DI bf16x4 trh(unsigned a) { return __builtin_amdgcn_ds_read_tr16_b64_v4i16((LAS bf16x4*)(size_t)a); }
DI bf16x8 cat(bf16x4 lo, bf16x4 hi) { return __builtin_shufflevector(lo, hi, 0, 1, 2, 3, 4, 5, 6, 7); }
template <class T> DI T ldsr(unsigned a) { return *(LAS T*)(size_t)a; }
template <class T> DI void ldsw(unsigned a, T v) { *(LAS T*)(size_t)a = v; }
DI unsigned pk2(float a, float b) { f32x2 v = {a, b}; return __builtin_bit_cast(unsigned, __builtin_convertvector(v, bf2_t)); }
DI float bflo(unsigned u) { return __uint_as_float(u << 16); }
DI float bfhi(unsigned u) { return __uint_as_float(u & 0xffff0000u); }
DI f32x4 mfma16(bf16x8 a, bf16x8 b, f32x4 c) { return __builtin_amdgcn_mfma_f32_16x16x32_bf16(a, b, c, 0, 0, 0); }
DI void mfma16_ip(f32x4& c, bf16x8 a, bf16x8 b) { asm volatile("v_mfma_f32_16x16x32_bf16 %0, %1, %2, %0" : "+v"(c) : "v"(a), "v"(b)); }
DI void mfma16_z(f32x4& c, bf16x8 a, bf16x8 b) { asm volatile("v_mfma_f32_16x16x32_bf16 %0, %1, %2, 0" : "=&v"(c) : "v"(a), "v"(b)); }
DI f32x16 mfma32(bf16x8 a, bf16x8 b, f32x16 c) { return __builtin_amdgcn_mfma_f32_32x32x16_bf16(a, b, c, 0, 0, 0); }
DI float xor32(float v) { auto r = __builtin_amdgcn_permlane32_swap(__float_as_uint(v), __float_as_uint(v), false, false); return __uint_as_float((threadIdx.x & 32) ? r[0] : r[1]); }
DI float sum_x32(float v) { auto r = __builtin_amdgcn_permlane32_swap(__float_as_uint(v), __float_as_uint(v), false, false); return __uint_as_float(r[0]) + __uint_as_float(r[1]); }
DI float max_x32(float v) { auto r = __builtin_amdgcn_permlane32_swap(__float_as_uint(v), __float_as_uint(v), false, false); return fmaxf(__uint_as_float(r[0]), __uint_as_float(r[1])); }
DI float sum_x16(float v) { auto r = __builtin_amdgcn_permlane16_swap(__float_as_uint(v), __float_as_uint(v), false, false); return __uint_as_float(r[0]) + __uint_as_float(r[1]); }
template <int AUX> DI void bstore16(u32x4 v, rsrc_t r, unsigned voff, unsigned soff) { __builtin_amdgcn_raw_buffer_store_b128(v, r, voff, soff, AUX); }
template <int AUX> DI void bstore8(u32x2 v, rsrc_t r, unsigned voff, unsigned soff) { __builtin_amdgcn_raw_buffer_store_b64(v, r, voff, soff, AUX); }
template <int AUX> DI void bstore4(unsigned v, rsrc_t r, unsigned voff, unsigned soff) { __builtin_amdgcn_raw_buffer_store_b32(v, r, voff, soff, AUX); }

template <int... I> struct iseq {};
template <int N, int... I> struct mkseq : mkseq<N - 1, N - 1, I...> {};
template <int... I> struct mkseq<0, I...> { typedef iseq<I...> type; };
template <int V> struct ic { static constexpr int value = V; };
template <class Fn, int... I> DI void sfor_(Fn&& f, iseq<I...>) { (f(ic<I>{}), ...); }
template <int N, class Fn> DI void sfor(Fn&& f) { sfor_(f, typename mkseq<N>::type{}); }

extern __shared__ __attribute__((aligned(16))) char smem[];
#define STAMP(ws, kid, i)
#define STAMP_DECL
#define STAMP_FLUSH(ws, kid)

constexpr int K1_SLOT = 32768, K1_RING = 4, K1_TBL = K1_SLOT * K1_RING;
constexpr int K1_LDS = K1_TBL + 1024 + 1792;

template <int NN> DI void k1_body(const void* x, const void* wqkv, char* ws, int mt, int nt, int m, int lane, unsigned lds0,
                                  unsigned xv0, unsigned xv1, unsigned wv0, unsigned wv1, rsrc_t rx, rsrc_t rw, int w) {
  const int t16 = lane & 15, g = lane >> 4, q4 = (lane >> 2) & 3, p = lane & 3;
  constexpr int NT = NN == 0 ? 4 : 3;
  auto issue = [&](int slab) {
    const unsigned dst = lds0 + (slab % K1_RING) * K1_SLOT + w * 2048;
    glds16(dst, xv0, rx, slab * 128);
    glds16(dst + 16384, wv0, rw, slab * 64 * 3456);
    glds16(dst + 1024, xv1, rx, slab * 128);
    glds16(dst + 16384 + 1024, wv1, rw, slab * 64 * 3456);
  };
  STAMP_DECL; STAMP(ws, 1, 0);
  auto issue1 = [&](int slab, int i) {
    const unsigned dst = lds0 + (slab % K1_RING) * K1_SLOT + w * 2048;
    if (i == 0) glds16(dst, xv0, rx, slab * 128);
    else if (i == 1) glds16(dst + 16384, wv0, rw, slab * 64 * 3456);
    else if (i == 2) glds16(dst + 1024, xv1, rx, slab * 128);
    else glds16(dst + 16384 + 1024, wv1, rw, slab * 64 * 3456);
  };
  issue(0); issue(1);
  STAMP(ws, 1, 1);
  const unsigned xa0 = lds0 + (32 * m + t16) * 128 + 16 * (g ^ ((t16 >> 1) & 7));
  const unsigned xa1 = xa0 ^ 64;
  const unsigned o8 = (nt & 1) * 8;
  const unsigned wa = lds0 + 16384 + (4 * g + q4) * 224 + o8 + 8 * p;
  const unsigned wt = lds0 + 16384 + (4 * g + q4) * 224 + o8 + 72 * (p < 3 ? p : 2) + 32;
  f32x4 acc[4][2] = {}, cs[NT] = {};
  const bf16x8 ones = __builtin_bit_cast(bf16x8, (u32x4){ONES2, ONES2, ONES2, ONES2});
  bf16x8 fb[2][2], fa[2][NT];
  auto rd = [&](auto KS) {
    constexpr int ks = KS.value, sp = ks & 1;
    constexpr unsigned so = ((ks >> 1) % K1_RING) * K1_SLOT;
    const unsigned xa = (sp ? xa1 : xa0) + so;
    fb[sp][0] = ldsr<bf16x8>(xa); fb[sp][1] = ldsr<bf16x8>(xa + 2048);
    sfor<NT>([&](auto TI) {
      constexpr int ti = TI.value, tile = NN * 4 + ti;
      if constexpr (tile < 6) {
        constexpr unsigned co = 72 * (tile >> 1) + 40 * (tile & 1);
        fa[sp][ti] = trfrag(wa + so + sp * 32 * 224 + co, wa + so + sp * 32 * 224 + 16 * 224 + co);
      } else fa[sp][ti] = trfrag(wt + so + sp * 32 * 224, wt + so + sp * 32 * 224 + 16 * 224);
    });
  };
  sfor<9>([&](auto SL) {
    constexpr int sl = SL.value;
    constexpr int iss = sl == 0 ? 2 : (sl + 3 < 9 ? sl + 3 : 9);
    constexpr int need = sl == 0 ? 0 : (sl + 1 < 9 ? sl + 1 : 8);
    wait_vm<4 * (iss - need - 1)>();
    bar();
    if constexpr (sl == 0) STAMP(ws, 1, 2);
    if constexpr (sl == 4) STAMP(ws, 1, 3);
    if constexpr (sl <= 1) rd(ic<2 * sl>{});
    sfor<2>([&](auto SP) {
      constexpr int sp = SP.value, ks = 2 * sl + sp;
      if constexpr (ks + 1 < 18 && ks != 1) rd(ic<ks + 1>{});
      SBAR();
      if constexpr (NN == 1) fa[sp][2] = t16 >= 12 ? ones : fa[sp][2];
      sfor<4>([&](auto TI) {
        constexpr int ti = TI.value;
        if constexpr (ti < NT) {
          acc[ti][0] = mfma16(fa[sp][ti], fb[sp][0], acc[ti][0]);
          acc[ti][1] = mfma16(fa[sp][ti], fb[sp][1], acc[ti][1]);
        } else {
          acc[3][0] = mfma16(fb[sp][0], fb[sp][0], acc[3][0]);
          acc[3][1] = mfma16(fb[sp][1], fb[sp][1], acc[3][1]);
        }
        if constexpr (sl == 0) { SBAR(); issue1(2 + sp, ti); SBAR(); }
        else if constexpr (sl + 3 < 9 && ((ti + NN) & 1)) { SBAR(); issue1(sl + 3, 2 * sp + (ti >> 1)); SBAR(); }
      });
      if ((ks & 3) == m) {
        sfor<NT>([&](auto TI) { cs[TI.value] = mfma16(fa[sp][TI.value], ones, cs[TI.value]); });
      }
      SBAR();
    });
  });
  STAMP(ws, 1, 4);
  const unsigned tb = lds0 + K1_TBL, cb = tb + 1024;
  if (t16 == 0) sfor<NT>([&](auto TI) { ldsw<f32x4>(cb + (m * 112 + (NN * 4 + TI.value) * 16 + 4 * g) * 4, cs[TI.value]); });
  if constexpr (NN == 1) {
    sfor<2>([&](auto T) {
      const unsigned a = tb + (32 * m + 16 * T.value + t16) * 8;
      if (g == 3) ldsw<float>(a, acc[2][T.value][0]);
      const f32x4 gr = acc[3][T.value];
      const float d = p == 0 ? gr[0] : p == 1 ? gr[1] : p == 2 ? gr[2] : gr[3];
      if (g == (t16 >> 2)) ldsw<float>(a + 4, d);
    });
  }
  lds_bar();
  if (w < 7) {
    if (lane < 16) {
      const unsigned a = cb + (w * 16 + lane) * 4;
      ldsw<float>(a, (ldsr<float>(a) + ldsr<float>(a + 448)) + (ldsr<float>(a + 896) + ldsr<float>(a + 1344)));
    }
  } else {
#pragma unroll
    for (int i = 0; i < 2; ++i) {
      const f32x2 st = ldsr<f32x2>(tb + (64 * i + lane) * 8);
      const float mu = st[0] * (1.f / D_), r = rsqrtf(fmaxf(st[1] * (1.f / D_) - mu * mu, 0.f) + 1e-5f);
      ldsw<f32x2>(tb + (64 * i + lane) * 8, (f32x2){r, -r * mu});
    }
  }
  lds_bar();
  f32x2 st[2];
  st[0] = ldsr<f32x2>(tb + (32 * m + t16) * 8); st[1] = ldsr<f32x2>(tb + (32 * m + 16 + t16) * 8);
  const rsrc_t rq = mk_rsrc(ws + WS_QF, 32 * 64 * QREC), rkv = mk_rsrc(ws + WS_KV, 32 * 64 * KVREC);
  const int tile32 = 4 * mt + m;
  sfor<NT>([&](auto TI) {
    constexpr int ti = TI.value, tile = NN * 4 + ti;
    const f32x4 c = ldsr<f32x4>(cb + (tile * 16 + 4 * g) * 4);
    if constexpr (tile < 6) {
      const int pg = 3 * nt + (tile >> 1), type = pg >> 4, head = 2 * (pg & 15) + (tile & 1);
      const float sc = type == 0 ? QSCALE : 1.f;
      unsigned d[2][2];
      sfor<2>([&](auto T) {
        const f32x4 v = (acc[ti][T.value] * st[T.value][0] + c * st[T.value][1]) * sc;
        d[T.value][0] = pk2(v[0], v[1]);
        d[T.value][1] = pk2(v[2], v[3]);
      });
      auto s0 = __builtin_amdgcn_permlane32_swap(d[0][0], d[1][0], false, false);
      auto s1 = __builtin_amdgcn_permlane32_swap(d[0][1], d[1][1], false, false);
      const u32x4 v = {s0[0], s1[0], s0[1], s1[1]};
      const int key = 16 * (g >> 1) + t16;
      if (type == 0) bstore16<0>(v, rq, (g & 1) * 512 + key * 16, (head * 64 + tile32) * QREC);
      else if (type == 1) bstore16<0>(v, rkv, (g & 1) * 512 + key * 16, (head * 64 + tile32) * KVREC);
      else bstore16<0>(v, rkv, 1152 + key * 48 + (g & 1) * 16, (head * 64 + tile32) * KVREC);
    } else {
      const int pg = 3 * nt + (g < 3 ? g : 2), type = pg >> 4, head = 2 * (pg & 15);
      const float sc = type == 0 ? QSCALE : 1.f;
      sfor<2>([&](auto T) {
        const f32x4 v = (acc[ti][T.value] * st[T.value][0] + c * st[T.value][1]) * sc;
        const unsigned d0 = pk2(v[0], v[1]), d1 = pk2(v[2], v[3]);
        const int key = 16 * T.value + t16;
        if (g < 3) {
          if (type == 0) {
            const unsigned o = (head * 64 + tile32) * QREC + 1024 + key * 4;
            bstore4<0>(d0, rq, o, 0); bstore4<0>(d1, rq, o + 64 * QREC, 0);
          } else if (type == 1) {
            const unsigned o = (head * 64 + tile32) * KVREC + 1024 + key * 4;
            bstore4<0>(d0, rkv, o, 0); bstore4<0>(d1, rkv, o + 64 * KVREC, 0);
          } else {
            const unsigned o = (head * 64 + tile32) * KVREC + 1152 + key * 48 + 32;
            bstore8<0>((u32x2){d0, 0x00003f80u}, rkv, o, 0); bstore8<0>((u32x2){d1, 0x00003f80u}, rkv, o + 64 * KVREC, 0);
          }
        }
      });
    }
  });
  STAMP(ws, 1, 5);
  wait_vm<0>();
  STAMP(ws, 1, 6);
  STAMP_FLUSH(ws, 1);
}

__global__ __launch_bounds__(512, 2) void k1_qkv(const void* x, const void* wqkv, char* ws) {
  const int tid = threadIdx.x, lane = tid & 63, w = rfl(tid >> 6);
  const int xcd = blockIdx.x & 7, r = blockIdx.x >> 3;
  const int mt = 4 * (xcd & 3) + (r & 3), nt = 8 * (xcd >> 2) + (r >> 2);
  const unsigned lds0 = (unsigned)(size_t)smem;
  const rsrc_t rx = mk_rsrc(x, S_ * D_ * 2), rw = mk_rsrc(wqkv, D_ * 3 * D_ * 2);
  unsigned xv[2], wv[2];
#pragma unroll
  for (int i = 0; i < 2; ++i) {
    const int row = 8 * (2 * w + i) + (lane >> 3);
    xv[i] = (128 * mt + row) * 1152 + 16 * ((lane & 7) ^ ((row >> 1) & 7));
    int li = 64 * (2 * w + i) + lane;
    if (li >= 896) li -= 128;
    const int r64 = (li * 4682) >> 16, c = li - 14 * r64;
    const int rho = r64 & 31, k = (r64 & 32) + 8 * ((rho >> 2) & 3) + 4 * (rho >> 4) + (rho & 3);
    wv[i] = k * 3456 + ((216 * nt) & ~15) + 16 * c;
  }
  if (w < 4) k1_body<0>(x, wqkv, ws, mt, nt, w, lane, lds0, xv[0], xv[1], wv[0], wv[1], rx, rw, w);
  else k1_body<1>(x, wqkv, ws, mt, nt, w - 4, lane, lds0, xv[0], xv[1], wv[0], wv[1], rx, rw, w);
}

constexpr int K2_SIDE = 43008;
constexpr int K2_LDS = K2_SIDE + 73728 + 2048;

template <bool EDGE> DI void k2_body(char* ws, const void* wo, const void* wg, const void* wu, const void* wd, int xcd, int r, int head, int qb, int w, int lane, unsigned lds0) {
  const int q = lane & 31, h = lane >> 5, G = lane >> 4;
  const int qt = 8 * qb + w;
  const rsrc_t rq = mk_rsrc(ws + WS_QF, 32 * 64 * QREC), rkv = mk_rsrc(ws + WS_KV, 32 * 64 * KVREC);
  STAMP_DECL; STAMP(ws, 2, 0);
  bf16x8 qm; unsigned qtl;
  bload16(qm, lane * 16, rq, (head * 64 + qt) * QREC);
  bload4(qtl, 1024 + q * 4, rq, (head * 64 + qt) * QREC);
  if constexpr (EDGE) {
    const unsigned src = (head * 64) * KVREC;
#pragma unroll
    for (int i = 0; i < 3; ++i) { const int pc = min(w + 8 * i, 20); glds16(lds0 + 8 * KVREC + pc * 1024, lane * 16, rkv, src + pc * 1024); }
  } else {
    const unsigned src = (head * 64 + 8 * qb - 8) * KVREC;
#pragma unroll
    for (int i = 0; i < 6; ++i) { const int pc = min(w + 8 * i, 41); glds16(lds0 + pc * 1024, lane * 16, rkv, src + pc * 1024); }
  }
  STAMP(ws, 2, 1);
  asm volatile("s_waitcnt vmcnt(0)" : "+v"(qm), "+v"(qtl)::"memory");
  bar();
  STAMP(ws, 2, 2);
  const bf16x4 qt4 = __builtin_bit_cast(bf16x4, (u32x2){h ? 0u : qtl, 0u});
  const unsigned kb = lds0 + w * KVREC + lane * 16, ktb = lds0 + w * KVREC + 1024 + q * 4;
  const unsigned vb = lds0 + w * KVREC + 1152 + (4 * (G >> 1) + ((lane >> 2) & 3)) * 48 + ((G & 1) ? 32 : 8 * (lane & 3));
  f32x16 s[9];
  sfor<9>([&](auto I) {
    constexpr int i = I.value;
    const unsigned off = EDGE ? (unsigned)(max(w + i, 8) - w) * KVREC : i * KVREC;
    const bf16x8 km = ldsr<bf16x8>(kb + off);
    const unsigned kt = ldsr<unsigned>(ktb + off);
    f32x16 a = mfma32(km, qm, (f32x16){});
    s[i] = __builtin_amdgcn_mfma_f32_32x32x8bf16_1k(__builtin_bit_cast(bf16x4, (u32x2){kt, 0u}), qt4, a, 0, 0, 0);
    if (EDGE && w + i < 8) {
#pragma unroll
      for (int r = 0; r < 16; ++r) s[i][r] = -1e30f;
    }
  });
  const int qq = q - 4 * h;
#pragma unroll
  for (int r = 0; r < 16; ++r) s[8][r] = ((r & 3) + 8 * (r >> 2) <= qq) ? s[8][r] : s[0][r];
  float m4[4];
#pragma unroll
  for (int c = 0; c < 4; ++c) m4[c] = fmaxf(s[1][2 * c], s[1][2 * c + 1]);
#pragma unroll
  for (int i = 1; i < 9; ++i)
#pragma unroll
    for (int e = (i == 1 ? 4 : 0); e < 8; ++e) m4[e & 3] = fmaxf(fmaxf(m4[e & 3], s[i][2 * e]), s[i][2 * e + 1]);
  float mx = max_x32(fmaxf(fmaxf(m4[0], m4[1]), fmaxf(m4[2], m4[3])));
  STAMP(ws, 2, 3);
  f32x16 o = {};
  const rsrc_t rs1 = mk_rsrc(r < 9 ? wg : r < 18 ? wu : wo, r < 18 ? D_ * F_ * 2 : D_ * D_ * 2), rs2 = mk_rsrc(wd, F_ * D_ * 2);
  const unsigned svo = r < 18 ? (72 * w + (lane >> 3)) * 9216 + (9 * xcd + (r < 9 ? r : r - 9)) * 128 + (lane & 7) * 16 : lane * 16;
  const int pw = (r - 18) * 8 + w;
  auto side = [&](int i) {
    if (r < 18) glds16(lds0 + K2_SIDE + (w * 9 + i) * 1024, svo, rs1, i * 8 * 9216);
    else {
      if (i < 6) glds16(lds0 + K2_SIDE + w * 1024, svo, rs1, min(pw * 6 + i, 647) * 1024);
      if (i >= 3) glds16(lds0 + K2_SIDE + w * 1024, svo, rs2, min((xcd * 112 + pw) * 6 + i - 3, 5183) * 1024);
    }
  };
  side(0);
  sfor<8>([&](auto I) {
    constexpr int i = I.value + 1;
    side(i);
    const unsigned off = EDGE ? (unsigned)(max(w + i, 8) - w) * KVREC : i * KVREC;
    float pv[16];
#pragma unroll
    for (int e = 0; e < 8; ++e) {
      const f32x2 dd = (f32x2){s[i][2 * e], s[i][2 * e + 1]} - (f32x2){mx, mx};
      pv[2 * e] = __builtin_amdgcn_exp2f(dd[0]); pv[2 * e + 1] = __builtin_amdgcn_exp2f(dd[1]);
    }
    if constexpr (i < 8) {
#pragma unroll
      for (int ks = 0; ks < 2; ++ks) {
        const u32x4 pp = {pk2(pv[8 * ks], pv[8 * ks + 1]), pk2(pv[8 * ks + 2], pv[8 * ks + 3]), pk2(pv[8 * ks + 4], pv[8 * ks + 5]), pk2(pv[8 * ks + 6], pv[8 * ks + 7])};
        o = mfma32(trfrag(vb + off + ks * 768, vb + off + ks * 768 + 384), __builtin_bit_cast(bf16x8, pp), o);
      }
    } else {
      const unsigned off0 = EDGE ? (unsigned)(8 - w) * KVREC : 0;
#pragma unroll
      for (int ks = 0; ks < 2; ++ks) {
        u32x4 pd, po;
#pragma unroll
        for (int e = 0; e < 4; ++e) {
          const int r = 8 * ks + 2 * e;
          const bool v0 = ((r & 3) + 8 * (r >> 2) <= qq), v1 = (((r + 1) & 3) + 8 * ((r + 1) >> 2) <= qq);
          pd[e] = pk2(v0 ? pv[r] : 0.f, v1 ? pv[r + 1] : 0.f);
          po[e] = pk2(v0 ? 0.f : pv[r], v1 ? 0.f : pv[r + 1]);
        }
        o = mfma32(trfrag(vb + off + ks * 768, vb + off + ks * 768 + 384), __builtin_bit_cast(bf16x8, pd), o);
        o = mfma32(trfrag(vb + off0 + ks * 768, vb + off0 + ks * 768 + 384), __builtin_bit_cast(bf16x8, po), o);
      }
    }
  });
  STAMP(ws, 2, 4);
  auto lr = __builtin_amdgcn_permlane32_swap(__float_as_uint(o[10]), __float_as_uint(o[10]), false, false);
  const float inv = 1.f / __uint_as_float(lr[0]);
  const rsrc_t ro = mk_rsrc(ws + WS_OF, 128 * 18 * 1024);
  const u32x4 om = {pk2(o[0] * inv, o[1] * inv), pk2(o[2] * inv, o[3] * inv), pk2(o[4] * inv, o[5] * inv), pk2(o[6] * inv, o[7] * inv)};
  const unsigned tt = 2 * qt + (q >> 4);
  bstore16<0>(om, ro, (tt * 18 + (head >> 1)) * 1024 + ((q & 15) + 16 * (2 * (head & 1) + h)) * 16, 0);
  if (h == 0) bstore4<0>(pk2(o[8] * inv, o[9] * inv), ro, (tt * 18 + 16 + (head >> 4)) * 1024 + ((q & 15) + 16 * ((head >> 2) & 3)) * 16 + (head & 3) * 4, 0);
  STAMP(ws, 2, 5);
  if (r < 18) {
    wait_vm<2>();
    float a[8] = {};
#pragma unroll
    for (int i = 0; i < 9; ++i) {
      const u32x4 v = ldsr<u32x4>(lds0 + K2_SIDE + (w * 9 + i) * 1024 + lane * 16);
#pragma unroll
      for (int e = 0; e < 4; ++e) { a[2 * e] += bflo(v[e]); a[2 * e + 1] += bfhi(v[e]); }
    }
#pragma unroll
    for (int e = 0; e < 8; ++e) a[e] = sum_x32(sum_x16(a[e]));
    const unsigned rb = lds0 + K2_SIDE + 73728 - 2048;
    (void)rb;
    const unsigned red = lds0 + K2_SIDE + w * 9216;
    if (lane < 16) { ldsw<f32x4>(red + lane * 32, (f32x4){a[0], a[1], a[2], a[3]}); ldsw<f32x4>(red + lane * 32 + 16, (f32x4){a[4], a[5], a[6], a[7]}); }
    lds_bar();
    if (w == 0) {
      float t = 0.f;
#pragma unroll
      for (int i = 0; i < 8; ++i) t += ldsr<float>(lds0 + K2_SIDE + i * 9216 + lane * 4) + ldsr<float>(lds0 + K2_SIDE + i * 9216 + 256 + lane * 4);
      *(GAS float*)(ws + WS_CS + ((r < 9 ? 0 : 4608) + (9 * xcd + (r < 9 ? r : r - 9)) * 64 + lane) * 4) = t;
    }
  }
  wait_vm<0>();
  STAMP(ws, 2, 6);
  STAMP_FLUSH(ws, 2);
}

__global__ __launch_bounds__(512, 2) void k2_attn(char* ws, const void* wo, const void* wg, const void* wu, const void* wd) {
  const int tid = threadIdx.x, lane = tid & 63, w = rfl(tid >> 6);
  const int xcd = blockIdx.x & 7, r = blockIdx.x >> 3;
  const int head = 4 * xcd + (r & 3), qb = r >> 2;
  const unsigned lds0 = (unsigned)(size_t)smem;
  if (blockIdx.x == 0) *(GAS unsigned*)(ws + WS_CNT + tid * 64) = 0u;
  if (qb == 0) k2_body<true>(ws, wo, wg, wu, wd, xcd, r, head, qb, w, lane, lds0);
  else k2_body<false>(ws, wo, wg, wu, wd, xcd, r, head, qb, w, lane, lds0);
}

constexpr int K3_LDS = 72 * 1024;

__global__ __launch_bounds__(512, 2) void k3_wo(const void* x, const void* wo, char* ws) {
  const int tid = threadIdx.x, lane = tid & 63, w = rfl(tid >> 6);
  const unsigned lds0 = (unsigned)(size_t)smem;
  const int t16 = lane & 15, g = lane >> 4;
  const int xcd = blockIdx.x & 7, br = blockIdx.x >> 3;
  const int mt = 2 * xcd + (br >= 9), nt = br >= 9 ? br - 9 : br;
  const int T = 8 * mt + w;
  const rsrc_t rwo = mk_rsrc(wo, D_ * D_ * 2), rof = mk_rsrc(ws + WS_OF, 128 * 18 * 1024), rx = mk_rsrc(x, S_ * D_ * 2);
  const int rho = 8 * (w & 3) + (lane >> 3), rg = (rho >> 2) & 3, rj = 4 * (rho >> 4) + (rho & 3);
  const unsigned cbyte = 128 * nt + 32 * (((lane & 7) >> 1) ^ ((rho >> 1) & 3)) + 16 * (lane & 1);
  const unsigned wv = (20 * (rg >> 1) + 8 * (rg & 1) + rj) * 1152 + cbyte;
  const int th = 16 * (w >> 2) + 4 * rg + (rj >> 1);
  const unsigned wvt = (18 * th + ((th & 1) ? 0 : 16) + (rj & 1)) * 1152 + cbyte;
  STAMP_DECL; STAMP(ws, 3, 0);
  bf16x8 of[18];
  u32x2 xr[4];
  auto op = [&](auto N) {
    constexpr int n = N.value, p = n / 3;
    if constexpr (n >= 27) bload8(xr[n - 27], (16 * T + t16) * 1152 + 128 * nt + 32 * (n - 27) + 8 * g, rx, 0);
    else if constexpr (n % 3 == 0) { if constexpr (p < 8) glds16(lds0 + (w + 8 * p) * 1024, wv, rwo, (2 * p + (w >> 2)) * 36 * 1152); else glds16(lds0 + (w + 8 * p) * 1024, wvt, rwo, 0); }
    else bload16(of[2 * p + n % 3 - 1], lane * 16, rof, (T * 18 + 2 * p + n % 3 - 1) * 1024);
  };
  constexpr int PRO = 12;
  sfor<PRO>([&](auto N) { op(N); });
  STAMP(ws, 3, 1);
  unsigned wa[4];
  {
    const int r2 = 4 * g + ((lane >> 2) & 3);
#pragma unroll
    for (int c = 0; c < 4; ++c) wa[c] = lds0 + r2 * 128 + 32 * (c ^ ((r2 >> 1) & 3)) + 8 * (lane & 3);
  }
  f32x4 acc[4] = {};
  bf16x8 fr[2][4];
  auto rd = [&](auto KS) { constexpr int ks = KS.value; sfor<4>([&](auto C) { fr[ks & 1][C.value] = trfrag(wa[C.value] + ks * 4096, wa[C.value] + ks * 4096 + 2048); }); };
  sfor<9>([&](auto P) {
    constexpr int p = P.value;
    constexpr int iss = PRO + 4 * p < 31 ? PRO + 4 * p : 31;
    constexpr int need = p == 0 ? 2 : (p < 8 ? 3 * p + 3 : 26);
    asm volatile("s_waitcnt vmcnt(%2)" : "+v"(of[2 * p]), "+v"(of[2 * p + 1]) : "i"(iss - need - 1) : "memory");
    bar();
    if constexpr (p == 0) STAMP(ws, 3, 2);
    if constexpr (p == 3) STAMP(ws, 3, 3);
    if constexpr (p == 6) STAMP(ws, 3, 4);
    if constexpr (p <= 1) rd(ic<2 * p>{});
    sfor<2>([&](auto SP) {
      constexpr int ks = 2 * p + SP.value;
      if constexpr (ks + 1 < 18 && ks != 1) rd(ic<ks + 1>{});
      SBAR();
      sfor<4>([&](auto C) {
        acc[C.value] = mfma16(fr[ks & 1][C.value], of[ks], acc[C.value]);
        constexpr int n = PRO + 4 * p + 2 * SP.value + (C.value >> 1);
        if constexpr ((C.value & 1) && n < 31) { SBAR(); op(ic<n>{}); SBAR(); }
      });
      SBAR();
    });
  });
  asm volatile("s_waitcnt vmcnt(0)" : "+v"(xr[0]), "+v"(xr[1]), "+v"(xr[2]), "+v"(xr[3])::"memory");
  unsigned d[4][2];
  float s1 = 0.f, s2 = 0.f;
#pragma unroll
  for (int c = 0; c < 4; ++c) {
    d[c][0] = pk2(acc[c][0] + bflo(xr[c][0]), acc[c][1] + bfhi(xr[c][0]));
    d[c][1] = pk2(acc[c][2] + bflo(xr[c][1]), acc[c][3] + bfhi(xr[c][1]));
#pragma unroll
    for (int e = 0; e < 2; ++e) { const float a = bflo(d[c][e]), b = bfhi(d[c][e]); s1 += a + b; s2 += a * a + b * b; }
  }
  s1 = sum_x32(sum_x16(s1)); s2 = sum_x32(sum_x16(s2));
  const rsrc_t rhf = mk_rsrc(ws + WS_HF, 128 * 18 * 1024);
  bstore16<0>((u32x4){d[0][0], d[0][1], d[1][0], d[1][1]}, rhf, lane * 16, (T * 18 + 2 * nt) * 1024);
  bstore16<0>((u32x4){d[2][0], d[2][1], d[3][0], d[3][1]}, rhf, lane * 16, (T * 18 + 2 * nt + 1) * 1024);
  if (g == 0) *(GAS f32x2*)(ws + WS_ST + (nt * 2048 + 16 * T + t16) * 8) = (f32x2){s1, s2};
  STAMP(ws, 3, 5);
  wait_vm<0>();
  STAMP(ws, 3, 6);
  STAMP_FLUSH(ws, 3);
}

#define K4_HAUX 16
constexpr int K4_NS = 2, K4_R = 4, K4_P = 2;
constexpr int K4_LG = K4_R * K4_NS * 8192;
constexpr int K4_CS = K4_LG + 36864 + 8192;
constexpr int K4_STT = K4_CS + 2048;
constexpr int K4_LDS = K4_STT + 2048;
struct K4Op { int kind, idx; };
constexpr int k4_lo(int S) { return S == 0 ? K4_P * K4_NS : (S + K4_R - 1) * K4_NS; }
constexpr int k4_hi(int S) { return (S + K4_R) * K4_NS < 36 ? (S + K4_R) * K4_NS : 36; }
constexpr int k4_nops(int S) { int n = 0; for (int u = k4_lo(S); u < k4_hi(S); ++u) n += u < 18 ? 3 : 1; if (S >= 9 && S < 14) ++n; if (S >= 10 && S < 16) ++n; return n; }
constexpr K4Op k4_op(int S, int i) {
  for (int u = k4_lo(S); u < k4_hi(S); ++u) { const int c = u < 18 ? 3 : 1; if (i < c) return {i, u}; i -= c; }
  if (S >= 9 && S < 14) { if (i == 0) return {3, S - 9}; --i; }
  return {4, S - 10};
}
constexpr int k4_nst(int S) { return (S == 8 || S == 9 || S == 17) ? 2 : 0; }
constexpr int K4_PRO = 7 + 3 * K4_P * K4_NS;
constexpr int k4_issued(int S) { int n = K4_PRO; for (int s = 0; s < S; ++s) n += k4_nops(s) + k4_nst(s); return n; }
constexpr int k4_posW(int u) {
  if (u < K4_P * K4_NS) return 7 + 3 * u;
  for (int S = 0;; ++S) if (u >= k4_lo(S) && u < k4_hi(S)) { int n = k4_issued(S) + k4_nst(S); for (int v = k4_lo(S); v < u; ++v) n += v < 18 ? 3 : 1; return n; }
}
constexpr unsigned k4_so(int u) { return (((u / K4_NS) % K4_R) * K4_NS + u % K4_NS) * 8192; }

__global__ __launch_bounds__(512, 2) void k4_ffn1(const void* wg, const void* wu, const void* wd, char* ws) {
  const int tid = threadIdx.x, lane = tid & 63, w = rfl(tid >> 6);
  const int xcd = blockIdx.x & 7, r = blockIdx.x >> 3;
  const int mt = r >> 2, Qd = xcd, j = r & 3;
  const unsigned lds0 = (unsigned)(size_t)smem;
  const int t16 = lane & 15, g = lane >> 4;
  const int T0 = 16 * mt + 2 * w;
  const rsrc_t rwm = mk_rsrc(w < 4 ? wg : wu, D_ * F_ * 2), rhf = mk_rsrc(ws + WS_HF, 128 * 18 * 1024), rwd = mk_rsrc(wd, F_ * D_ * 2);
  const rsrc_t rst = mk_rsrc(ws + WS_ST, 9 * 2048 * 8), rcs = mk_rsrc(ws + WS_CS, 2 * 4608 * 4), rhh = mk_rsrc(ws + WS_HH, 128 * 144 * 1024);
  STAMP_DECL; STAMP(ws, 4, 0);
  u32x2 stv[2][3];
  sfor<2>([&](auto T) { sfor<3>([&](auto I) {
    const int n = g + 4 * I.value;
    bload8(stv[T.value][I.value], n < 9 ? (n * 2048 + 16 * (T0 + T.value) + t16) * 8 : 0x7fffff00u, rst, 0);
  }); });
  {
    const unsigned f = lane < 32 ? 576 * Qd + 128 * j + 4 * lane : 576 * Qd + 512 + 16 * j + 4 * (lane - 32);
    glds16(lds0 + K4_CS + (w & 1) * 1024, lane < 36 ? f * 4 : 0x7fffff00u, rcs, (w & 1) * 4608 * 4);
  }
  const int rho = 8 * (w & 3) + (lane >> 3);
  const unsigned wv = rho * 9216 + 1152 * Qd + 256 * j + 32 * (((lane & 7) >> 1) ^ ((rho >> 1) & 3)) + 16 * (lane & 1);
  const unsigned wdst = lds0 + w * 1024, l16 = lane * 16;
  bf16x8 act[18][2];
  auto doop = [&](auto KIND, auto IDX) {
    constexpr int kind = KIND.value, u = IDX.value;
    if constexpr (kind == 0) glds16(wdst + k4_so(u), wv, rwm, (u % 18) * 32 * 9216 + (u / 18) * 128);
    else if constexpr (kind <= 2) bload16(act[u][kind - 1], l16, rhf, (T0 * 18 + 18 * (kind - 1) + u) * 1024);
    else if constexpr (kind == 3) {
      const int kk = (w & 3) + 4 * u;
      const unsigned lv = (l16 >> 5) * 9216 + 1152 * Qd + 1024 + 32 * j + (l16 & 16);
      glds16(lds0 + K4_LG + (kk < 18 ? (w >> 2) * 18432 + kk * 1024 : 36864 + w * 1024), lv, rwm, (kk < 18 ? kk : 17) * 32 * 9216);
    } else {
      glds16(lds0 + K4_LG + 36864 + w * 1024, l16, rwd, ((xcd >> 1) * 1296 + min((r * 8 + w) * 6 + u, 1295)) * 1024);
    }
  };
  sfor<K4_P * K4_NS>([&](auto U) { doop(ic<0>{}, U); doop(ic<1>{}, U); doop(ic<2>{}, U); });
  unsigned wa[4];
  {
    const int r2 = 4 * g + ((lane >> 2) & 3);
#pragma unroll
    for (int c = 0; c < 4; ++c) wa[c] = lds0 + r2 * 128 + 32 * (c ^ ((r2 >> 1) & 3)) + 8 * (lane & 3);
  }
  f32x4 ag[4][2], au[4][2];
  bf16x4 fr[3][2][2];
  unsigned hd[2][2];
  auto rd1 = [&](auto N, auto I) {
    constexpr int n = N.value, i = I.value;
    if constexpr (n < 144) fr[n % 3][i >> 1][i & 1] = trh(wa[n % 4] + k4_so(n / 4) + 2048 * i);
    else if constexpr (n < 162) fr[n % 3][i >> 1][i & 1] = trh(lds0 + K4_LG + (l16 >> 6) * 32 + ((l16 >> 1) & 24) + (n - 144) * 1024 + (i >> 1) * 18432 + (i & 1) * 512);
  };
  auto rd = [&](auto N) { sfor<4>([&](auto I) { rd1(N, I); }); };
  auto mm = [&](auto N, auto KS, f32x4& g0, f32x4& g1, f32x4& u0, f32x4& u1) {
    constexpr int n = N.value, ks = KS.value;
    const bf16x8 fg = cat(fr[n % 3][0][0], fr[n % 3][0][1]), fu = cat(fr[n % 3][1][0], fr[n % 3][1][1]);
    bf16x4 nx[4];
    if constexpr (ks == 0) mfma16_z(g0, fg, act[ks][0]); else mfma16_ip(g0, fg, act[ks][0]);
    SBAR(); rd1(ic<n + 2>{}, ic<0>{}); SBAR();
    if constexpr (ks == 0) mfma16_z(g1, fg, act[ks][1]); else mfma16_ip(g1, fg, act[ks][1]);
    SBAR(); rd1(ic<n + 2>{}, ic<1>{}); SBAR();
    if constexpr (ks == 0) mfma16_z(u0, fu, act[ks][0]); else mfma16_ip(u0, fu, act[ks][0]);
    SBAR(); rd1(ic<n + 2>{}, ic<2>{}); SBAR();
    if constexpr (ks == 0) mfma16_z(u1, fu, act[ks][1]); else mfma16_ip(u1, fu, act[ks][1]);
    SBAR(); rd1(ic<n + 2>{}, ic<3>{}); SBAR();
    (void)nx;
  };
  auto epi = [&](const f32x4& G_, const f32x4& U_, float nmu, const f32x4& cg, const f32x4& cu, unsigned& d0, unsigned& d1) {
    const f32x4 v = (G_ + cg * nmu) * (U_ + cu * nmu);
    d0 = pk2(v[0], v[1]); d1 = pk2(v[2], v[3]);
  };
  auto epichunk = [&](auto GRP, auto C) {
    constexpr int grp = GRP.value, c = C.value;
    asm volatile("s_nop 4" : "+v"(ag[c][0]), "+v"(ag[c][1]), "+v"(au[c][0]), "+v"(au[c][1]));
    const unsigned csa = lds0 + K4_CS + ((l16 >> 4) & 48) + 256 * grp + 64 * c, sta = lds0 + K4_STT + 128 * w + ((l16 >> 2) & 60);
    const f32x4 cg = ldsr<f32x4>(csa), cu = ldsr<f32x4>(csa + 1024);
    sfor<2>([&](auto T) {
      const float nmu = ldsr<float>(sta + 64 * T.value);
      if constexpr ((c & 1) == 0) epi(ag[c][T.value], au[c][T.value], nmu, cg, cu, hd[T.value][0], hd[T.value][1]);
      else {
        unsigned d2, d3;
        epi(ag[c][T.value], au[c][T.value], nmu, cg, cu, d2, d3);
        bstore16<K4_HAUX>((u32x4){hd[T.value][0], hd[T.value][1], d2, d3}, rhh, l16, ((T0 + T.value) * 144 + 18 * Qd + 4 * j + 2 * grp + (c >> 1)) * 1024);
      }
    });
  };
  sfor<18>([&](auto SS) {
    constexpr int S = SS.value;
    constexpr int nw = k4_issued(S) - k4_posW((S + 1) * K4_NS < 36 ? (S + 1) * K4_NS : 35) - 1;
    static_assert(nw >= 0 && nw < 64, "");
    if constexpr (S < 9) asm volatile("s_waitcnt vmcnt(%4)" : "+v"(act[2 * S][0]), "+v"(act[2 * S][1]), "+v"(act[2 * S + 1][0]), "+v"(act[2 * S + 1][1]) : "i"(nw) : "memory");
    else wait_vm<nw>();
    bar();
    if constexpr (S == 0) STAMP(ws, 4, 1);
    if constexpr (S == 4) STAMP(ws, 4, 2);
    if constexpr (S == 9) STAMP(ws, 4, 3);
    if constexpr (S == 17) STAMP(ws, 4, 4);
    if constexpr (S == 0) {
      rd(ic<0>{}); rd(ic<1>{});
      sfor<2>([&](auto T) {
        PIN(stv[T.value][0]); PIN(stv[T.value][1]); PIN(stv[T.value][2]);
        float a = __uint_as_float(stv[T.value][0][0]) + __uint_as_float(stv[T.value][1][0]) + __uint_as_float(stv[T.value][2][0]);
        float b = __uint_as_float(stv[T.value][0][1]) + __uint_as_float(stv[T.value][1][1]) + __uint_as_float(stv[T.value][2][1]);
        a = sum_x32(sum_x16(a)); b = sum_x32(sum_x16(b));
        const float mu = a * (1.f / D_), var = fmaxf(b * (1.f / D_) - mu * mu, 0.f);
        if (g < 2) ldsw<float>(lds0 + K4_STT + 1024 * g + (32 * w + 16 * T.value + t16) * 4, g ? 1.f / (var + 1e-5f) : -mu);
      });
    }
    sfor<4 * K4_NS>([&](auto L) {
      constexpr int l = L.value, n = 4 * K4_NS * S + l, t = n / 4, c = n % 4, ks = t % 18;
      mm(ic<n>{}, ic<ks>{}, ag[c][0], ag[c][1], au[c][0], au[c][1]);
      constexpr int no = k4_nops(S), NL = 4 * K4_NS;
      sfor<no>([&](auto I) { if constexpr (I.value * NL / no == l) doop(ic<k4_op(S, I.value).kind>{}, ic<k4_op(S, I.value).idx>{}); });
      if constexpr (ks == 17 && c >= 1) epichunk(ic<t / 18>{}, ic<c - 1>{});
      if constexpr (t == 18 && c == 0) epichunk(ic<0>{}, ic<3>{});
      SBAR();
    });
  });
  STAMP(ws, 4, 5);
  {
    f32x4 g0, g1, u0, u1;
    sfor<18>([&](auto K) {
      constexpr int ks = K.value, n = 144 + ks;
      mm(ic<n>{}, ic<ks>{}, g0, g1, u0, u1);
      if constexpr (ks == 0) epichunk(ic<1>{}, ic<3>{});
      SBAR();
    });
    const unsigned csa = lds0 + K4_CS + ((l16 >> 4) & 48) + 512, sta = lds0 + K4_STT + 128 * w + ((l16 >> 2) & 60);
    const f32x4 cg = ldsr<f32x4>(csa), cu = ldsr<f32x4>(csa + 1024);
    unsigned a0, a1, b0, b1;
    asm volatile("s_nop 9" : "+v"(g0), "+v"(g1), "+v"(u0), "+v"(u1));
    epi(g0, u0, ldsr<float>(sta), cg, cu, a0, a1);
    epi(g1, u1, ldsr<float>(sta + 64), cg, cu, b0, b1);
    auto s0 = __builtin_amdgcn_permlane32_swap(a0, b0, false, false);
    auto s1 = __builtin_amdgcn_permlane32_swap(a1, b1, false, false);
    bstore16<K4_HAUX>((u32x4){s0[0], s1[0], s0[1], s1[1]}, rhh, (l16 >> 9) * 144 * 1024 + (l16 & 511) + 512 * (j & 1), (T0 * 144 + 18 * Qd + 16 + (j >> 1)) * 1024);
    if (Qd == 0 && j == 0 && l16 < 512) *(GAS float*)(ws + WS_RS + (16 * T0) * 4 + (l16 >> 2)) = ldsr<float>(lds0 + K4_STT + 1024 + 128 * w + (l16 >> 2));
    STAMP(ws, 4, 6);
    wait_vm<0>();
    STAMP(ws, 4, 7);
    STAMP_FLUSH(ws, 4);
  }
}

constexpr int K5_SLOT = 17408, K5_R = 8, K5_P = 4;
constexpr int K5_LDS = K5_SLOT * K5_R;
constexpr int k5_E(int i) { int e = 36; if (K5_P + 2 * (i + 1) < e) e = K5_P + 2 * (i + 1); if (i >= 0 && i + K5_R < e) e = i + K5_R; return e; }

__global__ __launch_bounds__(512, 2) void k5_ffn2(const void* wd, char* ws, void* out) {
  const int tid = threadIdx.x, lane = tid & 63, w = rfl(tid >> 6);
  const int xcd = blockIdx.x & 7, r = blockIdx.x >> 3;
  const int sg = xcd >> 1, mt = 8 * (xcd & 1) + (r >> 2), nt = r & 3;
  const unsigned lds0 = (unsigned)(size_t)smem;
  const int t16 = lane & 15, g = lane >> 4;
  const int tile = 4 * mt + nt;
  const rsrc_t rpt = mk_rsrc(ws + WS_PT, 64 * 8 * 4 * PT_REC);
  STAMP_DECL; STAMP(ws, 5, 0);
  if (w >= 4) {
    const int l = w - 4;
    const rsrc_t rwd = mk_rsrc(wd, F_ * D_ * 2), rhh = mk_rsrc(ws + WS_HH, 128 * 144 * 1024);
    unsigned mv[2][2], tv[2];
#pragma unroll
    for (int sp = 0; sp < 2; ++sp) {
#pragma unroll
      for (int i = 0; i < 2; ++i) {
        const int rho = 4 * (2 * l + i) + g, rr = sp ? ((rho & 7) | ((rho & 8) << 1) | ((rho & 16) >> 1)) : rho;
        mv[sp][i] = rr * 1152 + 256 * nt + 32 * ((t16 >> 1) ^ (rho & 7)) + 16 * (t16 & 1);
      }
      const int rho = lane >> 1, rr = sp ? ((rho & 7) | ((rho & 8) << 1) | ((rho & 16) >> 1)) : rho;
      tv[sp] = rr * 1152 + 1024 + 32 * nt + 16 * (lane & 1);
    }
    const unsigned hs = ((8 * mt + 2 * l) * 144 + 36 * sg) * 1024, wsb = sg * 1152 * 1152, dl = lds0 + l * 2048;
    auto issue = [&](auto U) {
      constexpr int u = U.value, sp = (u % 18) >= 16;
      constexpr unsigned so = (u % K5_R) * K5_SLOT;
      glds16(dl + so + 8192, mv[sp][0], rwd, wsb + u * 32 * 1152);
      glds16(dl + so, lane * 16, rhh, hs + u * 1024);
      glds16(dl + so + 8192 + 1024, mv[sp][1], rwd, wsb + u * 32 * 1152);
      glds16(dl + so + 1024, lane * 16, rhh, hs + (144 + u) * 1024);
      if ((u & 3) == l) glds16(lds0 + so + 16384, tv[sp], rwd, wsb + u * 32 * 1152);
    };
    sfor<K5_P>([&](auto U) { issue(U); });
    sfor<36>([&](auto I) {
      constexpr int i = I.value, y = k5_E(i - 1) - (i + 2 < 36 ? i + 2 : 36);
      wait_vm<4 * y + y / 4>();
      bar();
      if constexpr (i == 0) STAMP(ws, 5, 1);
      if constexpr (i == 18) STAMP(ws, 5, 2);
      sfor<k5_E(i) - k5_E(i - 1)>([&](auto U) { issue(ic<k5_E(i - 1) + U.value>{}); });
    });
    bar();
    STAMP(ws, 5, 3);
  } else {
    const int r2 = 4 * g + ((lane >> 2) & 3);
    unsigned wa[8];
#pragma unroll
    for (int c = 0; c < 8; ++c) wa[c] = lds0 + 8192 + r2 * 256 + 32 * (c ^ (r2 & 7)) + 8 * (lane & 3);
    const unsigned wt = lds0 + 16384 + r2 * 32 + 8 * (lane & 3), ha = lds0 + w * 2048 + lane * 16;
    f32x4 acc[9][2] = {};
    bf16x4 fr[3][3][2];
    bf16x8 hb[2][2];
    auto rd1 = [&](auto N, auto I) {
      constexpr int n = N.value, u = n / 3, i = I.value;
      if constexpr (u < 36) {
        constexpr unsigned so = (u % K5_R) * K5_SLOT;
        constexpr int c = 3 * (n % 3) + i / 2;
        if constexpr (i >= 6) hb[u & 1][i - 6] = ldsr<bf16x8>(ha + so + (i - 6) * 1024);
        else if constexpr (c < 8) fr[n % 3][i / 2][i & 1] = trh(wa[c] + so + (i & 1) * 4096);
        else fr[n % 3][i / 2][i & 1] = trh(wt + so + (i & 1) * 512);
      }
    };
    sfor<36>([&](auto U) {
      constexpr int u = U.value;
      bar();
      if constexpr (u == 0) STAMP(ws, 5, 1);
      if constexpr (u == 18) STAMP(ws, 5, 2);
      if constexpr (u == 0) { rd1(ic<0>{}, ic<6>{}); rd1(ic<0>{}, ic<7>{}); sfor<6>([&](auto I) { rd1(ic<0>{}, I); }); sfor<6>([&](auto I) { rd1(ic<1>{}, I); }); }
      sfor<18>([&](auto M) {
        constexpr int mi = M.value, v = mi / 6, n = 3 * u + v, c = mi / 2;
        acc[c][mi & 1] = mfma16(cat(fr[n % 3][c % 3][0], fr[n % 3][c % 3][1]), hb[u & 1][mi & 1], acc[c][mi & 1]);
        SBAR();
        if constexpr (mi == 6 || mi == 8) rd1(ic<n + 2>{}, ic<6 + (mi - 6) / 2>{});
        rd1(ic<n + 2>{}, ic<mi % 6>{});
        SBAR();
      });
    });
    bar();
    STAMP(ws, 5, 3);
    if (w == sg) {
      sfor<2>([&](auto T) { sfor<9>([&](auto C) { ldsw<f32x4>(lds0 + (T.value * 9 + C.value) * 1024 + lane * 16, acc[C.value][T.value]); }); });
    } else {
      sfor<2>([&](auto T) {
        const unsigned pb = (((tile * 4 + w) * 4 + sg) * 2 + T.value) * PT_REC;
        sfor<4>([&](auto I) {
          constexpr int i = I.value;
          const f32x4 a = acc[2 * i][T.value], b = acc[2 * i + 1][T.value];
          bstore16<16>((u32x4){pk2(a[0], a[1]), pk2(a[2], a[3]), pk2(b[0], b[1]), pk2(b[2], b[3])}, rpt, lane * 16 + i * 1024, pb);
        });
        const f32x4 a = acc[8][T.value];
        bstore8<16>((u32x2){pk2(a[0], a[1]), pk2(a[2], a[3])}, rpt, lane * 8 + 4096, pb);
      });
    }
  }
  const int T = w >> 2, pi = w & 3, tt = 8 * mt + 2 * sg + T;
  const rsrc_t rhf = mk_rsrc(ws + WS_HF, 128 * 18 * 1024);
  const u32x4 hv = __builtin_amdgcn_raw_buffer_load_b128(rhf, lane * 16, (tt * 18 + 4 * nt + pi) * 1024, 0);
  const u32x2 hl = __builtin_amdgcn_raw_buffer_load_b64(rhf, lane * 16 + 8 * (nt & 1), (tt * 18 + 16 + (nt >> 1)) * 1024, 0);
  const float rs2 = *(const GAS float*)(ws + WS_RS + (16 * tt + t16) * 4);
  STAMP(ws, 5, 4);
  asm volatile("s_waitcnt vmcnt(0) lgkmcnt(0)" ::: "memory");
  STAMP(ws, 5, 5);
  bar();
  if (w == 4) {
    GAS unsigned* cnt = (GAS unsigned*)(ws + WS_CNT + tile * 64);
    if (lane == 0) __hip_atomic_fetch_add(cnt, 1u, __ATOMIC_RELAXED, __HIP_MEMORY_SCOPE_AGENT);
    for (int spin = 0; spin < (1 << 22); ++spin) {
      if (rfl(__hip_atomic_load(cnt, __ATOMIC_RELAXED, __HIP_MEMORY_SCOPE_AGENT)) >= 4u) break;
      __builtin_amdgcn_s_sleep(1);
    }
  }
  bar();
  STAMP(ws, 5, 6);
  u32x4 pv[3];
  u32x2 pl[3];
#pragma unroll
  for (int o = 0; o < 3; ++o) {
    const unsigned so = (((tile * 4 + sg) * 4 + ((sg + 1 + o) & 3)) * 2 + T) * PT_REC;
    pv[o] = __builtin_amdgcn_raw_buffer_load_b128(rpt, lane * 16 + pi * 1024, so, 16);
    if (pi == 0) pl[o] = __builtin_amdgcn_raw_buffer_load_b64(rpt, lane * 8 + 4096, so, 16);
  }
  const rsrc_t rout = mk_rsrc(out, S_ * D_ * 2);
  auto fin = [&](const f32x4& own, unsigned a0, unsigned a1, unsigned b0, unsigned b1, unsigned c0, unsigned c1, unsigned h0, unsigned h1, unsigned& d0, unsigned& d1) {
    const float v0 = ((own[0] + bflo(b0)) + (bflo(a0) + bflo(c0))) * rs2 + bflo(h0);
    const float v1 = ((own[1] + bfhi(b0)) + (bfhi(a0) + bfhi(c0))) * rs2 + bfhi(h0);
    const float v2 = ((own[2] + bflo(b1)) + (bflo(a1) + bflo(c1))) * rs2 + bflo(h1);
    const float v3 = ((own[3] + bfhi(b1)) + (bfhi(a1) + bfhi(c1))) * rs2 + bfhi(h1);
    d0 = pk2(v0, v1); d1 = pk2(v2, v3);
  };
  const unsigned orow = (16 * tt + t16) * 1152, own = lds0 + T * 9216 + lane * 16;
  {
    unsigned a0, a1, b0, b1;
    fin(ldsr<f32x4>(own + pi * 2048), pv[0][0], pv[0][1], pv[1][0], pv[1][1], pv[2][0], pv[2][1], hv[0], hv[1], a0, a1);
    fin(ldsr<f32x4>(own + pi * 2048 + 1024), pv[0][2], pv[0][3], pv[1][2], pv[1][3], pv[2][2], pv[2][3], hv[2], hv[3], b0, b1);
    auto s0 = __builtin_amdgcn_permlane16_swap(a0, b0, false, false);
    auto s1 = __builtin_amdgcn_permlane16_swap(a1, b1, false, false);
    bstore16<0>((u32x4){s0[0], s1[0], s0[1], s1[1]}, rout, orow + 256 * nt + 64 * pi + 32 * (g & 1) + 16 * (g >> 1), 0);
  }
  if (pi == 0) {
    unsigned e0, e1;
    fin(ldsr<f32x4>(own + 8192), pl[0][0], pl[0][1], pl[1][0], pl[1][1], pl[2][0], pl[2][1], hl[0], hl[1], e0, e1);
    bstore8<0>((u32x2){e0, e1}, rout, orow + 1024 + 32 * nt + 8 * g, 0);
  }
  STAMP(ws, 5, 7);
  STAMP_FLUSH(ws, 5);
}

extern "C" void kernel_launch(void* const* d_in, const int* in_sizes, int n_in, void* d_out, int out_size, void* d_ws, size_t ws_size,
                              hipStream_t stream) {
  const void *x = d_in[0], *wqkv = d_in[1], *wo = d_in[2], *wg = d_in[4], *wu = d_in[5], *wd = d_in[6];
  char* ws = (char*)d_ws;
  (void)hipFuncSetAttribute((const void*)k1_qkv, hipFuncAttributeMaxDynamicSharedMemorySize, K1_LDS);
  (void)hipFuncSetAttribute((const void*)k2_attn, hipFuncAttributeMaxDynamicSharedMemorySize, K2_LDS);
  (void)hipFuncSetAttribute((const void*)k3_wo, hipFuncAttributeMaxDynamicSharedMemorySize, K3_LDS);
  (void)hipFuncSetAttribute((const void*)k4_ffn1, hipFuncAttributeMaxDynamicSharedMemorySize, K4_LDS);
  (void)hipFuncSetAttribute((const void*)k5_ffn2, hipFuncAttributeMaxDynamicSharedMemorySize, K5_LDS);
  k1_qkv<<<256, 512, K1_LDS, stream>>>(x, wqkv, ws);
  k2_attn<<<256, 512, K2_LDS, stream>>>(ws, wo, wg, wu, wd);
  k3_wo<<<144, 512, K3_LDS, stream>>>(x, wo, ws);
  k4_ffn1<<<256, 512, K4_LDS, stream>>>(wg, wu, wd, ws);
  k5_ffn2<<<256, 512, K5_LDS, stream>>>(wd, ws, d_out);
}
```

```cpp
#include <hip/hip_runtime.h>
#include <stdint.h>

typedef short bf16x8 __attribute__((ext_vector_type(8)));
typedef short bf16x4 __attribute__((ext_vector_type(4)));
typedef float f32x2 __attribute__((ext_vector_type(2)));
typedef float f32x4 __attribute__((ext_vector_type(4)));
typedef float f32x16 __attribute__((ext_vector_type(16)));
typedef unsigned u32x2 __attribute__((ext_vector_type(2)));
typedef unsigned u32x4 __attribute__((ext_vector_type(4)));
typedef __bf16 bf2_t __attribute__((ext_vector_type(2)));
typedef __amdgpu_buffer_rsrc_t rsrc_t;
#define LAS __attribute__((address_space(3)))
#define GAS __attribute__((address_space(1)))
#define DI __device__ __forceinline__

constexpr int S_ = 2048, D_ = 576, F_ = 4608;
constexpr float QSCALE = 0.23570226039551584f * 1.4426950408889634f;
constexpr unsigned ONES2 = 0x3f803f80u;

constexpr size_t QREC = 1280, KVREC = 2816;
constexpr unsigned OOB = 0x7fffff00u;
constexpr size_t WS_QF = 0;
constexpr size_t WS_KV = WS_QF + 32 * 64 * QREC;
constexpr size_t WS_OF = WS_KV + 32 * 64 * KVREC;
constexpr size_t WS_HF = WS_OF + 128 * 18 * 1024;
constexpr size_t WS_ST = WS_HF + 128 * 18 * 1024;
constexpr size_t WS_CS = WS_ST + 9 * 2048 * 8;
constexpr size_t WS_CNT = WS_CS + 2 * 4608 * 4;
constexpr size_t WS_RS = WS_CNT + 512 * 64;
constexpr size_t WS_LG = WS_RS + 2048 * 4;
constexpr size_t WS_WT = WS_LG + 8 * 4 * 36864;
constexpr size_t WS_HH = WS_WT + 4 * 4608 * 32;
constexpr size_t PT_REC = 4608;
constexpr size_t WS_PT = WS_HH + (size_t)128 * 144 * 1024;
constexpr size_t WS_END = WS_PT + (size_t)64 * 8 * 4 * PT_REC;

DI unsigned rfl(unsigned v) { return __builtin_amdgcn_readfirstlane(v); }
DI rsrc_t mk_rsrc(const void* p, unsigned bytes) { return __builtin_amdgcn_make_buffer_rsrc((void*)p, (short)0, (int)bytes, 0x00020000); }
DI void glds16(unsigned lds, unsigned voff, rsrc_t r, unsigned soff) {
  asm volatile("s_mov_b32 m0, %0\n\ts_nop 0\n\tbuffer_load_dwordx4 %1, %2, %3 offen lds" ::"s"(lds), "v"(voff), "s"(r), "s"(soff) : "memory", "m0");
}
DI void bload16(bf16x8& d, unsigned voff, rsrc_t r, unsigned soff) {
  asm volatile("buffer_load_dwordx4 %0, %1, %2, %3 offen" : "=v"(d) : "v"(voff), "s"(r), "s"(soff) : "memory");
}
DI void bload8(u32x2& d, unsigned voff, rsrc_t r, unsigned soff) {
  asm volatile("buffer_load_dwordx2 %0, %1, %2, %3 offen" : "=v"(d) : "v"(voff), "s"(r), "s"(soff) : "memory");
}
DI void bload4(unsigned& d, unsigned voff, rsrc_t r, unsigned soff) {
  asm volatile("buffer_load_dword %0, %1, %2, %3 offen" : "=v"(d) : "v"(voff), "s"(r), "s"(soff) : "memory");
}
DI void warm(unsigned lds, unsigned voff, rsrc_t r, unsigned soff) {
  asm volatile("s_mov_b32 m0, %0\n\ts_nop 0\n\tbuffer_load_dword %1, %2, %3 offen lds" ::"s"(lds), "v"(voff), "s"(r), "s"(soff) : "memory", "m0");
}
template <int N> DI void wait_vm() { asm volatile("s_waitcnt vmcnt(%0)" ::"i"(N) : "memory"); }
#define PIN(x) asm volatile("" : "+v"(x))
#define SBAR() __builtin_amdgcn_sched_barrier(0)
DI void bar() { SBAR(); asm volatile("s_barrier" ::: "memory"); SBAR(); }
DI void lds_bar() { SBAR(); asm volatile("s_waitcnt lgkmcnt(0)\n\ts_barrier" ::: "memory"); SBAR(); }
DI bf16x8 trfrag(unsigned a0, unsigned a1) {
  bf16x4 lo = __builtin_amdgcn_ds_read_tr16_b64_v4i16((LAS bf16x4*)(size_t)a0);
  bf16x4 hi = __builtin_amdgcn_ds_read_tr16_b64_v4i16((LAS bf16x4*)(size_t)a1);
  return __builtin_shufflevector(lo, hi, 0, 1, 2, 3, 4, 5, 6, 7);
}
DI bf16x4 trh(unsigned a) { return __builtin_amdgcn_ds_read_tr16_b64_v4i16((LAS bf16x4*)(size_t)a); }
DI bf16x8 cat(bf16x4 lo, bf16x4 hi) { return __builtin_shufflevector(lo, hi, 0, 1, 2, 3, 4, 5, 6, 7); }
template <class T> DI T ldsr(unsigned a) { return *(LAS T*)(size_t)a; }
template <class T> DI void ldsw(unsigned a, T v) { *(LAS T*)(size_t)a = v; }
DI unsigned pk2(float a, float b) { f32x2 v = {a, b}; return __builtin_bit_cast(unsigned, __builtin_convertvector(v, bf2_t)); }
DI float bflo(unsigned u) { return __uint_as_float(u << 16); }
DI float bfhi(unsigned u) { return __uint_as_float(u & 0xffff0000u); }
DI f32x4 mfma16(bf16x8 a, bf16x8 b, f32x4 c) { return __builtin_amdgcn_mfma_f32_16x16x32_bf16(a, b, c, 0, 0, 0); }
DI void mfma16_ip(f32x4& c, bf16x8 a, bf16x8 b) { asm volatile("v_mfma_f32_16x16x32_bf16 %0, %1, %2, %0" : "+v"(c) : "v"(a), "v"(b)); }
DI void mfma16_z(f32x4& c, bf16x8 a, bf16x8 b) { asm volatile("v_mfma_f32_16x16x32_bf16 %0, %1, %2, 0" : "=&v"(c) : "v"(a), "v"(b)); }
DI f32x16 mfma32(bf16x8 a, bf16x8 b, f32x16 c) { return __builtin_amdgcn_mfma_f32_32x32x16_bf16(a, b, c, 0, 0, 0); }
DI float xor32(float v) { auto r = __builtin_amdgcn_permlane32_swap(__float_as_uint(v), __float_as_uint(v), false, false); return __uint_as_float((threadIdx.x & 32) ? r[0] : r[1]); }
DI float sum_x32(float v) { auto r = __builtin_amdgcn_permlane32_swap(__float_as_uint(v), __float_as_uint(v), false, false); return __uint_as_float(r[0]) + __uint_as_float(r[1]); }
DI float max_x32(float v) { auto r = __builtin_amdgcn_permlane32_swap(__float_as_uint(v), __float_as_uint(v), false, false); return fmaxf(__uint_as_float(r[0]), __uint_as_float(r[1])); }
DI float sum_x16(float v) { auto r = __builtin_amdgcn_permlane16_swap(__float_as_uint(v), __float_as_uint(v), false, false); return __uint_as_float(r[0]) + __uint_as_float(r[1]); }
template <int AUX> DI void bstore16(u32x4 v, rsrc_t r, unsigned voff, unsigned soff) {
  if constexpr (AUX == 16) asm volatile("buffer_store_dwordx4 %0, %1, %2, %3 offen sc1\n\ts_nop 1" ::"v"(v), "v"(voff), "s"(r), "s"(soff) : "memory");
  else asm volatile("buffer_store_dwordx4 %0, %1, %2, %3 offen\n\ts_nop 1" ::"v"(v), "v"(voff), "s"(r), "s"(soff) : "memory");
}
template <int AUX> DI void bstore8(u32x2 v, rsrc_t r, unsigned voff, unsigned soff) { __builtin_amdgcn_raw_buffer_store_b64(v, r, voff, soff, AUX); }
template <int AUX> DI void bstore4(unsigned v, rsrc_t r, unsigned voff, unsigned soff) { __builtin_amdgcn_raw_buffer_store_b32(v, r, voff, soff, AUX); }

template <int... I> struct iseq {};
template <int N, int... I> struct mkseq : mkseq<N - 1, N - 1, I...> {};
template <int... I> struct mkseq<0, I...> { typedef iseq<I...> type; };
template <int V> struct ic { static constexpr int value = V; };
template <class Fn, int... I> DI void sfor_(Fn&& f, iseq<I...>) { (f(ic<I>{}), ...); }
template <int N, class Fn> DI void sfor(Fn&& f) { sfor_(f, typename mkseq<N>::type{}); }

extern __shared__ __attribute__((aligned(16))) char smem[];
#define K1_AUX 0
#define STAMP(ws, kid, i)
#define STAMP_DECL
#define STAMP_FLUSH(ws, kid)

constexpr int K1_SLOT = 32768, K1_TBL = K1_SLOT * 4;
constexpr int K1_LDS = K1_TBL + 1024 + 512 + 64 + 256;
extern "C" __global__ void k9_end();

template <int NN> DI void k1_body(char* ws, rsrc_t rx, rsrc_t rw, int mt, int nt, int w, int lane, unsigned lds0) {
  const int t16 = lane & 15, g = lane >> 4, q4 = (lane >> 2) & 3, p = lane & 3, m = w & 3;
  constexpr int NT = NN ? 3 : 4;
  unsigned xv[2], wv[2];
#pragma unroll
  for (int i = 0; i < 2; ++i) {
    const int row = 8 * (2 * w + i) + (lane >> 3);
    xv[i] = (128 * mt + row) * 1152 + 16 * ((lane & 7) ^ ((row >> 1) & 7));
    int li = 64 * (2 * w + i) + lane;
    if (li >= 896) li -= 128;
    const int r64 = (li * 4682) >> 16, c = li - 14 * r64;
    const int rho = r64 & 31, k = (r64 & 32) + 8 * ((rho >> 2) & 3) + 4 * (rho >> 4) + (rho & 3);
    wv[i] = k * 3456 + ((216 * nt) & ~15) + 16 * c;
  }
  STAMP_DECL; STAMP(ws, 1, 0);
  auto issue1 = [&](int slab, int i) {
    const unsigned dst = lds0 + (slab & 3) * K1_SLOT + w * 2048 + (i >> 1) * 1024;
    if (i & 1) glds16(dst + 16384, wv[i >> 1], rw, slab * 64 * 3456);
    else glds16(dst, xv[i >> 1], rx, slab * 128);
  };
#pragma unroll
  for (int sl = 0; sl < 2; ++sl)
#pragma unroll
    for (int i = 0; i < 4; ++i) issue1(sl, i);
  STAMP(ws, 1, 1);
  const unsigned wa = lds0 + 16384 + (4 * g + q4) * 224 + (nt & 1) * 8;
  unsigned ad[2][5];
  ad[0][0] = wa + 8 * p + 144 * NN;
  ad[0][1] = wa + 72 * (p < 3 ? p : 2) + 32;
  ad[0][2] = NN && m >= 2 ? ad[0][1] : ad[0][0] + 72 * (m >> 1) + 40 * (m & 1);
  ad[0][3] = lds0 + (32 * m + t16) * 128 + 16 * (g ^ ((t16 >> 1) & 7));
  ad[0][4] = ad[0][3] ^ 64;
#pragma unroll
  for (int i = 0; i < 5; ++i) { ad[1][i] = ad[0][i] + 2 * K1_SLOT; PIN(ad[1][i]); }
  f32x4 acc[4][2] = {}, cs = {};
  bf16x8 ones = __builtin_bit_cast(bf16x8, (u32x4){ONES2, ONES2, ONES2, ONES2});
  PIN(ones);
  bf16x8 fb[2][2];
  bf16x4 fa[2][NT + 1][2];
  constexpr int NR = 2 * NT + 4;
  auto rd1 = [&](auto KS, auto I) {
    constexpr int ks = KS.value, i = I.value, sp = ks & 1, sl = (ks >> 1) & 3, ti = (i - 2) >> 1;
    constexpr unsigned so = (sl & 1) * K1_SLOT;
    if constexpr (ks < 18 && i < NR) {
      if constexpr (i < 2) fb[sp][i] = ldsr<bf16x8>(ad[sl >> 1][3 + sp] + so + i * 2048);
      else {
        constexpr int ai = ti == NT ? 2 : (NN && ti == 2) ? 1 : 0;
        constexpr unsigned co = ai ? 0 : 72 * (ti >> 1) + 40 * (ti & 1);
        fa[sp][ti][i & 1] = trh(ad[sl >> 1][ai] + so + co + sp * 32 * 224 + (i & 1) * 16 * 224);
      }
    }
  };
  sfor<9>([&](auto SL) {
    constexpr int sl = SL.value;
    constexpr int iss = sl == 0 ? 2 : (sl + 3 < 9 ? sl + 3 : 9);
    constexpr int need = sl == 0 ? 0 : (sl + 1 < 9 ? sl + 1 : 8);
    wait_vm<4 * (iss - need - 1)>();
    bar();
    if constexpr (sl == 0) STAMP(ws, 1, 2);
    if constexpr (sl == 4) STAMP(ws, 1, 3);
    if constexpr (sl <= 1) sfor<NR>([&](auto I) { rd1(ic<2 * sl>{}, I); });
    sfor<2>([&](auto SP) {
      constexpr int sp = SP.value, ks = 2 * sl + sp;
      bf16x8 tl;
      if constexpr (NN) tl = t16 >= 12 ? ones : cat(fa[sp][2][0], fa[sp][2][1]);
      sfor<9>([&](auto M) {
        constexpr int mi = M.value, ti = mi >> 1;
        if constexpr (mi == 8) cs = mfma16(cat(fa[sp][NT][0], fa[sp][NT][1]), ones, cs);
        else if constexpr (NN && ti == 3) acc[3][mi & 1] = mfma16(fb[sp][mi & 1], fb[sp][mi & 1], acc[3][mi & 1]);
        else if constexpr (NN && ti == 2) acc[2][mi & 1] = mfma16(tl, fb[sp][mi & 1], acc[2][mi & 1]);
        else acc[ti][mi & 1] = mfma16(cat(fa[sp][ti][0], fa[sp][ti][1]), fb[sp][mi & 1], acc[ti][mi & 1]);
        SBAR();
        if constexpr (ks != 1) {
          if constexpr (mi < 3) { rd1(ic<ks + 1>{}, ic<2 * mi>{}); rd1(ic<ks + 1>{}, ic<2 * mi + 1>{}); }
          else rd1(ic<ks + 1>{}, ic<3 + mi>{});
        }
        if constexpr (sl == 0) { if constexpr (mi & 1) issue1(2 + sp, mi >> 1); }
        else if constexpr (sl + 3 < 9 && (mi == 2 + 2 * NN || mi == 6 + 2 * NN)) issue1(sl + 3, 2 * sp + (mi >= 6));
        SBAR();
      });
    });
  });
  STAMP(ws, 1, 4);
  const unsigned tb = lds0 + K1_TBL, cb = tb + 1024, dummy = cb + 512;
  ldsw<f32x4>(t16 == 0 ? cb + (w * 16 + 4 * g) * 4 : dummy, cs);
  if constexpr (NN) sfor<2>([&](auto T) {
    const unsigned a = tb + (32 * m + 16 * T.value + t16) * 8;
    const f32x4 gr = acc[3][T.value];
    const float d01 = (p & 1) ? gr[1] : gr[0], d23 = (p & 1) ? gr[3] : gr[2];
    ldsw<float>(g == 3 ? a : dummy, acc[2][T.value][0]);
    ldsw<float>(g == q4 ? a + 4 : dummy, (p & 2) ? d23 : d01);
  });
  lds_bar();
  f32x2 st[2];
  sfor<2>([&](auto T) {
    const f32x2 v = ldsr<f32x2>(tb + (32 * m + 16 * T.value + t16) * 8);
    const float mu = v[0] * (1.f / D_), rs = __builtin_amdgcn_rsqf(fmaxf(v[1] * (1.f / D_) - mu * mu, 0.f) + 1e-5f);
    st[T.value] = (f32x2){rs, -rs * mu};
  });
  const rsrc_t ro = mk_rsrc(ws + WS_QF, 32 * 64 * (QREC + KVREC));
  const int tile32 = 4 * mt + m, key = 16 * (g >> 1) + t16;
  const unsigned lo_qk = (g & 1) * 512 + key * 16, lo_v = 1280 + key * 48 + (g & 1) * 16;
  sfor<(NN ? 2 : 4)>([&](auto TI) {
    constexpr int ti = TI.value, tile = 4 * NN + ti;
    const int pg = 3 * nt + (tile >> 1), type = pg >> 4, rec = (2 * (pg & 15) + (tile & 1)) * 64 + tile32;
    const f32x4 c = ldsr<f32x4>(cb + (tile * 16 + 4 * g) * 4);
    const float sc = type == 0 ? QSCALE : 1.f;
    const f32x4 v0 = acc[ti][0] * (st[0][0] * sc) + c * (st[0][1] * sc), v1 = acc[ti][1] * (st[1][0] * sc) + c * (st[1][1] * sc);
    auto s0 = __builtin_amdgcn_permlane32_swap(pk2(v0[0], v0[1]), pk2(v1[0], v1[1]), false, false);
    auto s1 = __builtin_amdgcn_permlane32_swap(pk2(v0[2], v0[3]), pk2(v1[2], v1[3]), false, false);
    bstore16<K1_AUX>((u32x4){s0[0], s1[0], s0[1], s1[1]}, ro, type == 2 ? lo_v : lo_qk, type == 0 ? rec * QREC : WS_KV + rec * KVREC);
  });
  if constexpr (NN) {
    const int pg = 3 * nt + g, type = pg >> 4, rec = 2 * (pg & 15) * 64 + tile32;
    const f32x4 c = ldsr<f32x4>(cb + (96 + 4 * g) * 4);
    const float sc = type == 0 ? QSCALE : 1.f;
    const unsigned rb = type == 0 ? rec * QREC : WS_KV + rec * KVREC, hs = type == 0 ? 64 * QREC : 64 * KVREC;
    const unsigned oq = g < 3 && type < 2 ? rb + 1024 + t16 * 8 : OOB, ov = g < 3 && type == 2 ? rb + 1280 + 32 + t16 * 48 : OOB;
    sfor<2>([&](auto T) {
      const f32x4 v = acc[2][T.value] * (st[T.value][0] * sc) + c * (st[T.value][1] * sc);
      const unsigned d0 = pk2(v[0], v[1]), d1 = pk2(v[2], v[3]);
      if (nt <= 10) { bstore8<K1_AUX>((u32x2){d0, 0u}, ro, oq + T.value * 128, 0); bstore8<K1_AUX>((u32x2){d1, 0u}, ro, oq + hs + T.value * 128, 0); }
      if (nt >= 10) { bstore16<K1_AUX>((u32x4){d0, 0x00003f80u, 0u, 0u}, ro, ov, T.value * 768); bstore16<K1_AUX>((u32x4){d1, 0x00003f80u, 0u, 0u}, ro, ov, 64 * KVREC + T.value * 768); }
    });
  }
  STAMP(ws, 1, 5);
  STAMP(ws, 1, 6);
  STAMP_FLUSH(ws, 1);
}

extern "C" __global__ __launch_bounds__(512, 2) void k1_qkv(const void* x, const void* wqkv, char* ws) {
  const int tid = threadIdx.x, lane = tid & 63, w = rfl(tid >> 6);
  const int xcd = blockIdx.x & 7, r = blockIdx.x >> 3;
  const int mt = 4 * (xcd & 3) + (r & 3), nt = 8 * (xcd >> 2) + (r >> 2);
  const unsigned lds0 = (unsigned)(size_t)smem;
  const rsrc_t rx = mk_rsrc(x, S_ * D_ * 2), rw = mk_rsrc(wqkv, D_ * 3 * D_ * 2);
  if (w == 7) {
    unsigned long long pc; unsigned back, size;
    asm volatile("s_getpc_b64 %0\n.Lk1pc%=:\n\ts_mov_b32 %1, .Lk1pc%=-k1_qkv\n\ts_mov_b32 %2, k9_end-k1_qkv" : "=s"(pc), "=s"(back), "=s"(size));
    warm(lds0 + K1_TBL + 1024 + 512 + 64, lane < 24 ? (r * 24 + lane) * 128 : OOB, mk_rsrc((const void*)(pc - back), size), 0);
  }
  if (w < 4) k1_body<0>(ws, rx, rw, mt, nt, w, lane, lds0);
  else k1_body<1>(ws, rx, rw, mt, nt, w, lane, lds0);
}

constexpr int K2_SCR = 16 * KVREC;
constexpr int K2_LDS = K2_SCR + 256;

template <bool EDGE> DI void k2_body(char* ws, const void* x, const void* wo, const void* wg, const void* wu, int xcd, int r, int head, int qb, int w, int lane, unsigned lds0) {
  const int q = lane & 31, h = lane >> 5, G = lane >> 4;
  const int qt = 8 * qb + w;
  const rsrc_t rq = mk_rsrc(ws + WS_QF, 32 * 64 * QREC), rkv = mk_rsrc(ws + WS_KV, 32 * 64 * KVREC);
  STAMP_DECL; STAMP(ws, 2, 0);
  bf16x8 qm; unsigned qtl;
  bload16(qm, lane * 16, rq, (head * 64 + qt) * QREC);
  bload4(qtl, 1024 + q * 8, rq, (head * 64 + qt) * QREC);
  if constexpr (EDGE) {
    const unsigned src = (head * 64) * KVREC;
#pragma unroll
    for (int i = 0; i < 3; ++i) { const int pc = min(w + 8 * i, 21); glds16(lds0 + 8 * KVREC + pc * 1024, lane * 16, rkv, src + pc * 1024); }
  } else {
    const unsigned src = (head * 64 + 8 * qb - 8) * KVREC;
#pragma unroll
    for (int i = 0; i < 6; ++i) { const int pc = min(w + 8 * i, 43); glds16(lds0 + pc * 1024, lane * 16, rkv, src + pc * 1024); }
  }
  STAMP(ws, 2, 1);
  asm volatile("s_waitcnt vmcnt(0)" : "+v"(qm), "+v"(qtl)::"memory");
  bar();
  STAMP(ws, 2, 2);
  {
    const int v = r * 8 + w;
    const int l = (v % 81) * 64 + lane, row = (l * 7282) >> 16;
    if (v < 162) warm(lds0 + K2_SCR, row * 9216 + xcd * 1152 + (l - 9 * row) * 128, mk_rsrc(v < 81 ? wg : wu, D_ * F_ * 2), 0);
    else if (v < 243) warm(lds0 + K2_SCR, l * 128, mk_rsrc(wo, D_ * D_ * 2), 0);
    if (v < 36) warm(lds0 + K2_SCR, (v * 64 + lane) * 128, mk_rsrc(x, S_ * D_ * 2), xcd * 256 * 1152);
  }
  const bf16x4 qt4 = __builtin_bit_cast(bf16x4, (u32x2){h ? 0u : qtl, 0u});
  const unsigned kb = lds0 + w * KVREC + lane * 16, ktb = lds0 + w * KVREC + 1024 + q * 8;
  const unsigned vb = lds0 + w * KVREC + 1280 + (4 * (G >> 1) + ((lane >> 2) & 3)) * 48 + ((G & 1) ? 32 : 8 * (lane & 3));
  f32x16 s[9];
  sfor<9>([&](auto I) {
    constexpr int i = I.value;
    const unsigned off = EDGE ? (unsigned)(max(w + i, 8) - w) * KVREC : i * KVREC;
    const bf16x8 km = ldsr<bf16x8>(kb + off);
    const bf16x4 kt = ldsr<bf16x4>(ktb + off);
    f32x16 a = mfma32(km, qm, (f32x16){});
    s[i] = __builtin_amdgcn_mfma_f32_32x32x8bf16_1k(kt, qt4, a, 0, 0, 0);
    if (EDGE && w + i < 8) {
#pragma unroll
      for (int r = 0; r < 16; ++r) s[i][r] = -1e30f;
    }
  });
  const int qq = q - 4 * h;
#pragma unroll
  for (int r = 0; r < 16; ++r) s[8][r] = ((r & 3) + 8 * (r >> 2) <= qq) ? s[8][r] : s[0][r];
  float m4[4];
#pragma unroll
  for (int c = 0; c < 4; ++c) m4[c] = fmaxf(s[1][2 * c], s[1][2 * c + 1]);
#pragma unroll
  for (int i = 1; i < 9; ++i)
#pragma unroll
    for (int e = (i == 1 ? 4 : 0); e < 8; ++e) m4[e & 3] = fmaxf(fmaxf(m4[e & 3], s[i][2 * e]), s[i][2 * e + 1]);
  float mx = max_x32(fmaxf(fmaxf(m4[0], m4[1]), fmaxf(m4[2], m4[3])));
  STAMP(ws, 2, 3);
  f32x16 o = {};
  sfor<8>([&](auto I) {
    constexpr int i = I.value + 1;
    const unsigned off = EDGE ? (unsigned)(max(w + i, 8) - w) * KVREC : i * KVREC;
    float pv[16];
#pragma unroll
    for (int e = 0; e < 8; ++e) {
      const f32x2 dd = (f32x2){s[i][2 * e], s[i][2 * e + 1]} - (f32x2){mx, mx};
      pv[2 * e] = __builtin_amdgcn_exp2f(dd[0]); pv[2 * e + 1] = __builtin_amdgcn_exp2f(dd[1]);
    }
    if constexpr (i < 8) {
#pragma unroll
      for (int ks = 0; ks < 2; ++ks) {
        const u32x4 pp = {pk2(pv[8 * ks], pv[8 * ks + 1]), pk2(pv[8 * ks + 2], pv[8 * ks + 3]), pk2(pv[8 * ks + 4], pv[8 * ks + 5]), pk2(pv[8 * ks + 6], pv[8 * ks + 7])};
        o = mfma32(trfrag(vb + off + ks * 768, vb + off + ks * 768 + 384), __builtin_bit_cast(bf16x8, pp), o);
      }
    } else {
      const unsigned off0 = EDGE ? (unsigned)(8 - w) * KVREC : 0;
#pragma unroll
      for (int ks = 0; ks < 2; ++ks) {
        u32x4 pd, po;
#pragma unroll
        for (int e = 0; e < 4; ++e) {
          const int r = 8 * ks + 2 * e;
          const bool v0 = ((r & 3) + 8 * (r >> 2) <= qq), v1 = (((r + 1) & 3) + 8 * ((r + 1) >> 2) <= qq);
          pd[e] = pk2(v0 ? pv[r] : 0.f, v1 ? pv[r + 1] : 0.f);
          po[e] = pk2(v0 ? 0.f : pv[r], v1 ? 0.f : pv[r + 1]);
        }
        o = mfma32(trfrag(vb + off + ks * 768, vb + off + ks * 768 + 384), __builtin_bit_cast(bf16x8, pd), o);
        o = mfma32(trfrag(vb + off0 + ks * 768, vb + off0 + ks * 768 + 384), __builtin_bit_cast(bf16x8, po), o);
      }
    }
  });
  STAMP(ws, 2, 4);
  auto lr = __builtin_amdgcn_permlane32_swap(__float_as_uint(o[10]), __float_as_uint(o[10]), false, false);
  const float inv = 1.f / __uint_as_float(lr[0]);
  const rsrc_t ro = mk_rsrc(ws + WS_OF, 128 * 18 * 1024);
  const u32x4 om = {pk2(o[0] * inv, o[1] * inv), pk2(o[2] * inv, o[3] * inv), pk2(o[4] * inv, o[5] * inv), pk2(o[6] * inv, o[7] * inv)};
  const unsigned tt = 2 * qt + (q >> 4);
  bstore16<0>(om, ro, (tt * 18 + (head >> 1)) * 1024 + ((q & 15) + 16 * (2 * (head & 1) + h)) * 16, 0);
  if (h == 0) bstore4<0>(pk2(o[8] * inv, o[9] * inv), ro, (tt * 18 + 16 + (head >> 4)) * 1024 + ((q & 15) + 16 * ((head >> 2) & 3)) * 16 + (head & 3) * 4, 0);
  STAMP(ws, 2, 5);
  STAMP(ws, 2, 6);
  STAMP_FLUSH(ws, 2);
}

extern "C" __global__ __launch_bounds__(512, 2) void k2_attn(char* ws, const void* x, const void* wo, const void* wg, const void* wu) {
  const int tid = threadIdx.x, lane = tid & 63, w = rfl(tid >> 6);
  const int xcd = blockIdx.x & 7, r = blockIdx.x >> 3;
  const int head = 4 * xcd + (r & 3), qb = r >> 2;
  const unsigned lds0 = (unsigned)(size_t)smem;
  if (blockIdx.x == 0) *(GAS unsigned*)(ws + WS_CNT + tid * 64) = 0u;
  if (qb == 0) k2_body<true>(ws, x, wo, wg, wu, xcd, r, head, qb, w, lane, lds0);
  else k2_body<false>(ws, x, wo, wg, wu, xcd, r, head, qb, w, lane, lds0);
}

constexpr int K3_LDS = 72 * 1024;

extern "C" __global__ __launch_bounds__(512, 2) void k3_wo(const void* x, const void* wo, const void* wg, const void* wu, const void* wd, char* ws) {
  const int tid = threadIdx.x, lane = tid & 63, w = rfl(tid >> 6);
  const unsigned lds0 = (unsigned)(size_t)smem;
  const int t16 = lane & 15, g = lane >> 4;
  const int xcd = blockIdx.x & 7, br = blockIdx.x >> 3;
  if (br >= 18) {
    const int sb = br - 18, gw = (sb * 8 + xcd) * 8 + w;
    u32x4 dv;
    const int drow = 8 * gw + (lane >> 3);
    if (gw < 576) dv = *(const GAS u32x4*)((const char*)wd + (size_t)drow * 1152 + 1024 + (lane & 7) * 16);
    for (int u = sb; u < 18; u += 14) {
      const int mat = u >= 9, un = u - 9 * mat;
      const char* base = (const char*)(mat ? wu : wg) + (9 * xcd + un) * 128 + (lane & 7) * 16 + (size_t)(72 * w + (lane >> 3)) * 9216;
      u32x4 v[9];
#pragma unroll
      for (int i = 0; i < 9; ++i) v[i] = *(const GAS u32x4*)(base + (size_t)i * 8 * 9216);
      if (un == 8) {
#pragma unroll
        for (int i = 0; i < 9; ++i)
          *(GAS u32x4*)(ws + WS_LG + ((xcd * 4 + ((lane & 7) >> 1)) * 2 + mat) * 18432 + (72 * w + 8 * i + (lane >> 3)) * 32 + (lane & 1) * 16) = v[i];
      }
      float a[8] = {};
#pragma unroll
      for (int i = 0; i < 9; ++i)
#pragma unroll
        for (int e = 0; e < 4; ++e) { a[2 * e] += bflo(v[i][e]); a[2 * e + 1] += bfhi(v[i][e]); }
#pragma unroll
      for (int e = 0; e < 8; ++e) a[e] = sum_x32(sum_x16(a[e]));
      __syncthreads();
      if (lane < 16) { ldsw<f32x4>(lds0 + w * 512 + lane * 32, (f32x4){a[0], a[1], a[2], a[3]}); ldsw<f32x4>(lds0 + w * 512 + lane * 32 + 16, (f32x4){a[4], a[5], a[6], a[7]}); }
      __syncthreads();
      if (tid < 64) {
        float t = 0.f;
#pragma unroll
        for (int i = 0; i < 16; ++i) t += ldsr<float>(lds0 + i * 256 + tid * 4);
        *(GAS float*)(ws + WS_CS + (mat * 4608 + (9 * xcd + un) * 64 + tid) * 4) = t;
      }
    }
    if (gw < 576) {
      const int pr = (drow % 576) >= 512 ? ((drow & ~24) | ((drow & 8) << 1) | ((drow & 16) >> 1)) : drow;
      *(GAS u32x4*)(ws + WS_WT + (((lane & 7) >> 1) * 4608 + pr) * 32 + (lane & 1) * 16) = dv;
    }
    return;
  }
  const int mt = 2 * xcd + (br >= 9), nt = br >= 9 ? br - 9 : br;
  const int T = 8 * mt + w;
  const rsrc_t rwo = mk_rsrc(wo, D_ * D_ * 2), rof = mk_rsrc(ws + WS_OF, 128 * 18 * 1024), rx = mk_rsrc(x, S_ * D_ * 2);
  const int rho = 8 * (w & 3) + (lane >> 3), rg = (rho >> 2) & 3, rj = 4 * (rho >> 4) + (rho & 3);
  const unsigned cbyte = 128 * nt + 32 * (((lane & 7) >> 1) ^ ((rho >> 1) & 3)) + 16 * (lane & 1);
  const unsigned wv = (20 * (rg >> 1) + 8 * (rg & 1) + rj) * 1152 + cbyte;
  const int th = 16 * (w >> 2) + 4 * rg + (rj >> 1);
  const unsigned wvt = (18 * th + ((th & 1) ? 0 : 16) + (rj & 1)) * 1152 + cbyte;
  STAMP_DECL; STAMP(ws, 3, 0);
  bf16x8 of[18];
  u32x2 xr[4];
  auto op = [&](auto N) {
    constexpr int n = N.value, p = n / 3;
    if constexpr (n >= 27) bload8(xr[n - 27], (16 * T + t16) * 1152 + 128 * nt + 32 * (n - 27) + 8 * g, rx, 0);
    else if constexpr (n % 3 == 0) { if constexpr (p < 8) glds16(lds0 + (w + 8 * p) * 1024, wv, rwo, (2 * p + (w >> 2)) * 36 * 1152); else glds16(lds0 + (w + 8 * p) * 1024, wvt, rwo, 0); }
    else bload16(of[2 * p + n % 3 - 1], lane * 16, rof, (T * 18 + 2 * p + n % 3 - 1) * 1024);
  };
  constexpr int PRO = 12;
  sfor<PRO>([&](auto N) { op(N); });
  STAMP(ws, 3, 1);
  unsigned wa[4];
  {
    const int r2 = 4 * g + ((lane >> 2) & 3);
#pragma unroll
    for (int c = 0; c < 4; ++c) wa[c] = lds0 + r2 * 128 + 32 * (c ^ ((r2 >> 1) & 3)) + 8 * (lane & 3);
  }
  f32x4 acc[4] = {};
  bf16x4 fr[2][4][2];
  auto rd1 = [&](auto KS, auto I) { constexpr int ks = KS.value, i = I.value; fr[ks & 1][i >> 1][i & 1] = trh(wa[i >> 1] + ks * 4096 + (i & 1) * 2048); };
  sfor<9>([&](auto P) {
    constexpr int p = P.value;
    constexpr int iss = PRO + 4 * p < 31 ? PRO + 4 * p : 31;
    constexpr int need = p == 0 ? 2 : (p < 8 ? 3 * p + 3 : 26);
    asm volatile("s_waitcnt vmcnt(%2)" : "+v"(of[2 * p]), "+v"(of[2 * p + 1]) : "i"(iss - need - 1) : "memory");
    bar();
    if constexpr (p == 0) STAMP(ws, 3, 2);
    if constexpr (p == 3) STAMP(ws, 3, 3);
    if constexpr (p == 6) STAMP(ws, 3, 4);
    if constexpr (p <= 1) sfor<8>([&](auto I) { rd1(ic<2 * p>{}, I); });
    sfor<2>([&](auto SP) {
      constexpr int ks = 2 * p + SP.value;
      sfor<4>([&](auto C) {
        acc[C.value] = mfma16(cat(fr[ks & 1][C.value][0], fr[ks & 1][C.value][1]), of[ks], acc[C.value]);
        SBAR();
        if constexpr (ks + 1 < 18 && ks != 1) { rd1(ic<ks + 1>{}, ic<2 * C.value>{}); rd1(ic<ks + 1>{}, ic<2 * C.value + 1>{}); }
        constexpr int n = PRO + 4 * p + 2 * SP.value + (C.value >> 1);
        if constexpr ((C.value & 1) && n < 31) op(ic<n>{});
        SBAR();
      });
    });
  });
  asm volatile("s_waitcnt vmcnt(0)" : "+v"(xr[0]), "+v"(xr[1]), "+v"(xr[2]), "+v"(xr[3])::"memory");
  unsigned d[4][2];
  float s1 = 0.f, s2 = 0.f;
#pragma unroll
  for (int c = 0; c < 4; ++c) {
    d[c][0] = pk2(acc[c][0] + bflo(xr[c][0]), acc[c][1] + bfhi(xr[c][0]));
    d[c][1] = pk2(acc[c][2] + bflo(xr[c][1]), acc[c][3] + bfhi(xr[c][1]));
#pragma unroll
    for (int e = 0; e < 2; ++e) { const float a = bflo(d[c][e]), b = bfhi(d[c][e]); s1 += a + b; s2 += a * a + b * b; }
  }
  s1 = sum_x32(sum_x16(s1)); s2 = sum_x32(sum_x16(s2));
  const rsrc_t rhf = mk_rsrc(ws + WS_HF, 128 * 18 * 1024);
  bstore16<0>((u32x4){d[0][0], d[0][1], d[1][0], d[1][1]}, rhf, lane * 16, (T * 18 + 2 * nt) * 1024);
  bstore16<0>((u32x4){d[2][0], d[2][1], d[3][0], d[3][1]}, rhf, lane * 16, (T * 18 + 2 * nt + 1) * 1024);
  if (g == 0) *(GAS f32x2*)(ws + WS_ST + ((T * 9 + nt) * 16 + t16) * 8) = (f32x2){s1, s2};
  STAMP(ws, 3, 5);
  STAMP(ws, 3, 6);
  STAMP_FLUSH(ws, 3);
}

#define K4_HAUX 16
constexpr int K4_NS = 2, K4_R = 4, K4_P = 2;
constexpr int K4_LG = K4_R * K4_NS * 8192;
constexpr int K4_STP = K4_LG + 36864;
constexpr int K4_CS = K4_STP + 24576;
constexpr int K4_STT = K4_CS + 2048;
constexpr int K4_SCR = K4_STT + 2048;
constexpr int K4_LDS = K4_SCR + 256;
struct K4Op { int kind, idx; };
constexpr int k4_lo(int S) { return S == 0 ? K4_P * K4_NS : (S + K4_R - 1) * K4_NS; }
constexpr int k4_hi(int S) { return (S + K4_R) * K4_NS < 36 ? (S + K4_R) * K4_NS : 36; }
constexpr int k4_nx(int S) { return S == 6 ? 4 : (S >= 10 && S < 15) || S == 16 ? 1 : 0; }
constexpr int k4_nops(int S) { int n = k4_nx(S); for (int u = k4_lo(S); u < k4_hi(S); ++u) n += u < 18 ? 3 : 1; return n; }
constexpr K4Op k4_op(int S, int i) {
  for (int u = k4_lo(S); u < k4_hi(S); ++u) { const int c = u < 18 ? 3 : 1; if (i < c) return {i, u}; i -= c; }
  if (S == 6) return {i < 3 ? 5 : 6, i};
  if (S == 16) return {4, 0};
  return {3, S - 10};
}
constexpr int k4_nst(int S) { return (S == 8 || S == 9 || S == 17) ? 2 : 0; }
constexpr int K4_PRO = 3 * K4_P * K4_NS;
constexpr int k4_issued(int S) { int n = K4_PRO; for (int s = 0; s < S; ++s) n += k4_nops(s) + k4_nst(s); return n; }
constexpr int k4_posW(int u) {
  if (u < K4_P * K4_NS) return 3 * u;
  for (int S = 0;; ++S) if (u >= k4_lo(S) && u < k4_hi(S)) { int n = k4_issued(S) + k4_nst(S); for (int v = k4_lo(S); v < u; ++v) n += v < 18 ? 3 : 1; return n; }
}
constexpr int k4_need(int S) {
  if (S == 8) return k4_issued(7) - 1;
  if (S >= 16) return k4_issued(15) - 1;
  return k4_posW((S + 1) * K4_NS);
}
constexpr unsigned k4_so(int u) { return (((u / K4_NS) % K4_R) * K4_NS + u % K4_NS) * 8192; }

extern "C" __global__ __launch_bounds__(512, 2) void k4_ffn1(const void* wg, const void* wu, const void* wd, char* ws) {
  const int tid = threadIdx.x, lane = tid & 63, w = rfl(tid >> 6);
  const int xcd = blockIdx.x & 7, r = blockIdx.x >> 3;
  const int mt = r >> 2, Qd = xcd, j = r & 3;
  const unsigned lds0 = (unsigned)(size_t)smem;
  const int t16 = lane & 15, g = lane >> 4;
  const int T0 = 16 * mt + 2 * w;
  const rsrc_t rwm = mk_rsrc(w < 4 ? wg : wu, D_ * F_ * 2), rhf = mk_rsrc(ws + WS_HF, 128 * 18 * 1024), rwd = mk_rsrc(wd, F_ * D_ * 2);
  const rsrc_t rst = mk_rsrc(ws + WS_ST, 9 * 2048 * 8), rcs = mk_rsrc(ws + WS_CS, 2 * 4608 * 4), rhh = mk_rsrc(ws + WS_HH, 128 * 144 * 1024);
  const rsrc_t rlg = mk_rsrc(ws + WS_LG, 8 * 4 * 36864);
  STAMP_DECL; STAMP(ws, 4, 0);
  const int rho = 8 * (w & 3) + (lane >> 3);
  const unsigned wv = rho * 9216 + 1152 * Qd + 256 * j + 32 * (((lane & 7) >> 1) ^ ((rho >> 1) & 3)) + 16 * (lane & 1);
  const unsigned wdst = lds0 + w * 1024, l16 = lane * 16;
  bf16x8 act[18][2];
  auto doop = [&](auto KIND, auto IDX) {
    constexpr int kind = KIND.value, u = IDX.value;
    if constexpr (kind == 0) glds16(wdst + k4_so(u), wv, rwm, (u % 18) * 32 * 9216 + (u / 18) * 128);
    else if constexpr (kind <= 2) bload16(act[u][kind - 1], l16, rhf, (T0 * 18 + 18 * (kind - 1) + u) * 1024);
    else if constexpr (kind == 3) { const int pc = min(w + 8 * u, 35); glds16(lds0 + K4_LG + pc * 1024, l16, rlg, (Qd * 4 + j) * 36864 + pc * 1024); }
    else if constexpr (kind == 4) warm(lds0 + K4_SCR, l16 * 8, rwd, (xcd >> 1) * 1327104 + min(r * 8 + w, 161) * 8192);
    else if constexpr (kind == 5) glds16(lds0 + K4_STP + w * 3072 + u * 1024, l16, rst, T0 * 1152 + u * 1024);
    else {
      const unsigned f = l16 < 512 ? 576 * Qd + 128 * j + (l16 >> 2) : 576 * Qd + 512 + 16 * j + (l16 >> 2) - 128;
      glds16(lds0 + K4_CS + (w & 1) * 1024, l16 < 576 ? f * 4 : OOB, rcs, (w & 1) * 4608 * 4);
    }
  };
  sfor<K4_P * K4_NS>([&](auto U) { doop(ic<0>{}, U); doop(ic<1>{}, U); doop(ic<2>{}, U); });
  unsigned wa[4];
  {
    const int r2 = 4 * g + ((lane >> 2) & 3);
#pragma unroll
    for (int c = 0; c < 4; ++c) wa[c] = lds0 + r2 * 128 + 32 * (c ^ ((r2 >> 1) & 3)) + 8 * (lane & 3);
  }
  f32x4 ag[4][2], au[4][2];
  bf16x4 fr[3][2][2];
  unsigned hd[2][2];
  auto rd1 = [&](auto N, auto I) {
    constexpr int n = N.value, i = I.value;
    if constexpr (n < 144) fr[n % 3][i >> 1][i & 1] = trh(wa[n % 4] + k4_so(n / 4) + 2048 * i);
    else if constexpr (n < 162) fr[n % 3][i >> 1][i & 1] = trh(lds0 + K4_LG + (l16 >> 6) * 32 + ((l16 >> 1) & 24) + (n - 144) * 1024 + (i >> 1) * 18432 + (i & 1) * 512);
  };
  auto rd = [&](auto N) { sfor<4>([&](auto I) { rd1(N, I); }); };
  auto mm = [&](auto N, auto KS, f32x4& g0, f32x4& g1, f32x4& u0, f32x4& u1) {
    constexpr int n = N.value, ks = KS.value;
    const bf16x8 fg = cat(fr[n % 3][0][0], fr[n % 3][0][1]), fu = cat(fr[n % 3][1][0], fr[n % 3][1][1]);
    bf16x4 nx[4];
    if constexpr (ks == 0) mfma16_z(g0, fg, act[ks][0]); else mfma16_ip(g0, fg, act[ks][0]);
    SBAR(); rd1(ic<n + 2>{}, ic<0>{}); SBAR();
    if constexpr (ks == 0) mfma16_z(g1, fg, act[ks][1]); else mfma16_ip(g1, fg, act[ks][1]);
    SBAR(); rd1(ic<n + 2>{}, ic<1>{}); SBAR();
    if constexpr (ks == 0) mfma16_z(u0, fu, act[ks][0]); else mfma16_ip(u0, fu, act[ks][0]);
    SBAR(); rd1(ic<n + 2>{}, ic<2>{}); SBAR();
    if constexpr (ks == 0) mfma16_z(u1, fu, act[ks][1]); else mfma16_ip(u1, fu, act[ks][1]);
    SBAR(); rd1(ic<n + 2>{}, ic<3>{}); SBAR();
    (void)nx;
  };
  auto epi = [&](const f32x4& G_, const f32x4& U_, float nmu, const f32x4& cg, const f32x4& cu, unsigned& d0, unsigned& d1) {
    const f32x4 v = (G_ + cg * nmu) * (U_ + cu * nmu);
    d0 = pk2(v[0], v[1]); d1 = pk2(v[2], v[3]);
  };
  auto epichunk = [&](auto GRP, auto C) {
    constexpr int grp = GRP.value, c = C.value;
    asm volatile("s_nop 4" : "+v"(ag[c][0]), "+v"(ag[c][1]), "+v"(au[c][0]), "+v"(au[c][1]));
    const unsigned csa = lds0 + K4_CS + ((l16 >> 4) & 48) + 256 * grp + 64 * c, sta = lds0 + K4_STT + 128 * w + ((l16 >> 2) & 60);
    const f32x4 cg = ldsr<f32x4>(csa), cu = ldsr<f32x4>(csa + 1024);
    sfor<2>([&](auto T) {
      const float nmu = ldsr<float>(sta + 64 * T.value);
      if constexpr ((c & 1) == 0) epi(ag[c][T.value], au[c][T.value], nmu, cg, cu, hd[T.value][0], hd[T.value][1]);
      else {
        unsigned d2, d3;
        epi(ag[c][T.value], au[c][T.value], nmu, cg, cu, d2, d3);
        bstore16<K4_HAUX>((u32x4){hd[T.value][0], hd[T.value][1], d2, d3}, rhh, l16, ((T0 + T.value) * 144 + 18 * Qd + 4 * j + 2 * grp + (c >> 1)) * 1024);
      }
    });
  };
  sfor<18>([&](auto SS) {
    constexpr int S = SS.value;
    constexpr int nw = k4_issued(S) - k4_need(S) - 1;
    static_assert(nw >= 0 && nw < 64, "");
    if constexpr (S < 9) asm volatile("s_waitcnt vmcnt(%4)" : "+v"(act[2 * S][0]), "+v"(act[2 * S][1]), "+v"(act[2 * S + 1][0]), "+v"(act[2 * S + 1][1]) : "i"(nw) : "memory");
    else wait_vm<nw>();
    bar();
    if constexpr (S == 0) STAMP(ws, 4, 1);
    if constexpr (S == 4) STAMP(ws, 4, 2);
    if constexpr (S == 9) STAMP(ws, 4, 3);
    if constexpr (S == 17) STAMP(ws, 4, 4);
    if constexpr (S == 0) { rd(ic<0>{}); rd(ic<1>{}); }
    if constexpr (S == 8) {
      sfor<2>([&](auto T) {
        const unsigned sa = lds0 + K4_STP + w * 3072 + T.value * 1152 + (l16 >> 1);
        const f32x2 p0 = ldsr<f32x2>(sa), p1 = ldsr<f32x2>(sa + 512), p2 = ldsr<f32x2>(sa + 1024);
        const f32x2 ps = p0 + p1 + (l16 < 256 ? p2 : (f32x2){0.f, 0.f});
        const float a = sum_x32(sum_x16(ps[0])), b = sum_x32(sum_x16(ps[1]));
        const float mu = a * (1.f / D_), var = fmaxf(b * (1.f / D_) - mu * mu, 0.f);
        ldsw<float>(lds0 + K4_STT + ((l16 & 256) << 2) + 128 * w + 64 * T.value + ((l16 >> 2) & 60), (l16 & 256) ? __builtin_amdgcn_rcpf(var + 1e-5f) : -mu);
      });
      SBAR();
    }
    sfor<4 * K4_NS>([&](auto L) {
      constexpr int l = L.value, n = 4 * K4_NS * S + l, t = n / 4, c = n % 4, ks = t % 18;
      mm(ic<n>{}, ic<ks>{}, ag[c][0], ag[c][1], au[c][0], au[c][1]);
      constexpr int no = k4_nops(S), NL = 4 * K4_NS;
      sfor<no>([&](auto I) {
        constexpr int nd = no ? no : 1, b0 = I.value * NL / nd;
        if constexpr (b0 == l) doop(ic<k4_op(S, I.value).kind>{}, ic<k4_op(S, I.value).idx>{});
      });
      if constexpr (ks == 17 && c >= 1) epichunk(ic<t / 18>{}, ic<c - 1>{});
      if constexpr (t == 18 && c == 0) epichunk(ic<0>{}, ic<3>{});
      SBAR();
    });
  });
  STAMP(ws, 4, 5);
  {
    f32x4 g0, g1, u0, u1;
    sfor<18>([&](auto K) {
      constexpr int ks = K.value, n = 144 + ks;
      mm(ic<n>{}, ic<ks>{}, g0, g1, u0, u1);
      if constexpr (ks == 0) epichunk(ic<1>{}, ic<3>{});
      SBAR();
    });
    const unsigned csa = lds0 + K4_CS + ((l16 >> 4) & 48) + 512, sta = lds0 + K4_STT + 128 * w + ((l16 >> 2) & 60);
    const f32x4 cg = ldsr<f32x4>(csa), cu = ldsr<f32x4>(csa + 1024);
    unsigned a0, a1, b0, b1;
    asm volatile("s_nop 9" : "+v"(g0), "+v"(g1), "+v"(u0), "+v"(u1));
    epi(g0, u0, ldsr<float>(sta), cg, cu, a0, a1);
    epi(g1, u1, ldsr<float>(sta + 64), cg, cu, b0, b1);
    auto s0 = __builtin_amdgcn_permlane32_swap(a0, b0, false, false);
    auto s1 = __builtin_amdgcn_permlane32_swap(a1, b1, false, false);
    bstore16<K4_HAUX>((u32x4){s0[0], s1[0], s0[1], s1[1]}, rhh, (l16 >> 9) * 144 * 1024 + (l16 & 511) + 512 * (j & 1), (T0 * 144 + 18 * Qd + 16 + (j >> 1)) * 1024);
    if (Qd == 0 && j == 0 && l16 < 512) *(GAS float*)(ws + WS_RS + (16 * T0) * 4 + (l16 >> 2)) = ldsr<float>(lds0 + K4_STT + 1024 + 128 * w + (l16 >> 2));
    STAMP(ws, 4, 6);
    STAMP(ws, 4, 7);
    STAMP_FLUSH(ws, 4);
  }
}

constexpr int K5_SLOT = 17408, K5_R = 8, K5_P = 4;
constexpr int K5_LDS = K5_SLOT * K5_R + 256;
constexpr int k5_E(int i) { int e = 36; if (K5_P + 2 * (i + 1) < e) e = K5_P + 2 * (i + 1); if (i >= 0 && i + K5_R < e) e = i + K5_R; return e; }

extern "C" __global__ __launch_bounds__(512, 2) void k5_ffn2(const void* wd, char* ws, void* out) {
  const int tid = threadIdx.x, lane = tid & 63, w = rfl(tid >> 6);
  const int xcd = blockIdx.x & 7, r = blockIdx.x >> 3;
  const int sg = xcd >> 1, mt = 8 * (xcd & 1) + (r >> 2), nt = r & 3;
  const unsigned lds0 = (unsigned)(size_t)smem;
  const int t16 = lane & 15, g = lane >> 4;
  const int tile = 4 * mt + nt;
  const rsrc_t rpt = mk_rsrc(ws + WS_PT, 64 * 8 * 4 * PT_REC);
  STAMP_DECL; STAMP(ws, 5, 0);
  if (w >= 4) {
    const int l = w - 4;
    const rsrc_t rwd = mk_rsrc(wd, F_ * D_ * 2), rhh = mk_rsrc(ws + WS_HH, 128 * 144 * 1024);
    const rsrc_t rwt = mk_rsrc(ws + WS_WT, 4 * 4608 * 32);
    unsigned mv[2][2];
#pragma unroll
    for (int sp = 0; sp < 2; ++sp) {
#pragma unroll
      for (int i = 0; i < 2; ++i) {
        const int rho = 4 * (2 * l + i) + g, rr = sp ? ((rho & 7) | ((rho & 8) << 1) | ((rho & 16) >> 1)) : rho;
        mv[sp][i] = rr * 1152 + 256 * nt + 32 * ((t16 >> 1) ^ (rho & 7)) + 16 * (t16 & 1);
      }
    }
    const unsigned hs = ((8 * mt + 2 * l) * 144 + 36 * sg) * 1024, wsb = sg * 1152 * 1152, dl = lds0 + l * 2048;
    auto issue = [&](auto U) {
      constexpr int u = U.value, sp = (u % 18) >= 16;
      constexpr unsigned so = (u % K5_R) * K5_SLOT;
      glds16(dl + so + 8192, mv[sp][0], rwd, wsb + u * 32 * 1152);
      glds16(dl + so, lane * 16, rhh, hs + u * 1024);
      glds16(dl + so + 8192 + 1024, mv[sp][1], rwd, wsb + u * 32 * 1152);
      glds16(dl + so + 1024, lane * 16, rhh, hs + (144 + u) * 1024);
      if ((u & 3) == l) glds16(lds0 + so + 16384, lane * 16, rwt, (nt * 144 + 36 * sg + u) * 1024);
    };
    sfor<K5_P>([&](auto U) { issue(U); });
    sfor<36>([&](auto I) {
      constexpr int i = I.value, y = k5_E(i - 1) - (i + 2 < 36 ? i + 2 : 36);
      wait_vm<4 * y + y / 4>();
      bar();
      if constexpr (i == 0) STAMP(ws, 5, 1);
      if constexpr (i == 18) STAMP(ws, 5, 2);
      sfor<k5_E(i) - k5_E(i - 1)>([&](auto U) { issue(ic<k5_E(i - 1) + U.value>{}); });
    });
    bar();
    STAMP(ws, 5, 3);
  } else {
    const int r2 = 4 * g + ((lane >> 2) & 3);
    unsigned wa[8];
#pragma unroll
    for (int c = 0; c < 8; ++c) wa[c] = lds0 + 8192 + r2 * 256 + 32 * (c ^ (r2 & 7)) + 8 * (lane & 3);
    const unsigned wt = lds0 + 16384 + r2 * 32 + 8 * (lane & 3), ha = lds0 + w * 2048 + lane * 16;
    f32x4 acc[9][2] = {};
    bf16x4 fr[3][3][2];
    bf16x8 hb[2][2];
    auto rd1 = [&](auto N, auto I) {
      constexpr int n = N.value, u = n / 3, i = I.value;
      if constexpr (u < 36) {
        constexpr unsigned so = (u % K5_R) * K5_SLOT;
        constexpr int c = 3 * (n % 3) + i / 2;
        if constexpr (i >= 6) hb[u & 1][i - 6] = ldsr<bf16x8>(ha + so + (i - 6) * 1024);
        else if constexpr (c < 8) fr[n % 3][i / 2][i & 1] = trh(wa[c] + so + (i & 1) * 4096);
        else fr[n % 3][i / 2][i & 1] = trh(wt + so + (i & 1) * 512);
      }
    };
    sfor<36>([&](auto U) {
      constexpr int u = U.value;
      bar();
      if constexpr (u == 0) STAMP(ws, 5, 1);
      if constexpr (u == 18) STAMP(ws, 5, 2);
      if constexpr (u == 0) { rd1(ic<0>{}, ic<6>{}); rd1(ic<0>{}, ic<7>{}); sfor<6>([&](auto I) { rd1(ic<0>{}, I); }); sfor<6>([&](auto I) { rd1(ic<1>{}, I); }); }
      sfor<18>([&](auto M) {
        constexpr int mi = M.value, v = mi / 6, n = 3 * u + v, c = mi / 2;
        acc[c][mi & 1] = mfma16(cat(fr[n % 3][c % 3][0], fr[n % 3][c % 3][1]), hb[u & 1][mi & 1], acc[c][mi & 1]);
        SBAR();
        if constexpr (mi == 6 || mi == 8) rd1(ic<n + 2>{}, ic<6 + (mi - 6) / 2>{});
        rd1(ic<n + 2>{}, ic<mi % 6>{});
        SBAR();
      });
    });
    bar();
    STAMP(ws, 5, 3);
    if (w == sg) {
      sfor<2>([&](auto T) { sfor<9>([&](auto C) { ldsw<f32x4>(lds0 + (T.value * 9 + C.value) * 1024 + lane * 16, acc[C.value][T.value]); }); });
    } else {
      sfor<2>([&](auto T) {
        const unsigned pb = (((tile * 4 + w) * 4 + sg) * 2 + T.value) * PT_REC;
        sfor<4>([&](auto I) {
          constexpr int i = I.value;
          const f32x4 a = acc[2 * i][T.value], b = acc[2 * i + 1][T.value];
          bstore16<16>((u32x4){pk2(a[0], a[1]), pk2(a[2], a[3]), pk2(b[0], b[1]), pk2(b[2], b[3])}, rpt, lane * 16 + i * 1024, pb);
        });
        const f32x4 a = acc[8][T.value];
        bstore8<16>((u32x2){pk2(a[0], a[1]), pk2(a[2], a[3])}, rpt, lane * 8 + 4096, pb);
      });
    }
  }
  const int T = w >> 2, pi = w & 3, tt = 8 * mt + 2 * sg + T;
  const rsrc_t rhf = mk_rsrc(ws + WS_HF, 128 * 18 * 1024);
  const u32x4 hv = __builtin_amdgcn_raw_buffer_load_b128(rhf, lane * 16, (tt * 18 + 4 * nt + pi) * 1024, 0);
  const u32x2 hl = __builtin_amdgcn_raw_buffer_load_b64(rhf, lane * 16 + 8 * (nt & 1), (tt * 18 + 16 + (nt >> 1)) * 1024, 0);
  const float rs2 = *(const GAS float*)(ws + WS_RS + (16 * tt + t16) * 4);
  STAMP(ws, 5, 4);
  asm volatile("s_waitcnt vmcnt(0) lgkmcnt(0)" ::: "memory");
  STAMP(ws, 5, 5);
  bar();
  if (w == 4) {
    GAS unsigned* cnt = (GAS unsigned*)(ws + WS_CNT + tile * 64);
    if (lane == 0) __hip_atomic_fetch_add(cnt, 1u, __ATOMIC_RELAXED, __HIP_MEMORY_SCOPE_AGENT);
    for (int spin = 0; spin < (1 << 22); ++spin) {
      if (rfl(__hip_atomic_load(cnt, __ATOMIC_RELAXED, __HIP_MEMORY_SCOPE_AGENT)) >= 4u) break;
      __builtin_amdgcn_s_sleep(1);
    }
  }
  bar();
  STAMP(ws, 5, 6);
  u32x4 pv[3];
  u32x2 pl[3];
#pragma unroll
  for (int o = 0; o < 3; ++o) {
    const unsigned so = (((tile * 4 + sg) * 4 + ((sg + 1 + o) & 3)) * 2 + T) * PT_REC;
    pv[o] = __builtin_amdgcn_raw_buffer_load_b128(rpt, lane * 16 + pi * 1024, so, 16);
    if (pi == 0) pl[o] = __builtin_amdgcn_raw_buffer_load_b64(rpt, lane * 8 + 4096, so, 16);
  }
  const rsrc_t rout = mk_rsrc(out, S_ * D_ * 2);
  auto fin = [&](const f32x4& own, unsigned a0, unsigned a1, unsigned b0, unsigned b1, unsigned c0, unsigned c1, unsigned h0, unsigned h1, unsigned& d0, unsigned& d1) {
    const float v0 = ((own[0] + bflo(b0)) + (bflo(a0) + bflo(c0))) * rs2 + bflo(h0);
    const float v1 = ((own[1] + bfhi(b0)) + (bfhi(a0) + bfhi(c0))) * rs2 + bfhi(h0);
    const float v2 = ((own[2] + bflo(b1)) + (bflo(a1) + bflo(c1))) * rs2 + bflo(h1);
    const float v3 = ((own[3] + bfhi(b1)) + (bfhi(a1) + bfhi(c1))) * rs2 + bfhi(h1);
    d0 = pk2(v0, v1); d1 = pk2(v2, v3);
  };
  const unsigned orow = (16 * tt + t16) * 1152, own = lds0 + T * 9216 + lane * 16;
  {
    unsigned a0, a1, b0, b1;
    fin(ldsr<f32x4>(own + pi * 2048), pv[0][0], pv[0][1], pv[1][0], pv[1][1], pv[2][0], pv[2][1], hv[0], hv[1], a0, a1);
    fin(ldsr<f32x4>(own + pi * 2048 + 1024), pv[0][2], pv[0][3], pv[1][2], pv[1][3], pv[2][2], pv[2][3], hv[2], hv[3], b0, b1);
    auto s0 = __builtin_amdgcn_permlane16_swap(a0, b0, false, false);
    auto s1 = __builtin_amdgcn_permlane16_swap(a1, b1, false, false);
    bstore16<0>((u32x4){s0[0], s1[0], s0[1], s1[1]}, rout, orow + 256 * nt + 64 * pi + 32 * (g & 1) + 16 * (g >> 1), 0);
  }
  if (pi == 0) {
    unsigned e0, e1;
    fin(ldsr<f32x4>(own + 8192), pl[0][0], pl[0][1], pl[1][0], pl[1][1], pl[2][0], pl[2][1], hl[0], hl[1], e0, e1);
    bstore8<0>((u32x2){e0, e1}, rout, orow + 1024 + 32 * nt + 8 * g, 0);
  }
  STAMP(ws, 5, 7);
  STAMP_FLUSH(ws, 5);
}

extern "C" __global__ void k9_end() {}

extern "C" void kernel_launch(void* const* d_in, const int* in_sizes, int n_in, void* d_out, int out_size, void* d_ws, size_t ws_size,
                              hipStream_t stream) {
  const void *x = d_in[0], *wqkv = d_in[1], *wo = d_in[2], *wg = d_in[4], *wu = d_in[5], *wd = d_in[6];
  char* ws = (char*)d_ws;
  (void)hipFuncSetAttribute((const void*)k1_qkv, hipFuncAttributeMaxDynamicSharedMemorySize, K1_LDS);
  (void)hipFuncSetAttribute((const void*)k2_attn, hipFuncAttributeMaxDynamicSharedMemorySize, K2_LDS);
  (void)hipFuncSetAttribute((const void*)k3_wo, hipFuncAttributeMaxDynamicSharedMemorySize, K3_LDS);
  (void)hipFuncSetAttribute((const void*)k4_ffn1, hipFuncAttributeMaxDynamicSharedMemorySize, K4_LDS);
  (void)hipFuncSetAttribute((const void*)k5_ffn2, hipFuncAttributeMaxDynamicSharedMemorySize, K5_LDS);
  k1_qkv<<<256, 512, K1_LDS, stream>>>(x, wqkv, ws);
  k2_attn<<<256, 512, K2_LDS, stream>>>(ws, x, wo, wg, wu);
  k3_wo<<<256, 512, K3_LDS, stream>>>(x, wo, wg, wu, wd, ws);
  k4_ffn1<<<256, 512, K4_LDS, stream>>>(wg, wu, wd, ws);
  k5_ffn2<<<256, 512, K5_LDS, stream>>>(wd, ws, d_out);
}
```

```cpp
#define STAMPS
#define KA 1
#define IA 0
#define MA 0
#define KB 1
#define IB 0
#define MB 0
#define MULT 10
#include <hip/hip_runtime.h>
#include <stdint.h>

typedef short bf16x8 __attribute__((ext_vector_type(8)));
typedef short bf16x4 __attribute__((ext_vector_type(4)));
typedef float f32x2 __attribute__((ext_vector_type(2)));
typedef float f32x4 __attribute__((ext_vector_type(4)));
typedef float f32x16 __attribute__((ext_vector_type(16)));
typedef unsigned u32x2 __attribute__((ext_vector_type(2)));
typedef unsigned u32x4 __attribute__((ext_vector_type(4)));
typedef __bf16 bf2_t __attribute__((ext_vector_type(2)));
typedef __amdgpu_buffer_rsrc_t rsrc_t;
#define LAS __attribute__((address_space(3)))
#define GAS __attribute__((address_space(1)))
#define DI __device__ __forceinline__

constexpr int S_ = 2048, D_ = 576, F_ = 4608;
constexpr float QSCALE = 0.23570226039551584f * 1.4426950408889634f;
constexpr unsigned ONES2 = 0x3f803f80u;

constexpr size_t QREC = 1280, KVREC = 2816;
constexpr unsigned OOB = 0x7fffff00u;
constexpr size_t WS_QF = 0;
constexpr size_t WS_KV = WS_QF + 32 * 64 * QREC;
constexpr size_t WS_OF = WS_KV + 32 * 64 * KVREC;
constexpr size_t WS_HF = WS_OF + 128 * 18 * 1024;
constexpr size_t WS_ST = WS_HF + 128 * 18 * 1024;
constexpr size_t WS_CS = WS_ST + 9 * 2048 * 8;
constexpr size_t WS_CNT = WS_CS + 2 * 4608 * 4;
constexpr size_t WS_RS = WS_CNT + 512 * 64;
constexpr size_t WS_LG = WS_RS + 2048 * 4;
constexpr size_t WS_WT = WS_LG + 8 * 4 * 36864;
constexpr size_t WS_HH = WS_WT + 4 * 4608 * 32;
constexpr size_t PT_REC = 4608;
constexpr size_t WS_PT = WS_HH + (size_t)128 * 144 * 1024;
constexpr size_t WS_END = WS_PT + (size_t)64 * 8 * 4 * PT_REC;

DI unsigned rfl(unsigned v) { return __builtin_amdgcn_readfirstlane(v); }
DI rsrc_t mk_rsrc(const void* p, unsigned bytes) { return __builtin_amdgcn_make_buffer_rsrc((void*)p, (short)0, (int)bytes, 0x00020000); }
DI void glds16(unsigned lds, unsigned voff, rsrc_t r, unsigned soff) {
  asm volatile("s_mov_b32 m0, %0\n\ts_nop 0\n\tbuffer_load_dwordx4 %1, %2, %3 offen lds" ::"s"(lds), "v"(voff), "s"(r), "s"(soff) : "memory", "m0");
}
DI void bload16(bf16x8& d, unsigned voff, rsrc_t r, unsigned soff) {
  asm volatile("buffer_load_dwordx4 %0, %1, %2, %3 offen" : "=v"(d) : "v"(voff), "s"(r), "s"(soff) : "memory");
}
DI void bload8(u32x2& d, unsigned voff, rsrc_t r, unsigned soff) {
  asm volatile("buffer_load_dwordx2 %0, %1, %2, %3 offen" : "=v"(d) : "v"(voff), "s"(r), "s"(soff) : "memory");
}
DI void bload4(unsigned& d, unsigned voff, rsrc_t r, unsigned soff) {
  asm volatile("buffer_load_dword %0, %1, %2, %3 offen" : "=v"(d) : "v"(voff), "s"(r), "s"(soff) : "memory");
}
DI void warm(unsigned lds, unsigned voff, rsrc_t r, unsigned soff) {
  asm volatile("s_mov_b32 m0, %0\n\ts_nop 0\n\tbuffer_load_dword %1, %2, %3 offen lds" ::"s"(lds), "v"(voff), "s"(r), "s"(soff) : "memory", "m0");
}
template <int N> DI void wait_vm() { asm volatile("s_waitcnt vmcnt(%0)" ::"i"(N) : "memory"); }
#define PIN(x) asm volatile("" : "+v"(x))
#define SBAR() __builtin_amdgcn_sched_barrier(0)
DI void bar() { SBAR(); asm volatile("s_barrier" ::: "memory"); SBAR(); }
DI void lds_bar() { SBAR(); asm volatile("s_waitcnt lgkmcnt(0)\n\ts_barrier" ::: "memory"); SBAR(); }
DI bf16x8 trfrag(unsigned a0, unsigned a1) {
  bf16x4 lo = __builtin_amdgcn_ds_read_tr16_b64_v4i16((LAS bf16x4*)(size_t)a0);
  bf16x4 hi = __builtin_amdgcn_ds_read_tr16_b64_v4i16((LAS bf16x4*)(size_t)a1);
  return __builtin_shufflevector(lo, hi, 0, 1, 2, 3, 4, 5, 6, 7);
}
DI bf16x4 trh(unsigned a) { return __builtin_amdgcn_ds_read_tr16_b64_v4i16((LAS bf16x4*)(size_t)a); }
DI bf16x8 cat(bf16x4 lo, bf16x4 hi) { return __builtin_shufflevector(lo, hi, 0, 1, 2, 3, 4, 5, 6, 7); }
template <class T> DI T ldsr(unsigned a) { return *(LAS T*)(size_t)a; }
template <class T> DI void ldsw(unsigned a, T v) { *(LAS T*)(size_t)a = v; }
DI unsigned pk2(float a, float b) { f32x2 v = {a, b}; return __builtin_bit_cast(unsigned, __builtin_convertvector(v, bf2_t)); }
DI float bflo(unsigned u) { return __uint_as_float(u << 16); }
DI float bfhi(unsigned u) { return __uint_as_float(u & 0xffff0000u); }
DI f32x4 mfma16(bf16x8 a, bf16x8 b, f32x4 c) { return __builtin_amdgcn_mfma_f32_16x16x32_bf16(a, b, c, 0, 0, 0); }
DI void mfma16_ip(f32x4& c, bf16x8 a, bf16x8 b) { asm volatile("v_mfma_f32_16x16x32_bf16 %0, %1, %2, %0" : "+v"(c) : "v"(a), "v"(b)); }
DI void mfma16_z(f32x4& c, bf16x8 a, bf16x8 b) { asm volatile("v_mfma_f32_16x16x32_bf16 %0, %1, %2, 0" : "=&v"(c) : "v"(a), "v"(b)); }
DI f32x16 mfma32(bf16x8 a, bf16x8 b, f32x16 c) { return __builtin_amdgcn_mfma_f32_32x32x16_bf16(a, b, c, 0, 0, 0); }
DI float xor32(float v) { auto r = __builtin_amdgcn_permlane32_swap(__float_as_uint(v), __float_as_uint(v), false, false); return __uint_as_float((threadIdx.x & 32) ? r[0] : r[1]); }
DI float sum_x32(float v) { auto r = __builtin_amdgcn_permlane32_swap(__float_as_uint(v), __float_as_uint(v), false, false); return __uint_as_float(r[0]) + __uint_as_float(r[1]); }
DI float max_x32(float v) { auto r = __builtin_amdgcn_permlane32_swap(__float_as_uint(v), __float_as_uint(v), false, false); return fmaxf(__uint_as_float(r[0]), __uint_as_float(r[1])); }
DI float sum_x16(float v) { auto r = __builtin_amdgcn_permlane16_swap(__float_as_uint(v), __float_as_uint(v), false, false); return __uint_as_float(r[0]) + __uint_as_float(r[1]); }
template <int AUX> DI void bstore16(u32x4 v, rsrc_t r, unsigned voff, unsigned soff) {
  if constexpr (AUX == 16) asm volatile("buffer_store_dwordx4 %0, %1, %2, %3 offen sc1\n\ts_nop 1" ::"v"(v), "v"(voff), "s"(r), "s"(soff) : "memory");
  else asm volatile("buffer_store_dwordx4 %0, %1, %2, %3 offen\n\ts_nop 1" ::"v"(v), "v"(voff), "s"(r), "s"(soff) : "memory");
}
template <int AUX> DI void bstore8(u32x2 v, rsrc_t r, unsigned voff, unsigned soff) { __builtin_amdgcn_raw_buffer_store_b64(v, r, voff, soff, AUX); }
template <int AUX> DI void bstore4(unsigned v, rsrc_t r, unsigned voff, unsigned soff) { __builtin_amdgcn_raw_buffer_store_b32(v, r, voff, soff, AUX); }

template <int... I> struct iseq {};
template <int N, int... I> struct mkseq : mkseq<N - 1, N - 1, I...> {};
template <int... I> struct mkseq<0, I...> { typedef iseq<I...> type; };
template <int V> struct ic { static constexpr int value = V; };
template <class Fn, int... I> DI void sfor_(Fn&& f, iseq<I...>) { (f(ic<I>{}), ...); }
template <int N, class Fn> DI void sfor(Fn&& f) { sfor_(f, typename mkseq<N>::type{}); }

extern __shared__ __attribute__((aligned(16))) char smem[];
#define K1_AUX 0
constexpr size_t WS_DBG = 64u << 20;
#define STAMP(ws, kid, i) do { SBAR(); unsigned long long t_, c_; asm volatile("s_memrealtime %0\n\ts_memtime %1\n\ts_waitcnt lgkmcnt(0)" : "=s"(t_), "=s"(c_) :: "memory"); \
  stamp_buf[i] = t_; stamp_buf[8 + i] = c_; SBAR(); } while (0)
#define STAMP_DECL unsigned long long stamp_buf[16] = {}
#define STAMP_FLUSH(ws, kid) do { if ((threadIdx.x & 63) == 0) { for (int i_ = 0; i_ < 16; ++i_) *(GAS unsigned long long*)(ws + WS_DBG + ((((size_t)kid * 256 + blockIdx.x) * 8 + (threadIdx.x >> 6)) * 32 + i_) * 8) = stamp_buf[i_]; } } while (0)

constexpr int K1_SLOT = 32768, K1_TBL = K1_SLOT * 4;
constexpr int K1_LDS = K1_TBL + 1024 + 512 + 64 + 256;
extern "C" __global__ void k9_end();

template <int NN> DI void k1_body(char* ws, rsrc_t rx, rsrc_t rw, int mt, int nt, int w, int lane, unsigned lds0) {
  const int t16 = lane & 15, g = lane >> 4, q4 = (lane >> 2) & 3, p = lane & 3, m = w & 3;
  constexpr int NT = NN ? 3 : 4;
  unsigned xv[2], wv[2];
#pragma unroll
  for (int i = 0; i < 2; ++i) {
    const int row = 8 * (2 * w + i) + (lane >> 3);
    xv[i] = (128 * mt + row) * 1152 + 16 * ((lane & 7) ^ ((row >> 1) & 7));
    int li = 64 * (2 * w + i) + lane;
    if (li >= 896) li -= 128;
    const int r64 = (li * 4682) >> 16, c = li - 14 * r64;
    const int rho = r64 & 31, k = (r64 & 32) + 8 * ((rho >> 2) & 3) + 4 * (rho >> 4) + (rho & 3);
    wv[i] = k * 3456 + ((216 * nt) & ~15) + 16 * c;
  }
  STAMP_DECL; STAMP(ws, 1, 0);
  auto issue1 = [&](int slab, int i) {
    const unsigned dst = lds0 + (slab & 3) * K1_SLOT + w * 2048 + (i >> 1) * 1024;
    if (i & 1) glds16(dst + 16384, wv[i >> 1], rw, slab * 64 * 3456);
    else glds16(dst, xv[i >> 1], rx, slab * 128);
  };
#pragma unroll
  for (int sl = 0; sl < 2; ++sl)
#pragma unroll
    for (int i = 0; i < 4; ++i) issue1(sl, i);
  STAMP(ws, 1, 1);
  const unsigned wa = lds0 + 16384 + (4 * g + q4) * 224 + (nt & 1) * 8;
  unsigned ad[2][5];
  ad[0][0] = wa + 8 * p + 144 * NN;
  ad[0][1] = wa + 72 * (p < 3 ? p : 2) + 32;
  ad[0][2] = NN && m >= 2 ? ad[0][1] : ad[0][0] + 72 * (m >> 1) + 40 * (m & 1);
  ad[0][3] = lds0 + (32 * m + t16) * 128 + 16 * (g ^ ((t16 >> 1) & 7));
  ad[0][4] = ad[0][3] ^ 64;
#pragma unroll
  for (int i = 0; i < 5; ++i) { ad[1][i] = ad[0][i] + 2 * K1_SLOT; PIN(ad[1][i]); }
  f32x4 acc[4][2] = {}, cs = {};
  bf16x8 ones = __builtin_bit_cast(bf16x8, (u32x4){ONES2, ONES2, ONES2, ONES2});
  PIN(ones);
  bf16x8 fb[2][2];
  bf16x4 fa[2][NT + 1][2];
  constexpr int NR = 2 * NT + 4;
  auto rd1 = [&](auto KS, auto I) {
    constexpr int ks = KS.value, i = I.value, sp = ks & 1, sl = (ks >> 1) & 3, ti = (i - 2) >> 1;
    constexpr unsigned so = (sl & 1) * K1_SLOT;
    if constexpr (ks < 18 && i < NR) {
      if constexpr (i < 2) fb[sp][i] = ldsr<bf16x8>(ad[sl >> 1][3 + sp] + so + i * 2048);
      else {
        constexpr int ai = ti == NT ? 2 : (NN && ti == 2) ? 1 : 0;
        constexpr unsigned co = ai ? 0 : 72 * (ti >> 1) + 40 * (ti & 1);
        fa[sp][ti][i & 1] = trh(ad[sl >> 1][ai] + so + co + sp * 32 * 224 + (i & 1) * 16 * 224);
      }
    }
  };
  sfor<9>([&](auto SL) {
    constexpr int sl = SL.value;
    constexpr int iss = sl == 0 ? 2 : (sl + 3 < 9 ? sl + 3 : 9);
    constexpr int need = sl == 0 ? 0 : (sl + 1 < 9 ? sl + 1 : 8);
    wait_vm<4 * (iss - need - 1)>();
    bar();
    if constexpr (sl == 0) STAMP(ws, 1, 2);
    if constexpr (sl == 4) STAMP(ws, 1, 3);
    if constexpr (sl <= 1) sfor<NR>([&](auto I) { rd1(ic<2 * sl>{}, I); });
    sfor<2>([&](auto SP) {
      constexpr int sp = SP.value, ks = 2 * sl + sp;
      bf16x8 tl;
      if constexpr (NN) tl = t16 >= 12 ? ones : cat(fa[sp][2][0], fa[sp][2][1]);
      sfor<9>([&](auto M) {
        constexpr int mi = M.value, ti = mi >> 1;
        if constexpr (mi == 8) cs = mfma16(cat(fa[sp][NT][0], fa[sp][NT][1]), ones, cs);
        else if constexpr (NN && ti == 3) acc[3][mi & 1] = mfma16(fb[sp][mi & 1], fb[sp][mi & 1], acc[3][mi & 1]);
        else if constexpr (NN && ti == 2) acc[2][mi & 1] = mfma16(tl, fb[sp][mi & 1], acc[2][mi & 1]);
        else acc[ti][mi & 1] = mfma16(cat(fa[sp][ti][0], fa[sp][ti][1]), fb[sp][mi & 1], acc[ti][mi & 1]);
        SBAR();
        if constexpr (ks != 1) {
          if constexpr (mi < 3) { rd1(ic<ks + 1>{}, ic<2 * mi>{}); rd1(ic<ks + 1>{}, ic<2 * mi + 1>{}); }
          else rd1(ic<ks + 1>{}, ic<3 + mi>{});
        }
        if constexpr (sl == 0) { if constexpr (mi & 1) issue1(2 + sp, mi >> 1); }
        else if constexpr (sl + 3 < 9 && (mi == 2 + 2 * NN || mi == 6 + 2 * NN)) issue1(sl + 3, 2 * sp + (mi >= 6));
        SBAR();
      });
    });
  });
  STAMP(ws, 1, 4);
  const unsigned tb = lds0 + K1_TBL, cb = tb + 1024, dummy = cb + 512;
  ldsw<f32x4>(t16 == 0 ? cb + (w * 16 + 4 * g) * 4 : dummy, cs);
  if constexpr (NN) sfor<2>([&](auto T) {
    const unsigned a = tb + (32 * m + 16 * T.value + t16) * 8;
    const f32x4 gr = acc[3][T.value];
    const float d01 = (p & 1) ? gr[1] : gr[0], d23 = (p & 1) ? gr[3] : gr[2];
    ldsw<float>(g == 3 ? a : dummy, acc[2][T.value][0]);
    ldsw<float>(g == q4 ? a + 4 : dummy, (p & 2) ? d23 : d01);
  });
  lds_bar();
  f32x2 st[2];
  sfor<2>([&](auto T) {
    const f32x2 v = ldsr<f32x2>(tb + (32 * m + 16 * T.value + t16) * 8);
    const float mu = v[0] * (1.f / D_), rs = __builtin_amdgcn_rsqf(fmaxf(v[1] * (1.f / D_) - mu * mu, 0.f) + 1e-5f);
    st[T.value] = (f32x2){rs, -rs * mu};
  });
  const rsrc_t ro = mk_rsrc(ws + WS_QF, 32 * 64 * (QREC + KVREC));
  const int tile32 = 4 * mt + m, key = 16 * (g >> 1) + t16;
  const unsigned lo_qk = (g & 1) * 512 + key * 16, lo_v = 1280 + key * 48 + (g & 1) * 16;
  sfor<(NN ? 2 : 4)>([&](auto TI) {
    constexpr int ti = TI.value, tile = 4 * NN + ti;
    const int pg = 3 * nt + (tile >> 1), type = pg >> 4, rec = (2 * (pg & 15) + (tile & 1)) * 64 + tile32;
    const f32x4 c = ldsr<f32x4>(cb + (tile * 16 + 4 * g) * 4);
    const float sc = type == 0 ? QSCALE : 1.f;
    const f32x4 v0 = acc[ti][0] * (st[0][0] * sc) + c * (st[0][1] * sc), v1 = acc[ti][1] * (st[1][0] * sc) + c * (st[1][1] * sc);
    auto s0 = __builtin_amdgcn_permlane32_swap(pk2(v0[0], v0[1]), pk2(v1[0], v1[1]), false, false);
    auto s1 = __builtin_amdgcn_permlane32_swap(pk2(v0[2], v0[3]), pk2(v1[2], v1[3]), false, false);
    bstore16<K1_AUX>((u32x4){s0[0], s1[0], s0[1], s1[1]}, ro, type == 2 ? lo_v : lo_qk, type == 0 ? rec * QREC : WS_KV + rec * KVREC);
  });
  if constexpr (NN) {
    const int pg = 3 * nt + g, type = pg >> 4, rec = 2 * (pg & 15) * 64 + tile32;
    const f32x4 c = ldsr<f32x4>(cb + (96 + 4 * g) * 4);
    const float sc = type == 0 ? QSCALE : 1.f;
    const unsigned rb = type == 0 ? rec * QREC : WS_KV + rec * KVREC, hs = type == 0 ? 64 * QREC : 64 * KVREC;
    const unsigned oq = g < 3 && type < 2 ? rb + 1024 + t16 * 8 : OOB, ov = g < 3 && type == 2 ? rb + 1280 + 32 + t16 * 48 : OOB;
    sfor<2>([&](auto T) {
      const f32x4 v = acc[2][T.value] * (st[T.value][0] * sc) + c * (st[T.value][1] * sc);
      const unsigned d0 = pk2(v[0], v[1]), d1 = pk2(v[2], v[3]);
      if (nt <= 10) { bstore8<K1_AUX>((u32x2){d0, 0u}, ro, oq + T.value * 128, 0); bstore8<K1_AUX>((u32x2){d1, 0u}, ro, oq + hs + T.value * 128, 0); }
      if (nt >= 10) { bstore16<K1_AUX>((u32x4){d0, 0x00003f80u, 0u, 0u}, ro, ov, T.value * 768); bstore16<K1_AUX>((u32x4){d1, 0x00003f80u, 0u, 0u}, ro, ov, 64 * KVREC + T.value * 768); }
    });
  }
  STAMP(ws, 1, 5);
  wait_vm<0>();
  STAMP(ws, 1, 6);
  STAMP_FLUSH(ws, 1);
}

extern "C" __global__ __launch_bounds__(512, 2) void k1_qkv(const void* x, const void* wqkv, char* ws) {
  const int tid = threadIdx.x, lane = tid & 63, w = rfl(tid >> 6);
  const int xcd = blockIdx.x & 7, r = blockIdx.x >> 3;
  const int mt = 4 * (xcd & 3) + (r & 3), nt = 8 * (xcd >> 2) + (r >> 2);
  const unsigned lds0 = (unsigned)(size_t)smem;
  const rsrc_t rx = mk_rsrc(x, S_ * D_ * 2), rw = mk_rsrc(wqkv, D_ * 3 * D_ * 2);
  if (w == 7) {
    unsigned long long pc; unsigned back, size;
    asm volatile("s_getpc_b64 %0\n.Lk1pc%=:\n\ts_mov_b32 %1, .Lk1pc%=-k1_qkv\n\ts_mov_b32 %2, k9_end-k1_qkv" : "=s"(pc), "=s"(back), "=s"(size));
    warm(lds0 + K1_TBL + 1024 + 512 + 64, lane < 24 ? (r * 24 + lane) * 128 : OOB, mk_rsrc((const void*)(pc - back), size), 0);
  }
  if (w < 4) k1_body<0>(ws, rx, rw, mt, nt, w, lane, lds0);
  else k1_body<1>(ws, rx, rw, mt, nt, w, lane, lds0);
}

constexpr int K2_SCR = 16 * KVREC;
constexpr int K2_LDS = K2_SCR + 256;

template <bool EDGE> DI void k2_body(char* ws, const void* x, const void* wo, const void* wg, const void* wu, int xcd, int r, int head, int qb, int w, int lane, unsigned lds0) {
  const int q = lane & 31, h = lane >> 5, G = lane >> 4;
  const int qt = 8 * qb + w;
  const rsrc_t rq = mk_rsrc(ws + WS_QF, 32 * 64 * QREC), rkv = mk_rsrc(ws + WS_KV, 32 * 64 * KVREC);
  STAMP_DECL; STAMP(ws, 2, 0);
  bf16x8 qm; unsigned qtl;
  bload16(qm, lane * 16, rq, (head * 64 + qt) * QREC);
  bload4(qtl, 1024 + q * 8, rq, (head * 64 + qt) * QREC);
  if constexpr (EDGE) {
    const unsigned src = (head * 64) * KVREC;
#pragma unroll
    for (int i = 0; i < 3; ++i) { const int pc = min(w + 8 * i, 21); glds16(lds0 + 8 * KVREC + pc * 1024, lane * 16, rkv, src + pc * 1024); }
  } else {
    const unsigned src = (head * 64 + 8 * qb - 8) * KVREC;
#pragma unroll
    for (int i = 0; i < 6; ++i) { const int pc = min(w + 8 * i, 43); glds16(lds0 + pc * 1024, lane * 16, rkv, src + pc * 1024); }
  }
  STAMP(ws, 2, 1);
  asm volatile("s_waitcnt vmcnt(0)" : "+v"(qm), "+v"(qtl)::"memory");
  bar();
  STAMP(ws, 2, 2);
  {
    const int v = r * 8 + w;
    const int l = (v % 81) * 64 + lane, row = (l * 7282) >> 16;
    if (v < 162) warm(lds0 + K2_SCR, row * 9216 + xcd * 1152 + (l - 9 * row) * 128, mk_rsrc(v < 81 ? wg : wu, D_ * F_ * 2), 0);
    else if (v < 243) warm(lds0 + K2_SCR, l * 128, mk_rsrc(wo, D_ * D_ * 2), 0);
    if (v < 36) warm(lds0 + K2_SCR, (v * 64 + lane) * 128, mk_rsrc(x, S_ * D_ * 2), xcd * 256 * 1152);
  }
  const bf16x4 qt4 = __builtin_bit_cast(bf16x4, (u32x2){h ? 0u : qtl, 0u});
  const unsigned kb = lds0 + w * KVREC + lane * 16, ktb = lds0 + w * KVREC + 1024 + q * 8;
  const unsigned vb = lds0 + w * KVREC + 1280 + (4 * (G >> 1) + ((lane >> 2) & 3)) * 48 + ((G & 1) ? 32 : 8 * (lane & 3));
  f32x16 s[9];
  sfor<9>([&](auto I) {
    constexpr int i = I.value;
    const unsigned off = EDGE ? (unsigned)(max(w + i, 8) - w) * KVREC : i * KVREC;
    const bf16x8 km = ldsr<bf16x8>(kb + off);
    const bf16x4 kt = ldsr<bf16x4>(ktb + off);
    f32x16 a = mfma32(km, qm, (f32x16){});
    s[i] = __builtin_amdgcn_mfma_f32_32x32x8bf16_1k(kt, qt4, a, 0, 0, 0);
    if (EDGE && w + i < 8) {
#pragma unroll
      for (int r = 0; r < 16; ++r) s[i][r] = -1e30f;
    }
  });
  const int qq = q - 4 * h;
#pragma unroll
  for (int r = 0; r < 16; ++r) s[8][r] = ((r & 3) + 8 * (r >> 2) <= qq) ? s[8][r] : s[0][r];
  float m4[4];
#pragma unroll
  for (int c = 0; c < 4; ++c) m4[c] = fmaxf(s[1][2 * c], s[1][2 * c + 1]);
#pragma unroll
  for (int i = 1; i < 9; ++i)
#pragma unroll
    for (int e = (i == 1 ? 4 : 0); e < 8; ++e) m4[e & 3] = fmaxf(fmaxf(m4[e & 3], s[i][2 * e]), s[i][2 * e + 1]);
  float mx = max_x32(fmaxf(fmaxf(m4[0], m4[1]), fmaxf(m4[2], m4[3])));
  PIN(mx);
  STAMP(ws, 2, 3);
  f32x16 o = {};
  sfor<8>([&](auto I) {
    constexpr int i = I.value + 1;
    const unsigned off = EDGE ? (unsigned)(max(w + i, 8) - w) * KVREC : i * KVREC;
    float pv[16];
#pragma unroll
    for (int e = 0; e < 8; ++e) {
      const f32x2 dd = (f32x2){s[i][2 * e], s[i][2 * e + 1]} - (f32x2){mx, mx};
      pv[2 * e] = __builtin_amdgcn_exp2f(dd[0]); pv[2 * e + 1] = __builtin_amdgcn_exp2f(dd[1]);
    }
    if constexpr (i < 8) {
#pragma unroll
      for (int ks = 0; ks < 2; ++ks) {
        const u32x4 pp = {pk2(pv[8 * ks], pv[8 * ks + 1]), pk2(pv[8 * ks + 2], pv[8 * ks + 3]), pk2(pv[8 * ks + 4], pv[8 * ks + 5]), pk2(pv[8 * ks + 6], pv[8 * ks + 7])};
        o = mfma32(trfrag(vb + off + ks * 768, vb + off + ks * 768 + 384), __builtin_bit_cast(bf16x8, pp), o);
      }
    } else {
      const unsigned off0 = EDGE ? (unsigned)(8 - w) * KVREC : 0;
#pragma unroll
      for (int ks = 0; ks < 2; ++ks) {
        u32x4 pd, po;
#pragma unroll
        for (int e = 0; e < 4; ++e) {
          const int r = 8 * ks + 2 * e;
          const bool v0 = ((r & 3) + 8 * (r >> 2) <= qq), v1 = (((r + 1) & 3) + 8 * ((r + 1) >> 2) <= qq);
          pd[e] = pk2(v0 ? pv[r] : 0.f, v1 ? pv[r + 1] : 0.f);
          po[e] = pk2(v0 ? 0.f : pv[r], v1 ? 0.f : pv[r + 1]);
        }
        o = mfma32(trfrag(vb + off + ks * 768, vb + off + ks * 768 + 384), __builtin_bit_cast(bf16x8, pd), o);
        o = mfma32(trfrag(vb + off0 + ks * 768, vb + off0 + ks * 768 + 384), __builtin_bit_cast(bf16x8, po), o);
      }
    }
  });
  PIN(o);
  STAMP(ws, 2, 4);
  auto lr = __builtin_amdgcn_permlane32_swap(__float_as_uint(o[10]), __float_as_uint(o[10]), false, false);
  const float inv = 1.f / __uint_as_float(lr[0]);
  const rsrc_t ro = mk_rsrc(ws + WS_OF, 128 * 18 * 1024);
  const u32x4 om = {pk2(o[0] * inv, o[1] * inv), pk2(o[2] * inv, o[3] * inv), pk2(o[4] * inv, o[5] * inv), pk2(o[6] * inv, o[7] * inv)};
  const unsigned tt = 2 * qt + (q >> 4);
  bstore16<0>(om, ro, (tt * 18 + (head >> 1)) * 1024 + ((q & 15) + 16 * (2 * (head & 1) + h)) * 16, 0);
  if (h == 0) bstore4<0>(pk2(o[8] * inv, o[9] * inv), ro, (tt * 18 + 16 + (head >> 4)) * 1024 + ((q & 15) + 16 * ((head >> 2) & 3)) * 16 + (head & 3) * 4, 0);
  STAMP(ws, 2, 5);
  wait_vm<0>();
  STAMP(ws, 2, 6);
  STAMP_FLUSH(ws, 2);
}

extern "C" __global__ __launch_bounds__(512, 2) void k2_attn(char* ws, const void* x, const void* wo, const void* wg, const void* wu) {
  const int tid = threadIdx.x, lane = tid & 63, w = rfl(tid >> 6);
  const int xcd = blockIdx.x & 7, r = blockIdx.x >> 3;
  const int head = 4 * xcd + (r & 3), qb = r >> 2;
  const unsigned lds0 = (unsigned)(size_t)smem;
  if (blockIdx.x == 0) *(GAS unsigned*)(ws + WS_CNT + tid * 64) = 0u;
  if (qb == 0) k2_body<true>(ws, x, wo, wg, wu, xcd, r, head, qb, w, lane, lds0);
  else k2_body<false>(ws, x, wo, wg, wu, xcd, r, head, qb, w, lane, lds0);
}

constexpr int K3_LDS = 72 * 1024;

extern "C" __global__ __launch_bounds__(512, 2) void k3_wo(const void* x, const void* wo, const void* wg, const void* wu, const void* wd, char* ws) {
  const int tid = threadIdx.x, lane = tid & 63, w = rfl(tid >> 6);
  const unsigned lds0 = (unsigned)(size_t)smem;
  const int t16 = lane & 15, g = lane >> 4;
  const int xcd = blockIdx.x & 7, br = blockIdx.x >> 3;
  if (br >= 18) {
    const int sb = br - 18, gw = (sb * 8 + xcd) * 8 + w;
    u32x4 dv;
    const int drow = 8 * gw + (lane >> 3);
    if (gw < 576) dv = *(const GAS u32x4*)((const char*)wd + (size_t)drow * 1152 + 1024 + (lane & 7) * 16);
    for (int u = sb; u < 18; u += 14) {
      const int mat = u >= 9, un = u - 9 * mat;
      const char* base = (const char*)(mat ? wu : wg) + (9 * xcd + un) * 128 + (lane & 7) * 16 + (size_t)(72 * w + (lane >> 3)) * 9216;
      u32x4 v[9];
#pragma unroll
      for (int i = 0; i < 9; ++i) v[i] = *(const GAS u32x4*)(base + (size_t)i * 8 * 9216);
      if (un == 8) {
#pragma unroll
        for (int i = 0; i < 9; ++i)
          *(GAS u32x4*)(ws + WS_LG + ((xcd * 4 + ((lane & 7) >> 1)) * 2 + mat) * 18432 + (72 * w + 8 * i + (lane >> 3)) * 32 + (lane & 1) * 16) = v[i];
      }
      float a[8] = {};
#pragma unroll
      for (int i = 0; i < 9; ++i)
#pragma unroll
        for (int e = 0; e < 4; ++e) { a[2 * e] += bflo(v[i][e]); a[2 * e + 1] += bfhi(v[i][e]); }
#pragma unroll
      for (int e = 0; e < 8; ++e) a[e] = sum_x32(sum_x16(a[e]));
      __syncthreads();
      if (lane < 16) { ldsw<f32x4>(lds0 + w * 512 + lane * 32, (f32x4){a[0], a[1], a[2], a[3]}); ldsw<f32x4>(lds0 + w * 512 + lane * 32 + 16, (f32x4){a[4], a[5], a[6], a[7]}); }
      __syncthreads();
      if (tid < 64) {
        float t = 0.f;
#pragma unroll
        for (int i = 0; i < 16; ++i) t += ldsr<float>(lds0 + i * 256 + tid * 4);
        *(GAS float*)(ws + WS_CS + (mat * 4608 + (9 * xcd + un) * 64 + tid) * 4) = t;
      }
    }
    if (gw < 576) {
      const int pr = (drow % 576) >= 512 ? ((drow & ~24) | ((drow & 8) << 1) | ((drow & 16) >> 1)) : drow;
      *(GAS u32x4*)(ws + WS_WT + (((lane & 7) >> 1) * 4608 + pr) * 32 + (lane & 1) * 16) = dv;
    }
    return;
  }
  const int mt = 2 * xcd + (br >= 9), nt = br >= 9 ? br - 9 : br;
  const int T = 8 * mt + w;
  const rsrc_t rwo = mk_rsrc(wo, D_ * D_ * 2), rof = mk_rsrc(ws + WS_OF, 128 * 18 * 1024), rx = mk_rsrc(x, S_ * D_ * 2);
  const int rho = 8 * (w & 3) + (lane >> 3), rg = (rho >> 2) & 3, rj = 4 * (rho >> 4) + (rho & 3);
  const unsigned cbyte = 128 * nt + 32 * (((lane & 7) >> 1) ^ ((rho >> 1) & 3)) + 16 * (lane & 1);
  const unsigned wv = (20 * (rg >> 1) + 8 * (rg & 1) + rj) * 1152 + cbyte;
  const int th = 16 * (w >> 2) + 4 * rg + (rj >> 1);
  const unsigned wvt = (18 * th + ((th & 1) ? 0 : 16) + (rj & 1)) * 1152 + cbyte;
  STAMP_DECL; STAMP(ws, 3, 0);
  bf16x8 of[18];
  u32x2 xr[4];
  auto op = [&](auto N) {
    constexpr int n = N.value, p = n / 3;
    if constexpr (n >= 27) bload8(xr[n - 27], (16 * T + t16) * 1152 + 128 * nt + 32 * (n - 27) + 8 * g, rx, 0);
    else if constexpr (n % 3 == 0) { if constexpr (p < 8) glds16(lds0 + (w + 8 * p) * 1024, wv, rwo, (2 * p + (w >> 2)) * 36 * 1152); else glds16(lds0 + (w + 8 * p) * 1024, wvt, rwo, 0); }
    else bload16(of[2 * p + n % 3 - 1], lane * 16, rof, (T * 18 + 2 * p + n % 3 - 1) * 1024);
  };
  constexpr int PRO = 12;
  sfor<PRO>([&](auto N) { op(N); });
  STAMP(ws, 3, 1);
  unsigned wa[4];
  {
    const int r2 = 4 * g + ((lane >> 2) & 3);
#pragma unroll
    for (int c = 0; c < 4; ++c) wa[c] = lds0 + r2 * 128 + 32 * (c ^ ((r2 >> 1) & 3)) + 8 * (lane & 3);
  }
  f32x4 acc[4] = {};
  bf16x4 fr[2][4][2];
  auto rd1 = [&](auto KS, auto I) { constexpr int ks = KS.value, i = I.value; fr[ks & 1][i >> 1][i & 1] = trh(wa[i >> 1] + ks * 4096 + (i & 1) * 2048); };
  sfor<9>([&](auto P) {
    constexpr int p = P.value;
    constexpr int iss = PRO + 4 * p < 31 ? PRO + 4 * p : 31;
    constexpr int need = p == 0 ? 2 : (p < 8 ? 3 * p + 3 : 26);
    asm volatile("s_waitcnt vmcnt(%2)" : "+v"(of[2 * p]), "+v"(of[2 * p + 1]) : "i"(iss - need - 1) : "memory");
    bar();
    if constexpr (p == 0) STAMP(ws, 3, 2);
    if constexpr (p == 3) STAMP(ws, 3, 3);
    if constexpr (p == 6) STAMP(ws, 3, 4);
    if constexpr (p <= 1) sfor<8>([&](auto I) { rd1(ic<2 * p>{}, I); });
    sfor<2>([&](auto SP) {
      constexpr int ks = 2 * p + SP.value;
      sfor<4>([&](auto C) {
        acc[C.value] = mfma16(cat(fr[ks & 1][C.value][0], fr[ks & 1][C.value][1]), of[ks], acc[C.value]);
        SBAR();
        if constexpr (ks + 1 < 18 && ks != 1) { rd1(ic<ks + 1>{}, ic<2 * C.value>{}); rd1(ic<ks + 1>{}, ic<2 * C.value + 1>{}); }
        constexpr int n = PRO + 4 * p + 2 * SP.value + (C.value >> 1);
        if constexpr ((C.value & 1) && n < 31) op(ic<n>{});
        SBAR();
      });
    });
  });
  asm volatile("s_waitcnt vmcnt(0)" : "+v"(xr[0]), "+v"(xr[1]), "+v"(xr[2]), "+v"(xr[3])::"memory");
  unsigned d[4][2];
  float s1 = 0.f, s2 = 0.f;
#pragma unroll
  for (int c = 0; c < 4; ++c) {
    d[c][0] = pk2(acc[c][0] + bflo(xr[c][0]), acc[c][1] + bfhi(xr[c][0]));
    d[c][1] = pk2(acc[c][2] + bflo(xr[c][1]), acc[c][3] + bfhi(xr[c][1]));
#pragma unroll
    for (int e = 0; e < 2; ++e) { const float a = bflo(d[c][e]), b = bfhi(d[c][e]); s1 += a + b; s2 += a * a + b * b; }
  }
  s1 = sum_x32(sum_x16(s1)); s2 = sum_x32(sum_x16(s2));
  const rsrc_t rhf = mk_rsrc(ws + WS_HF, 128 * 18 * 1024);
  bstore16<0>((u32x4){d[0][0], d[0][1], d[1][0], d[1][1]}, rhf, lane * 16, (T * 18 + 2 * nt) * 1024);
  bstore16<0>((u32x4){d[2][0], d[2][1], d[3][0], d[3][1]}, rhf, lane * 16, (T * 18 + 2 * nt + 1) * 1024);
  if (g == 0) *(GAS f32x2*)(ws + WS_ST + ((T * 9 + nt) * 16 + t16) * 8) = (f32x2){s1, s2};
  STAMP(ws, 3, 5);
  wait_vm<0>();
  STAMP(ws, 3, 6);
  STAMP_FLUSH(ws, 3);
}

#define K4_HAUX 16
constexpr int K4_NS = 2, K4_R = 4, K4_P = 2;
constexpr int K4_LG = K4_R * K4_NS * 8192;
constexpr int K4_STP = K4_LG + 36864;
constexpr int K4_CS = K4_STP + 24576;
constexpr int K4_STT = K4_CS + 2048;
constexpr int K4_SCR = K4_STT + 2048;
constexpr int K4_LDS = K4_SCR + 256;
struct K4Op { int kind, idx; };
constexpr int k4_lo(int S) { return S == 0 ? K4_P * K4_NS : (S + K4_R - 1) * K4_NS; }
constexpr int k4_hi(int S) { return (S + K4_R) * K4_NS < 36 ? (S + K4_R) * K4_NS : 36; }
constexpr int k4_nx(int S) { return S == 6 ? 4 : (S >= 10 && S < 15) || S == 16 ? 1 : 0; }
constexpr int k4_nops(int S) { int n = k4_nx(S); for (int u = k4_lo(S); u < k4_hi(S); ++u) n += u < 18 ? 3 : 1; return n; }
constexpr K4Op k4_op(int S, int i) {
  for (int u = k4_lo(S); u < k4_hi(S); ++u) { const int c = u < 18 ? 3 : 1; if (i < c) return {i, u}; i -= c; }
  if (S == 6) return {i < 3 ? 5 : 6, i};
  if (S == 16) return {4, 0};
  return {3, S - 10};
}
constexpr int k4_nst(int S) { return (S == 8 || S == 9 || S == 17) ? 2 : 0; }
constexpr int K4_PRO = 3 * K4_P * K4_NS;
constexpr int k4_issued(int S) { int n = K4_PRO; for (int s = 0; s < S; ++s) n += k4_nops(s) + k4_nst(s); return n; }
constexpr int k4_posW(int u) {
  if (u < K4_P * K4_NS) return 3 * u;
  for (int S = 0;; ++S) if (u >= k4_lo(S) && u < k4_hi(S)) { int n = k4_issued(S) + k4_nst(S); for (int v = k4_lo(S); v < u; ++v) n += v < 18 ? 3 : 1; return n; }
}
constexpr int k4_need(int S) {
  if (S == 8) return k4_issued(7) - 1;
  if (S >= 16) return k4_issued(15) - 1;
  return k4_posW((S + 1) * K4_NS);
}
constexpr unsigned k4_so(int u) { return (((u / K4_NS) % K4_R) * K4_NS + u % K4_NS) * 8192; }

extern "C" __global__ __launch_bounds__(512, 2) void k4_ffn1(const void* wg, const void* wu, const void* wd, char* ws) {
  const int tid = threadIdx.x, lane = tid & 63, w = rfl(tid >> 6);
  const int xcd = blockIdx.x & 7, r = blockIdx.x >> 3;
  const int mt = r >> 2, Qd = xcd, j = r & 3;
  const unsigned lds0 = (unsigned)(size_t)smem;
  const int t16 = lane & 15, g = lane >> 4;
  const int T0 = 16 * mt + 2 * w;
  const rsrc_t rwm = mk_rsrc(w < 4 ? wg : wu, D_ * F_ * 2), rhf = mk_rsrc(ws + WS_HF, 128 * 18 * 1024), rwd = mk_rsrc(wd, F_ * D_ * 2);
  const rsrc_t rst = mk_rsrc(ws + WS_ST, 9 * 2048 * 8), rcs = mk_rsrc(ws + WS_CS, 2 * 4608 * 4), rhh = mk_rsrc(ws + WS_HH, 128 * 144 * 1024);
  const rsrc_t rlg = mk_rsrc(ws + WS_LG, 8 * 4 * 36864);
  STAMP_DECL; STAMP(ws, 4, 0);
  const int rho = 8 * (w & 3) + (lane >> 3);
  const unsigned wv = rho * 9216 + 1152 * Qd + 256 * j + 32 * (((lane & 7) >> 1) ^ ((rho >> 1) & 3)) + 16 * (lane & 1);
  const unsigned wdst = lds0 + w * 1024, l16 = lane * 16;
  bf16x8 act[18][2];
  auto doop = [&](auto KIND, auto IDX) {
    constexpr int kind = KIND.value, u = IDX.value;
    if constexpr (kind == 0) glds16(wdst + k4_so(u), wv, rwm, (u % 18) * 32 * 9216 + (u / 18) * 128);
    else if constexpr (kind <= 2) bload16(act[u][kind - 1], l16, rhf, (T0 * 18 + 18 * (kind - 1) + u) * 1024);
    else if constexpr (kind == 3) { const int pc = min(w + 8 * u, 35); glds16(lds0 + K4_LG + pc * 1024, l16, rlg, (Qd * 4 + j) * 36864 + pc * 1024); }
    else if constexpr (kind == 4) warm(lds0 + K4_SCR, l16 * 8, rwd, (xcd >> 1) * 1327104 + min(r * 8 + w, 161) * 8192);
    else if constexpr (kind == 5) glds16(lds0 + K4_STP + w * 3072 + u * 1024, l16, rst, T0 * 1152 + u * 1024);
    else {
      const unsigned f = l16 < 512 ? 576 * Qd + 128 * j + (l16 >> 2) : 576 * Qd + 512 + 16 * j + (l16 >> 2) - 128;
      glds16(lds0 + K4_CS + (w & 1) * 1024, l16 < 576 ? f * 4 : OOB, rcs, (w & 1) * 4608 * 4);
    }
  };
  sfor<K4_P * K4_NS>([&](auto U) { doop(ic<0>{}, U); doop(ic<1>{}, U); doop(ic<2>{}, U); });
  unsigned wa[4];
  {
    const int r2 = 4 * g + ((lane >> 2) & 3);
#pragma unroll
    for (int c = 0; c < 4; ++c) wa[c] = lds0 + r2 * 128 + 32 * (c ^ ((r2 >> 1) & 3)) + 8 * (lane & 3);
  }
  f32x4 ag[4][2], au[4][2];
  bf16x4 fr[3][2][2];
  unsigned hd[2][2];
  auto rd1 = [&](auto N, auto I) {
    constexpr int n = N.value, i = I.value;
    if constexpr (n < 144) fr[n % 3][i >> 1][i & 1] = trh(wa[n % 4] + k4_so(n / 4) + 2048 * i);
    else if constexpr (n < 162) fr[n % 3][i >> 1][i & 1] = trh(lds0 + K4_LG + (l16 >> 6) * 32 + ((l16 >> 1) & 24) + (n - 144) * 1024 + (i >> 1) * 18432 + (i & 1) * 512);
  };
  auto rd = [&](auto N) { sfor<4>([&](auto I) { rd1(N, I); }); };
  auto mm = [&](auto N, auto KS, f32x4& g0, f32x4& g1, f32x4& u0, f32x4& u1) {
    constexpr int n = N.value, ks = KS.value;
    const bf16x8 fg = cat(fr[n % 3][0][0], fr[n % 3][0][1]), fu = cat(fr[n % 3][1][0], fr[n % 3][1][1]);
    bf16x4 nx[4];
    if constexpr (ks == 0) mfma16_z(g0, fg, act[ks][0]); else mfma16_ip(g0, fg, act[ks][0]);
    SBAR(); rd1(ic<n + 2>{}, ic<0>{}); SBAR();
    if constexpr (ks == 0) mfma16_z(g1, fg, act[ks][1]); else mfma16_ip(g1, fg, act[ks][1]);
    SBAR(); rd1(ic<n + 2>{}, ic<1>{}); SBAR();
    if constexpr (ks == 0) mfma16_z(u0, fu, act[ks][0]); else mfma16_ip(u0, fu, act[ks][0]);
    SBAR(); rd1(ic<n + 2>{}, ic<2>{}); SBAR();
    if constexpr (ks == 0) mfma16_z(u1, fu, act[ks][1]); else mfma16_ip(u1, fu, act[ks][1]);
    SBAR(); rd1(ic<n + 2>{}, ic<3>{}); SBAR();
    (void)nx;
  };
  auto epi = [&](const f32x4& G_, const f32x4& U_, float nmu, const f32x4& cg, const f32x4& cu, unsigned& d0, unsigned& d1) {
    const f32x4 v = (G_ + cg * nmu) * (U_ + cu * nmu);
    d0 = pk2(v[0], v[1]); d1 = pk2(v[2], v[3]);
  };
  auto epichunk = [&](auto GRP, auto C) {
    constexpr int grp = GRP.value, c = C.value;
    asm volatile("s_nop 4" : "+v"(ag[c][0]), "+v"(ag[c][1]), "+v"(au[c][0]), "+v"(au[c][1]));
    const unsigned csa = lds0 + K4_CS + ((l16 >> 4) & 48) + 256 * grp + 64 * c, sta = lds0 + K4_STT + 128 * w + ((l16 >> 2) & 60);
    const f32x4 cg = ldsr<f32x4>(csa), cu = ldsr<f32x4>(csa + 1024);
    sfor<2>([&](auto T) {
      const float nmu = ldsr<float>(sta + 64 * T.value);
      if constexpr ((c & 1) == 0) epi(ag[c][T.value], au[c][T.value], nmu, cg, cu, hd[T.value][0], hd[T.value][1]);
      else {
        unsigned d2, d3;
        epi(ag[c][T.value], au[c][T.value], nmu, cg, cu, d2, d3);
        bstore16<K4_HAUX>((u32x4){hd[T.value][0], hd[T.value][1], d2, d3}, rhh, l16, ((T0 + T.value) * 144 + 18 * Qd + 4 * j + 2 * grp + (c >> 1)) * 1024);
      }
    });
  };
  sfor<18>([&](auto SS) {
    constexpr int S = SS.value;
    constexpr int nw = k4_issued(S) - k4_need(S) - 1;
    static_assert(nw >= 0 && nw < 64, "");
    if constexpr (S < 9) asm volatile("s_waitcnt vmcnt(%4)" : "+v"(act[2 * S][0]), "+v"(act[2 * S][1]), "+v"(act[2 * S + 1][0]), "+v"(act[2 * S + 1][1]) : "i"(nw) : "memory");
    else wait_vm<nw>();
    bar();
    if constexpr (S == 0) STAMP(ws, 4, 1);
    if constexpr (S == 4) STAMP(ws, 4, 2);
    if constexpr (S == 9) STAMP(ws, 4, 3);
    if constexpr (S == 17) STAMP(ws, 4, 4);
    if constexpr (S == 0) { rd(ic<0>{}); rd(ic<1>{}); }
    if constexpr (S == 8) {
      sfor<2>([&](auto T) {
        const unsigned sa = lds0 + K4_STP + w * 3072 + T.value * 1152 + (l16 >> 1);
        const f32x2 p0 = ldsr<f32x2>(sa), p1 = ldsr<f32x2>(sa + 512), p2 = ldsr<f32x2>(sa + 1024);
        const f32x2 ps = p0 + p1 + (l16 < 256 ? p2 : (f32x2){0.f, 0.f});
        const float a = sum_x32(sum_x16(ps[0])), b = sum_x32(sum_x16(ps[1]));
        const float mu = a * (1.f / D_), var = fmaxf(b * (1.f / D_) - mu * mu, 0.f);
        ldsw<float>(lds0 + K4_STT + ((l16 & 256) << 2) + 128 * w + 64 * T.value + ((l16 >> 2) & 60), (l16 & 256) ? __builtin_amdgcn_rcpf(var + 1e-5f) : -mu);
      });
      SBAR();
    }
    sfor<4 * K4_NS>([&](auto L) {
      constexpr int l = L.value, n = 4 * K4_NS * S + l, t = n / 4, c = n % 4, ks = t % 18;
      mm(ic<n>{}, ic<ks>{}, ag[c][0], ag[c][1], au[c][0], au[c][1]);
      constexpr int no = k4_nops(S), NL = 4 * K4_NS;
      sfor<no>([&](auto I) {
        constexpr int nd = no ? no : 1, b0 = I.value * NL / nd;
        if constexpr (b0 == l) doop(ic<k4_op(S, I.value).kind>{}, ic<k4_op(S, I.value).idx>{});
      });
      if constexpr (ks == 17 && c >= 1) epichunk(ic<t / 18>{}, ic<c - 1>{});
      if constexpr (t == 18 && c == 0) epichunk(ic<0>{}, ic<3>{});
      SBAR();
    });
  });
  STAMP(ws, 4, 5);
  {
    f32x4 g0, g1, u0, u1;
    sfor<18>([&](auto K) {
      constexpr int ks = K.value, n = 144 + ks;
      mm(ic<n>{}, ic<ks>{}, g0, g1, u0, u1);
      if constexpr (ks == 0) epichunk(ic<1>{}, ic<3>{});
      SBAR();
    });
    const unsigned csa = lds0 + K4_CS + ((l16 >> 4) & 48) + 512, sta = lds0 + K4_STT + 128 * w + ((l16 >> 2) & 60);
    const f32x4 cg = ldsr<f32x4>(csa), cu = ldsr<f32x4>(csa + 1024);
    unsigned a0, a1, b0, b1;
    asm volatile("s_nop 9" : "+v"(g0), "+v"(g1), "+v"(u0), "+v"(u1));
    epi(g0, u0, ldsr<float>(sta), cg, cu, a0, a1);
    epi(g1, u1, ldsr<float>(sta + 64), cg, cu, b0, b1);
    auto s0 = __builtin_amdgcn_permlane32_swap(a0, b0, false, false);
    auto s1 = __builtin_amdgcn_permlane32_swap(a1, b1, false, false);
    bstore16<K4_HAUX>((u32x4){s0[0], s1[0], s0[1], s1[1]}, rhh, (l16 >> 9) * 144 * 1024 + (l16 & 511) + 512 * (j & 1), (T0 * 144 + 18 * Qd + 16 + (j >> 1)) * 1024);
    if (Qd == 0 && j == 0 && l16 < 512) *(GAS float*)(ws + WS_RS + (16 * T0) * 4 + (l16 >> 2)) = ldsr<float>(lds0 + K4_STT + 1024 + 128 * w + (l16 >> 2));
    STAMP(ws, 4, 6);
    wait_vm<0>();
    STAMP(ws, 4, 7);
    STAMP_FLUSH(ws, 4);
  }
}

constexpr int K5_SLOT = 17408, K5_R = 8, K5_P = 4;
constexpr int K5_LDS = K5_SLOT * K5_R + 256;
constexpr int k5_E(int i) { int e = 36; if (K5_P + 2 * (i + 1) < e) e = K5_P + 2 * (i + 1); if (i >= 0 && i + K5_R < e) e = i + K5_R; return e; }

extern "C" __global__ __launch_bounds__(512, 2) void k5_ffn2(const void* wd, char* ws, void* out) {
  const int tid = threadIdx.x, lane = tid & 63, w = rfl(tid >> 6);
  const int xcd = blockIdx.x & 7, r = blockIdx.x >> 3;
  const int sg = xcd >> 1, mt = 8 * (xcd & 1) + (r >> 2), nt = r & 3;
  const unsigned lds0 = (unsigned)(size_t)smem;
  const int t16 = lane & 15, g = lane >> 4;
  const int tile = 4 * mt + nt;
  const rsrc_t rpt = mk_rsrc(ws + WS_PT, 64 * 8 * 4 * PT_REC);
  STAMP_DECL; STAMP(ws, 5, 0);
  if (w >= 4) {
    const int l = w - 4;
    const rsrc_t rwd = mk_rsrc(wd, F_ * D_ * 2), rhh = mk_rsrc(ws + WS_HH, 128 * 144 * 1024);
    const rsrc_t rwt = mk_rsrc(ws + WS_WT, 4 * 4608 * 32);
    unsigned mv[2][2];
#pragma unroll
    for (int sp = 0; sp < 2; ++sp) {
#pragma unroll
      for (int i = 0; i < 2; ++i) {
        const int rho = 4 * (2 * l + i) + g, rr = sp ? ((rho & 7) | ((rho & 8) << 1) | ((rho & 16) >> 1)) : rho;
        mv[sp][i] = rr * 1152 + 256 * nt + 32 * ((t16 >> 1) ^ (rho & 7)) + 16 * (t16 & 1);
      }
    }
    const unsigned hs = ((8 * mt + 2 * l) * 144 + 36 * sg) * 1024, wsb = sg * 1152 * 1152, dl = lds0 + l * 2048;
    auto issue = [&](auto U) {
      constexpr int u = U.value, sp = (u % 18) >= 16;
      constexpr unsigned so = (u % K5_R) * K5_SLOT;
      glds16(dl + so + 8192, mv[sp][0], rwd, wsb + u * 32 * 1152);
      glds16(dl + so, lane * 16, rhh, hs + u * 1024);
      glds16(dl + so + 8192 + 1024, mv[sp][1], rwd, wsb + u * 32 * 1152);
      glds16(dl + so + 1024, lane * 16, rhh, hs + (144 + u) * 1024);
      if ((u & 3) == l) glds16(lds0 + so + 16384, lane * 16, rwt, (nt * 144 + 36 * sg + u) * 1024);
    };
    sfor<K5_P>([&](auto U) { issue(U); });
    sfor<36>([&](auto I) {
      constexpr int i = I.value, y = k5_E(i - 1) - (i + 2 < 36 ? i + 2 : 36);
      wait_vm<4 * y + y / 4>();
      bar();
      if constexpr (i == 0) STAMP(ws, 5, 1);
      if constexpr (i == 18) STAMP(ws, 5, 2);
      sfor<k5_E(i) - k5_E(i - 1)>([&](auto U) { issue(ic<k5_E(i - 1) + U.value>{}); });
    });
    bar();
    STAMP(ws, 5, 3);
  } else {
    const int r2 = 4 * g + ((lane >> 2) & 3);
    unsigned wa[8];
#pragma unroll
    for (int c = 0; c < 8; ++c) wa[c] = lds0 + 8192 + r2 * 256 + 32 * (c ^ (r2 & 7)) + 8 * (lane & 3);
    const unsigned wt = lds0 + 16384 + r2 * 32 + 8 * (lane & 3), ha = lds0 + w * 2048 + lane * 16;
    f32x4 acc[9][2] = {};
    bf16x4 fr[3][3][2];
    bf16x8 hb[2][2];
    auto rd1 = [&](auto N, auto I) {
      constexpr int n = N.value, u = n / 3, i = I.value;
      if constexpr (u < 36) {
        constexpr unsigned so = (u % K5_R) * K5_SLOT;
        constexpr int c = 3 * (n % 3) + i / 2;
        if constexpr (i >= 6) hb[u & 1][i - 6] = ldsr<bf16x8>(ha + so + (i - 6) * 1024);
        else if constexpr (c < 8) fr[n % 3][i / 2][i & 1] = trh(wa[c] + so + (i & 1) * 4096);
        else fr[n % 3][i / 2][i & 1] = trh(wt + so + (i & 1) * 512);
      }
    };
    sfor<36>([&](auto U) {
      constexpr int u = U.value;
      bar();
      if constexpr (u == 0) STAMP(ws, 5, 1);
      if constexpr (u == 18) STAMP(ws, 5, 2);
      if constexpr (u == 0) { rd1(ic<0>{}, ic<6>{}); rd1(ic<0>{}, ic<7>{}); sfor<6>([&](auto I) { rd1(ic<0>{}, I); }); sfor<6>([&](auto I) { rd1(ic<1>{}, I); }); }
      sfor<18>([&](auto M) {
        constexpr int mi = M.value, v = mi / 6, n = 3 * u + v, c = mi / 2;
        acc[c][mi & 1] = mfma16(cat(fr[n % 3][c % 3][0], fr[n % 3][c % 3][1]), hb[u & 1][mi & 1], acc[c][mi & 1]);
        SBAR();
        if constexpr (mi == 6 || mi == 8) rd1(ic<n + 2>{}, ic<6 + (mi - 6) / 2>{});
        rd1(ic<n + 2>{}, ic<mi % 6>{});
        SBAR();
      });
    });
    bar();
    STAMP(ws, 5, 3);
    if (w == sg) {
      sfor<2>([&](auto T) { sfor<9>([&](auto C) { ldsw<f32x4>(lds0 + (T.value * 9 + C.value) * 1024 + lane * 16, acc[C.value][T.value]); }); });
    } else {
      sfor<2>([&](auto T) {
        const unsigned pb = (((tile * 4 + w) * 4 + sg) * 2 + T.value) * PT_REC;
        sfor<4>([&](auto I) {
          constexpr int i = I.value;
          const f32x4 a = acc[2 * i][T.value], b = acc[2 * i + 1][T.value];
          bstore16<16>((u32x4){pk2(a[0], a[1]), pk2(a[2], a[3]), pk2(b[0], b[1]), pk2(b[2], b[3])}, rpt, lane * 16 + i * 1024, pb);
        });
        const f32x4 a = acc[8][T.value];
        bstore8<16>((u32x2){pk2(a[0], a[1]), pk2(a[2], a[3])}, rpt, lane * 8 + 4096, pb);
      });
    }
  }
  const int T = w >> 2, pi = w & 3, tt = 8 * mt + 2 * sg + T;
  const rsrc_t rhf = mk_rsrc(ws + WS_HF, 128 * 18 * 1024);
  const u32x4 hv = __builtin_amdgcn_raw_buffer_load_b128(rhf, lane * 16, (tt * 18 + 4 * nt + pi) * 1024, 0);
  const u32x2 hl = __builtin_amdgcn_raw_buffer_load_b64(rhf, lane * 16 + 8 * (nt & 1), (tt * 18 + 16 + (nt >> 1)) * 1024, 0);
  const float rs2 = *(const GAS float*)(ws + WS_RS + (16 * tt + t16) * 4);
  STAMP(ws, 5, 4);
  asm volatile("s_waitcnt vmcnt(0) lgkmcnt(0)" ::: "memory");
  STAMP(ws, 5, 5);
  bar();
  if (w == 4) {
    GAS unsigned* cnt = (GAS unsigned*)(ws + WS_CNT + tile * 64);
    if (lane == 0) __hip_atomic_fetch_add(cnt, 1u, __ATOMIC_RELAXED, __HIP_MEMORY_SCOPE_AGENT);
    for (int spin = 0; spin < (1 << 22); ++spin) {
      if (rfl(__hip_atomic_load(cnt, __ATOMIC_RELAXED, __HIP_MEMORY_SCOPE_AGENT)) >= 4u) break;
      __builtin_amdgcn_s_sleep(1);
    }
  }
  bar();
  STAMP(ws, 5, 6);
  u32x4 pv[3];
  u32x2 pl[3];
#pragma unroll
  for (int o = 0; o < 3; ++o) {
    const unsigned so = (((tile * 4 + sg) * 4 + ((sg + 1 + o) & 3)) * 2 + T) * PT_REC;
    pv[o] = __builtin_amdgcn_raw_buffer_load_b128(rpt, lane * 16 + pi * 1024, so, 16);
    if (pi == 0) pl[o] = __builtin_amdgcn_raw_buffer_load_b64(rpt, lane * 8 + 4096, so, 16);
  }
  const rsrc_t rout = mk_rsrc(out, S_ * D_ * 2);
  auto fin = [&](const f32x4& own, unsigned a0, unsigned a1, unsigned b0, unsigned b1, unsigned c0, unsigned c1, unsigned h0, unsigned h1, unsigned& d0, unsigned& d1) {
    const float v0 = ((own[0] + bflo(b0)) + (bflo(a0) + bflo(c0))) * rs2 + bflo(h0);
    const float v1 = ((own[1] + bfhi(b0)) + (bfhi(a0) + bfhi(c0))) * rs2 + bfhi(h0);
    const float v2 = ((own[2] + bflo(b1)) + (bflo(a1) + bflo(c1))) * rs2 + bflo(h1);
    const float v3 = ((own[3] + bfhi(b1)) + (bfhi(a1) + bfhi(c1))) * rs2 + bfhi(h1);
    d0 = pk2(v0, v1); d1 = pk2(v2, v3);
  };
  const unsigned orow = (16 * tt + t16) * 1152, own = lds0 + T * 9216 + lane * 16;
  {
    unsigned a0, a1, b0, b1;
    fin(ldsr<f32x4>(own + pi * 2048), pv[0][0], pv[0][1], pv[1][0], pv[1][1], pv[2][0], pv[2][1], hv[0], hv[1], a0, a1);
    fin(ldsr<f32x4>(own + pi * 2048 + 1024), pv[0][2], pv[0][3], pv[1][2], pv[1][3], pv[2][2], pv[2][3], hv[2], hv[3], b0, b1);
    auto s0 = __builtin_amdgcn_permlane16_swap(a0, b0, false, false);
    auto s1 = __builtin_amdgcn_permlane16_swap(a1, b1, false, false);
    bstore16<0>((u32x4){s0[0], s1[0], s0[1], s1[1]}, rout, orow + 256 * nt + 64 * pi + 32 * (g & 1) + 16 * (g >> 1), 0);
  }
  if (pi == 0) {
    unsigned e0, e1;
    fin(ldsr<f32x4>(own + 8192), pl[0][0], pl[0][1], pl[1][0], pl[1][1], pl[2][0], pl[2][1], hl[0], hl[1], e0, e1);
    bstore8<0>((u32x2){e0, e1}, rout, orow + 1024 + 32 * nt + 8 * g, 0);
  }
  wait_vm<0>();
  STAMP(ws, 5, 7);
  STAMP_FLUSH(ws, 5);
}

extern "C" __global__ void k9_end() {}

extern "C" __global__ __launch_bounds__(256) void k6_diag(char* ws) {
  __shared__ unsigned long long ra[256], rb[256];
  const int b = threadIdx.x;
  unsigned long long t0 = __builtin_amdgcn_s_memrealtime();
  unsigned long long a = MA ? 0ull : ~0ull, c = MB ? 0ull : ~0ull;
  const int nba = KA == 3 ? 144 : 256, nbb = KB == 3 ? 144 : 256;
  for (int w = 0; w < 8; ++w) {
    if (b < nba) { unsigned long long v = *(GAS unsigned long long*)(ws + WS_DBG + ((((size_t)KA * 256 + b) * 8 + w) * 32 + IA) * 8); a = MA ? (v > a ? v : a) : (v < a ? v : a); }
    if (b < nbb) { unsigned long long v = *(GAS unsigned long long*)(ws + WS_DBG + ((((size_t)KB * 256 + b) * 8 + w) * 32 + IB) * 8); c = MB ? (v > c ? v : c) : (v < c ? v : c); }
  }
  ra[b] = a; rb[b] = c;
  __syncthreads();
  if (b == 0) {
    for (int i = 1; i < 256; ++i) { a = MA ? (ra[i] > a ? ra[i] : a) : (ra[i] < a ? ra[i] : a); c = MB ? (rb[i] > c ? rb[i] : c) : (rb[i] < c ? rb[i] : c); }
    unsigned long long d = c - a;
    if (d > 10000) d = 10000;
    d *= MULT;
    while (__builtin_amdgcn_s_memrealtime() - t0 < d + 300) __builtin_amdgcn_s_sleep(2);
  }
}

extern "C" void kernel_launch(void* const* d_in, const int* in_sizes, int n_in, void* d_out, int out_size, void* d_ws, size_t ws_size,
                              hipStream_t stream) {
  const void *x = d_in[0], *wqkv = d_in[1], *wo = d_in[2], *wg = d_in[4], *wu = d_in[5], *wd = d_in[6];
  char* ws = (char*)d_ws;
  (void)hipFuncSetAttribute((const void*)k1_qkv, hipFuncAttributeMaxDynamicSharedMemorySize, K1_LDS);
  (void)hipFuncSetAttribute((const void*)k2_attn, hipFuncAttributeMaxDynamicSharedMemorySize, K2_LDS);
  (void)hipFuncSetAttribute((const void*)k3_wo, hipFuncAttributeMaxDynamicSharedMemorySize, K3_LDS);
  (void)hipFuncSetAttribute((const void*)k4_ffn1, hipFuncAttributeMaxDynamicSharedMemorySize, K4_LDS);
  (void)hipFuncSetAttribute((const void*)k5_ffn2, hipFuncAttributeMaxDynamicSharedMemorySize, K5_LDS);
  k1_qkv<<<256, 512, K1_LDS, stream>>>(x, wqkv, ws);
  k2_attn<<<256, 512, K2_LDS, stream>>>(ws, x, wo, wg, wu);
  k3_wo<<<256, 512, K3_LDS, stream>>>(x, wo, wg, wu, wd, ws);
  k4_ffn1<<<256, 512, K4_LDS, stream>>>(wg, wu, wd, ws);
  k5_ffn2<<<256, 512, K5_LDS, stream>>>(wd, ws, d_out);
  k6_diag<<<1, 256, 0, stream>>>(ws);
}
```

```cpp
constexpr unsigned long WS_DBG = 64u << 20;
#define KA 1
#define IA 0
#define MA 0
#define KB 5
#define IB 7
#define MB 1
#define MULT 10
#include <hip/hip_runtime.h>
#include <stdint.h>

typedef short bf16x8 __attribute__((ext_vector_type(8)));
typedef short bf16x4 __attribute__((ext_vector_type(4)));
typedef float f32x2 __attribute__((ext_vector_type(2)));
typedef float f32x4 __attribute__((ext_vector_type(4)));
typedef float f32x16 __attribute__((ext_vector_type(16)));
typedef unsigned u32x2 __attribute__((ext_vector_type(2)));
typedef unsigned u32x4 __attribute__((ext_vector_type(4)));
typedef __bf16 bf2_t __attribute__((ext_vector_type(2)));
typedef __amdgpu_buffer_rsrc_t rsrc_t;
#define LAS __attribute__((address_space(3)))
#define GAS __attribute__((address_space(1)))
#define DI __device__ __forceinline__

constexpr int S_ = 2048, D_ = 576, F_ = 4608;
constexpr float QSCALE = 0.23570226039551584f * 1.4426950408889634f;
constexpr unsigned ONES2 = 0x3f803f80u;

constexpr size_t QREC = 1280, KVREC = 2816;
constexpr unsigned OOB = 0x7fffff00u;
constexpr size_t WS_QF = 0;
constexpr size_t WS_KV = WS_QF + 32 * 64 * QREC;
constexpr size_t WS_OF = WS_KV + 32 * 64 * KVREC;
constexpr size_t WS_HF = WS_OF + 128 * 18 * 1024;
constexpr size_t WS_ST = WS_HF + 128 * 18 * 1024;
constexpr size_t WS_CS = WS_ST + 9 * 2048 * 8;
constexpr size_t WS_CNT = WS_CS + 2 * 4608 * 4;
constexpr size_t WS_RS = WS_CNT + 512 * 64;
constexpr size_t WS_LG = WS_RS + 2048 * 4;
constexpr size_t WS_WT = WS_LG + 8 * 4 * 36864;
constexpr size_t WS_HH = WS_WT + 4 * 4608 * 32;
constexpr size_t PT_REC = 4608;
constexpr size_t WS_PT = WS_HH + (size_t)128 * 144 * 1024;
constexpr size_t WS_END = WS_PT + (size_t)64 * 8 * 4 * PT_REC;

DI unsigned rfl(unsigned v) { return __builtin_amdgcn_readfirstlane(v); }
DI rsrc_t mk_rsrc(const void* p, unsigned bytes) { return __builtin_amdgcn_make_buffer_rsrc((void*)p, (short)0, (int)bytes, 0x00020000); }
DI void glds16(unsigned lds, unsigned voff, rsrc_t r, unsigned soff) {
  asm volatile("s_mov_b32 m0, %0\n\ts_nop 0\n\tbuffer_load_dwordx4 %1, %2, %3 offen lds" ::"s"(lds), "v"(voff), "s"(r), "s"(soff) : "memory", "m0");
}
DI void bload16(bf16x8& d, unsigned voff, rsrc_t r, unsigned soff) {
  asm volatile("buffer_load_dwordx4 %0, %1, %2, %3 offen" : "=v"(d) : "v"(voff), "s"(r), "s"(soff) : "memory");
}
DI void bload8(u32x2& d, unsigned voff, rsrc_t r, unsigned soff) {
  asm volatile("buffer_load_dwordx2 %0, %1, %2, %3 offen" : "=v"(d) : "v"(voff), "s"(r), "s"(soff) : "memory");
}
DI void bload4(unsigned& d, unsigned voff, rsrc_t r, unsigned soff) {
  asm volatile("buffer_load_dword %0, %1, %2, %3 offen" : "=v"(d) : "v"(voff), "s"(r), "s"(soff) : "memory");
}
DI void warm(unsigned lds, unsigned voff, rsrc_t r, unsigned soff) {
  asm volatile("s_mov_b32 m0, %0\n\ts_nop 0\n\tbuffer_load_dword %1, %2, %3 offen lds" ::"s"(lds), "v"(voff), "s"(r), "s"(soff) : "memory", "m0");
}
template <int N> DI void wait_vm() { asm volatile("s_waitcnt vmcnt(%0)" ::"i"(N) : "memory"); }
#define PIN(x) asm volatile("" : "+v"(x))
#define SBAR() __builtin_amdgcn_sched_barrier(0)
DI void bar() { SBAR(); asm volatile("s_barrier" ::: "memory"); SBAR(); }
DI void lds_bar() { SBAR(); asm volatile("s_waitcnt lgkmcnt(0)\n\ts_barrier" ::: "memory"); SBAR(); }
DI bf16x8 trfrag(unsigned a0, unsigned a1) {
  bf16x4 lo = __builtin_amdgcn_ds_read_tr16_b64_v4i16((LAS bf16x4*)(size_t)a0);
  bf16x4 hi = __builtin_amdgcn_ds_read_tr16_b64_v4i16((LAS bf16x4*)(size_t)a1);
  return __builtin_shufflevector(lo, hi, 0, 1, 2, 3, 4, 5, 6, 7);
}
DI bf16x4 trh(unsigned a) { return __builtin_amdgcn_ds_read_tr16_b64_v4i16((LAS bf16x4*)(size_t)a); }
DI bf16x8 cat(bf16x4 lo, bf16x4 hi) { return __builtin_shufflevector(lo, hi, 0, 1, 2, 3, 4, 5, 6, 7); }
template <class T> DI T ldsr(unsigned a) { return *(LAS T*)(size_t)a; }
template <class T> DI void ldsw(unsigned a, T v) { *(LAS T*)(size_t)a = v; }
DI unsigned pk2(float a, float b) { f32x2 v = {a, b}; return __builtin_bit_cast(unsigned, __builtin_convertvector(v, bf2_t)); }
DI float bflo(unsigned u) { return __uint_as_float(u << 16); }
DI float bfhi(unsigned u) { return __uint_as_float(u & 0xffff0000u); }
DI f32x4 mfma16(bf16x8 a, bf16x8 b, f32x4 c) { return __builtin_amdgcn_mfma_f32_16x16x32_bf16(a, b, c, 0, 0, 0); }
DI void mfma16_ip(f32x4& c, bf16x8 a, bf16x8 b) { asm volatile("v_mfma_f32_16x16x32_bf16 %0, %1, %2, %0" : "+v"(c) : "v"(a), "v"(b)); }
DI void mfma16_z(f32x4& c, bf16x8 a, bf16x8 b) { asm volatile("v_mfma_f32_16x16x32_bf16 %0, %1, %2, 0" : "=&v"(c) : "v"(a), "v"(b)); }
DI f32x16 mfma32(bf16x8 a, bf16x8 b, f32x16 c) { return __builtin_amdgcn_mfma_f32_32x32x16_bf16(a, b, c, 0, 0, 0); }
DI float xor32(float v) { auto r = __builtin_amdgcn_permlane32_swap(__float_as_uint(v), __float_as_uint(v), false, false); return __uint_as_float((threadIdx.x & 32) ? r[0] : r[1]); }
DI float sum_x32(float v) { auto r = __builtin_amdgcn_permlane32_swap(__float_as_uint(v), __float_as_uint(v), false, false); return __uint_as_float(r[0]) + __uint_as_float(r[1]); }
DI float max_x32(float v) { auto r = __builtin_amdgcn_permlane32_swap(__float_as_uint(v), __float_as_uint(v), false, false); return fmaxf(__uint_as_float(r[0]), __uint_as_float(r[1])); }
DI float sum_x16(float v) { auto r = __builtin_amdgcn_permlane16_swap(__float_as_uint(v), __float_as_uint(v), false, false); return __uint_as_float(r[0]) + __uint_as_float(r[1]); }
template <int AUX> DI void bstore16(u32x4 v, rsrc_t r, unsigned voff, unsigned soff) {
  if constexpr (AUX == 16) asm volatile("buffer_store_dwordx4 %0, %1, %2, %3 offen sc1\n\ts_nop 1" ::"v"(v), "v"(voff), "s"(r), "s"(soff) : "memory");
  else asm volatile("buffer_store_dwordx4 %0, %1, %2, %3 offen\n\ts_nop 1" ::"v"(v), "v"(voff), "s"(r), "s"(soff) : "memory");
}
template <int AUX> DI void bstore8(u32x2 v, rsrc_t r, unsigned voff, unsigned soff) { __builtin_amdgcn_raw_buffer_store_b64(v, r, voff, soff, AUX); }
template <int AUX> DI void bstore4(unsigned v, rsrc_t r, unsigned voff, unsigned soff) { __builtin_amdgcn_raw_buffer_store_b32(v, r, voff, soff, AUX); }

template <int... I> struct iseq {};
template <int N, int... I> struct mkseq : mkseq<N - 1, N - 1, I...> {};
template <int... I> struct mkseq<0, I...> { typedef iseq<I...> type; };
template <int V> struct ic { static constexpr int value = V; };
template <class Fn, int... I> DI void sfor_(Fn&& f, iseq<I...>) { (f(ic<I>{}), ...); }
template <int N, class Fn> DI void sfor(Fn&& f) { sfor_(f, typename mkseq<N>::type{}); }

extern __shared__ __attribute__((aligned(16))) char smem[];
#define K1_AUX 0
#define STAMP(ws, kid, i)
#define STAMP_DECL
#define STAMP_FLUSH(ws, kid)

constexpr int K1_SLOT = 32768, K1_TBL = K1_SLOT * 4;
constexpr int K1_LDS = K1_TBL + 1024 + 512 + 64 + 256;
extern "C" __global__ void k9_end();

template <int NN> DI void k1_body(char* ws, rsrc_t rx, rsrc_t rw, int mt, int nt, int w, int lane, unsigned lds0) {
  const int t16 = lane & 15, g = lane >> 4, q4 = (lane >> 2) & 3, p = lane & 3, m = w & 3;
  constexpr int NT = NN ? 3 : 4;
  unsigned xv[2], wv[2];
#pragma unroll
  for (int i = 0; i < 2; ++i) {
    const int row = 8 * (2 * w + i) + (lane >> 3);
    xv[i] = (128 * mt + row) * 1152 + 16 * ((lane & 7) ^ ((row >> 1) & 7));
    int li = 64 * (2 * w + i) + lane;
    if (li >= 896) li -= 128;
    const int r64 = (li * 4682) >> 16, c = li - 14 * r64;
    const int rho = r64 & 31, k = (r64 & 32) + 8 * ((rho >> 2) & 3) + 4 * (rho >> 4) + (rho & 3);
    wv[i] = k * 3456 + ((216 * nt) & ~15) + 16 * c;
  }
  STAMP_DECL; STAMP(ws, 1, 0);
  auto issue1 = [&](int slab, int i) {
    const unsigned dst = lds0 + (slab & 3) * K1_SLOT + w * 2048 + (i >> 1) * 1024;
    if (i & 1) glds16(dst + 16384, wv[i >> 1], rw, slab * 64 * 3456);
    else glds16(dst, xv[i >> 1], rx, slab * 128);
  };
#pragma unroll
  for (int sl = 0; sl < 2; ++sl)
#pragma unroll
    for (int i = 0; i < 4; ++i) issue1(sl, i);
  STAMP(ws, 1, 1);
  const unsigned wa = lds0 + 16384 + (4 * g + q4) * 224 + (nt & 1) * 8;
  unsigned ad[2][5];
  ad[0][0] = wa + 8 * p + 144 * NN;
  ad[0][1] = wa + 72 * (p < 3 ? p : 2) + 32;
  ad[0][2] = NN && m >= 2 ? ad[0][1] : ad[0][0] + 72 * (m >> 1) + 40 * (m & 1);
  ad[0][3] = lds0 + (32 * m + t16) * 128 + 16 * (g ^ ((t16 >> 1) & 7));
  ad[0][4] = ad[0][3] ^ 64;
#pragma unroll
  for (int i = 0; i < 5; ++i) { ad[1][i] = ad[0][i] + 2 * K1_SLOT; PIN(ad[1][i]); }
  f32x4 acc[4][2] = {}, cs = {};
  bf16x8 ones = __builtin_bit_cast(bf16x8, (u32x4){ONES2, ONES2, ONES2, ONES2});
  PIN(ones);
  bf16x8 fb[2][2];
  bf16x4 fa[2][NT + 1][2];
  constexpr int NR = 2 * NT + 4;
  auto rd1 = [&](auto KS, auto I) {
    constexpr int ks = KS.value, i = I.value, sp = ks & 1, sl = (ks >> 1) & 3, ti = (i - 2) >> 1;
    constexpr unsigned so = (sl & 1) * K1_SLOT;
    if constexpr (ks < 18 && i < NR) {
      if constexpr (i < 2) fb[sp][i] = ldsr<bf16x8>(ad[sl >> 1][3 + sp] + so + i * 2048);
      else {
        constexpr int ai = ti == NT ? 2 : (NN && ti == 2) ? 1 : 0;
        constexpr unsigned co = ai ? 0 : 72 * (ti >> 1) + 40 * (ti & 1);
        fa[sp][ti][i & 1] = trh(ad[sl >> 1][ai] + so + co + sp * 32 * 224 + (i & 1) * 16 * 224);
      }
    }
  };
  sfor<9>([&](auto SL) {
    constexpr int sl = SL.value;
    constexpr int iss = sl == 0 ? 2 : (sl + 3 < 9 ? sl + 3 : 9);
    constexpr int need = sl == 0 ? 0 : (sl + 1 < 9 ? sl + 1 : 8);
    wait_vm<4 * (iss - need - 1)>();
    bar();
    if constexpr (sl == 0) STAMP(ws, 1, 2);
    if constexpr (sl == 4) STAMP(ws, 1, 3);
    if constexpr (sl <= 1) sfor<NR>([&](auto I) { rd1(ic<2 * sl>{}, I); });
    sfor<2>([&](auto SP) {
      constexpr int sp = SP.value, ks = 2 * sl + sp;
      bf16x8 tl;
      if constexpr (NN) tl = t16 >= 12 ? ones : cat(fa[sp][2][0], fa[sp][2][1]);
      sfor<9>([&](auto M) {
        constexpr int mi = M.value, ti = mi >> 1;
        if constexpr (mi == 8) cs = mfma16(cat(fa[sp][NT][0], fa[sp][NT][1]), ones, cs);
        else if constexpr (NN && ti == 3) acc[3][mi & 1] = mfma16(fb[sp][mi & 1], fb[sp][mi & 1], acc[3][mi & 1]);
        else if constexpr (NN && ti == 2) acc[2][mi & 1] = mfma16(tl, fb[sp][mi & 1], acc[2][mi & 1]);
        else acc[ti][mi & 1] = mfma16(cat(fa[sp][ti][0], fa[sp][ti][1]), fb[sp][mi & 1], acc[ti][mi & 1]);
        SBAR();
        if constexpr (ks != 1) {
          if constexpr (mi < 3) { rd1(ic<ks + 1>{}, ic<2 * mi>{}); rd1(ic<ks + 1>{}, ic<2 * mi + 1>{}); }
          else rd1(ic<ks + 1>{}, ic<3 + mi>{});
        }
        if constexpr (sl == 0) { if constexpr (mi & 1) issue1(2 + sp, mi >> 1); }
        else if constexpr (sl + 3 < 9 && (mi == 2 + 2 * NN || mi == 6 + 2 * NN)) issue1(sl + 3, 2 * sp + (mi >= 6));
        SBAR();
      });
    });
  });
  STAMP(ws, 1, 4);
  const unsigned tb = lds0 + K1_TBL, cb = tb + 1024, dummy = cb + 512;
  ldsw<f32x4>(t16 == 0 ? cb + (w * 16 + 4 * g) * 4 : dummy, cs);
  if constexpr (NN) sfor<2>([&](auto T) {
    const unsigned a = tb + (32 * m + 16 * T.value + t16) * 8;
    const f32x4 gr = acc[3][T.value];
    const float d01 = (p & 1) ? gr[1] : gr[0], d23 = (p & 1) ? gr[3] : gr[2];
    ldsw<float>(g == 3 ? a : dummy, acc[2][T.value][0]);
    ldsw<float>(g == q4 ? a + 4 : dummy, (p & 2) ? d23 : d01);
  });
  lds_bar();
  f32x2 st[2];
  sfor<2>([&](auto T) {
    const f32x2 v = ldsr<f32x2>(tb + (32 * m + 16 * T.value + t16) * 8);
    const float mu = v[0] * (1.f / D_), rs = __builtin_amdgcn_rsqf(fmaxf(v[1] * (1.f / D_) - mu * mu, 0.f) + 1e-5f);
    st[T.value] = (f32x2){rs, -rs * mu};
  });
  const rsrc_t ro = mk_rsrc(ws + WS_QF, 32 * 64 * (QREC + KVREC));
  const int tile32 = 4 * mt + m, key = 16 * (g >> 1) + t16;
  const unsigned lo_qk = (g & 1) * 512 + key * 16, lo_v = 1280 + key * 48 + (g & 1) * 16;
  sfor<(NN ? 2 : 4)>([&](auto TI) {
    constexpr int ti = TI.value, tile = 4 * NN + ti;
    const int pg = 3 * nt + (tile >> 1), type = pg >> 4, rec = (2 * (pg & 15) + (tile & 1)) * 64 + tile32;
    const f32x4 c = ldsr<f32x4>(cb + (tile * 16 + 4 * g) * 4);
    const float sc = type == 0 ? QSCALE : 1.f;
    const f32x4 v0 = acc[ti][0] * (st[0][0] * sc) + c * (st[0][1] * sc), v1 = acc[ti][1] * (st[1][0] * sc) + c * (st[1][1] * sc);
    auto s0 = __builtin_amdgcn_permlane32_swap(pk2(v0[0], v0[1]), pk2(v1[0], v1[1]), false, false);
    auto s1 = __builtin_amdgcn_permlane32_swap(pk2(v0[2], v0[3]), pk2(v1[2], v1[3]), false, false);
    bstore16<K1_AUX>((u32x4){s0[0], s1[0], s0[1], s1[1]}, ro, type == 2 ? lo_v : lo_qk, type == 0 ? rec * QREC : WS_KV + rec * KVREC);
  });
  if constexpr (NN) {
    const int pg = 3 * nt + g, type = pg >> 4, rec = 2 * (pg & 15) * 64 + tile32;
    const f32x4 c = ldsr<f32x4>(cb + (96 + 4 * g) * 4);
    const float sc = type == 0 ? QSCALE : 1.f;
    const unsigned rb = type == 0 ? rec * QREC : WS_KV + rec * KVREC, hs = type == 0 ? 64 * QREC : 64 * KVREC;
    const unsigned oq = g < 3 && type < 2 ? rb + 1024 + t16 * 8 : OOB, ov = g < 3 && type == 2 ? rb + 1280 + 32 + t16 * 48 : OOB;
    sfor<2>([&](auto T) {
      const f32x4 v = acc[2][T.value] * (st[T.value][0] * sc) + c * (st[T.value][1] * sc);
      const unsigned d0 = pk2(v[0], v[1]), d1 = pk2(v[2], v[3]);
      if (nt <= 10) { bstore8<K1_AUX>((u32x2){d0, 0u}, ro, oq + T.value * 128, 0); bstore8<K1_AUX>((u32x2){d1, 0u}, ro, oq + hs + T.value * 128, 0); }
      if (nt >= 10) { bstore16<K1_AUX>((u32x4){d0, 0x00003f80u, 0u, 0u}, ro, ov, T.value * 768); bstore16<K1_AUX>((u32x4){d1, 0x00003f80u, 0u, 0u}, ro, ov, 64 * KVREC + T.value * 768); }
    });
  }
  STAMP(ws, 1, 5);
  STAMP(ws, 1, 6);
  STAMP_FLUSH(ws, 1);
}

extern "C" __global__ __launch_bounds__(512, 2) void k1_qkv(const void* x, const void* wqkv, char* ws) {
  const int tid = threadIdx.x, lane = tid & 63, w = rfl(tid >> 6);
  const int xcd = blockIdx.x & 7, r = blockIdx.x >> 3;
  const int mt = 4 * (xcd & 3) + (r & 3), nt = 8 * (xcd >> 2) + (r >> 2);
  const unsigned lds0 = (unsigned)(size_t)smem;
  const rsrc_t rx = mk_rsrc(x, S_ * D_ * 2), rw = mk_rsrc(wqkv, D_ * 3 * D_ * 2);
  if (w == 7) {
    unsigned long long pc; unsigned back, size;
    asm volatile("s_getpc_b64 %0\n.Lk1pc%=:\n\ts_mov_b32 %1, .Lk1pc%=-k1_qkv\n\ts_mov_b32 %2, k9_end-k1_qkv" : "=s"(pc), "=s"(back), "=s"(size));
    warm(lds0 + K1_TBL + 1024 + 512 + 64, lane < 24 ? (r * 24 + lane) * 128 : OOB, mk_rsrc((const void*)(pc - back), size), 0);
  }
  if (w < 4) k1_body<0>(ws, rx, rw, mt, nt, w, lane, lds0);
  else k1_body<1>(ws, rx, rw, mt, nt, w, lane, lds0);
}

constexpr int K2_SCR = 16 * KVREC;
constexpr int K2_LDS = K2_SCR + 256;

template <bool EDGE> DI void k2_body(char* ws, const void* x, const void* wo, const void* wg, const void* wu, int xcd, int r, int head, int qb, int w, int lane, unsigned lds0) {
  const int q = lane & 31, h = lane >> 5, G = lane >> 4;
  const int qt = 8 * qb + w;
  const rsrc_t rq = mk_rsrc(ws + WS_QF, 32 * 64 * QREC), rkv = mk_rsrc(ws + WS_KV, 32 * 64 * KVREC);
  STAMP_DECL; STAMP(ws, 2, 0);
  bf16x8 qm; unsigned qtl;
  bload16(qm, lane * 16, rq, (head * 64 + qt) * QREC);
  bload4(qtl, 1024 + q * 8, rq, (head * 64 + qt) * QREC);
  if constexpr (EDGE) {
    const unsigned src = (head * 64) * KVREC;
#pragma unroll
    for (int i = 0; i < 3; ++i) { const int pc = min(w + 8 * i, 21); glds16(lds0 + 8 * KVREC + pc * 1024, lane * 16, rkv, src + pc * 1024); }
  } else {
    const unsigned src = (head * 64 + 8 * qb - 8) * KVREC;
#pragma unroll
    for (int i = 0; i < 6; ++i) { const int pc = min(w + 8 * i, 43); glds16(lds0 + pc * 1024, lane * 16, rkv, src + pc * 1024); }
  }
  STAMP(ws, 2, 1);
  asm volatile("s_waitcnt vmcnt(0)" : "+v"(qm), "+v"(qtl)::"memory");
  bar();
  STAMP(ws, 2, 2);
  {
    const int v = r * 8 + w;
    const int l = (v % 81) * 64 + lane, row = (l * 7282) >> 16;
    if (v < 162) warm(lds0 + K2_SCR, row * 9216 + xcd * 1152 + (l - 9 * row) * 128, mk_rsrc(v < 81 ? wg : wu, D_ * F_ * 2), 0);
    else if (v < 243) warm(lds0 + K2_SCR, l * 128, mk_rsrc(wo, D_ * D_ * 2), 0);
    if (v < 36) warm(lds0 + K2_SCR, (v * 64 + lane) * 128, mk_rsrc(x, S_ * D_ * 2), xcd * 256 * 1152);
  }
  const bf16x4 qt4 = __builtin_bit_cast(bf16x4, (u32x2){h ? 0u : qtl, 0u});
  const unsigned kb = lds0 + w * KVREC + lane * 16, ktb = lds0 + w * KVREC + 1024 + q * 8;
  const unsigned vb = lds0 + w * KVREC + 1280 + (4 * (G >> 1) + ((lane >> 2) & 3)) * 48 + ((G & 1) ? 32 : 8 * (lane & 3));
  f32x16 s[9];
  sfor<9>([&](auto I) {
    constexpr int i = I.value;
    const unsigned off = EDGE ? (unsigned)(max(w + i, 8) - w) * KVREC : i * KVREC;
    const bf16x8 km = ldsr<bf16x8>(kb + off);
    const bf16x4 kt = ldsr<bf16x4>(ktb + off);
    f32x16 a = mfma32(km, qm, (f32x16){});
    s[i] = __builtin_amdgcn_mfma_f32_32x32x8bf16_1k(kt, qt4, a, 0, 0, 0);
    if (EDGE && w + i < 8) {
#pragma unroll
      for (int r = 0; r < 16; ++r) s[i][r] = -1e30f;
    }
  });
  const int qq = q - 4 * h;
#pragma unroll
  for (int r = 0; r < 16; ++r) s[8][r] = ((r & 3) + 8 * (r >> 2) <= qq) ? s[8][r] : s[0][r];
  float m4[4];
#pragma unroll
  for (int c = 0; c < 4; ++c) m4[c] = fmaxf(s[1][2 * c], s[1][2 * c + 1]);
#pragma unroll
  for (int i = 1; i < 9; ++i)
#pragma unroll
    for (int e = (i == 1 ? 4 : 0); e < 8; ++e) m4[e & 3] = fmaxf(fmaxf(m4[e & 3], s[i][2 * e]), s[i][2 * e + 1]);
  float mx = max_x32(fmaxf(fmaxf(m4[0], m4[1]), fmaxf(m4[2], m4[3])));
  STAMP(ws, 2, 3);
  f32x16 o = {};
  sfor<8>([&](auto I) {
    constexpr int i = I.value + 1;
    const unsigned off = EDGE ? (unsigned)(max(w + i, 8) - w) * KVREC : i * KVREC;
    float pv[16];
#pragma unroll
    for (int e = 0; e < 8; ++e) {
      const f32x2 dd = (f32x2){s[i][2 * e], s[i][2 * e + 1]} - (f32x2){mx, mx};
      pv[2 * e] = __builtin_amdgcn_exp2f(dd[0]); pv[2 * e + 1] = __builtin_amdgcn_exp2f(dd[1]);
    }
    if constexpr (i < 8) {
#pragma unroll
      for (int ks = 0; ks < 2; ++ks) {
        const u32x4 pp = {pk2(pv[8 * ks], pv[8 * ks + 1]), pk2(pv[8 * ks + 2], pv[8 * ks + 3]), pk2(pv[8 * ks + 4], pv[8 * ks + 5]), pk2(pv[8 * ks + 6], pv[8 * ks + 7])};
        o = mfma32(trfrag(vb + off + ks * 768, vb + off + ks * 768 + 384), __builtin_bit_cast(bf16x8, pp), o);
      }
    } else {
      const unsigned off0 = EDGE ? (unsigned)(8 - w) * KVREC : 0;
#pragma unroll
      for (int ks = 0; ks < 2; ++ks) {
        u32x4 pd, po;
#pragma unroll
        for (int e = 0; e < 4; ++e) {
          const int r = 8 * ks + 2 * e;
          const bool v0 = ((r & 3) + 8 * (r >> 2) <= qq), v1 = (((r + 1) & 3) + 8 * ((r + 1) >> 2) <= qq);
          pd[e] = pk2(v0 ? pv[r] : 0.f, v1 ? pv[r + 1] : 0.f);
          po[e] = pk2(v0 ? 0.f : pv[r], v1 ? 0.f : pv[r + 1]);
        }
        o = mfma32(trfrag(vb + off + ks * 768, vb + off + ks * 768 + 384), __builtin_bit_cast(bf16x8, pd), o);
        o = mfma32(trfrag(vb + off0 + ks * 768, vb + off0 + ks * 768 + 384), __builtin_bit_cast(bf16x8, po), o);
      }
    }
  });
  STAMP(ws, 2, 4);
  auto lr = __builtin_amdgcn_permlane32_swap(__float_as_uint(o[10]), __float_as_uint(o[10]), false, false);
  const float inv = 1.f / __uint_as_float(lr[0]);
  const rsrc_t ro = mk_rsrc(ws + WS_OF, 128 * 18 * 1024);
  const u32x4 om = {pk2(o[0] * inv, o[1] * inv), pk2(o[2] * inv, o[3] * inv), pk2(o[4] * inv, o[5] * inv), pk2(o[6] * inv, o[7] * inv)};
  const unsigned tt = 2 * qt + (q >> 4);
  bstore16<0>(om, ro, (tt * 18 + (head >> 1)) * 1024 + ((q & 15) + 16 * (2 * (head & 1) + h)) * 16, 0);
  if (h == 0) bstore4<0>(pk2(o[8] * inv, o[9] * inv), ro, (tt * 18 + 16 + (head >> 4)) * 1024 + ((q & 15) + 16 * ((head >> 2) & 3)) * 16 + (head & 3) * 4, 0);
  STAMP(ws, 2, 5);
  STAMP(ws, 2, 6);
  STAMP_FLUSH(ws, 2);
}

extern "C" __global__ __launch_bounds__(512, 2) void k2_attn(char* ws, const void* x, const void* wo, const void* wg, const void* wu) {
  const int tid = threadIdx.x, lane = tid & 63, w = rfl(tid >> 6);
  const int xcd = blockIdx.x & 7, r = blockIdx.x >> 3;
  const int head = 4 * xcd + (r & 3), qb = r >> 2;
  const unsigned lds0 = (unsigned)(size_t)smem;
  if (blockIdx.x == 0) *(GAS unsigned*)(ws + WS_CNT + tid * 64) = 0u;
  if (qb == 0) k2_body<true>(ws, x, wo, wg, wu, xcd, r, head, qb, w, lane, lds0);
  else k2_body<false>(ws, x, wo, wg, wu, xcd, r, head, qb, w, lane, lds0);
}

constexpr int K3_LDS = 72 * 1024;

extern "C" __global__ __launch_bounds__(512, 2) void k3_wo(const void* x, const void* wo, const void* wg, const void* wu, const void* wd, char* ws) {
  const int tid = threadIdx.x, lane = tid & 63, w = rfl(tid >> 6);
  const unsigned lds0 = (unsigned)(size_t)smem;
  const int t16 = lane & 15, g = lane >> 4;
  const int xcd = blockIdx.x & 7, br = blockIdx.x >> 3;
  if (br >= 18) {
    const int sb = br - 18, gw = (sb * 8 + xcd) * 8 + w;
    u32x4 dv;
    const int drow = 8 * gw + (lane >> 3);
    if (gw < 576) dv = *(const GAS u32x4*)((const char*)wd + (size_t)drow * 1152 + 1024 + (lane & 7) * 16);
    for (int u = sb; u < 18; u += 14) {
      const int mat = u >= 9, un = u - 9 * mat;
      const char* base = (const char*)(mat ? wu : wg) + (9 * xcd + un) * 128 + (lane & 7) * 16 + (size_t)(72 * w + (lane >> 3)) * 9216;
      u32x4 v[9];
#pragma unroll
      for (int i = 0; i < 9; ++i) v[i] = *(const GAS u32x4*)(base + (size_t)i * 8 * 9216);
      if (un == 8) {
#pragma unroll
        for (int i = 0; i < 9; ++i)
          *(GAS u32x4*)(ws + WS_LG + ((xcd * 4 + ((lane & 7) >> 1)) * 2 + mat) * 18432 + (72 * w + 8 * i + (lane >> 3)) * 32 + (lane & 1) * 16) = v[i];
      }
      float a[8] = {};
#pragma unroll
      for (int i = 0; i < 9; ++i)
#pragma unroll
        for (int e = 0; e < 4; ++e) { a[2 * e] += bflo(v[i][e]); a[2 * e + 1] += bfhi(v[i][e]); }
#pragma unroll
      for (int e = 0; e < 8; ++e) a[e] = sum_x32(sum_x16(a[e]));
      __syncthreads();
      if (lane < 16) { ldsw<f32x4>(lds0 + w * 512 + lane * 32, (f32x4){a[0], a[1], a[2], a[3]}); ldsw<f32x4>(lds0 + w * 512 + lane * 32 + 16, (f32x4){a[4], a[5], a[6], a[7]}); }
      __syncthreads();
      if (tid < 64) {
        float t = 0.f;
#pragma unroll
        for (int i = 0; i < 16; ++i) t += ldsr<float>(lds0 + i * 256 + tid * 4);
        *(GAS float*)(ws + WS_CS + (mat * 4608 + (9 * xcd + un) * 64 + tid) * 4) = t;
      }
    }
    if (gw < 576) {
      const int pr = (drow % 576) >= 512 ? ((drow & ~24) | ((drow & 8) << 1) | ((drow & 16) >> 1)) : drow;
      *(GAS u32x4*)(ws + WS_WT + (((lane & 7) >> 1) * 4608 + pr) * 32 + (lane & 1) * 16) = dv;
    }
    return;
  }
  const int mt = 2 * xcd + (br >= 9), nt = br >= 9 ? br - 9 : br;
  const int T = 8 * mt + w;
  const rsrc_t rwo = mk_rsrc(wo, D_ * D_ * 2), rof = mk_rsrc(ws + WS_OF, 128 * 18 * 1024), rx = mk_rsrc(x, S_ * D_ * 2);
  const int rho = 8 * (w & 3) + (lane >> 3), rg = (rho >> 2) & 3, rj = 4 * (rho >> 4) + (rho & 3);
  const unsigned cbyte = 128 * nt + 32 * (((lane & 7) >> 1) ^ ((rho >> 1) & 3)) + 16 * (lane & 1);
  const unsigned wv = (20 * (rg >> 1) + 8 * (rg & 1) + rj) * 1152 + cbyte;
  const int th = 16 * (w >> 2) + 4 * rg + (rj >> 1);
  const unsigned wvt = (18 * th + ((th & 1) ? 0 : 16) + (rj & 1)) * 1152 + cbyte;
  STAMP_DECL; STAMP(ws, 3, 0);
  bf16x8 of[18];
  u32x2 xr[4];
  auto op = [&](auto N) {
    constexpr int n = N.value, p = n / 3;
    if constexpr (n >= 27) bload8(xr[n - 27], (16 * T + t16) * 1152 + 128 * nt + 32 * (n - 27) + 8 * g, rx, 0);
    else if constexpr (n % 3 == 0) { if constexpr (p < 8) glds16(lds0 + (w + 8 * p) * 1024, wv, rwo, (2 * p + (w >> 2)) * 36 * 1152); else glds16(lds0 + (w + 8 * p) * 1024, wvt, rwo, 0); }
    else bload16(of[2 * p + n % 3 - 1], lane * 16, rof, (T * 18 + 2 * p + n % 3 - 1) * 1024);
  };
  constexpr int PRO = 12;
  sfor<PRO>([&](auto N) { op(N); });
  STAMP(ws, 3, 1);
  unsigned wa[4];
  {
    const int r2 = 4 * g + ((lane >> 2) & 3);
#pragma unroll
    for (int c = 0; c < 4; ++c) wa[c] = lds0 + r2 * 128 + 32 * (c ^ ((r2 >> 1) & 3)) + 8 * (lane & 3);
  }
  f32x4 acc[4] = {};
  bf16x4 fr[2][4][2];
  auto rd1 = [&](auto KS, auto I) { constexpr int ks = KS.value, i = I.value; fr[ks & 1][i >> 1][i & 1] = trh(wa[i >> 1] + ks * 4096 + (i & 1) * 2048); };
  sfor<9>([&](auto P) {
    constexpr int p = P.value;
    constexpr int iss = PRO + 4 * p < 31 ? PRO + 4 * p : 31;
    constexpr int need = p == 0 ? 2 : (p < 8 ? 3 * p + 3 : 26);
    asm volatile("s_waitcnt vmcnt(%2)" : "+v"(of[2 * p]), "+v"(of[2 * p + 1]) : "i"(iss - need - 1) : "memory");
    bar();
    if constexpr (p == 0) STAMP(ws, 3, 2);
    if constexpr (p == 3) STAMP(ws, 3, 3);
    if constexpr (p == 6) STAMP(ws, 3, 4);
    if constexpr (p <= 1) sfor<8>([&](auto I) { rd1(ic<2 * p>{}, I); });
    sfor<2>([&](auto SP) {
      constexpr int ks = 2 * p + SP.value;
      sfor<4>([&](auto C) {
        acc[C.value] = mfma16(cat(fr[ks & 1][C.value][0], fr[ks & 1][C.value][1]), of[ks], acc[C.value]);
        SBAR();
        if constexpr (ks + 1 < 18 && ks != 1) { rd1(ic<ks + 1>{}, ic<2 * C.value>{}); rd1(ic<ks + 1>{}, ic<2 * C.value + 1>{}); }
        constexpr int n = PRO + 4 * p + 2 * SP.value + (C.value >> 1);
        if constexpr ((C.value & 1) && n < 31) op(ic<n>{});
        SBAR();
      });
    });
  });
  asm volatile("s_waitcnt vmcnt(0)" : "+v"(xr[0]), "+v"(xr[1]), "+v"(xr[2]), "+v"(xr[3])::"memory");
  unsigned d[4][2];
  float s1 = 0.f, s2 = 0.f;
#pragma unroll
  for (int c = 0; c < 4; ++c) {
    d[c][0] = pk2(acc[c][0] + bflo(xr[c][0]), acc[c][1] + bfhi(xr[c][0]));
    d[c][1] = pk2(acc[c][2] + bflo(xr[c][1]), acc[c][3] + bfhi(xr[c][1]));
#pragma unroll
    for (int e = 0; e < 2; ++e) { const float a = bflo(d[c][e]), b = bfhi(d[c][e]); s1 += a + b; s2 += a * a + b * b; }
  }
  s1 = sum_x32(sum_x16(s1)); s2 = sum_x32(sum_x16(s2));
  const rsrc_t rhf = mk_rsrc(ws + WS_HF, 128 * 18 * 1024);
  bstore16<0>((u32x4){d[0][0], d[0][1], d[1][0], d[1][1]}, rhf, lane * 16, (T * 18 + 2 * nt) * 1024);
  bstore16<0>((u32x4){d[2][0], d[2][1], d[3][0], d[3][1]}, rhf, lane * 16, (T * 18 + 2 * nt + 1) * 1024);
  if (g == 0) *(GAS f32x2*)(ws + WS_ST + ((T * 9 + nt) * 16 + t16) * 8) = (f32x2){s1, s2};
  STAMP(ws, 3, 5);
  STAMP(ws, 3, 6);
  STAMP_FLUSH(ws, 3);
}

#define K4_HAUX 16
constexpr int K4_NS = 2, K4_R = 4, K4_P = 2;
constexpr int K4_LG = K4_R * K4_NS * 8192;
constexpr int K4_STP = K4_LG + 36864;
constexpr int K4_CS = K4_STP + 24576;
constexpr int K4_STT = K4_CS + 2048;
constexpr int K4_SCR = K4_STT + 2048;
constexpr int K4_LDS = K4_SCR + 256;
struct K4Op { int kind, idx; };
constexpr int k4_lo(int S) { return S == 0 ? K4_P * K4_NS : (S + K4_R - 1) * K4_NS; }
constexpr int k4_hi(int S) { return (S + K4_R) * K4_NS < 36 ? (S + K4_R) * K4_NS : 36; }
constexpr int k4_nx(int S) { return S == 6 ? 4 : (S >= 10 && S < 15) || S == 16 ? 1 : 0; }
constexpr int k4_nops(int S) { int n = k4_nx(S); for (int u = k4_lo(S); u < k4_hi(S); ++u) n += u < 18 ? 3 : 1; return n; }
constexpr K4Op k4_op(int S, int i) {
  for (int u = k4_lo(S); u < k4_hi(S); ++u) { const int c = u < 18 ? 3 : 1; if (i < c) return {i, u}; i -= c; }
  if (S == 6) return {i < 3 ? 5 : 6, i};
  if (S == 16) return {4, 0};
  return {3, S - 10};
}
constexpr int k4_nst(int S) { return (S == 8 || S == 9 || S == 17) ? 2 : 0; }
constexpr int K4_PRO = 3 * K4_P * K4_NS;
constexpr int k4_issued(int S) { int n = K4_PRO; for (int s = 0; s < S; ++s) n += k4_nops(s) + k4_nst(s); return n; }
constexpr int k4_posW(int u) {
  if (u < K4_P * K4_NS) return 3 * u;
  for (int S = 0;; ++S) if (u >= k4_lo(S) && u < k4_hi(S)) { int n = k4_issued(S) + k4_nst(S); for (int v = k4_lo(S); v < u; ++v) n += v < 18 ? 3 : 1; return n; }
}
constexpr int k4_need(int S) {
  if (S == 8) return k4_issued(7) - 1;
  if (S >= 16) return k4_issued(15) - 1;
  return k4_posW((S + 1) * K4_NS);
}
constexpr unsigned k4_so(int u) { return (((u / K4_NS) % K4_R) * K4_NS + u % K4_NS) * 8192; }

extern "C" __global__ __launch_bounds__(512, 2) void k4_ffn1(const void* wg, const void* wu, const void* wd, char* ws) {
  const int tid = threadIdx.x, lane = tid & 63, w = rfl(tid >> 6);
  const int xcd = blockIdx.x & 7, r = blockIdx.x >> 3;
  const int mt = r >> 2, Qd = xcd, j = r & 3;
  const unsigned lds0 = (unsigned)(size_t)smem;
  const int t16 = lane & 15, g = lane >> 4;
  const int T0 = 16 * mt + 2 * w;
  const rsrc_t rwm = mk_rsrc(w < 4 ? wg : wu, D_ * F_ * 2), rhf = mk_rsrc(ws + WS_HF, 128 * 18 * 1024), rwd = mk_rsrc(wd, F_ * D_ * 2);
  const rsrc_t rst = mk_rsrc(ws + WS_ST, 9 * 2048 * 8), rcs = mk_rsrc(ws + WS_CS, 2 * 4608 * 4), rhh = mk_rsrc(ws + WS_HH, 128 * 144 * 1024);
  const rsrc_t rlg = mk_rsrc(ws + WS_LG, 8 * 4 * 36864);
  STAMP_DECL; STAMP(ws, 4, 0);
  const int rho = 8 * (w & 3) + (lane >> 3);
  const unsigned wv = rho * 9216 + 1152 * Qd + 256 * j + 32 * (((lane & 7) >> 1) ^ ((rho >> 1) & 3)) + 16 * (lane & 1);
  const unsigned wdst = lds0 + w * 1024, l16 = lane * 16;
  bf16x8 act[18][2];
  auto doop = [&](auto KIND, auto IDX) {
    constexpr int kind = KIND.value, u = IDX.value;
    if constexpr (kind == 0) glds16(wdst + k4_so(u), wv, rwm, (u % 18) * 32 * 9216 + (u / 18) * 128);
    else if constexpr (kind <= 2) bload16(act[u][kind - 1], l16, rhf, (T0 * 18 + 18 * (kind - 1) + u) * 1024);
    else if constexpr (kind == 3) { const int pc = min(w + 8 * u, 35); glds16(lds0 + K4_LG + pc * 1024, l16, rlg, (Qd * 4 + j) * 36864 + pc * 1024); }
    else if constexpr (kind == 4) warm(lds0 + K4_SCR, l16 * 8, rwd, (xcd >> 1) * 1327104 + min(r * 8 + w, 161) * 8192);
    else if constexpr (kind == 5) glds16(lds0 + K4_STP + w * 3072 + u * 1024, l16, rst, T0 * 1152 + u * 1024);
    else {
      const unsigned f = l16 < 512 ? 576 * Qd + 128 * j + (l16 >> 2) : 576 * Qd + 512 + 16 * j + (l16 >> 2) - 128;
      glds16(lds0 + K4_CS + (w & 1) * 1024, l16 < 576 ? f * 4 : OOB, rcs, (w & 1) * 4608 * 4);
    }
  };
  sfor<K4_P * K4_NS>([&](auto U) { doop(ic<0>{}, U); doop(ic<1>{}, U); doop(ic<2>{}, U); });
  unsigned wa[4];
  {
    const int r2 = 4 * g + ((lane >> 2) & 3);
#pragma unroll
    for (int c = 0; c < 4; ++c) wa[c] = lds0 + r2 * 128 + 32 * (c ^ ((r2 >> 1) & 3)) + 8 * (lane & 3);
  }
  f32x4 ag[4][2], au[4][2];
  bf16x4 fr[3][2][2];
  unsigned hd[2][2];
  auto rd1 = [&](auto N, auto I) {
    constexpr int n = N.value, i = I.value;
    if constexpr (n < 144) fr[n % 3][i >> 1][i & 1] = trh(wa[n % 4] + k4_so(n / 4) + 2048 * i);
    else if constexpr (n < 162) fr[n % 3][i >> 1][i & 1] = trh(lds0 + K4_LG + (l16 >> 6) * 32 + ((l16 >> 1) & 24) + (n - 144) * 1024 + (i >> 1) * 18432 + (i & 1) * 512);
  };
  auto rd = [&](auto N) { sfor<4>([&](auto I) { rd1(N, I); }); };
  auto mm = [&](auto N, auto KS, f32x4& g0, f32x4& g1, f32x4& u0, f32x4& u1) {
    constexpr int n = N.value, ks = KS.value;
    const bf16x8 fg = cat(fr[n % 3][0][0], fr[n % 3][0][1]), fu = cat(fr[n % 3][1][0], fr[n % 3][1][1]);
    bf16x4 nx[4];
    if constexpr (ks == 0) mfma16_z(g0, fg, act[ks][0]); else mfma16_ip(g0, fg, act[ks][0]);
    SBAR(); rd1(ic<n + 2>{}, ic<0>{}); SBAR();
    if constexpr (ks == 0) mfma16_z(g1, fg, act[ks][1]); else mfma16_ip(g1, fg, act[ks][1]);
    SBAR(); rd1(ic<n + 2>{}, ic<1>{}); SBAR();
    if constexpr (ks == 0) mfma16_z(u0, fu, act[ks][0]); else mfma16_ip(u0, fu, act[ks][0]);
    SBAR(); rd1(ic<n + 2>{}, ic<2>{}); SBAR();
    if constexpr (ks == 0) mfma16_z(u1, fu, act[ks][1]); else mfma16_ip(u1, fu, act[ks][1]);
    SBAR(); rd1(ic<n + 2>{}, ic<3>{}); SBAR();
    (void)nx;
  };
  auto epi = [&](const f32x4& G_, const f32x4& U_, float nmu, const f32x4& cg, const f32x4& cu, unsigned& d0, unsigned& d1) {
    const f32x4 v = (G_ + cg * nmu) * (U_ + cu * nmu);
    d0 = pk2(v[0], v[1]); d1 = pk2(v[2], v[3]);
  };
  auto epichunk = [&](auto GRP, auto C) {
    constexpr int grp = GRP.value, c = C.value;
    asm volatile("s_nop 4" : "+v"(ag[c][0]), "+v"(ag[c][1]), "+v"(au[c][0]), "+v"(au[c][1]));
    const unsigned csa = lds0 + K4_CS + ((l16 >> 4) & 48) + 256 * grp + 64 * c, sta = lds0 + K4_STT + 128 * w + ((l16 >> 2) & 60);
    const f32x4 cg = ldsr<f32x4>(csa), cu = ldsr<f32x4>(csa + 1024);
    sfor<2>([&](auto T) {
      const float nmu = ldsr<float>(sta + 64 * T.value);
      if constexpr ((c & 1) == 0) epi(ag[c][T.value], au[c][T.value], nmu, cg, cu, hd[T.value][0], hd[T.value][1]);
      else {
        unsigned d2, d3;
        epi(ag[c][T.value], au[c][T.value], nmu, cg, cu, d2, d3);
        bstore16<K4_HAUX>((u32x4){hd[T.value][0], hd[T.value][1], d2, d3}, rhh, l16, ((T0 + T.value) * 144 + 18 * Qd + 4 * j + 2 * grp + (c >> 1)) * 1024);
      }
    });
  };
  sfor<18>([&](auto SS) {
    constexpr int S = SS.value;
    constexpr int nw = k4_issued(S) - k4_need(S) - 1;
    static_assert(nw >= 0 && nw < 64, "");
    if constexpr (S < 9) asm volatile("s_waitcnt vmcnt(%4)" : "+v"(act[2 * S][0]), "+v"(act[2 * S][1]), "+v"(act[2 * S + 1][0]), "+v"(act[2 * S + 1][1]) : "i"(nw) : "memory");
    else wait_vm<nw>();
    bar();
    if constexpr (S == 0) STAMP(ws, 4, 1);
    if constexpr (S == 4) STAMP(ws, 4, 2);
    if constexpr (S == 9) STAMP(ws, 4, 3);
    if constexpr (S == 17) STAMP(ws, 4, 4);
    if constexpr (S == 0) { rd(ic<0>{}); rd(ic<1>{}); }
    if constexpr (S == 8) {
      sfor<2>([&](auto T) {
        const unsigned sa = lds0 + K4_STP + w * 3072 + T.value * 1152 + (l16 >> 1);
        const f32x2 p0 = ldsr<f32x2>(sa), p1 = ldsr<f32x2>(sa + 512), p2 = ldsr<f32x2>(sa + 1024);
        const f32x2 ps = p0 + p1 + (l16 < 256 ? p2 : (f32x2){0.f, 0.f});
        const float a = sum_x32(sum_x16(ps[0])), b = sum_x32(sum_x16(ps[1]));
        const float mu = a * (1.f / D_), var = fmaxf(b * (1.f / D_) - mu * mu, 0.f);
        ldsw<float>(lds0 + K4_STT + ((l16 & 256) << 2) + 128 * w + 64 * T.value + ((l16 >> 2) & 60), (l16 & 256) ? __builtin_amdgcn_rcpf(var + 1e-5f) : -mu);
      });
      SBAR();
    }
    sfor<4 * K4_NS>([&](auto L) {
      constexpr int l = L.value, n = 4 * K4_NS * S + l, t = n / 4, c = n % 4, ks = t % 18;
      mm(ic<n>{}, ic<ks>{}, ag[c][0], ag[c][1], au[c][0], au[c][1]);
      constexpr int no = k4_nops(S), NL = 4 * K4_NS;
      sfor<no>([&](auto I) {
        constexpr int nd = no ? no : 1, b0 = I.value * NL / nd;
        if constexpr (b0 == l) doop(ic<k4_op(S, I.value).kind>{}, ic<k4_op(S, I.value).idx>{});
      });
      if constexpr (ks == 17 && c >= 1) epichunk(ic<t / 18>{}, ic<c - 1>{});
      if constexpr (t == 18 && c == 0) epichunk(ic<0>{}, ic<3>{});
      SBAR();
    });
  });
  STAMP(ws, 4, 5);
  {
    f32x4 g0, g1, u0, u1;
    sfor<18>([&](auto K) {
      constexpr int ks = K.value, n = 144 + ks;
      mm(ic<n>{}, ic<ks>{}, g0, g1, u0, u1);
      if constexpr (ks == 0) epichunk(ic<1>{}, ic<3>{});
      SBAR();
    });
    const unsigned csa = lds0 + K4_CS + ((l16 >> 4) & 48) + 512, sta = lds0 + K4_STT + 128 * w + ((l16 >> 2) & 60);
    const f32x4 cg = ldsr<f32x4>(csa), cu = ldsr<f32x4>(csa + 1024);
    unsigned a0, a1, b0, b1;
    asm volatile("s_nop 9" : "+v"(g0), "+v"(g1), "+v"(u0), "+v"(u1));
    epi(g0, u0, ldsr<float>(sta), cg, cu, a0, a1);
    epi(g1, u1, ldsr<float>(sta + 64), cg, cu, b0, b1);
    auto s0 = __builtin_amdgcn_permlane32_swap(a0, b0, false, false);
    auto s1 = __builtin_amdgcn_permlane32_swap(a1, b1, false, false);
    bstore16<K4_HAUX>((u32x4){s0[0], s1[0], s0[1], s1[1]}, rhh, (l16 >> 9) * 144 * 1024 + (l16 & 511) + 512 * (j & 1), (T0 * 144 + 18 * Qd + 16 + (j >> 1)) * 1024);
    if (Qd == 0 && j == 0 && l16 < 512) *(GAS float*)(ws + WS_RS + (16 * T0) * 4 + (l16 >> 2)) = ldsr<float>(lds0 + K4_STT + 1024 + 128 * w + (l16 >> 2));
    STAMP(ws, 4, 6);
    STAMP(ws, 4, 7);
    STAMP_FLUSH(ws, 4);
  }
}

constexpr int K5_SLOT = 17408, K5_R = 8, K5_P = 4;
constexpr int K5_LDS = K5_SLOT * K5_R + 256;
constexpr int k5_E(int i) { int e = 36; if (K5_P + 2 * (i + 1) < e) e = K5_P + 2 * (i + 1); if (i >= 0 && i + K5_R < e) e = i + K5_R; return e; }

extern "C" __global__ __launch_bounds__(512, 2) void k5_ffn2(const void* wd, char* ws, void* out) {
  const int tid = threadIdx.x, lane = tid & 63, w = rfl(tid >> 6);
  const int xcd = blockIdx.x & 7, r = blockIdx.x >> 3;
  const int sg = xcd >> 1, mt = 8 * (xcd & 1) + (r >> 2), nt = r & 3;
  const unsigned lds0 = (unsigned)(size_t)smem;
  const int t16 = lane & 15, g = lane >> 4;
  const int tile = 4 * mt + nt;
  const rsrc_t rpt = mk_rsrc(ws + WS_PT, 64 * 8 * 4 * PT_REC);
  STAMP_DECL; STAMP(ws, 5, 0);
  if (w >= 4) {
    const int l = w - 4;
    const rsrc_t rwd = mk_rsrc(wd, F_ * D_ * 2), rhh = mk_rsrc(ws + WS_HH, 128 * 144 * 1024);
    const rsrc_t rwt = mk_rsrc(ws + WS_WT, 4 * 4608 * 32);
    unsigned mv[2][2];
#pragma unroll
    for (int sp = 0; sp < 2; ++sp) {
#pragma unroll
      for (int i = 0; i < 2; ++i) {
        const int rho = 4 * (2 * l + i) + g, rr = sp ? ((rho & 7) | ((rho & 8) << 1) | ((rho & 16) >> 1)) : rho;
        mv[sp][i] = rr * 1152 + 256 * nt + 32 * ((t16 >> 1) ^ (rho & 7)) + 16 * (t16 & 1);
      }
    }
    const unsigned hs = ((8 * mt + 2 * l) * 144 + 36 * sg) * 1024, wsb = sg * 1152 * 1152, dl = lds0 + l * 2048;
    auto issue = [&](auto U) {
      constexpr int u = U.value, sp = (u % 18) >= 16;
      constexpr unsigned so = (u % K5_R) * K5_SLOT;
      glds16(dl + so + 8192, mv[sp][0], rwd, wsb + u * 32 * 1152);
      glds16(dl + so, lane * 16, rhh, hs + u * 1024);
      glds16(dl + so + 8192 + 1024, mv[sp][1], rwd, wsb + u * 32 * 1152);
      glds16(dl + so + 1024, lane * 16, rhh, hs + (144 + u) * 1024);
      if ((u & 3) == l) glds16(lds0 + so + 16384, lane * 16, rwt, (nt * 144 + 36 * sg + u) * 1024);
    };
    sfor<K5_P>([&](auto U) { issue(U); });
    sfor<36>([&](auto I) {
      constexpr int i = I.value, y = k5_E(i - 1) - (i + 2 < 36 ? i + 2 : 36);
      wait_vm<4 * y + y / 4>();
      bar();
      if constexpr (i == 0) STAMP(ws, 5, 1);
      if constexpr (i == 18) STAMP(ws, 5, 2);
      sfor<k5_E(i) - k5_E(i - 1)>([&](auto U) { issue(ic<k5_E(i - 1) + U.value>{}); });
    });
    bar();
    STAMP(ws, 5, 3);
  } else {
    const int r2 = 4 * g + ((lane >> 2) & 3);
    unsigned wa[8];
#pragma unroll
    for (int c = 0; c < 8; ++c) wa[c] = lds0 + 8192 + r2 * 256 + 32 * (c ^ (r2 & 7)) + 8 * (lane & 3);
    const unsigned wt = lds0 + 16384 + r2 * 32 + 8 * (lane & 3), ha = lds0 + w * 2048 + lane * 16;
    f32x4 acc[9][2] = {};
    bf16x4 fr[3][3][2];
    bf16x8 hb[2][2];
    auto rd1 = [&](auto N, auto I) {
      constexpr int n = N.value, u = n / 3, i = I.value;
      if constexpr (u < 36) {
        constexpr unsigned so = (u % K5_R) * K5_SLOT;
        constexpr int c = 3 * (n % 3) + i / 2;
        if constexpr (i >= 6) hb[u & 1][i - 6] = ldsr<bf16x8>(ha + so + (i - 6) * 1024);
        else if constexpr (c < 8) fr[n % 3][i / 2][i & 1] = trh(wa[c] + so + (i & 1) * 4096);
        else fr[n % 3][i / 2][i & 1] = trh(wt + so + (i & 1) * 512);
      }
    };
    sfor<36>([&](auto U) {
      constexpr int u = U.value;
      bar();
      if constexpr (u == 0) STAMP(ws, 5, 1);
      if constexpr (u == 18) STAMP(ws, 5, 2);
      if constexpr (u == 0) { rd1(ic<0>{}, ic<6>{}); rd1(ic<0>{}, ic<7>{}); sfor<6>([&](auto I) { rd1(ic<0>{}, I); }); sfor<6>([&](auto I) { rd1(ic<1>{}, I); }); }
      sfor<18>([&](auto M) {
        constexpr int mi = M.value, v = mi / 6, n = 3 * u + v, c = mi / 2;
        acc[c][mi & 1] = mfma16(cat(fr[n % 3][c % 3][0], fr[n % 3][c % 3][1]), hb[u & 1][mi & 1], acc[c][mi & 1]);
        SBAR();
        if constexpr (mi == 6 || mi == 8) rd1(ic<n + 2>{}, ic<6 + (mi - 6) / 2>{});
        rd1(ic<n + 2>{}, ic<mi % 6>{});
        SBAR();
      });
    });
    bar();
    STAMP(ws, 5, 3);
    if (w == sg) {
      sfor<2>([&](auto T) { sfor<9>([&](auto C) { ldsw<f32x4>(lds0 + (T.value * 9 + C.value) * 1024 + lane * 16, acc[C.value][T.value]); }); });
    } else {
      sfor<2>([&](auto T) {
        const unsigned pb = (((tile * 4 + w) * 4 + sg) * 2 + T.value) * PT_REC;
        sfor<4>([&](auto I) {
          constexpr int i = I.value;
          const f32x4 a = acc[2 * i][T.value], b = acc[2 * i + 1][T.value];
          bstore16<16>((u32x4){pk2(a[0], a[1]), pk2(a[2], a[3]), pk2(b[0], b[1]), pk2(b[2], b[3])}, rpt, lane * 16 + i * 1024, pb);
        });
        const f32x4 a = acc[8][T.value];
        bstore8<16>((u32x2){pk2(a[0], a[1]), pk2(a[2], a[3])}, rpt, lane * 8 + 4096, pb);
      });
    }
  }
  const int T = w >> 2, pi = w & 3, tt = 8 * mt + 2 * sg + T;
  const rsrc_t rhf = mk_rsrc(ws + WS_HF, 128 * 18 * 1024);
  const u32x4 hv = __builtin_amdgcn_raw_buffer_load_b128(rhf, lane * 16, (tt * 18 + 4 * nt + pi) * 1024, 0);
  const u32x2 hl = __builtin_amdgcn_raw_buffer_load_b64(rhf, lane * 16 + 8 * (nt & 1), (tt * 18 + 16 + (nt >> 1)) * 1024, 0);
  const float rs2 = *(const GAS float*)(ws + WS_RS + (16 * tt + t16) * 4);
  STAMP(ws, 5, 4);
  asm volatile("s_waitcnt vmcnt(0) lgkmcnt(0)" ::: "memory");
  STAMP(ws, 5, 5);
  bar();
  if (w == 4) {
    GAS unsigned* cnt = (GAS unsigned*)(ws + WS_CNT + tile * 64);
    if (lane == 0) __hip_atomic_fetch_add(cnt, 1u, __ATOMIC_RELAXED, __HIP_MEMORY_SCOPE_AGENT);
    for (int spin = 0; spin < (1 << 22); ++spin) {
      if (rfl(__hip_atomic_load(cnt, __ATOMIC_RELAXED, __HIP_MEMORY_SCOPE_AGENT)) >= 4u) break;
      __builtin_amdgcn_s_sleep(1);
    }
  }
  bar();
  STAMP(ws, 5, 6);
  u32x4 pv[3];
  u32x2 pl[3];
#pragma unroll
  for (int o = 0; o < 3; ++o) {
    const unsigned so = (((tile * 4 + sg) * 4 + ((sg + 1 + o) & 3)) * 2 + T) * PT_REC;
    pv[o] = __builtin_amdgcn_raw_buffer_load_b128(rpt, lane * 16 + pi * 1024, so, 16);
    if (pi == 0) pl[o] = __builtin_amdgcn_raw_buffer_load_b64(rpt, lane * 8 + 4096, so, 16);
  }
  const rsrc_t rout = mk_rsrc(out, S_ * D_ * 2);
  auto fin = [&](const f32x4& own, unsigned a0, unsigned a1, unsigned b0, unsigned b1, unsigned c0, unsigned c1, unsigned h0, unsigned h1, unsigned& d0, unsigned& d1) {
    const float v0 = ((own[0] + bflo(b0)) + (bflo(a0) + bflo(c0))) * rs2 + bflo(h0);
    const float v1 = ((own[1] + bfhi(b0)) + (bfhi(a0) + bfhi(c0))) * rs2 + bfhi(h0);
    const float v2 = ((own[2] + bflo(b1)) + (bflo(a1) + bflo(c1))) * rs2 + bflo(h1);
    const float v3 = ((own[3] + bfhi(b1)) + (bfhi(a1) + bfhi(c1))) * rs2 + bfhi(h1);
    d0 = pk2(v0, v1); d1 = pk2(v2, v3);
  };
  const unsigned orow = (16 * tt + t16) * 1152, own = lds0 + T * 9216 + lane * 16;
  {
    unsigned a0, a1, b0, b1;
    fin(ldsr<f32x4>(own + pi * 2048), pv[0][0], pv[0][1], pv[1][0], pv[1][1], pv[2][0], pv[2][1], hv[0], hv[1], a0, a1);
    fin(ldsr<f32x4>(own + pi * 2048 + 1024), pv[0][2], pv[0][3], pv[1][2], pv[1][3], pv[2][2], pv[2][3], hv[2], hv[3], b0, b1);
    auto s0 = __builtin_amdgcn_permlane16_swap(a0, b0, false, false);
    auto s1 = __builtin_amdgcn_permlane16_swap(a1, b1, false, false);
    bstore16<0>((u32x4){s0[0], s1[0], s0[1], s1[1]}, rout, orow + 256 * nt + 64 * pi + 32 * (g & 1) + 16 * (g >> 1), 0);
  }
  if (pi == 0) {
    unsigned e0, e1;
    fin(ldsr<f32x4>(own + 8192), pl[0][0], pl[0][1], pl[1][0], pl[1][1], pl[2][0], pl[2][1], hl[0], hl[1], e0, e1);
    bstore8<0>((u32x2){e0, e1}, rout, orow + 1024 + 32 * nt + 8 * g, 0);
  }
  STAMP(ws, 5, 7);
  STAMP_FLUSH(ws, 5);
}

extern "C" __global__ void k9_end() {}

extern "C" __global__ __launch_bounds__(256) void k6_diag(char* ws) {
  __shared__ unsigned long long ra[256], rb[256];
  const int b = threadIdx.x;
  unsigned long long t0 = __builtin_amdgcn_s_memrealtime();
  unsigned long long a = MA ? 0ull : ~0ull, c = MB ? 0ull : ~0ull;
  const int nba = KA == 3 ? 144 : 256, nbb = KB == 3 ? 144 : 256;
  for (int w = 0; w < 8; ++w) {
    if (b < nba) { unsigned long long v = *(GAS unsigned long long*)(ws + WS_DBG + ((((size_t)KA * 256 + b) * 8 + w) * 32 + IA) * 8); a = MA ? (v > a ? v : a) : (v < a ? v : a); }
    if (b < nbb) { unsigned long long v = *(GAS unsigned long long*)(ws + WS_DBG + ((((size_t)KB * 256 + b) * 8 + w) * 32 + IB) * 8); c = MB ? (v > c ? v : c) : (v < c ? v : c); }
  }
  ra[b] = a; rb[b] = c;
  __syncthreads();
  if (b == 0) {
    for (int i = 1; i < 256; ++i) { a = MA ? (ra[i] > a ? ra[i] : a) : (ra[i] < a ? ra[i] : a); c = MB ? (rb[i] > c ? rb[i] : c) : (rb[i] < c ? rb[i] : c); }
    unsigned long long d = c - a;
    d = 200;
    d *= MULT;
    while (__builtin_amdgcn_s_memrealtime() - t0 < d + 300) __builtin_amdgcn_s_sleep(2);
  }
}

extern "C" void kernel_launch(void* const* d_in, const int* in_sizes, int n_in, void* d_out, int out_size, void* d_ws, size_t ws_size,
                              hipStream_t stream) {
  const void *x = d_in[0], *wqkv = d_in[1], *wo = d_in[2], *wg = d_in[4], *wu = d_in[5], *wd = d_in[6];
  char* ws = (char*)d_ws;
  (void)hipFuncSetAttribute((const void*)k1_qkv, hipFuncAttributeMaxDynamicSharedMemorySize, K1_LDS);
  (void)hipFuncSetAttribute((const void*)k2_attn, hipFuncAttributeMaxDynamicSharedMemorySize, K2_LDS);
  (void)hipFuncSetAttribute((const void*)k3_wo, hipFuncAttributeMaxDynamicSharedMemorySize, K3_LDS);
  (void)hipFuncSetAttribute((const void*)k4_ffn1, hipFuncAttributeMaxDynamicSharedMemorySize, K4_LDS);
  (void)hipFuncSetAttribute((const void*)k5_ffn2, hipFuncAttributeMaxDynamicSharedMemorySize, K5_LDS);
  k1_qkv<<<256, 512, K1_LDS, stream>>>(x, wqkv, ws);
  k2_attn<<<256, 512, K2_LDS, stream>>>(ws, x, wo, wg, wu);
  k3_wo<<<256, 512, K3_LDS, stream>>>(x, wo, wg, wu, wd, ws);
  k4_ffn1<<<256, 512, K4_LDS, stream>>>(wg, wu, wd, ws);
  k5_ffn2<<<256, 512, K5_LDS, stream>>>(wd, ws, d_out);
  k6_diag<<<1, 256, 0, stream>>>(ws);
}
```

```cpp
#include <hip/hip_runtime.h>
#include <stdint.h>

typedef short bf16x8 __attribute__((ext_vector_type(8)));
typedef short bf16x4 __attribute__((ext_vector_type(4)));
typedef float f32x2 __attribute__((ext_vector_type(2)));
typedef float f32x4 __attribute__((ext_vector_type(4)));
typedef float f32x16 __attribute__((ext_vector_type(16)));
typedef unsigned u32x2 __attribute__((ext_vector_type(2)));
typedef unsigned u32x4 __attribute__((ext_vector_type(4)));
typedef __bf16 bf2_t __attribute__((ext_vector_type(2)));
typedef __amdgpu_buffer_rsrc_t rsrc_t;
#define LAS __attribute__((address_space(3)))
#define GAS __attribute__((address_space(1)))
#define DI __device__ __forceinline__

constexpr int S_ = 2048, D_ = 576, F_ = 4608;
constexpr float QSCALE = 0.23570226039551584f * 1.4426950408889634f;
constexpr unsigned ONES2 = 0x3f803f80u;

constexpr size_t QREC = 1280, KVREC = 2816;
constexpr unsigned OOB = 0x7fffff00u;
constexpr unsigned FL1 = 128;
constexpr unsigned FL2 = FL1 + 256 * 64;
constexpr unsigned FL3 = FL2 + 2048 * 64;
constexpr unsigned FLS = FL3 + 2048 * 64;
constexpr unsigned FL4 = FLS + 128 * 64;
constexpr unsigned FL5 = FL4 + 3 * 2048 * 64;
constexpr unsigned FL_END = FL5 + 1024 * 64;
constexpr size_t WS_QF = 1u << 20;
constexpr size_t WS_KV = WS_QF + 32 * 64 * QREC;
constexpr size_t WS_OF = WS_KV + 32 * 64 * KVREC;
constexpr size_t WS_HF = WS_OF + 128 * 18 * 1024;
constexpr size_t WS_ST = WS_HF + 128 * 18 * 1024;
constexpr size_t WS_CS = WS_ST + 9 * 2048 * 8;
constexpr size_t WS_RS = WS_CS + 2 * 4608 * 4;
constexpr size_t WS_LG = WS_RS + 4 * 2048 * 4;
constexpr size_t WS_WT = WS_LG + 8 * 4 * 36864;
constexpr size_t WS_HH = WS_WT + 4 * 4608 * 32;
constexpr size_t PT_REC = 4608;
constexpr size_t WS_PT = WS_HH + (size_t)128 * 144 * 1024;
constexpr size_t WS_END = WS_PT + (size_t)64 * 8 * 4 * PT_REC;

DI unsigned rfl(unsigned v) { return __builtin_amdgcn_readfirstlane(v); }
DI rsrc_t mk_rsrc(const void* p, unsigned bytes) { return __builtin_amdgcn_make_buffer_rsrc((void*)p, (short)0, (int)bytes, 0x00020000); }
DI void glds16(unsigned lds, unsigned voff, rsrc_t r, unsigned soff) {
  asm volatile("s_mov_b32 m0, %0\n\ts_nop 0\n\tbuffer_load_dwordx4 %1, %2, %3 offen lds" ::"s"(lds), "v"(voff), "s"(r), "s"(soff) : "memory", "m0");
}
DI void glds16c(unsigned lds, unsigned voff, rsrc_t r, unsigned soff) {
  asm volatile("s_mov_b32 m0, %0\n\ts_nop 0\n\tbuffer_load_dwordx4 %1, %2, %3 offen sc1 lds" ::"s"(lds), "v"(voff), "s"(r), "s"(soff) : "memory", "m0");
}
template <class T> DI void bload16c(T& d, unsigned voff, rsrc_t r, unsigned soff) {
  asm volatile("buffer_load_dwordx4 %0, %1, %2, %3 offen sc1" : "=v"(d) : "v"(voff), "s"(r), "s"(soff) : "memory");
}
DI void bload8c(u32x2& d, unsigned voff, rsrc_t r, unsigned soff) {
  asm volatile("buffer_load_dwordx2 %0, %1, %2, %3 offen sc1" : "=v"(d) : "v"(voff), "s"(r), "s"(soff) : "memory");
}
DI void bload4c(unsigned& d, unsigned voff, rsrc_t r, unsigned soff) {
  asm volatile("buffer_load_dword %0, %1, %2, %3 offen sc1" : "=v"(d) : "v"(voff), "s"(r), "s"(soff) : "memory");
}
DI void bload8(u32x2& d, unsigned voff, rsrc_t r, unsigned soff) {
  asm volatile("buffer_load_dwordx2 %0, %1, %2, %3 offen" : "=v"(d) : "v"(voff), "s"(r), "s"(soff) : "memory");
}
DI void warm(unsigned lds, unsigned voff, rsrc_t r, unsigned soff) {
  asm volatile("s_mov_b32 m0, %0\n\ts_nop 0\n\tbuffer_load_dword %1, %2, %3 offen lds" ::"s"(lds), "v"(voff), "s"(r), "s"(soff) : "memory", "m0");
}
template <int N> DI void wait_vm() { asm volatile("s_waitcnt vmcnt(%0)" ::"i"(N) : "memory"); }
#define PIN(x) asm volatile("" : "+v"(x))
#define SBAR() __builtin_amdgcn_sched_barrier(0)
DI void bar() { SBAR(); asm volatile("s_barrier" ::: "memory"); SBAR(); }
DI void lds_bar() { SBAR(); asm volatile("s_waitcnt lgkmcnt(0)\n\ts_barrier" ::: "memory"); SBAR(); }
DI bf16x8 trfrag(unsigned a0, unsigned a1) {
  bf16x4 lo = __builtin_amdgcn_ds_read_tr16_b64_v4i16((LAS bf16x4*)(size_t)a0);
  bf16x4 hi = __builtin_amdgcn_ds_read_tr16_b64_v4i16((LAS bf16x4*)(size_t)a1);
  return __builtin_shufflevector(lo, hi, 0, 1, 2, 3, 4, 5, 6, 7);
}
DI bf16x4 trh(unsigned a) { return __builtin_amdgcn_ds_read_tr16_b64_v4i16((LAS bf16x4*)(size_t)a); }
DI bf16x8 cat(bf16x4 lo, bf16x4 hi) { return __builtin_shufflevector(lo, hi, 0, 1, 2, 3, 4, 5, 6, 7); }
template <class T> DI T ldsr(unsigned a) { return *(LAS T*)(size_t)a; }
template <class T> DI void ldsw(unsigned a, T v) { *(LAS T*)(size_t)a = v; }
DI unsigned pk2(float a, float b) { f32x2 v = {a, b}; return __builtin_bit_cast(unsigned, __builtin_convertvector(v, bf2_t)); }
DI float bflo(unsigned u) { return __uint_as_float(u << 16); }
DI float bfhi(unsigned u) { return __uint_as_float(u & 0xffff0000u); }
DI f32x4 mfma16(bf16x8 a, bf16x8 b, f32x4 c) { return __builtin_amdgcn_mfma_f32_16x16x32_bf16(a, b, c, 0, 0, 0); }
DI void mfma16_ip(f32x4& c, bf16x8 a, bf16x8 b) { asm volatile("v_mfma_f32_16x16x32_bf16 %0, %1, %2, %0" : "+v"(c) : "v"(a), "v"(b)); }
DI void mfma16_z(f32x4& c, bf16x8 a, bf16x8 b) { asm volatile("v_mfma_f32_16x16x32_bf16 %0, %1, %2, 0" : "=&v"(c) : "v"(a), "v"(b)); }
DI f32x16 mfma32(bf16x8 a, bf16x8 b, f32x16 c) { return __builtin_amdgcn_mfma_f32_32x32x16_bf16(a, b, c, 0, 0, 0); }
DI float xor32(float v) { auto r = __builtin_amdgcn_permlane32_swap(__float_as_uint(v), __float_as_uint(v), false, false); return __uint_as_float((threadIdx.x & 32) ? r[0] : r[1]); }
DI float sum_x32(float v) { auto r = __builtin_amdgcn_permlane32_swap(__float_as_uint(v), __float_as_uint(v), false, false); return __uint_as_float(r[0]) + __uint_as_float(r[1]); }
DI float max_x32(float v) { auto r = __builtin_amdgcn_permlane32_swap(__float_as_uint(v), __float_as_uint(v), false, false); return fmaxf(__uint_as_float(r[0]), __uint_as_float(r[1])); }
DI float sum_x16(float v) { auto r = __builtin_amdgcn_permlane16_swap(__float_as_uint(v), __float_as_uint(v), false, false); return __uint_as_float(r[0]) + __uint_as_float(r[1]); }
template <int AUX> DI void bstore16(u32x4 v, rsrc_t r, unsigned voff, unsigned soff) {
  if constexpr (AUX == 16) asm volatile("buffer_store_dwordx4 %0, %1, %2, %3 offen sc1\n\ts_nop 1" ::"v"(v), "v"(voff), "s"(r), "s"(soff) : "memory");
  else asm volatile("buffer_store_dwordx4 %0, %1, %2, %3 offen\n\ts_nop 1" ::"v"(v), "v"(voff), "s"(r), "s"(soff) : "memory");
}
template <int AUX> DI void bstore8(u32x2 v, rsrc_t r, unsigned voff, unsigned soff) { __builtin_amdgcn_raw_buffer_store_b64(v, r, voff, soff, AUX); }
template <int AUX> DI void bstore4(unsigned v, rsrc_t r, unsigned voff, unsigned soff) { __builtin_amdgcn_raw_buffer_store_b32(v, r, voff, soff, AUX); }

template <int... I> struct iseq {};
template <int N, int... I> struct mkseq : mkseq<N - 1, N - 1, I...> {};
template <int... I> struct mkseq<0, I...> { typedef iseq<I...> type; };
template <int V> struct ic { static constexpr int value = V; };
template <class Fn, int... I> DI void sfor_(Fn&& f, iseq<I...>) { (f(ic<I>{}), ...); }
template <int N, class Fn> DI void sfor(Fn&& f) { sfor_(f, typename mkseq<N>::type{}); }

extern __shared__ __attribute__((aligned(16))) char smem[];
#define STAMP(ws, kid, i)
#define STAMP_DECL
#define STAMP_FLUSH(ws, kid)

struct Ctx {
  const void *x, *wqkv, *wo, *wg, *wu, *wd;
  char* ws; void* out;
  int w, xcd, r;
  unsigned lds0, t0, t1;
};
constexpr int LDS_ALL = 160 * 1024, LDS_SCR = LDS_ALL - 256;
DI int fresh_lane() { int l = __builtin_amdgcn_mbcnt_hi(~0u, __builtin_amdgcn_mbcnt_lo(~0u, 0u)); PIN(l); return l; }
DI unsigned opq(unsigned v) { PIN(v); return v; }
DI rsrc_t fl_rsrc(const Ctx& c) { return mk_rsrc(c.ws, FL_END); }
DI void set_flag(const Ctx& c, int lane, unsigned off) {
  const u32x2 t = {c.t0, c.t1};
  asm volatile("buffer_store_dwordx2 %0, %1, %2, %3 offen sc1" ::"v"(t), "v"(lane == 0 ? 0u : OOB), "s"(fl_rsrc(c)), "s"(off) : "memory");
}
DI bool tag_ok(const Ctx& c, u32x2 v) { return __builtin_amdgcn_ballot_w64(v[0] != c.t0 || v[1] != c.t1) == 0; }
DI void poll(const Ctx& c, unsigned voff, unsigned soff) {
  for (int spin = 0; spin < (1 << 20); ++spin) {
    u32x2 v;
    asm volatile("buffer_load_dwordx2 %0, %1, %2, %3 offen sc1\n\ts_waitcnt vmcnt(0)" : "=&v"(v) : "v"(voff), "s"(fl_rsrc(c)), "s"(soff) : "memory");
    if (tag_ok(c, v)) break;
  }
}

constexpr int K1_SLOT = 32768, K1_TBL = K1_SLOT * 4;
extern "C" __global__ void k9_end();

template <int NN> DI void p1_body(Ctx& c, int mt, int nt) {
  char* ws = c.ws;
  const int w = c.w, lane = fresh_lane();
  const unsigned lds0 = c.lds0;
  const rsrc_t rx = mk_rsrc(c.x, S_ * D_ * 2), rw = mk_rsrc(c.wqkv, D_ * 3 * D_ * 2);
  const int t16 = lane & 15, g = lane >> 4, q4 = (lane >> 2) & 3, p = lane & 3, m = w & 3;
  constexpr int NT = NN ? 3 : 4;
  unsigned xv[2], wv[2];
#pragma unroll
  for (int i = 0; i < 2; ++i) {
    const int row = 8 * (2 * w + i) + (lane >> 3);
    xv[i] = (128 * mt + row) * 1152 + 16 * ((lane & 7) ^ ((row >> 1) & 7));
    int li = 64 * (2 * w + i) + lane;
    if (li >= 896) li -= 128;
    const int r64 = (li * 4682) >> 16, cc = li - 14 * r64;
    const int rho = r64 & 31, k = (r64 & 32) + 8 * ((rho >> 2) & 3) + 4 * (rho >> 4) + (rho & 3);
    wv[i] = k * 3456 + ((216 * nt) & ~15) + 16 * cc;
  }
  STAMP_DECL; STAMP(ws, 1, 0);
  auto issue1 = [&](int slab, int i) {
    const unsigned dst = lds0 + (slab & 3) * K1_SLOT + w * 2048 + (i >> 1) * 1024;
    if (i & 1) glds16(dst + 16384, wv[i >> 1], rw, slab * 64 * 3456);
    else glds16(dst, xv[i >> 1], rx, slab * 128);
  };
#pragma unroll
  for (int sl = 0; sl < 2; ++sl)
#pragma unroll
    for (int i = 0; i < 4; ++i) issue1(sl, i);
  u32x2 ev;
  bload8c(ev, 0u, fl_rsrc(c), 0);
  STAMP(ws, 1, 1);
  const unsigned wa = lds0 + 16384 + (4 * g + q4) * 224 + (nt & 1) * 8;
  unsigned ad[2][5];
  ad[0][0] = wa + 8 * p + 144 * NN;
  ad[0][1] = wa + 72 * (p < 3 ? p : 2) + 32;
  ad[0][2] = NN && m >= 2 ? ad[0][1] : ad[0][0] + 72 * (m >> 1) + 40 * (m & 1);
  ad[0][3] = lds0 + (32 * m + t16) * 128 + 16 * (g ^ ((t16 >> 1) & 7));
  ad[0][4] = ad[0][3] ^ 64;
#pragma unroll
  for (int i = 0; i < 5; ++i) { ad[1][i] = ad[0][i] + 2 * K1_SLOT; PIN(ad[1][i]); }
  f32x4 acc[4][2] = {}, cs = {};
  bf16x8 ones = __builtin_bit_cast(bf16x8, (u32x4){ONES2, ONES2, ONES2, ONES2});
  PIN(ones);
  bf16x8 fb[2][2];
  bf16x4 fa[2][NT + 1][2];
  constexpr int NR = 2 * NT + 4;
  auto rd1 = [&](auto KS, auto I) {
    constexpr int ks = KS.value, i = I.value, sp = ks & 1, sl = (ks >> 1) & 3, ti = (i - 2) >> 1;
    constexpr unsigned so = (sl & 1) * K1_SLOT;
    if constexpr (ks < 18 && i < NR) {
      if constexpr (i < 2) fb[sp][i] = ldsr<bf16x8>(ad[sl >> 1][3 + sp] + so + i * 2048);
      else {
        constexpr int ai = ti == NT ? 2 : (NN && ti == 2) ? 1 : 0;
        constexpr unsigned co = ai ? 0 : 72 * (ti >> 1) + 40 * (ti & 1);
        fa[sp][ti][i & 1] = trh(ad[sl >> 1][ai] + so + co + sp * 32 * 224 + (i & 1) * 16 * 224);
      }
    }
  };
  sfor<9>([&](auto SL) {
    constexpr int sl = SL.value;
    constexpr int iss = sl == 0 ? 2 : (sl + 3 < 9 ? sl + 3 : 9);
    constexpr int need = sl == 0 ? 0 : (sl + 1 < 9 ? sl + 1 : 8);
    if constexpr (sl == 1) asm volatile("s_waitcnt vmcnt(%1)" : "+v"(ev) : "i"(4 * (iss - need - 1)) : "memory");
    else wait_vm<4 * (iss - need - 1) + (sl == 0)>();
    bar();
    if constexpr (sl == 0) STAMP(ws, 1, 2);
    if constexpr (sl == 4) STAMP(ws, 1, 3);
    if constexpr (sl <= 1) sfor<NR>([&](auto I) { rd1(ic<2 * sl>{}, I); });
    if constexpr (sl == 1) {
      const unsigned long long e = (((unsigned long long)rfl(ev[1]) << 32) | rfl(ev[0])) + 1ull;
      c.t0 = (unsigned)e; c.t1 = (unsigned)(e >> 32);
    }
    sfor<2>([&](auto SP) {
      constexpr int sp = SP.value, ks = 2 * sl + sp;
      bf16x8 tl;
      if constexpr (NN) tl = t16 >= 12 ? ones : cat(fa[sp][2][0], fa[sp][2][1]);
      sfor<9>([&](auto M) {
        constexpr int mi = M.value, ti = mi >> 1;
        if constexpr (mi == 8) cs = mfma16(cat(fa[sp][NT][0], fa[sp][NT][1]), ones, cs);
        else if constexpr (NN && ti == 3) acc[3][mi & 1] = mfma16(fb[sp][mi & 1], fb[sp][mi & 1], acc[3][mi & 1]);
        else if constexpr (NN && ti == 2) acc[2][mi & 1] = mfma16(tl, fb[sp][mi & 1], acc[2][mi & 1]);
        else acc[ti][mi & 1] = mfma16(cat(fa[sp][ti][0], fa[sp][ti][1]), fb[sp][mi & 1], acc[ti][mi & 1]);
        SBAR();
        if constexpr (ks != 1) {
          if constexpr (mi < 3) { rd1(ic<ks + 1>{}, ic<2 * mi>{}); rd1(ic<ks + 1>{}, ic<2 * mi + 1>{}); }
          else rd1(ic<ks + 1>{}, ic<3 + mi>{});
        }
        if constexpr (sl == 0) { if constexpr (mi & 1) issue1(2 + sp, mi >> 1); }
        else if constexpr (sl + 3 < 9 && (mi == 2 + 2 * NN || mi == 6 + 2 * NN)) issue1(sl + 3, 2 * sp + (mi >= 6));
        SBAR();
      });
    });
  });
  STAMP(ws, 1, 4);
  const unsigned tb = lds0 + K1_TBL, cb = tb + 1024, dummy = cb + 512;
  ldsw<f32x4>(t16 == 0 ? cb + (w * 16 + 4 * g) * 4 : dummy, cs);
  if constexpr (NN) sfor<2>([&](auto T) {
    const unsigned a = tb + (32 * m + 16 * T.value + t16) * 8;
    const f32x4 gr = acc[3][T.value];
    const float d01 = (p & 1) ? gr[1] : gr[0], d23 = (p & 1) ? gr[3] : gr[2];
    ldsw<float>(g == 3 ? a : dummy, acc[2][T.value][0]);
    ldsw<float>(g == q4 ? a + 4 : dummy, (p & 2) ? d23 : d01);
  });
  lds_bar();
  f32x2 st[2];
  sfor<2>([&](auto T) {
    const f32x2 v = ldsr<f32x2>(tb + (32 * m + 16 * T.value + t16) * 8);
    const float mu = v[0] * (1.f / D_), rs = __builtin_amdgcn_rsqf(fmaxf(v[1] * (1.f / D_) - mu * mu, 0.f) + 1e-5f);
    st[T.value] = (f32x2){rs, -rs * mu};
  });
  const rsrc_t ro = mk_rsrc(ws + WS_QF, 32 * 64 * (QREC + KVREC));
  constexpr unsigned KV0 = WS_KV - WS_QF;
  const int tile32 = 4 * mt + m, key = 16 * (g >> 1) + t16;
  const unsigned lo_qk = (g & 1) * 512 + key * 16, lo_v = 1280 + key * 48 + (g & 1) * 16;
  sfor<(NN ? 2 : 4)>([&](auto TI) {
    constexpr int ti = TI.value, tile = 4 * NN + ti;
    const int pg = 3 * nt + (tile >> 1), type = pg >> 4, rec = (2 * (pg & 15) + (tile & 1)) * 64 + tile32;
    const f32x4 cc = ldsr<f32x4>(cb + (tile * 16 + 4 * g) * 4);
    const float sc = type == 0 ? QSCALE : 1.f;
    const f32x4 v0 = acc[ti][0] * (st[0][0] * sc) + cc * (st[0][1] * sc), v1 = acc[ti][1] * (st[1][0] * sc) + cc * (st[1][1] * sc);
    auto s0 = __builtin_amdgcn_permlane32_swap(pk2(v0[0], v0[1]), pk2(v1[0], v1[1]), false, false);
    auto s1 = __builtin_amdgcn_permlane32_swap(pk2(v0[2], v0[3]), pk2(v1[2], v1[3]), false, false);
    bstore16<16>((u32x4){s0[0], s1[0], s0[1], s1[1]}, ro, type == 2 ? lo_v : lo_qk, type == 0 ? rec * QREC : KV0 + rec * KVREC);
  });
  if constexpr (NN) {
    const int pg = 3 * nt + g, type = pg >> 4, rec = 2 * (pg & 15) * 64 + tile32;
    const f32x4 cc = ldsr<f32x4>(cb + (96 + 4 * g) * 4);
    const float sc = type == 0 ? QSCALE : 1.f;
    const unsigned rb = type == 0 ? rec * QREC : KV0 + rec * KVREC, hs = type == 0 ? 64 * QREC : 64 * KVREC;
    const unsigned oq = g < 3 && type < 2 ? rb + 1024 + t16 * 8 : OOB, ov = g < 3 && type == 2 ? rb + 1280 + 32 + t16 * 48 : OOB;
    sfor<2>([&](auto T) {
      const f32x4 v = acc[2][T.value] * (st[T.value][0] * sc) + cc * (st[T.value][1] * sc);
      const unsigned d0 = pk2(v[0], v[1]), d1 = pk2(v[2], v[3]);
      if (nt <= 10) { bstore8<16>((u32x2){d0, 0u}, ro, oq + T.value * 128, 0); bstore8<16>((u32x2){d1, 0u}, ro, oq + hs + T.value * 128, 0); }
      if (nt >= 10) { bstore16<16>((u32x4){d0, 0x00003f80u, 0u, 0u}, ro, ov, T.value * 768); bstore16<16>((u32x4){d1, 0x00003f80u, 0u, 0u}, ro, ov, 64 * KVREC + T.value * 768); }
    });
  }
  STAMP(ws, 1, 5);
  wait_vm<0>();
  STAMP(ws, 1, 6);
  bar();
  if (w == 0) set_flag(c, lane, FL1 + (mt * 16 + nt) * 64);
  STAMP(ws, 1, 7);
  STAMP_FLUSH(ws, 1);
}

constexpr int P3_WO = 49152;
constexpr int K4_RING = 90112;
constexpr unsigned k4_so(int u) { return ((((u >> 1) + 2) & 3) * 2 + (u & 1)) * 8192; }
DI unsigned k4_wv(int lane, int w, int Qd, int j) {
  const int rho = 8 * (w & 3) + (lane >> 3);
  return rho * 9216 + 1152 * Qd + 256 * j + 32 * (((lane & 7) >> 1) ^ ((rho >> 1) & 3)) + 16 * (lane & 1);
}

template <bool EDGE> DI void p2_body(const Ctx& c, int head, int qb, u32x2 (&xr)[4]) {
  char* ws = c.ws;
  const int w = c.w, xcd = c.xcd, r = c.r, lane = fresh_lane();
  const unsigned lds0 = c.lds0;
  const int q = lane & 31, h = lane >> 5, G = lane >> 4;
  const int qt = 8 * qb + w;
  const rsrc_t rq = mk_rsrc(ws + WS_QF, 32 * 64 * QREC), rkv = mk_rsrc(ws + WS_KV, 32 * 64 * KVREC);
  STAMP_DECL; STAMP(ws, 2, 0);
  if (w == 0) {
    const int ty = lane < 4 ? 1 : lane < 8 ? 2 : 0, pm = max(2 * qb + (ty ? (lane & 3) - 2 : (lane & 1)), 0);
    poll(c, (pm * 16 + (((16 * ty + (head >> 1)) * 43) >> 7)) * 64, FL1);
  }
  bar();
  STAMP(ws, 2, 7);
  bf16x8 qm; unsigned qtl;
  if constexpr (EDGE) {
    const unsigned src = (head * 64) * KVREC;
#pragma unroll
    for (int i = 0; i < 3; ++i) { const int pc = min(w + 8 * i, 21); glds16c(lds0 + 8 * KVREC + pc * 1024, lane * 16, rkv, src + pc * 1024); }
  } else {
    const unsigned src = (head * 64 + 8 * qb - 8) * KVREC;
#pragma unroll
    for (int i = 0; i < 6; ++i) { const int pc = min(w + 8 * i, 43); glds16c(lds0 + pc * 1024, lane * 16, rkv, src + pc * 1024); }
  }
  bload16c(qm, lane * 16, rq, (head * 64 + qt) * QREC);
  bload4c(qtl, 1024 + q * 8, rq, (head * 64 + qt) * QREC);
  STAMP(ws, 2, 1);
  asm volatile("s_waitcnt vmcnt(0)" : "+v"(qm), "+v"(qtl)::"memory");
  bar();
  STAMP(ws, 2, 2);
  {
    if (r < 18) {
      const rsrc_t rwo = mk_rsrc(c.wo, D_ * D_ * 2);
      const int nt = r >= 9 ? r - 9 : r;
      const int rho = 8 * (w & 3) + (lane >> 3), rg = (rho >> 2) & 3, rj = 4 * (rho >> 4) + (rho & 3);
      const unsigned cbyte = 128 * nt + 32 * (((lane & 7) >> 1) ^ ((rho >> 1) & 3)) + 16 * (lane & 1);
      const unsigned wv = (20 * (rg >> 1) + 8 * (rg & 1) + rj) * 1152 + cbyte;
      const int th = 16 * (w >> 2) + 4 * rg + (rj >> 1);
      const unsigned wvt = (18 * th + ((th & 1) ? 0 : 16) + (rj & 1)) * 1152 + cbyte;
      sfor<8>([&](auto P) { glds16(lds0 + P3_WO + (w + 8 * P.value) * 1024, wv, rwo, (2 * P.value + (w >> 2)) * 36 * 1152); });
      glds16(lds0 + P3_WO + (w + 64) * 1024, wvt, rwo, 0);
    }
    {
      const int nt = r >= 9 ? r - 9 : r, T = 8 * (2 * xcd + (r >= 9)) + w;
      const unsigned xo = r < 18 ? (16 * T + (lane & 15)) * 1152 + 128 * nt + 8 * G : OOB;
      sfor<4>([&](auto I) { bload8(xr[I.value], xo, mk_rsrc(c.x, S_ * D_ * 2), 32 * I.value); });
    }
    const rsrc_t rwm = mk_rsrc(w < 4 ? c.wg : c.wu, D_ * F_ * 2);
    const unsigned wv4 = k4_wv(lane, w, xcd, r & 3);
    sfor<4>([&](auto U) { glds16(lds0 + K4_RING + w * 1024 + k4_so(U.value), wv4, rwm, U.value * 32 * 9216); });
    const int v = r * 8 + w;
    const int l = (v % 81) * 64 + lane, row = (l * 7282) >> 16;
    if (v < 162) warm(lds0 + LDS_SCR, row * 9216 + xcd * 1152 + (l - 9 * row) * 128, mk_rsrc(v < 81 ? c.wg : c.wu, D_ * F_ * 2), 0);
  }
  const bf16x4 qt4 = __builtin_bit_cast(bf16x4, (u32x2){h ? 0u : qtl, 0u});
  const unsigned kb = lds0 + w * KVREC + lane * 16, ktb = lds0 + w * KVREC + 1024 + q * 8;
  const unsigned vb = lds0 + w * KVREC + 1280 + (4 * (G >> 1) + ((lane >> 2) & 3)) * 48 + ((G & 1) ? 32 : 8 * (lane & 3));
  f32x16 s[9];
  sfor<9>([&](auto I) {
    constexpr int i = I.value;
    const unsigned off = EDGE ? (unsigned)(max(w + i, 8) - w) * KVREC : i * KVREC;
    const bf16x8 km = ldsr<bf16x8>(kb + off);
    const bf16x4 kt = ldsr<bf16x4>(ktb + off);
    f32x16 a = mfma32(km, qm, (f32x16){});
    s[i] = __builtin_amdgcn_mfma_f32_32x32x8bf16_1k(kt, qt4, a, 0, 0, 0);
    if (EDGE && w + i < 8) {
#pragma unroll
      for (int e = 0; e < 16; ++e) s[i][e] = -1e30f;
    }
  });
  const int qq = q - 4 * h;
#pragma unroll
  for (int e = 0; e < 16; ++e) s[8][e] = ((e & 3) + 8 * (e >> 2) <= qq) ? s[8][e] : s[0][e];
  float m4[4];
#pragma unroll
  for (int e = 0; e < 4; ++e) m4[e] = fmaxf(s[1][2 * e], s[1][2 * e + 1]);
#pragma unroll
  for (int i = 1; i < 9; ++i)
#pragma unroll
    for (int e = (i == 1 ? 4 : 0); e < 8; ++e) m4[e & 3] = fmaxf(fmaxf(m4[e & 3], s[i][2 * e]), s[i][2 * e + 1]);
  float mx = max_x32(fmaxf(fmaxf(m4[0], m4[1]), fmaxf(m4[2], m4[3])));
  STAMP(ws, 2, 3);
  f32x16 o = {};
  sfor<8>([&](auto I) {
    constexpr int i = I.value + 1;
    const unsigned off = EDGE ? (unsigned)(max(w + i, 8) - w) * KVREC : i * KVREC;
    float pv[16];
#pragma unroll
    for (int e = 0; e < 8; ++e) {
      const f32x2 dd = (f32x2){s[i][2 * e], s[i][2 * e + 1]} - (f32x2){mx, mx};
      pv[2 * e] = __builtin_amdgcn_exp2f(dd[0]); pv[2 * e + 1] = __builtin_amdgcn_exp2f(dd[1]);
    }
    if constexpr (i < 8) {
#pragma unroll
      for (int ks = 0; ks < 2; ++ks) {
        const u32x4 pp = {pk2(pv[8 * ks], pv[8 * ks + 1]), pk2(pv[8 * ks + 2], pv[8 * ks + 3]), pk2(pv[8 * ks + 4], pv[8 * ks + 5]), pk2(pv[8 * ks + 6], pv[8 * ks + 7])};
        o = mfma32(trfrag(vb + off + ks * 768, vb + off + ks * 768 + 384), __builtin_bit_cast(bf16x8, pp), o);
      }
    } else {
      const unsigned off0 = EDGE ? (unsigned)(8 - w) * KVREC : 0;
#pragma unroll
      for (int ks = 0; ks < 2; ++ks) {
        u32x4 pd, po;
#pragma unroll
        for (int e = 0; e < 4; ++e) {
          const int rr = 8 * ks + 2 * e;
          const bool v0 = ((rr & 3) + 8 * (rr >> 2) <= qq), v1 = (((rr + 1) & 3) + 8 * ((rr + 1) >> 2) <= qq);
          pd[e] = pk2(v0 ? pv[rr] : 0.f, v1 ? pv[rr + 1] : 0.f);
          po[e] = pk2(v0 ? 0.f : pv[rr], v1 ? 0.f : pv[rr + 1]);
        }
        o = mfma32(trfrag(vb + off + ks * 768, vb + off + ks * 768 + 384), __builtin_bit_cast(bf16x8, pd), o);
        o = mfma32(trfrag(vb + off0 + ks * 768, vb + off0 + ks * 768 + 384), __builtin_bit_cast(bf16x8, po), o);
      }
    }
  });
  STAMP(ws, 2, 4);
  auto lr = __builtin_amdgcn_permlane32_swap(__float_as_uint(o[10]), __float_as_uint(o[10]), false, false);
  const float inv = 1.f / __uint_as_float(lr[0]);
  const rsrc_t ro = mk_rsrc(ws + WS_OF, 128 * 18 * 1024);
  const u32x4 om = {pk2(o[0] * inv, o[1] * inv), pk2(o[2] * inv, o[3] * inv), pk2(o[4] * inv, o[5] * inv), pk2(o[6] * inv, o[7] * inv)};
  const unsigned tt = 2 * qt + (q >> 4);
  bstore16<16>(om, ro, (tt * 18 + (head >> 1)) * 1024 + ((q & 15) + 16 * (2 * (head & 1) + h)) * 16, 0);
  bstore4<16>(pk2(o[8] * inv, o[9] * inv), ro, h ? OOB : (tt * 18 + 16 + (head >> 4)) * 1024 + ((q & 15) + 16 * ((head >> 2) & 3)) * 16 + (head & 3) * 4, 0);
  STAMP(ws, 2, 5);
  asm volatile("s_waitcnt vmcnt(0)" : "+v"(xr[0]), "+v"(xr[1]), "+v"(xr[2]), "+v"(xr[3])::"memory");
  set_flag(c, lane, FL2 + (qt * 32 + head) * 64);
  STAMP(ws, 2, 6);
  STAMP_FLUSH(ws, 2);
}

DI void p3_side(const Ctx& c) {
  char* ws = c.ws;
  const int w = c.w, xcd = c.xcd, lane = fresh_lane();
  const unsigned lds0 = c.lds0;
  const int sb = c.r - 18, gw = (sb * 8 + xcd) * 8 + w;
  const rsrc_t rwt = mk_rsrc(ws + WS_WT, 4 * 4608 * 32), rlg = mk_rsrc(ws + WS_LG, 8 * 4 * 36864), rcs = mk_rsrc(ws + WS_CS, 2 * 4608 * 4);
  const int drow = 8 * gw + (lane >> 3);
  const u32x4 dv = __builtin_amdgcn_raw_buffer_load_b128(mk_rsrc(c.wd, F_ * D_ * 2), gw < 576 ? drow * 1152 + 1024 + (lane & 7) * 16 : OOB, 0, 0);
  for (int u = sb; u < 18; u += 14) {
    const int mat = (u + 7) >> 4, un = u - 9 * mat;
    const rsrc_t rw = mk_rsrc(mat ? c.wu : c.wg, D_ * F_ * 2);
    u32x4 v[9];
#pragma unroll
    for (int i = 0; i < 9; ++i) v[i] = __builtin_amdgcn_raw_buffer_load_b128(rw, (lane & 7) * 16 + (72 * w + (lane >> 3)) * 9216, (9 * xcd + un) * 128 + i * 8 * 9216, 0);
    if (un == 8) {
#pragma unroll
      for (int i = 0; i < 9; ++i)
        bstore16<16>(v[i], rlg, ((lane & 7) >> 1) * 36864 + (72 * w + (lane >> 3)) * 32 + (lane & 1) * 16, (xcd * 8 + mat) * 18432 + i * 256);
    }
    float a[8] = {};
#pragma unroll
    for (int i = 0; i < 9; ++i)
#pragma unroll
      for (int e = 0; e < 4; ++e) { a[2 * e] += bflo(v[i][e]); a[2 * e + 1] += bfhi(v[i][e]); }
#pragma unroll
    for (int e = 0; e < 8; ++e) a[e] = sum_x32(sum_x16(a[e]));
    lds_bar();
    if (lane < 16) { ldsw<f32x4>(lds0 + w * 512 + lane * 32, (f32x4){a[0], a[1], a[2], a[3]}); ldsw<f32x4>(lds0 + w * 512 + lane * 32 + 16, (f32x4){a[4], a[5], a[6], a[7]}); }
    lds_bar();
    if (w == 0) {
      float t = 0.f;
#pragma unroll
      for (int i = 0; i < 16; ++i) t += ldsr<float>(lds0 + i * 256 + lane * 4);
      bstore4<16>(__float_as_uint(t), rcs, lane * 4, (mat * 4608 + (9 * xcd + un) * 64) * 4);
    }
  }
  const int pr = (drow % 576) >= 512 ? ((drow & ~24) | ((drow & 8) << 1) | ((drow & 16) >> 1)) : drow;
  bstore16<16>(dv, rwt, gw < 576 ? (((lane & 7) >> 1) * 4608 + pr) * 32 + (lane & 1) * 16 : OOB, 0);
  wait_vm<0>();
  bar();
  if (w == 0) set_flag(c, lane, FLS + (sb * 8 + xcd) * 64);
  if (w == 1 && sb == 13 && xcd == 0) {
    for (int i = 0; i < 4; ++i) poll(c, lane * 64, FL1 + i * 4096);
    set_flag(c, lane, 0);
  }
}

DI void p3_gemm(const Ctx& c, const u32x2 (&xr)[4]) {
  char* ws = c.ws;
  const int w = c.w, lane = fresh_lane();
  const unsigned lds0 = c.lds0;
  const int t16 = lane & 15, g = lane >> 4;
  const int mt = 2 * c.xcd + (c.r >= 9), nt = c.r >= 9 ? c.r - 9 : c.r;
  const int T = 8 * mt + w;
  const rsrc_t rof = mk_rsrc(ws + WS_OF, 128 * 18 * 1024);
  STAMP_DECL; STAMP(ws, 3, 0);
  unsigned wa[4];
  {
    const int r2 = 4 * g + ((lane >> 2) & 3);
#pragma unroll
    for (int i = 0; i < 4; ++i) wa[i] = lds0 + P3_WO + r2 * 128 + 32 * (i ^ ((r2 >> 1) & 3)) + 8 * (lane & 3);
  }
  bar();
  STAMP(ws, 3, 1);
  bf16x8 of[18];
  f32x4 acc[4] = {};
  bf16x4 fr[2][4][2];
  auto rd1 = [&](auto KS, auto I) { constexpr int ks = KS.value, i = I.value; fr[ks & 1][i >> 1][i & 1] = trh(wa[i >> 1] + (ks & 15) * 4096 + (i & 1) * 2048); };
  sfor<8>([&](auto I) { rd1(ic<0>{}, I); });
  poll(c, (lane & 31) * 64, FL2 + (T >> 1) * 2048);
  STAMP(ws, 3, 2);
  constexpr int PRO = 6;
  auto ld = [&](auto K) { if constexpr (K.value < 18) bload16c(of[K.value], lane * 16, rof, (T * 18 + K.value) * 1024); };
  sfor<PRO>(ld);
  sfor<18>([&](auto KS) {
    constexpr int ks = KS.value, iss = PRO + ks < 18 ? PRO + ks : 18;
    asm volatile("s_waitcnt vmcnt(%1)" : "+v"(of[ks]) : "i"(iss - ks - 1) : "memory");
    if constexpr (ks == 0) STAMP(ws, 3, 3);
    if constexpr (ks == 9) STAMP(ws, 3, 4);
    if constexpr (ks == 15) sfor<4>([&](auto I) { wa[I.value] += 65536; PIN(wa[I.value]); });
    sfor<4>([&](auto C) {
      acc[C.value] = mfma16(cat(fr[ks & 1][C.value][0], fr[ks & 1][C.value][1]), of[ks], acc[C.value]);
      SBAR();
      if constexpr (ks + 1 < 18) { rd1(ic<ks + 1>{}, ic<2 * C.value>{}); rd1(ic<ks + 1>{}, ic<2 * C.value + 1>{}); }
      if constexpr (C.value == 1) ld(ic<PRO + ks>{});
      SBAR();
    });
  });
  unsigned d[4][2];
  float s1 = 0.f, s2 = 0.f;
#pragma unroll
  for (int i = 0; i < 4; ++i) {
    d[i][0] = pk2(acc[i][0] + bflo(xr[i][0]), acc[i][1] + bfhi(xr[i][0]));
    d[i][1] = pk2(acc[i][2] + bflo(xr[i][1]), acc[i][3] + bfhi(xr[i][1]));
  }
  const rsrc_t rhf = mk_rsrc(ws + WS_HF, 128 * 18 * 1024);
  bstore16<16>((u32x4){d[0][0], d[0][1], d[1][0], d[1][1]}, rhf, lane * 16, (T * 18 + 2 * nt) * 1024);
  bstore16<16>((u32x4){d[2][0], d[2][1], d[3][0], d[3][1]}, rhf, lane * 16, (T * 18 + 2 * nt + 1) * 1024);
#pragma unroll
  for (int i = 0; i < 4; ++i)
#pragma unroll
    for (int e = 0; e < 2; ++e) { const float a = bflo(d[i][e]), b = bfhi(d[i][e]); s1 += a + b; s2 += a * a + b * b; }
  s1 = sum_x32(sum_x16(s1)); s2 = sum_x32(sum_x16(s2));
  bstore8<16>((u32x2){__float_as_uint(s1), __float_as_uint(s2)}, mk_rsrc(ws + WS_ST, 9 * 2048 * 8), g == 0 ? t16 * 8 : OOB, (T * 9 + nt) * 128);
  STAMP(ws, 3, 5);
  wait_vm<0>();
  set_flag(c, lane, FL3 + (T * 16 + nt) * 64);
  STAMP(ws, 3, 6);
  STAMP_FLUSH(ws, 3);
}

constexpr int K4_LG = 0;
constexpr int K4_CS = K4_LG + 36864;
constexpr int K4_STT = K4_CS + 2048;
constexpr int K4_STP = K4_STT + 2048;
struct K4Op { int kind, idx; };
constexpr int k4_lo(int S) { return S == 0 ? 4 : (S + 3) * 2; }
constexpr int k4_hi(int S) { return (S + 4) * 2 < 36 ? (S + 4) * 2 : 36; }
constexpr int k4_nx(int S) { return S == 6 ? 4 : S == 11 || S == 14 ? 2 : (S >= 10 && S < 15) || S == 16 ? 1 : 0; }
constexpr int k4_nops(int S) { int n = k4_nx(S); for (int u = k4_lo(S); u < k4_hi(S); ++u) n += u < 18 ? 3 : 1; return n; }
constexpr K4Op k4_op(int S, int i) {
  for (int u = k4_lo(S); u < k4_hi(S); ++u) { const int c = u < 18 ? 3 : 1; if (i < c) return {i, u}; i -= c; }
  if (S == 6) return {i < 3 ? 5 : 6, i};
  if (S == 16) return {4, 0};
  if (i == 1) return {S == 11 ? 7 : 8, 0};
  return {3, S - 10};
}
constexpr int k4_nst(int S) { return (S == 8 || S == 9) ? 2 : 0; }
constexpr int k4_issued(int S) { int n = 8; for (int s = 0; s < S; ++s) n += k4_nops(s) + k4_nst(s); return n; }
constexpr int k4_posW(int u) {
  for (int S = 0;; ++S) if (u >= k4_lo(S) && u < k4_hi(S)) { int n = k4_issued(S) + k4_nst(S); for (int v = k4_lo(S); v < u; ++v) n += v < 18 ? 3 : 1; return n; }
}
constexpr int k4_need(int S) {
  if (S == 0) return 3;
  if (S == 8) return k4_issued(7) - 1;
  if (S >= 16) return k4_issued(15) - 1;
  return k4_posW((S + 1) * 2);
}
constexpr int K5_SLOT = 17408, K5_BASE = 16384, K5_P = 5;
constexpr unsigned k5_so(int v) { return K5_BASE + ((v + 2) & 7) * K5_SLOT; }
constexpr int k5_u(int v) { return v < 32 ? 18 * ((v >> 3) & 1) + 4 * ((v >> 1) & 3) + 2 * (v >> 4) + (v & 1) : 18 * ((v - 32) >> 1) + 16 + (v & 1); }
DI unsigned k5_mv(int rho, int sp, int nt, int t16) {
  const int rr = sp ? ((rho & 7) | ((rho & 8) << 1) | ((rho & 16) >> 1)) : rho;
  return rr * 1152 + 256 * nt + 32 * ((t16 >> 1) ^ (rho & 7)) + 16 * (t16 & 1);
}
DI unsigned k5_fl(int i, int sg, int mt5) { return (((((mt5 >> 1) * 4 + ((i >> 2) & 3)) * 8 + 2 * sg + ((i >> 4) & 1)) * 8) + 4 * (mt5 & 1) + (i & 3)) * 64; }

DI void p4(const Ctx& c) {
  char* ws = c.ws;
  const int w = c.w, r = c.r, xcd = c.xcd, lane = fresh_lane();
  const int mt = r >> 2, Qd = xcd, j = r & 3;
  const unsigned lds0 = c.lds0;
  const int g = lane >> 4;
  const int T0 = 16 * mt + 2 * w;
  const rsrc_t rwm = mk_rsrc(w < 4 ? c.wg : c.wu, D_ * F_ * 2), rhf = mk_rsrc(ws + WS_HF, 128 * 18 * 1024), rwd = mk_rsrc(c.wd, F_ * D_ * 2);
  const rsrc_t rhh = mk_rsrc(ws + WS_HH, 128 * 144 * 1024);
  const unsigned f4 = FL4 + ((r * 8 + xcd) * 8 + w) * 64;
  const int sg = xcd >> 1, mt5 = 8 * (xcd & 1) + (r >> 2);
  STAMP_DECL; STAMP(ws, 4, 0);
  const unsigned wv = k4_wv(lane, w, Qd, j);
  const unsigned wdst = lds0 + K4_RING + w * 1024, l16 = lane * 16;
  poll(c, lane < 18 ? FL3 + ((T0 + (lane >= 9)) * 16 + lane - 9 * (lane >= 9)) * 64 : FLS + (min(lane - 18, 13) * 8 + xcd) * 64, 0);
  bf16x8 act[18][2];
  u32x2 pf;
  auto pf_off = [&] { const int z = opq(l16) >> 4; return z < 32 ? FL4 + k5_fl(z, sg, mt5) : FLS + (18 * sg + min(z - 32, 17)) * 64; };
  auto doop = [&](auto KIND, auto IDX) {
    constexpr int kind = KIND.value, u = IDX.value;
    if constexpr (kind == 0) glds16(wdst + k4_so(u), wv, rwm, (u % 18) * 32 * 9216 + (u / 18) * 128);
    else if constexpr (kind <= 2) bload16c(act[u][kind - 1], l16, rhf, (T0 * 18 + 18 * (kind - 1) + u) * 1024);
    else if constexpr (kind == 3) { const int pc = min(w + 8 * u, 35); glds16c(lds0 + K4_LG + pc * 1024, l16, mk_rsrc(ws + WS_LG, 8 * 4 * 36864), (Qd * 4 + j) * 36864 + pc * 1024); }
    else if constexpr (kind == 4) warm(lds0 + LDS_SCR, l16 * 8, rwd, sg * 1327104 + min(r * 8 + w, 161) * 8192);
    else if constexpr (kind == 5) glds16c(lds0 + K4_STP + w * 3072 + u * 1024, l16, mk_rsrc(ws + WS_ST, 9 * 2048 * 8), T0 * 1152 + u * 1024);
    else if constexpr (kind == 6) {
      const unsigned f = l16 < 512 ? 576 * Qd + 128 * j + (l16 >> 2) : 576 * Qd + 512 + 16 * j + (l16 >> 2) - 128;
      glds16c(lds0 + K4_CS + (w & 1) * 1024, l16 < 576 ? f * 4 : OOB, mk_rsrc(ws + WS_CS, 2 * 4608 * 4), (w & 1) * 4608 * 4);
    } else if constexpr (kind == 7) set_flag(c, l16, f4);
    else bload8c(pf, pf_off(), fl_rsrc(c), 0);
  };
  sfor<4>([&](auto U) { doop(ic<1>{}, U); doop(ic<2>{}, U); });
  unsigned wa[4];
  {
    const int r2 = 4 * g + ((lane >> 2) & 3);
#pragma unroll
    for (int i = 0; i < 4; ++i) wa[i] = lds0 + K4_RING + r2 * 128 + 32 * (i ^ ((r2 >> 1) & 3)) + 8 * (lane & 3);
  }
  f32x4 ag[4][2], au[4][2];
  bf16x4 fr[3][2][2];
  unsigned hd[2][2];
  auto rd1 = [&](auto N, auto I) {
    constexpr int n = N.value, i = I.value;
    if constexpr (n < 144) fr[n % 3][i >> 1][i & 1] = trh(wa[n % 4] + k4_so(n / 4) + 2048 * i);
    else if constexpr (n < 162) fr[n % 3][i >> 1][i & 1] = trh(lds0 + K4_LG + (l16 >> 6) * 32 + ((l16 >> 1) & 24) + (n - 144) * 1024 + (i >> 1) * 18432 + (i & 1) * 512);
  };
  auto rd = [&](auto N) { sfor<4>([&](auto I) { rd1(N, I); }); };
  auto mm = [&](auto N, auto KS, f32x4& g0, f32x4& g1, f32x4& u0, f32x4& u1) {
    constexpr int n = N.value, ks = KS.value;
    const bf16x8 fg = cat(fr[n % 3][0][0], fr[n % 3][0][1]), fu = cat(fr[n % 3][1][0], fr[n % 3][1][1]);
    if constexpr (ks == 0) mfma16_z(g0, fg, act[ks][0]); else mfma16_ip(g0, fg, act[ks][0]);
    SBAR(); rd1(ic<n + 2>{}, ic<0>{}); SBAR();
    if constexpr (ks == 0) mfma16_z(g1, fg, act[ks][1]); else mfma16_ip(g1, fg, act[ks][1]);
    SBAR(); rd1(ic<n + 2>{}, ic<1>{}); SBAR();
    if constexpr (ks == 0) mfma16_z(u0, fu, act[ks][0]); else mfma16_ip(u0, fu, act[ks][0]);
    SBAR(); rd1(ic<n + 2>{}, ic<2>{}); SBAR();
    if constexpr (ks == 0) mfma16_z(u1, fu, act[ks][1]); else mfma16_ip(u1, fu, act[ks][1]);
    SBAR(); rd1(ic<n + 2>{}, ic<3>{}); SBAR();
  };
  auto epi = [&](const f32x4& G_, const f32x4& U_, float nmu, const f32x4& cg, const f32x4& cu, unsigned& d0, unsigned& d1) {
    const f32x4 v = (G_ + cg * nmu) * (U_ + cu * nmu);
    d0 = pk2(v[0], v[1]); d1 = pk2(v[2], v[3]);
  };
  auto epichunk = [&](auto GRP, auto C) {
    constexpr int grp = GRP.value, cc = C.value;
    asm volatile("s_nop 4" : "+v"(ag[cc][0]), "+v"(ag[cc][1]), "+v"(au[cc][0]), "+v"(au[cc][1]));
    const unsigned csa = lds0 + K4_CS + ((l16 >> 4) & 48) + 256 * grp + 64 * cc, sta = lds0 + K4_STT + 128 * w + ((l16 >> 2) & 60);
    const f32x4 cg = ldsr<f32x4>(csa), cu = ldsr<f32x4>(csa + 1024);
    sfor<2>([&](auto T) {
      const float nmu = ldsr<float>(sta + 64 * T.value);
      if constexpr ((cc & 1) == 0) epi(ag[cc][T.value], au[cc][T.value], nmu, cg, cu, hd[T.value][0], hd[T.value][1]);
      else {
        unsigned d2, d3;
        epi(ag[cc][T.value], au[cc][T.value], nmu, cg, cu, d2, d3);
        bstore16<16>((u32x4){hd[T.value][0], hd[T.value][1], d2, d3}, rhh, l16, ((T0 + T.value) * 144 + 18 * Qd + 4 * j + 2 * grp + (cc >> 1)) * 1024);
      }
    });
  };
  unsigned mv5;
  auto pf5 = [&](auto N) {
    constexpr int n = N.value, v = (n - 1) >> 1;
    if constexpr (n == 0) {
      const int vv = min(w, K5_P - 1), uu = 4 * (vv >> 1) + (vv & 1);
      glds16c(lds0 + K5_BASE + (vv + 2) * K5_SLOT + 16384, l16, mk_rsrc(ws + WS_WT, 4 * 4608 * 32), (j * 144 + 36 * sg + uu) * 1024);
      const unsigned z = opq(l16);
      mv5 = k5_mv(4 * w + (z >> 8), 0, j, (z >> 4) & 15);
    } else if constexpr (n & 1) glds16(lds0 + k5_so(v) + 8192 + w * 1024, mv5, rwd, sg * 1152 * 1152 + k5_u(v) * 32 * 1152);
    else glds16c(lds0 + k5_so(v) + w * 1024, l16, rhh, ((8 * mt5 + w) * 144 + 36 * sg + k5_u(v)) * 1024);
  };
  sfor<18>([&](auto SS) {
    constexpr int S = SS.value;
    constexpr int nw = k4_issued(S) - k4_need(S) - 1;
    static_assert(nw >= 0 && nw < 64, "");
    if constexpr (S < 9) asm volatile("s_waitcnt vmcnt(%4)" : "+v"(act[2 * S][0]), "+v"(act[2 * S][1]), "+v"(act[2 * S + 1][0]), "+v"(act[2 * S + 1][1]) : "i"(nw) : "memory");
    else if constexpr (S == 16) asm volatile("s_waitcnt vmcnt(%1)" : "+v"(pf) : "i"(nw) : "memory");
    else wait_vm<nw>();
    if constexpr (S == 16) { if (!tag_ok(c, pf)) poll(c, pf_off(), 0); }
    bar();
    if constexpr (S == 0) STAMP(ws, 4, 1);
    if constexpr (S == 4) STAMP(ws, 4, 2);
    if constexpr (S == 9) STAMP(ws, 4, 3);
    if constexpr (S == 17) STAMP(ws, 4, 4);
    if constexpr (S == 0) { rd(ic<0>{}); rd(ic<1>{}); }
    if constexpr (S == 8) {
      sfor<2>([&](auto T) {
        const unsigned sa = lds0 + K4_STP + w * 3072 + T.value * 1152 + (l16 >> 1);
        const f32x2 p0 = ldsr<f32x2>(sa), p1 = ldsr<f32x2>(sa + 512), p2 = ldsr<f32x2>(sa + 1024);
        const f32x2 ps = p0 + p1 + (l16 < 256 ? p2 : (f32x2){0.f, 0.f});
        const float a = sum_x32(sum_x16(ps[0])), b = sum_x32(sum_x16(ps[1]));
        const float mu = a * (1.f / D_), var = fmaxf(b * (1.f / D_) - mu * mu, 0.f);
        ldsw<float>(lds0 + K4_STT + ((l16 & 256) << 2) + 128 * w + 64 * T.value + ((l16 >> 2) & 60), (l16 & 256) ? __builtin_amdgcn_rcpf(var + 1e-5f) : -mu);
      });
      SBAR();
    }
    sfor<8>([&](auto L) {
      constexpr int l = L.value, n = 8 * S + l, t = n / 4, cc = n % 4, ks = t % 18;
      mm(ic<n>{}, ic<ks>{}, ag[cc][0], ag[cc][1], au[cc][0], au[cc][1]);
      constexpr int no = k4_nops(S);
      sfor<no>([&](auto I) {
        constexpr int nd = no ? no : 1, b0 = I.value * 8 / nd;
        if constexpr (b0 == l) doop(ic<k4_op(S, I.value).kind>{}, ic<k4_op(S, I.value).idx>{});
      });
      if constexpr (S == 17 && l < 5) pf5(L);
      if constexpr (ks == 17 && cc >= 1) epichunk(ic<t / 18>{}, ic<cc - 1>{});
      if constexpr (t == 18 && cc == 0) epichunk(ic<0>{}, ic<3>{});
      SBAR();
    });
  });
  STAMP(ws, 4, 5);
  {
    f32x4 g0, g1, u0, u1;
    sfor<18>([&](auto K) {
      constexpr int ks = K.value, n = 144 + ks;
      mm(ic<n>{}, ic<ks>{}, g0, g1, u0, u1);
      if constexpr (ks == 0) epichunk(ic<1>{}, ic<3>{});
      if constexpr (ks >= 1 && ks <= 6) pf5(ic<4 + ks>{});
      if constexpr (ks == 10) { wait_vm<6>(); set_flag(c, l16, f4 + 2048 * 64); }
      SBAR();
    });
    const unsigned csa = lds0 + K4_CS + ((l16 >> 4) & 48) + 512, sta = lds0 + K4_STT + 128 * w + ((l16 >> 2) & 60);
    const f32x4 cg = ldsr<f32x4>(csa), cu = ldsr<f32x4>(csa + 1024);
    unsigned a0, a1, b0, b1;
    asm volatile("s_nop 9" : "+v"(g0), "+v"(g1), "+v"(u0), "+v"(u1));
    epi(g0, u0, ldsr<float>(sta), cg, cu, a0, a1);
    epi(g1, u1, ldsr<float>(sta + 64), cg, cu, b0, b1);
    auto s0 = __builtin_amdgcn_permlane32_swap(a0, b0, false, false);
    auto s1 = __builtin_amdgcn_permlane32_swap(a1, b1, false, false);
    bstore16<16>((u32x4){s0[0], s1[0], s0[1], s1[1]}, rhh, (l16 >> 9) * 144 * 1024 + (l16 & 511) + 512 * (j & 1), (T0 * 144 + 18 * Qd + 16 + (j >> 1)) * 1024);
    bstore4<16>(ldsr<unsigned>(lds0 + K4_STT + 1024 + 128 * w + ((l16 >> 2) & 124)), mk_rsrc(ws + WS_RS, 4 * 2048 * 4), (Qd & 1) == 0 && j == 0 && l16 < 512 ? l16 >> 2 : OOB, (sg * 2048 + 16 * T0) * 4);
    STAMP(ws, 4, 6);
    STAMP_FLUSH(ws, 4);
  }
}

constexpr int k5_E(int i) { int e = 36; if (K5_P + 2 * (i + 1) < e) e = K5_P + 2 * (i + 1); if (i >= 0 && i + 8 < e) e = i + 8; return e; }
static_assert(k5_E(5) <= 16 && k5_E(20) <= 32, "");

DI void p5(const Ctx& c) {
  char* ws = c.ws;
  const int w = c.w, r = c.r, xcd = c.xcd, lane = fresh_lane();
  const int sg = xcd >> 1, mt = 8 * (xcd & 1) + (r >> 2), nt = r & 3;
  const unsigned lds0 = c.lds0;
  const int t16 = lane & 15, g = lane >> 4;
  const int tile = 4 * mt + nt;
  const rsrc_t rpt = mk_rsrc(ws + WS_PT, 64 * 8 * 4 * PT_REC);
  const unsigned f4 = FL4 + (4096 + (r * 8 + xcd) * 8 + w) * 64;
  STAMP_DECL; STAMP(ws, 5, 0);
  if (w >= 4) {
    const int l = w - 4;
    const rsrc_t rwd = mk_rsrc(c.wd, F_ * D_ * 2), rhh = mk_rsrc(ws + WS_HH, 128 * 144 * 1024);
    const rsrc_t rwt = mk_rsrc(ws + WS_WT, 4 * 4608 * 32);
    unsigned mv[2][2];
#pragma unroll
    for (int sp = 0; sp < 2; ++sp)
#pragma unroll
      for (int i = 0; i < 2; ++i) mv[sp][i] = k5_mv(4 * (2 * l + i) + g, sp, nt, t16);
    const unsigned hs = ((8 * mt + 2 * l) * 144 + 36 * sg) * 1024, wsb = sg * 1152 * 1152, dl = lds0 + l * 2048;
    auto issue = [&](auto V) {
      constexpr int v = V.value, u = k5_u(v), sp = v >= 32;
      constexpr unsigned so = k5_so(v);
      glds16(dl + so + 8192, mv[sp][0], rwd, wsb + u * 32 * 1152);
      glds16c(dl + so, lane * 16, rhh, hs + u * 1024);
      glds16(dl + so + 8192 + 1024, mv[sp][1], rwd, wsb + u * 32 * 1152);
      glds16c(dl + so + 1024, lane * 16, rhh, hs + (144 + u) * 1024);
      if ((v & 3) == l) glds16c(lds0 + so + 16384, lane * 16, rwt, (nt * 144 + 36 * sg + u) * 1024);
    };
    sfor<36>([&](auto I) {
      constexpr int i = I.value, y = k5_E(i - 1) - (i + 2 < 36 ? i + 2 : 36), yy = k5_E(i - 1) - K5_P;
      if constexpr (i == 0) wait_vm<3>();
      else if constexpr (i == 2) { wait_vm<4 * yy + yy / 4>(); set_flag(c, lane, f4); }
      else if constexpr (i + 2 > K5_P) wait_vm<4 * y + y / 4>();
      bar();
      if constexpr (i == 0) STAMP(ws, 5, 1);
      if constexpr (i == 18) STAMP(ws, 5, 2);
      sfor<k5_E(i) - k5_E(i - 1)>([&](auto U) { issue(ic<k5_E(i - 1) + U.value>{}); });
    });
    bar();
    STAMP(ws, 5, 3);
    bar();
  } else {
    const int r2 = 4 * g + ((lane >> 2) & 3);
    unsigned wa[8];
#pragma unroll
    for (int i = 0; i < 8; ++i) wa[i] = lds0 + 8192 + r2 * 256 + 32 * (i ^ (r2 & 7)) + 8 * (lane & 3);
    const unsigned wt = lds0 + 16384 + r2 * 32 + 8 * (lane & 3), ha = lds0 + w * 2048 + lane * 16;
    const unsigned fo = k5_fl(lane & 31, sg, mt);
    f32x4 acc[9][2] = {};
    bf16x4 fr[3][3][2];
    bf16x8 hb[2][2];
    u32x2 pf;
    auto rd1 = [&](auto N, auto I) {
      constexpr int n = N.value, u = n / 3, i = I.value;
      if constexpr (u < 36) {
        constexpr unsigned so = k5_so(u);
        constexpr int cc = 3 * (n % 3) + i / 2;
        if constexpr (i >= 6) hb[u & 1][i - 6] = ldsr<bf16x8>(ha + so + (i - 6) * 1024);
        else if constexpr (cc < 8) fr[n % 3][i / 2][i & 1] = trh(wa[cc] + so + (i & 1) * 4096);
        else fr[n % 3][i / 2][i & 1] = trh(wt + so + (i & 1) * 512);
      }
    };
    sfor<36>([&](auto U) {
      constexpr int u = U.value;
      if constexpr (u == 0) wait_vm<3>();
      if constexpr (u == 2) { wait_vm<0>(); set_flag(c, lane, f4); }
      if constexpr (u == 3 || u == 18) bload8c(pf, fo, fl_rsrc(c), FL4 + (u == 3 ? 1 : 2) * 2048 * 64);
      if constexpr (u == 6 || u == 21) {
        asm volatile("s_waitcnt vmcnt(0)" : "+v"(pf)::"memory");
        if (!tag_ok(c, pf)) poll(c, fo, FL4 + (u == 6 ? 1 : 2) * 2048 * 64);
      }
      bar();
      if constexpr (u == 0) STAMP(ws, 5, 1);
      if constexpr (u == 18) STAMP(ws, 5, 2);
      if constexpr (u == 0) { rd1(ic<0>{}, ic<6>{}); rd1(ic<0>{}, ic<7>{}); sfor<6>([&](auto I) { rd1(ic<0>{}, I); }); sfor<6>([&](auto I) { rd1(ic<1>{}, I); }); }
      sfor<18>([&](auto M) {
        constexpr int mi = M.value, v = mi / 6, n = 3 * u + v, cc = mi / 2;
        acc[cc][mi & 1] = mfma16(cat(fr[n % 3][cc % 3][0], fr[n % 3][cc % 3][1]), hb[u & 1][mi & 1], acc[cc][mi & 1]);
        SBAR();
        if constexpr (mi == 6 || mi == 8) rd1(ic<n + 2>{}, ic<6 + (mi - 6) / 2>{});
        rd1(ic<n + 2>{}, ic<mi % 6>{});
        SBAR();
      });
    });
    bar();
    STAMP(ws, 5, 3);
    if (w == sg) {
      sfor<2>([&](auto T) { sfor<9>([&](auto C) { ldsw<f32x4>(lds0 + (T.value * 9 + C.value) * 1024 + lane * 16, acc[C.value][T.value]); }); });
      lds_bar();
    } else {
      sfor<2>([&](auto T) {
        const unsigned pb = (((tile * 4 + w) * 4 + sg) * 2 + T.value) * PT_REC;
        sfor<4>([&](auto I) {
          constexpr int i = I.value;
          const f32x4 a = acc[2 * i][T.value], b = acc[2 * i + 1][T.value];
          bstore16<16>((u32x4){pk2(a[0], a[1]), pk2(a[2], a[3]), pk2(b[0], b[1]), pk2(b[2], b[3])}, rpt, lane * 16 + i * 1024, pb);
        });
        const f32x4 a = acc[8][T.value];
        bstore8<16>((u32x2){pk2(a[0], a[1]), pk2(a[2], a[3])}, rpt, lane * 8 + 4096, pb);
      });
      bar();
      wait_vm<0>();
      set_flag(c, lane, FL5 + ((tile * 4 + w) * 4 + sg) * 64);
    }
  }
  const int T = w >> 2, pi = w & 3, tt = 8 * mt + 2 * sg + T;
  const rsrc_t rhf = mk_rsrc(ws + WS_HF, 128 * 18 * 1024);
  u32x4 hv, pv[3];
  u32x2 hl, pl[3];
  unsigned rs2u;
  STAMP(ws, 5, 4);
  bload16c(hv, lane * 16, rhf, (tt * 18 + 4 * nt + pi) * 1024);
  bload8c(hl, lane * 16 + 8 * (nt & 1), rhf, (tt * 18 + 16 + (nt >> 1)) * 1024);
  bload4c(rs2u, t16 * 4, mk_rsrc(ws + WS_RS, 4 * 2048 * 4), (sg * 2048 + 16 * tt) * 4);
  poll(c, ((sg + 1 + min(lane, 2)) & 3) * 64, FL5 + (tile * 4 + sg) * 256);
  STAMP(ws, 5, 5);
#pragma unroll
  for (int o = 0; o < 3; ++o) {
    const unsigned so = (((tile * 4 + sg) * 4 + ((sg + 1 + o) & 3)) * 2 + T) * PT_REC;
    bload16c(pv[o], lane * 16 + pi * 1024, rpt, so);
    bload8c(pl[o], pi == 0 ? lane * 8 + 4096 : OOB, rpt, so);
  }
  const unsigned orow = (16 * tt + t16) * 1152, own = lds0 + T * 9216 + lane * 16;
  const f32x4 o0 = ldsr<f32x4>(own + pi * 2048), o1 = ldsr<f32x4>(own + pi * 2048 + 1024), o2 = ldsr<f32x4>(own + 8192);
  asm volatile("s_waitcnt vmcnt(0)" : "+v"(hv), "+v"(hl), "+v"(rs2u), "+v"(pv[0]), "+v"(pv[1]), "+v"(pv[2]), "+v"(pl[0]), "+v"(pl[1]), "+v"(pl[2])::"memory");
  STAMP(ws, 5, 6);
  const float rs2 = __uint_as_float(rs2u);
  const rsrc_t rout = mk_rsrc(c.out, S_ * D_ * 2);
  auto fin = [&](const f32x4& ow, unsigned a0, unsigned a1, unsigned b0, unsigned b1, unsigned c0, unsigned c1, unsigned h0, unsigned h1, unsigned& d0, unsigned& d1) {
    const float v0 = ((ow[0] + bflo(b0)) + (bflo(a0) + bflo(c0))) * rs2 + bflo(h0);
    const float v1 = ((ow[1] + bfhi(b0)) + (bfhi(a0) + bfhi(c0))) * rs2 + bfhi(h0);
    const float v2 = ((ow[2] + bflo(b1)) + (bflo(a1) + bflo(c1))) * rs2 + bflo(h1);
    const float v3 = ((ow[3] + bfhi(b1)) + (bfhi(a1) + bfhi(c1))) * rs2 + bfhi(h1);
    d0 = pk2(v0, v1); d1 = pk2(v2, v3);
  };
  {
    unsigned a0, a1, b0, b1;
    fin(o0, pv[0][0], pv[0][1], pv[1][0], pv[1][1], pv[2][0], pv[2][1], hv[0], hv[1], a0, a1);
    fin(o1, pv[0][2], pv[0][3], pv[1][2], pv[1][3], pv[2][2], pv[2][3], hv[2], hv[3], b0, b1);
    auto s0 = __builtin_amdgcn_permlane16_swap(a0, b0, false, false);
    auto s1 = __builtin_amdgcn_permlane16_swap(a1, b1, false, false);
    bstore16<0>((u32x4){s0[0], s1[0], s0[1], s1[1]}, rout, orow + 256 * nt + 64 * pi + 32 * (g & 1) + 16 * (g >> 1), 0);
  }
  {
    unsigned e0, e1;
    fin(o2, pl[0][0], pl[0][1], pl[1][0], pl[1][1], pl[2][0], pl[2][1], hl[0], hl[1], e0, e1);
    bstore8<0>((u32x2){e0, e1}, rout, pi == 0 ? orow + 1024 + 32 * nt + 8 * g : OOB, 0);
  }
  STAMP(ws, 5, 7);
  STAMP_FLUSH(ws, 5);
}

extern "C" __global__ __launch_bounds__(512, 2) void k1_all(const void* x, const void* wqkv, const void* wo, const void* wg, const void* wu, const void* wd, char* ws, void* out) {
  Ctx c = {x, wqkv, wo, wg, wu, wd, ws, out, (int)rfl(threadIdx.x >> 6), (int)(blockIdx.x & 7), (int)(blockIdx.x >> 3), (unsigned)(size_t)smem, 0u, 0u};
  if (c.w == 7) {
    unsigned long long pc; unsigned back, size;
    asm volatile("s_getpc_b64 %0\n.Lk1pc%=:\n\ts_mov_b32 %1, .Lk1pc%=-k1_all\n\ts_mov_b32 %2, k9_end-k1_all" : "=s"(pc), "=s"(back), "=s"(size));
    const int lane = fresh_lane();
    warm(c.lds0 + LDS_SCR, lane < 24 ? (c.r * 24 + lane) * 128 : OOB, mk_rsrc((const void*)(pc - back), size), 0);
  }
  {
    const int mt = 4 * (c.xcd & 3) + (c.r & 3), nt = 8 * (c.xcd >> 2) + (c.r >> 2);
    if (c.w < 4) p1_body<0>(c, mt, nt); else p1_body<1>(c, mt, nt);
  }
  u32x2 xr[4];
  {
    const int head = 4 * c.xcd + (c.r & 3), qb = c.r >> 2;
    if (qb == 0) p2_body<true>(c, head, qb, xr); else p2_body<false>(c, head, qb, xr);
  }
  if (c.r < 18) p3_gemm(c, xr); else p3_side(c);
  p4(c);
  p5(c);
}

extern "C" __global__ void k9_end() {}

extern "C" void kernel_launch(void* const* d_in, const int* in_sizes, int n_in, void* d_out, int out_size, void* d_ws, size_t ws_size,
                              hipStream_t stream) {
  (void)hipFuncSetAttribute((const void*)k1_all, hipFuncAttributeMaxDynamicSharedMemorySize, LDS_ALL);
  k1_all<<<256, 512, LDS_ALL, stream>>>(d_in[0], d_in[1], d_in[2], d_in[4], d_in[5], d_in[6], (char*)d_ws, d_out);
}
```

```cpp
#include <hip/hip_runtime.h>
#include <stdint.h>

typedef short bf16x8 __attribute__((ext_vector_type(8)));
typedef short bf16x4 __attribute__((ext_vector_type(4)));
typedef float f32x2 __attribute__((ext_vector_type(2)));
typedef float f32x4 __attribute__((ext_vector_type(4)));
typedef float f32x16 __attribute__((ext_vector_type(16)));
typedef unsigned u32x2 __attribute__((ext_vector_type(2)));
typedef unsigned u32x4 __attribute__((ext_vector_type(4)));
typedef __bf16 bf2_t __attribute__((ext_vector_type(2)));
typedef __amdgpu_buffer_rsrc_t rsrc_t;
#define LAS __attribute__((address_space(3)))
#define GAS __attribute__((address_space(1)))
#define DI __device__ __forceinline__

constexpr int S_ = 2048, D_ = 576, F_ = 4608;
constexpr float QSCALE = 0.23570226039551584f * 1.4426950408889634f;
constexpr unsigned ONES2 = 0x3f803f80u;

constexpr size_t QREC = 1280, KVREC = 2560;
constexpr unsigned OOB = 0x7fffff00u;
constexpr unsigned FL1 = 128;
constexpr unsigned FL2 = FL1 + 256 * 64;
constexpr unsigned FL3 = FL2 + 2048 * 64;
constexpr unsigned FLS = FL3 + 2048 * 64;
constexpr unsigned FL4 = FLS + 128 * 64;
constexpr unsigned FL5 = FL4 + 3 * 2048 * 64;
constexpr unsigned FL_END = FL5 + 1024 * 64;
constexpr size_t WS_QF = 1u << 20;
constexpr size_t WS_KV = WS_QF + 32 * 64 * QREC;
constexpr size_t WS_OF = WS_KV + 32 * 64 * KVREC;
constexpr size_t WS_HF = WS_OF + 128 * 18 * 1024;
constexpr size_t WS_ST = WS_HF + 128 * 18 * 1024;
constexpr size_t WS_CS = WS_ST + 9 * 2048 * 8;
constexpr size_t WS_RS = WS_CS + 2 * 4608 * 4;
constexpr size_t WS_LG = WS_RS + 4 * 2048 * 4;
constexpr size_t WS_WT = WS_LG + 8 * 4 * 36864;
constexpr size_t WS_HH = WS_WT + 4 * 4608 * 32;
constexpr size_t PT_REC = 4608;
constexpr size_t WS_PT = WS_HH + (size_t)128 * 144 * 1024;
constexpr size_t WS_END = WS_PT + (size_t)64 * 8 * 4 * PT_REC;

DI unsigned rfl(unsigned v) { return __builtin_amdgcn_readfirstlane(v); }
DI rsrc_t mk_rsrc(const void* p, unsigned bytes) { return __builtin_amdgcn_make_buffer_rsrc((void*)p, (short)0, (int)bytes, 0x00020000); }
DI void glds16(unsigned lds, unsigned voff, rsrc_t r, unsigned soff) {
  asm volatile("s_mov_b32 m0, %0\n\ts_nop 0\n\tbuffer_load_dwordx4 %1, %2, %3 offen lds" ::"s"(lds), "v"(voff), "s"(r), "s"(soff) : "memory", "m0");
}
DI void glds16c(unsigned lds, unsigned voff, rsrc_t r, unsigned soff) {
  asm volatile("s_mov_b32 m0, %0\n\ts_nop 0\n\tbuffer_load_dwordx4 %1, %2, %3 offen sc1 lds" ::"s"(lds), "v"(voff), "s"(r), "s"(soff) : "memory", "m0");
}
template <class T> DI void bload16c(T& d, unsigned voff, rsrc_t r, unsigned soff) {
  asm volatile("buffer_load_dwordx4 %0, %1, %2, %3 offen sc1" : "=v"(d) : "v"(voff), "s"(r), "s"(soff) : "memory");
}
DI void bload8c(u32x2& d, unsigned voff, rsrc_t r, unsigned soff) {
  asm volatile("buffer_load_dwordx2 %0, %1, %2, %3 offen sc1" : "=v"(d) : "v"(voff), "s"(r), "s"(soff) : "memory");
}
DI void bload4c(unsigned& d, unsigned voff, rsrc_t r, unsigned soff) {
  asm volatile("buffer_load_dword %0, %1, %2, %3 offen sc1" : "=v"(d) : "v"(voff), "s"(r), "s"(soff) : "memory");
}
DI void bload8(u32x2& d, unsigned voff, rsrc_t r, unsigned soff) {
  asm volatile("buffer_load_dwordx2 %0, %1, %2, %3 offen" : "=v"(d) : "v"(voff), "s"(r), "s"(soff) : "memory");
}
DI void warm(unsigned lds, unsigned voff, rsrc_t r, unsigned soff) {
  asm volatile("s_mov_b32 m0, %0\n\ts_nop 0\n\tbuffer_load_dword %1, %2, %3 offen lds" ::"s"(lds), "v"(voff), "s"(r), "s"(soff) : "memory", "m0");
}
template <int N> DI void wait_vm() { asm volatile("s_waitcnt vmcnt(%0)" ::"i"(N) : "memory"); }
#define PIN(x) asm volatile("" : "+v"(x))
#define SBAR() __builtin_amdgcn_sched_barrier(0)
DI void bar() { SBAR(); asm volatile("s_barrier" ::: "memory"); SBAR(); }
DI void lds_bar() { SBAR(); asm volatile("s_waitcnt lgkmcnt(0)\n\ts_barrier" ::: "memory"); SBAR(); }
DI bf16x8 trfrag(unsigned a0, unsigned a1) {
  bf16x4 lo = __builtin_amdgcn_ds_read_tr16_b64_v4i16((LAS bf16x4*)(size_t)a0);
  bf16x4 hi = __builtin_amdgcn_ds_read_tr16_b64_v4i16((LAS bf16x4*)(size_t)a1);
  return __builtin_shufflevector(lo, hi, 0, 1, 2, 3, 4, 5, 6, 7);
}
DI bf16x4 trh(unsigned a) { return __builtin_amdgcn_ds_read_tr16_b64_v4i16((LAS bf16x4*)(size_t)a); }
DI bf16x8 cat(bf16x4 lo, bf16x4 hi) { return __builtin_shufflevector(lo, hi, 0, 1, 2, 3, 4, 5, 6, 7); }
template <class T> DI T ldsr(unsigned a) { return *(LAS T*)(size_t)a; }
template <class T> DI void ldsw(unsigned a, T v) { *(LAS T*)(size_t)a = v; }
DI unsigned pk2(float a, float b) { f32x2 v = {a, b}; return __builtin_bit_cast(unsigned, __builtin_convertvector(v, bf2_t)); }
DI float bflo(unsigned u) { return __uint_as_float(u << 16); }
DI float bfhi(unsigned u) { return __uint_as_float(u & 0xffff0000u); }
DI f32x4 mfma16(bf16x8 a, bf16x8 b, f32x4 c) { return __builtin_amdgcn_mfma_f32_16x16x32_bf16(a, b, c, 0, 0, 0); }
DI void mfma16_ip(f32x4& c, bf16x8 a, bf16x8 b) { asm volatile("v_mfma_f32_16x16x32_bf16 %0, %1, %2, %0" : "+v"(c) : "v"(a), "v"(b)); }
DI void mfma16_z(f32x4& c, bf16x8 a, bf16x8 b) { asm volatile("v_mfma_f32_16x16x32_bf16 %0, %1, %2, 0" : "=&v"(c) : "v"(a), "v"(b)); }
DI f32x16 mfma32(bf16x8 a, bf16x8 b, f32x16 c) { return __builtin_amdgcn_mfma_f32_32x32x16_bf16(a, b, c, 0, 0, 0); }
DI float xor32(float v) { auto r = __builtin_amdgcn_permlane32_swap(__float_as_uint(v), __float_as_uint(v), false, false); return __uint_as_float((threadIdx.x & 32) ? r[0] : r[1]); }
DI float sum_x32(float v) { auto r = __builtin_amdgcn_permlane32_swap(__float_as_uint(v), __float_as_uint(v), false, false); return __uint_as_float(r[0]) + __uint_as_float(r[1]); }
DI float max_x32(float v) { auto r = __builtin_amdgcn_permlane32_swap(__float_as_uint(v), __float_as_uint(v), false, false); return fmaxf(__uint_as_float(r[0]), __uint_as_float(r[1])); }
DI float sum_x16(float v) { auto r = __builtin_amdgcn_permlane16_swap(__float_as_uint(v), __float_as_uint(v), false, false); return __uint_as_float(r[0]) + __uint_as_float(r[1]); }
template <int AUX> DI void bstore16(u32x4 v, rsrc_t r, unsigned voff, unsigned soff) {
  if constexpr (AUX == 16) asm volatile("buffer_store_dwordx4 %0, %1, %2, %3 offen sc1\n\ts_nop 1" ::"v"(v), "v"(voff), "s"(r), "s"(soff) : "memory");
  else asm volatile("buffer_store_dwordx4 %0, %1, %2, %3 offen\n\ts_nop 1" ::"v"(v), "v"(voff), "s"(r), "s"(soff) : "memory");
}
template <int AUX> DI void bstore8(u32x2 v, rsrc_t r, unsigned voff, unsigned soff) { __builtin_amdgcn_raw_buffer_store_b64(v, r, voff, soff, AUX); }
template <int AUX> DI void bstore4(unsigned v, rsrc_t r, unsigned voff, unsigned soff) { __builtin_amdgcn_raw_buffer_store_b32(v, r, voff, soff, AUX); }

template <int... I> struct iseq {};
template <int N, int... I> struct mkseq : mkseq<N - 1, N - 1, I...> {};
template <int... I> struct mkseq<0, I...> { typedef iseq<I...> type; };
template <int V> struct ic { static constexpr int value = V; };
template <class Fn, int... I> DI void sfor_(Fn&& f, iseq<I...>) { (f(ic<I>{}), ...); }
template <int N, class Fn> DI void sfor(Fn&& f) { sfor_(f, typename mkseq<N>::type{}); }

extern __shared__ __attribute__((aligned(16))) char smem[];
#define STAMP(ws, kid, i)
#define STAMP_DECL
#define STAMP_FLUSH(ws, kid)
#define STAMP_OUT()

struct Ctx {
  const void *x, *wqkv, *wo, *wg, *wu, *wd;
  char* ws; void* out;
  int w, xcd, r;
  unsigned lds0, t0, t1;
};
constexpr int LDS_ALL = 160 * 1024, LDS_SCR = LDS_ALL - 256;
DI int fresh_lane() { int l; asm volatile("v_mbcnt_lo_u32_b32 %0, -1, 0\n\tv_mbcnt_hi_u32_b32 %0, -1, %0" : "=v"(l)); __builtin_assume(l >= 0 && l < 64); return l; }
DI unsigned opq(unsigned v) { PIN(v); return v; }
constexpr int LDS_CNT = 147456;
DI void cnt_add(const Ctx& c, int i) {
  asm volatile("s_mov_b64 exec, 1\n\tds_add_u32 %0, %1\n\ts_mov_b64 exec, -1" ::"v"(c.lds0 + LDS_CNT + 4 * i), "v"(1u) : "memory");
}
DI unsigned cnt_get(const Ctx& c, int i) {
  unsigned v;
  asm volatile("ds_read_b32 %0, %1\n\ts_waitcnt lgkmcnt(0)" : "=v"(v) : "v"(c.lds0 + LDS_CNT + 4 * i) : "memory");
  return rfl(v);
}
DI void cnt_wait(const Ctx& c, int i, unsigned n) {
  if (__builtin_expect(cnt_get(c, i) < n, 0))
    for (int spin = 0; spin < (1 << 22) && cnt_get(c, i) < n; ++spin) __builtin_amdgcn_s_sleep(1);
}
DI rsrc_t fl_rsrc(const Ctx& c) { return mk_rsrc(c.ws, FL_END); }
DI void set_flag(const Ctx& c, unsigned off) {
  u32x2 t;
  asm volatile("v_mov_b32 %0, %1" : "=v"(t[0]) : "s"(c.t0));
  asm volatile("v_mov_b32 %0, %1" : "=v"(t[1]) : "s"(c.t1));
  asm volatile("s_mov_b64 exec, 1\n\tbuffer_store_dwordx2 %0, off, %1, %2 sc1\n\ts_mov_b64 exec, -1" ::"v"(t), "s"(fl_rsrc(c)), "s"(off) : "memory");
}
DI bool tag_ok(const Ctx& c, u32x2 v) { return __builtin_amdgcn_ballot_w64(v[0] != c.t0 || v[1] != c.t1) == 0; }
DI void poll(const Ctx& c, unsigned voff, unsigned soff) {
  for (int spin = 0; spin < (1 << 20); ++spin) {
    u32x2 v;
    asm volatile("buffer_load_dwordx2 %0, %1, %2, %3 offen sc1\n\ts_waitcnt vmcnt(0)" : "=&v"(v) : "v"(voff), "s"(fl_rsrc(c)), "s"(soff) : "memory");
    if (tag_ok(c, v)) break;
  }
}

constexpr int K1_SLOT = 32768, K1_TBL = K1_SLOT * 4;
extern "C" __global__ void k9_end();

template <int NN> DI void p1_body(Ctx& c, int mt, int nt) {
  char* ws = c.ws;
  const int w = c.w, lane = fresh_lane();
  const unsigned lds0 = c.lds0;
  const rsrc_t rx = mk_rsrc(c.x, S_ * D_ * 2), rw = mk_rsrc(c.wqkv, D_ * 3 * D_ * 2);
  const int t16 = lane & 15, g = lane >> 4, q4 = (lane >> 2) & 3, p = lane & 3, m = w & 3;
  constexpr int NT = NN ? 3 : 4;
  unsigned xv[2], wv[2];
#pragma unroll
  for (int i = 0; i < 2; ++i) {
    const int row = 8 * (2 * w + i) + (lane >> 3);
    xv[i] = (128 * mt + row) * 1152 + 16 * ((lane & 7) ^ ((row >> 1) & 7));
    int li = 64 * (2 * w + i) + lane;
    if (li >= 896) li -= 128;
    const int r64 = (li * 4682) >> 16, cc = li - 14 * r64;
    const int rho = r64 & 31, k = (r64 & 32) + 8 * ((rho >> 2) & 3) + 4 * (rho >> 4) + (rho & 3);
    wv[i] = k * 3456 + ((216 * nt) & ~15) + 16 * cc;
  }
  STAMP_DECL; STAMP(ws, 1, 0);
  auto issue1 = [&](int slab, int i) {
    const unsigned dst = lds0 + (slab & 3) * K1_SLOT + w * 2048 + (i >> 1) * 1024;
    if (i & 1) glds16(dst + 16384, wv[i >> 1], rw, slab * 64 * 3456);
    else glds16(dst, xv[i >> 1], rx, slab * 128);
  };
#pragma unroll
  for (int sl = 0; sl < 2; ++sl)
#pragma unroll
    for (int i = 0; i < 4; ++i) issue1(sl, i);
  u32x2 ev;
  bload8c(ev, 0u, fl_rsrc(c), 0);
  STAMP(ws, 1, 1);
  const unsigned wa = lds0 + 16384 + (4 * g + q4) * 224 + (nt & 1) * 8;
  unsigned ad[2][5];
  ad[0][0] = wa + 8 * p + 144 * NN;
  ad[0][1] = wa + 72 * (p < 3 ? p : 2) + 32;
  ad[0][2] = NN && m >= 2 ? ad[0][1] : ad[0][0] + 72 * (m >> 1) + 40 * (m & 1);
  ad[0][3] = lds0 + (32 * m + t16) * 128 + 16 * (g ^ ((t16 >> 1) & 7));
  ad[0][4] = ad[0][3] ^ 64;
#pragma unroll
  for (int i = 0; i < 5; ++i) { ad[1][i] = ad[0][i] + 2 * K1_SLOT; PIN(ad[1][i]); }
  f32x4 acc[4][2] = {}, cs = {};
  bf16x8 ones = __builtin_bit_cast(bf16x8, (u32x4){ONES2, ONES2, ONES2, ONES2});
  PIN(ones);
  bf16x8 fb[2][2];
  bf16x4 fa[2][NT + 1][2];
  constexpr int NR = 2 * NT + 4;
  auto rd1 = [&](auto KS, auto I) {
    constexpr int ks = KS.value, i = I.value, sp = ks & 1, sl = (ks >> 1) & 3, ti = (i - 2) >> 1;
    constexpr unsigned so = (sl & 1) * K1_SLOT;
    if constexpr (ks < 18 && i < NR) {
      if constexpr (i < 2) fb[sp][i] = ldsr<bf16x8>(ad[sl >> 1][3 + sp] + so + i * 2048);
      else {
        constexpr int ai = ti == NT ? 2 : (NN && ti == 2) ? 1 : 0;
        constexpr unsigned co = ai ? 0 : 72 * (ti >> 1) + 40 * (ti & 1);
        fa[sp][ti][i & 1] = trh(ad[sl >> 1][ai] + so + co + sp * 32 * 224 + (i & 1) * 16 * 224);
      }
    }
  };
  sfor<9>([&](auto SL) {
    constexpr int sl = SL.value;
    constexpr int iss = sl == 0 ? 2 : (sl + 3 < 9 ? sl + 3 : 9);
    constexpr int need = sl == 0 ? 0 : (sl + 1 < 9 ? sl + 1 : 8);
    if constexpr (sl == 1) asm volatile("s_waitcnt vmcnt(%1)" : "+v"(ev) : "i"(4 * (iss - need - 1)) : "memory");
    else wait_vm<4 * (iss - need - 1) + (sl == 0)>();
    bar();
    if constexpr (sl == 0) STAMP(ws, 1, 2);
    if constexpr (sl == 4) STAMP(ws, 1, 3);
    if constexpr (sl <= 1) sfor<NR>([&](auto I) { rd1(ic<2 * sl>{}, I); });
    if constexpr (sl == 1) {
      const unsigned long long e = (((unsigned long long)rfl(ev[1]) << 32) | rfl(ev[0])) + 1ull;
      c.t0 = (unsigned)e; c.t1 = (unsigned)(e >> 32);
    }
    sfor<2>([&](auto SP) {
      constexpr int sp = SP.value, ks = 2 * sl + sp;
      bf16x8 tl;
      if constexpr (NN) tl = t16 >= 12 ? ones : cat(fa[sp][2][0], fa[sp][2][1]);
      sfor<9>([&](auto M) {
        constexpr int mi = M.value, ti = mi >> 1;
        if constexpr (mi == 8) cs = mfma16(cat(fa[sp][NT][0], fa[sp][NT][1]), ones, cs);
        else if constexpr (NN && ti == 3) acc[3][mi & 1] = mfma16(fb[sp][mi & 1], fb[sp][mi & 1], acc[3][mi & 1]);
        else if constexpr (NN && ti == 2) acc[2][mi & 1] = mfma16(tl, fb[sp][mi & 1], acc[2][mi & 1]);
        else acc[ti][mi & 1] = mfma16(cat(fa[sp][ti][0], fa[sp][ti][1]), fb[sp][mi & 1], acc[ti][mi & 1]);
        SBAR();
        if constexpr (ks != 1) {
          if constexpr (mi < 3) { rd1(ic<ks + 1>{}, ic<2 * mi>{}); rd1(ic<ks + 1>{}, ic<2 * mi + 1>{}); }
          else rd1(ic<ks + 1>{}, ic<3 + mi>{});
        }
        if constexpr (sl == 0) { if constexpr (mi & 1) issue1(2 + sp, mi >> 1); }
        else if constexpr (sl + 3 < 9 && (mi == 2 + 2 * NN || mi == 6 + 2 * NN)) issue1(sl + 3, 2 * sp + (mi >= 6));
        SBAR();
      });
    });
  });
  STAMP(ws, 1, 4);
  const unsigned tb = lds0 + K1_TBL, cb = tb + 1024, dummy = cb + 512;
  ldsw<f32x4>(t16 == 0 ? cb + (w * 16 + 4 * g) * 4 : dummy, cs);
  if constexpr (NN) sfor<2>([&](auto T) {
    const unsigned a = tb + (32 * m + 16 * T.value + t16) * 8;
    const f32x4 gr = acc[3][T.value];
    const float d01 = (p & 1) ? gr[1] : gr[0], d23 = (p & 1) ? gr[3] : gr[2];
    ldsw<float>(g == 3 ? a : dummy, acc[2][T.value][0]);
    ldsw<float>(g == q4 ? a + 4 : dummy, (p & 2) ? d23 : d01);
  });
  lds_bar();
  f32x2 st[2];
  sfor<2>([&](auto T) {
    const f32x2 v = ldsr<f32x2>(tb + (32 * m + 16 * T.value + t16) * 8);
    const float mu = v[0] * (1.f / D_), rs = __builtin_amdgcn_rsqf(fmaxf(v[1] * (1.f / D_) - mu * mu, 0.f) + 1e-5f);
    st[T.value] = (f32x2){rs, -rs * mu};
  });
  const rsrc_t ro = mk_rsrc(ws + WS_QF, 32 * 64 * (QREC + KVREC));
  constexpr unsigned KV0 = WS_KV - WS_QF;
  const int tile32 = 4 * mt + m, key = 16 * (g >> 1) + t16;
  const unsigned lo_qk = (g & 1) * 512 + key * 16, lo_v = 1280 + key * 32 + (g & 1) * 16;
  sfor<(NN ? 2 : 4)>([&](auto TI) {
    constexpr int ti = TI.value, tile = 4 * NN + ti;
    const int pg = 3 * nt + (tile >> 1), type = pg >> 4, rec = (2 * (pg & 15) + (tile & 1)) * 64 + tile32;
    const f32x4 cc = ldsr<f32x4>(cb + (tile * 16 + 4 * g) * 4);
    const float sc = type == 0 ? QSCALE : 1.f;
    const f32x4 v0 = acc[ti][0] * (st[0][0] * sc) + cc * (st[0][1] * sc), v1 = acc[ti][1] * (st[1][0] * sc) + cc * (st[1][1] * sc);
    auto s0 = __builtin_amdgcn_permlane32_swap(pk2(v0[0], v0[1]), pk2(v1[0], v1[1]), false, false);
    auto s1 = __builtin_amdgcn_permlane32_swap(pk2(v0[2], v0[3]), pk2(v1[2], v1[3]), false, false);
    bstore16<16>((u32x4){s0[0], s1[0], s0[1], s1[1]}, ro, type == 2 ? lo_v : lo_qk, type == 0 ? rec * QREC : KV0 + rec * KVREC);
  });
  if constexpr (NN) {
    const int pg = 3 * nt + g, type = pg >> 4, rec = 2 * (pg & 15) * 64 + tile32;
    const f32x4 cc = ldsr<f32x4>(cb + (96 + 4 * g) * 4);
    const float sc = type == 0 ? QSCALE : 1.f;
    const unsigned rb = type == 0 ? rec * QREC : KV0 + rec * KVREC, hs = type == 0 ? 64 * QREC : 64 * KVREC;
    const unsigned oq = g < 3 ? rb + (type == 2 ? 2304 : 1024) + t16 * 8 : OOB, one = type == 1 ? 0u : 0x00003f80u;
    sfor<2>([&](auto T) {
      const f32x4 v = acc[2][T.value] * (st[T.value][0] * sc) + cc * (st[T.value][1] * sc);
      const unsigned d0 = pk2(v[0], v[1]), d1 = pk2(v[2], v[3]);
      bstore8<16>((u32x2){d0, one}, ro, oq, T.value * 128); bstore8<16>((u32x2){d1, one}, ro, oq + hs, T.value * 128);
    });
  }
  STAMP(ws, 1, 5);
  wait_vm<0>();
  STAMP(ws, 1, 6);
  bar();
  if (w == 0) set_flag(c, FL1 + (mt * 16 + nt) * 64);
  STAMP(ws, 1, 7);
  STAMP_FLUSH(ws, 1);
}

constexpr int P3_WO = 73728;
constexpr int K4_RING = 8192;
constexpr unsigned k4_so(int u) { return ((((u >> 1) + 2) & 3) * 2 + (u & 1)) * 8192; }
DI unsigned k4_wv(int lane, int w, int Qd, int j) {
  const int rho = 8 * (w & 3) + (lane >> 3);
  return rho * 9216 + 1152 * Qd + 256 * j + 32 * (((lane & 7) >> 1) ^ ((rho >> 1) & 3)) + 16 * (lane & 1);
}

template <int U0> DI void k4_first(const Ctx& c, int lane) {
  const rsrc_t rg = mk_rsrc(c.wg, D_ * F_ * 2), ru = mk_rsrc(c.wu, D_ * F_ * 2);
  const unsigned wv4 = k4_wv(lane, c.w, c.xcd, c.r & 3), dst = c.lds0 + K4_RING + (c.w & 3) * 1024;
  sfor<4>([&](auto U) {
    glds16(dst + k4_so(U0 + U.value), wv4, rg, (U0 + U.value) * 32 * 9216);
    glds16(dst + 4096 + k4_so(U0 + U.value), wv4, ru, (U0 + U.value) * 32 * 9216);
  });
}
DI void p2_ahead(const Ctx& c) {
  const int w = c.w, r = c.r, lane = fresh_lane();
  const rsrc_t rwo = mk_rsrc(c.wo, D_ * D_ * 2);
  const int nt = r >= 9 ? r - 9 : r;
  const int rho = 8 * w + (lane >> 3), rg = (rho >> 2) & 3, rj = 4 * (rho >> 4) + (rho & 3);
  const unsigned cbyte = 128 * nt + 32 * (((lane & 7) >> 1) ^ ((rho >> 1) & 3)) + 16 * (lane & 1);
  const int th = 4 * rg + (rj >> 1);
  const unsigned wv = (20 * (rg >> 1) + 8 * (rg & 1) + rj) * 1152 + cbyte, wvt = (18 * th + ((th & 1) ? 0 : 16) + (rj & 1)) * 1152 + cbyte;
  sfor<16>([&](auto K) { glds16(c.lds0 + P3_WO + (w + 4 * K.value) * 1024, wv, rwo, K.value * 36 * 1152); });
  sfor<2>([&](auto K) { glds16(c.lds0 + P3_WO + (w + 64 + 4 * K.value) * 1024, wvt, rwo, K.value * 288 * 1152); });
}

DI void p2(const Ctx& c) {
  const int head = 4 * c.xcd + (c.r & 3), qb = c.r >> 2;
  char* ws = c.ws;
  const int w = c.w, lane = fresh_lane();
  const unsigned lds0 = c.lds0;
  const int q = lane & 31, h = lane >> 5, G = lane >> 4;
  const int qt = 8 * qb + w;
  const rsrc_t rq = mk_rsrc(ws + WS_QF, 32 * 64 * QREC), rkv = mk_rsrc(ws + WS_KV, 32 * 64 * KVREC);
  STAMP_DECL; STAMP(ws, 2, 0);
  unsigned kb = lds0 + w * KVREC + lane * 16, ktb = lds0 + w * KVREC + 1024 + q * 8;
  unsigned vb[4];
#pragma unroll
  for (int i = 0; i < 4; ++i) { const int row = 8 * i + 4 * (G >> 1) + ((lane >> 2) & 3); vb[i] = lds0 + w * KVREC + ((G & 1) ? 2304 + row * 8 : 1280 + row * 32 + 8 * (lane & 3)); PIN(vb[i]); }
  unsigned mf[16], mk[8];
#pragma unroll
  for (int e = 0; e < 16; ++e) { mf[e] = ((e & 3) + 8 * (e >> 2) <= q - 4 * h) ? ~0u : 0u; PIN(mf[e]); }
#pragma unroll
  for (int e = 0; e < 8; ++e) { mk[e] = (mf[2 * e] & 0xffffu) | (mf[2 * e + 1] & 0xffff0000u); PIN(mk[e]); }
  const unsigned tt = 2 * qt + (q >> 4);
  unsigned om_o = (tt * 18 + (head >> 1)) * 1024 + ((q & 15) + 16 * (2 * (head & 1) + h)) * 16;
  unsigned ot_o = h ? OOB : (tt * 18 + 16 + (head >> 4)) * 1024 + ((q & 15) + 16 * ((head >> 2) & 3)) * 16 + (head & 3) * 4;
  unsigned l16 = lane * 16, qto = h ? OOB : 1024 + q * 8, wmo;
  const int wmv = c.r * 8 + w;
  {
    const int l = (wmv >= 162 ? wmv - 162 : wmv >= 81 ? wmv - 81 : wmv) * 64 + lane, row = (l * 7282) >> 16;
    wmo = wmv >= 162 ? l * 128 : row * 9216 + c.xcd * 1152 + (l - 9 * row) * 128;
    PIN(wmo);
  }
  PIN(kb); PIN(ktb); PIN(om_o); PIN(ot_o); PIN(l16); PIN(qto);
  if (qb == 0) {
#pragma unroll
    for (int i = 0; i < 3; ++i) {
      const unsigned o = min(w * 64 + lane + 512 * i, 1279) * 16, m = (o % KVREC - 1024 < 256) ? 0x0000f149u : 0u;
      ldsw<u32x4>(lds0 + o, (u32x4){0u, m, 0u, m});
    }
  }
  if (w == 0) {
    const int ty = lane < 4 ? 1 : lane < 8 ? 2 : 0, pm = max(2 * qb + (ty ? (lane & 3) - 2 : (lane & 1)), 0);
    ldsw<unsigned>(lds0 + LDS_CNT + 4 * (lane & 7), 0u);
    poll(c, (pm * 16 + (((16 * ty + (head >> 1)) * 43) >> 7)) * 64, FL1);
  }
  lds_bar();
  STAMP(ws, 2, 7);
  bf16x8 qm; u32x2 qt2;
#pragma unroll
  for (int i = 0; i < 5; ++i) { const int pc = max(w + 8 * i, qb ? 0 : 20 + w); glds16c(lds0 + pc * 1024, l16, rkv, (head * 64 + 8 * qb - 8) * KVREC + pc * 1024); }
  bload16c(qm, l16, rq, (head * 64 + qt) * QREC);
  bload8c(qt2, qto, rq, (head * 64 + qt) * QREC);
  auto toff = [&](int i) { return (unsigned)(i * KVREC); };
  STAMP(ws, 2, 1);
  asm volatile("s_waitcnt vmcnt(0)" : "+v"(qm), "+v"(qt2)::"memory");
  bar();
  STAMP(ws, 2, 2);
  const bf16x4 qt4 = __builtin_bit_cast(bf16x4, qt2);
  bf16x8 km[9]; bf16x4 kt[9];
  constexpr int ord[9] = {0, 8, 1, 2, 3, 4, 5, 6, 7};
  sfor<9>([&](auto I) { constexpr int i = ord[I.value]; km[i] = ldsr<bf16x8>(kb + toff(i)); kt[i] = ldsr<bf16x4>(ktb + toff(i)); });
  SBAR();
  f32x16 s[9];
  float m4[4];
  auto qk = [&](auto I, auto J) {
    constexpr int i = I.value, j = J.value;
    f32x16 a = mfma32(km[i], qm, (f32x16){}), b;
    if constexpr (j < 9) b = mfma32(km[j], qm, (f32x16){});
    s[i] = __builtin_amdgcn_mfma_f32_32x32x8bf16_1k(kt[i], qt4, a, 0, 0, 0);
    if constexpr (j < 9) s[j] = __builtin_amdgcn_mfma_f32_32x32x8bf16_1k(kt[j], qt4, b, 0, 0, 0);
    SBAR();
  };
  auto mx1 = [&](auto I) {
    constexpr int i = I.value;
#pragma unroll
    for (int e = 0; e < 8; ++e) m4[e & 3] = fmaxf(fmaxf(m4[e & 3], s[i][2 * e]), s[i][2 * e + 1]);
  };
  bf16x4 vf[2][4];
  auto rdv = [&](int set, unsigned off) {
#pragma unroll
    for (int i = 0; i < 4; ++i) vf[set][i] = trh(vb[i] + off);
  };
  qk(ic<0>{}, ic<8>{});
  qk(ic<1>{}, ic<2>{});
  {
    size_t p0 = (size_t)c.wg, p1 = (size_t)c.wu, p2 = (size_t)c.wo;
    asm("" : "+s"(p0), "+s"(p1), "+s"(p2));
    p0 = wmv >= 81 ? p1 : p0;
    warm(lds0 + LDS_SCR, wmo, mk_rsrc((const void*)(wmv >= 162 ? p2 : p0), wmv >= 162 ? D_ * D_ * 2 : D_ * F_ * 2), 0);
  }
  SBAR();
#pragma unroll
  for (int e = 0; e < 16; ++e) s[8][e] = __uint_as_float((__float_as_uint(s[8][e]) & mf[e]) | (__float_as_uint(s[0][e]) & ~mf[e]));
#pragma unroll
  for (int e = 0; e < 4; ++e) asm("v_max_f32 %0, %1, %2\n\tv_max3_f32 %0, %0, %3, %4" : "=&v"(m4[e]) : "v"(s[8][4 * e]), "v"(s[8][4 * e + 1]), "v"(s[8][4 * e + 2]), "v"(s[8][4 * e + 3]));
  SBAR();
  qk(ic<3>{}, ic<4>{});
  mx1(ic<1>{}); mx1(ic<2>{}); SBAR();
  qk(ic<5>{}, ic<6>{});
  mx1(ic<3>{}); mx1(ic<4>{}); SBAR();
  qk(ic<7>{}, ic<9>{});
  rdv(1, toff(1));
  mx1(ic<5>{}); mx1(ic<6>{}); SBAR();
  mx1(ic<7>{});
  float mx = max_x32(fmaxf(fmaxf(m4[0], m4[1]), fmaxf(m4[2], m4[3])));
  STAMP(ws, 2, 3);
  f32x16 o = {};
  u32x4 pp;
  sfor<16>([&](auto H) {
    constexpr int hh = H.value, i = 1 + hh / 2, ks = hh & 1;
    f32x2 dd[4];
#pragma unroll
    for (int e = 0; e < 4; ++e) dd[e] = (f32x2){s[i][8 * ks + 2 * e], s[i][8 * ks + 2 * e + 1]} - (f32x2){mx, mx};
    SBAR();
    if constexpr (hh > 0 && hh < 15) o = mfma32(cat(vf[(1 + (hh - 1) / 2) & 1][2 * (ks ^ 1)], vf[(1 + (hh - 1) / 2) & 1][2 * (ks ^ 1) + 1]), __builtin_bit_cast(bf16x8, pp), o);
    if constexpr (hh == 15) {
      const u32x4 pd = {pp[0] & mk[0], pp[1] & mk[1], pp[2] & mk[2], pp[3] & mk[3]};
      o = mfma32(cat(vf[0][0], vf[0][1]), __builtin_bit_cast(bf16x8, pd), o);
      o = mfma32(cat(vf[1][0], vf[1][1]), __builtin_bit_cast(bf16x8, pp ^ pd), o);
    }
    SBAR();
    if constexpr (ks == 0) rdv((i + 1) & 1, toff(i < 8 ? i + 1 : 0));
    SBAR();
    float pv[8];
#pragma unroll
    for (int e = 0; e < 4; ++e) { pv[2 * e] = __builtin_amdgcn_exp2f(dd[e][0]); pv[2 * e + 1] = __builtin_amdgcn_exp2f(dd[e][1]); }
    SBAR();
    pp = (u32x4){pk2(pv[0], pv[1]), pk2(pv[2], pv[3]), pk2(pv[4], pv[5]), pk2(pv[6], pv[7])};
    SBAR();
  });
  {
    const u32x4 pd = {pp[0] & mk[4], pp[1] & mk[5], pp[2] & mk[6], pp[3] & mk[7]};
    o = mfma32(cat(vf[0][2], vf[0][3]), __builtin_bit_cast(bf16x8, pd), o);
    o = mfma32(cat(vf[1][2], vf[1][3]), __builtin_bit_cast(bf16x8, pp ^ pd), o);
  }
  STAMP(ws, 2, 4);
  auto lr = __builtin_amdgcn_permlane32_swap(__float_as_uint(o[10]), __float_as_uint(o[10]), false, false);
  const float inv = __builtin_amdgcn_rcpf(__uint_as_float(lr[0]));
  const rsrc_t ro = mk_rsrc(ws + WS_OF, 128 * 18 * 1024);
  const u32x4 om = {pk2(o[0] * inv, o[1] * inv), pk2(o[2] * inv, o[3] * inv), pk2(o[4] * inv, o[5] * inv), pk2(o[6] * inv, o[7] * inv)};
  bstore16<16>(om, ro, om_o, 0);
  bstore4<16>(pk2(o[8] * inv, o[9] * inv), ro, ot_o, 0);
  STAMP(ws, 2, 5);
  cnt_add(c, 1);
  if (w < 4 && c.r < 18) { p2_ahead(c); wait_vm<18>(); } else wait_vm<0>();
  set_flag(c, FL2 + (qt * 32 + head) * 64);
  STAMP(ws, 2, 6);
  STAMP_FLUSH(ws, 2);
}

DI void p3_side(const Ctx& c) {
  char* ws = c.ws;
  const int w = c.w, xcd = c.xcd, lane = fresh_lane();
  const unsigned lds0 = c.lds0;
  const int sb = c.r - 18, blk = sb * 8 + xcd;
  const rsrc_t rwt = mk_rsrc(ws + WS_WT, 4 * 4608 * 32), rlg = mk_rsrc(ws + WS_LG, 8 * 4 * 36864), rcs = mk_rsrc(ws + WS_CS, 2 * 4608 * 4);
  STAMP_DECL; STAMP(ws, 3, 0);
  const int mat = sb >= 5, un = sb < 5 ? 2 * sb : sb - 5;
  const rsrc_t rw = mk_rsrc(mat ? c.wu : c.wg, D_ * F_ * 2);
  const unsigned vo = (lane & 7) * 16 + (72 * w + (lane >> 3)) * 9216;
  u32x4 v[2][9];
#pragma unroll
  for (int i = 0; i < 9; ++i) v[0][i] = __builtin_amdgcn_raw_buffer_load_b128(rw, vo, (9 * xcd + un) * 128 + i * 8 * 9216, 0);
#pragma unroll
  for (int i = 0; i < 9; ++i) v[1][i] = __builtin_amdgcn_raw_buffer_load_b128(rw, sb < 4 ? vo : OOB, (9 * xcd + un + 1) * 128 + i * 8 * 9216, 0);
  const int drow = 64 * (blk - 40) + 8 * w + (lane >> 3);
  const u32x4 dv = __builtin_amdgcn_raw_buffer_load_b128(mk_rsrc(c.wd, F_ * D_ * 2), mat ? drow * 1152 + 1024 + (lane & 7) * 16 : OOB, 0, 0);
  if (un == 8) {
    const unsigned lo = ((lane & 7) >> 1) * 36864 + (72 * w + (lane >> 3)) * 32 + (lane & 1) * 16;
#pragma unroll
    for (int i = 0; i < 9; ++i) bstore16<16>(v[0][i], rlg, lo, (xcd * 8 + (sb >> 3)) * 18432 + i * 256);
  }
  const int pr = (drow % 576) >= 512 ? ((drow & ~24) | ((drow & 8) << 1) | ((drow & 16) >> 1)) : drow;
  bstore16<16>(dv, rwt, mat ? (((lane & 7) >> 1) * 4608 + pr) * 32 + (lane & 1) * 16 : OOB, 0);
  float a[2][8] = {};
#pragma unroll
  for (int k = 0; k < 2; ++k) {
#pragma unroll
    for (int i = 0; i < 9; ++i)
#pragma unroll
      for (int e = 0; e < 4; ++e) { a[k][2 * e] += bflo(v[k][i][e]); a[k][2 * e + 1] += bfhi(v[k][i][e]); }
#pragma unroll
    for (int e = 0; e < 8; ++e) a[k][e] = sum_x32(sum_x16(a[k][e]));
  }
  if (lane < 16) {
#pragma unroll
    for (int k = 0; k < 2; ++k) {
      ldsw<f32x4>(lds0 + k * 4096 + w * 512 + lane * 32, (f32x4){a[k][0], a[k][1], a[k][2], a[k][3]});
      ldsw<f32x4>(lds0 + k * 4096 + w * 512 + lane * 32 + 16, (f32x4){a[k][4], a[k][5], a[k][6], a[k][7]});
    }
  }
  lds_bar();
  if (w < 2) {
    float t = 0.f;
#pragma unroll
    for (int i = 0; i < 16; ++i) t += ldsr<float>(lds0 + w * 4096 + i * 256 + lane * 4);
    bstore4<16>(__float_as_uint(t), rcs, w == 0 || sb < 4 ? lane * 4 : OOB, ((sb >= 5 ? 4608 : 0) + (9 * xcd + un + w) * 64) * 4);
  }
  STAMP(ws, 3, 5);
  wait_vm<0>();
  bar();
  if (w == 0) set_flag(c, FLS + blk * 64);
  STAMP(ws, 3, 6);
  if (w == 5 && blk == 48) {
    for (int i = 0; i < 4; ++i) poll(c, lane * 64, FL1 + i * 4096);
    set_flag(c, 0);
  }
  STAMP_FLUSH(ws, 6);
}

DI void p3_gemm(const Ctx& c) {
  char* ws = c.ws;
  const int w = c.w, lane = fresh_lane();
  const unsigned lds0 = c.lds0;
  const int t16 = lane & 15, g = lane >> 4;
  const int nt = c.r >= 9 ? c.r - 9 : c.r;
  const int T = 16 * c.xcd + 8 * (w >> 2) + 4 * (c.r >= 9) + (w & 3);
  const rsrc_t rof = mk_rsrc(ws + WS_OF, 128 * 18 * 1024);
  STAMP_DECL; STAMP(ws, 3, 0);
  unsigned wa[4];
  {
    const int r2 = 4 * g + ((lane >> 2) & 3);
#pragma unroll
    for (int i = 0; i < 4; ++i) wa[i] = lds0 + P3_WO + r2 * 128 + 32 * (i ^ ((r2 >> 1) & 3)) + 8 * (lane & 3);
  }
  u32x2 xr[4];
  sfor<4>([&](auto I) { bload8(xr[I.value], (16 * T + t16) * 1152 + 128 * nt + 8 * g, mk_rsrc(c.x, S_ * D_ * 2), 32 * I.value); });
  if (w < 4) { wait_vm<5>(); cnt_add(c, 0); }
  cnt_wait(c, 0, 4);
  STAMP(ws, 3, 1);
  bf16x8 of[18];
  f32x4 acc[4] = {};
  bf16x4 fr[2][4][2];
  auto rd1 = [&](auto KS, auto I) { constexpr int ks = KS.value, i = I.value; fr[ks & 1][i >> 1][i & 1] = trh(wa[i >> 1] + (ks & 15) * 4096 + (i & 1) * 2048); };
  sfor<8>([&](auto I) { rd1(ic<0>{}, I); });
  poll(c, (lane & 31) * 64, FL2 + (T >> 1) * 2048);
  asm volatile("" : "+v"(xr[0]), "+v"(xr[1]), "+v"(xr[2]), "+v"(xr[3]));
  STAMP(ws, 3, 2);
#define P3_PRO 18
  constexpr int PRO = P3_PRO;
  auto ld = [&](auto K) { if constexpr (K.value < 18) bload16c(of[K.value], lane * 16, rof, (T * 18 + K.value) * 1024); };
  sfor<PRO>(ld);
  sfor<18>([&](auto KS) {
    constexpr int ks = KS.value, iss = PRO + ks < 18 ? PRO + ks : 18;
    asm volatile("s_waitcnt vmcnt(%1)" : "+v"(of[ks]) : "i"(iss - ks - 1) : "memory");
    if constexpr (ks == 0) STAMP(ws, 3, 3);
    if constexpr (ks == 9) STAMP(ws, 3, 4);
    if constexpr (ks == 15) sfor<4>([&](auto I) { wa[I.value] += 65536; PIN(wa[I.value]); });
    sfor<4>([&](auto C) {
      acc[C.value] = mfma16(cat(fr[ks & 1][C.value][0], fr[ks & 1][C.value][1]), of[ks], acc[C.value]);
      SBAR();
      if constexpr (ks + 1 < 18) { rd1(ic<ks + 1>{}, ic<2 * C.value>{}); rd1(ic<ks + 1>{}, ic<2 * C.value + 1>{}); }
      if constexpr (C.value == 1) ld(ic<PRO + ks>{});
      SBAR();
    });
  });
  unsigned d[4][2];
  float s1 = 0.f, s2 = 0.f;
#pragma unroll
  for (int i = 0; i < 4; ++i) {
    d[i][0] = pk2(acc[i][0] + bflo(xr[i][0]), acc[i][1] + bfhi(xr[i][0]));
    d[i][1] = pk2(acc[i][2] + bflo(xr[i][1]), acc[i][3] + bfhi(xr[i][1]));
  }
  const rsrc_t rhf = mk_rsrc(ws + WS_HF, 128 * 18 * 1024);
  bstore16<16>((u32x4){d[0][0], d[0][1], d[1][0], d[1][1]}, rhf, lane * 16, (T * 18 + 2 * nt) * 1024);
  bstore16<16>((u32x4){d[2][0], d[2][1], d[3][0], d[3][1]}, rhf, lane * 16, (T * 18 + 2 * nt + 1) * 1024);
#pragma unroll
  for (int i = 0; i < 4; ++i)
#pragma unroll
    for (int e = 0; e < 2; ++e) { const float a = bflo(d[i][e]), b = bfhi(d[i][e]); s1 += a + b; s2 += a * a + b * b; }
  s1 = sum_x32(sum_x16(s1)); s2 = sum_x32(sum_x16(s2));
  bstore8<16>((u32x2){__float_as_uint(s1), __float_as_uint(s2)}, mk_rsrc(ws + WS_ST, 9 * 2048 * 8), g == 0 ? t16 * 8 : OOB, (T * 9 + nt) * 128);
  STAMP(ws, 3, 5);
  wait_vm<0>();
  set_flag(c, FL3 + (T * 16 + nt) * 64);
  STAMP(ws, 3, 6);
  STAMP_FLUSH(ws, 3);
}

constexpr int K4_LG = P3_WO;
constexpr int K4_CS = K4_LG + 36864;
constexpr int K4_STP = K4_CS + 4096;
struct K4Op { int kind, idx; };
struct K4Ops { int n; K4Op op[12]; };
constexpr K4Ops k4_ops(int S) {
  K4Ops o = {};
  auto add = [&](int k, int i) { o.op[o.n++] = {k, i}; };
  if (S == 3) add(9, 0);
  if (S == 0) for (int u = 4; u < 8; ++u) { add(1, u); add(2, u); }
  else for (int u = 2 * S + 6; u < 2 * S + 8 && u < 36; ++u) {
    if (S == 2) { add(0, 2 * u - 12); add(0, 2 * u - 11); }
    if (S > 2) add(0, u);
    if (u < 18) { add(1, u); add(2, u); }
  }
  if (S == 4) { add(5, 0); add(5, 1); add(5, 2); }
  if (S == 6) add(6, 0);
  if (S >= 10 && S < 15) add(3, S - 10);
  if (S == 12) add(7, 0);
  if (S == 14) add(8, 0);
  if (S == 16) add(4, 0);
  return o;
}
constexpr int k4_nst(int S) { return (S == 8 || S == 9) ? 2 : S == 7 ? 1 : 0; }
constexpr int k4_issued(int S) { int n = 8; for (int s = 0; s < S; ++s) n += k4_ops(s).n + k4_nst(s); return n; }
constexpr int k4_pos(int kind, int idx) {
  if (kind == 0 && idx < 8) return -1;
  if ((kind == 1 || kind == 2) && idx < 4) return 2 * idx + kind - 1;
  for (int S = 0;; ++S) for (int i = 0; i < k4_ops(S).n; ++i) if (k4_ops(S).op[i].kind == kind && k4_ops(S).op[i].idx == idx) return k4_issued(S) + k4_nst(S) + i;
}
constexpr int k4_need(int S) {
  if (S == 8) return k4_issued(7) - 1;
  if (S >= 16) return k4_issued(15) - 1;
  const int a = S < 9 ? k4_pos(2, 2 * S + 1) : -1, b = k4_pos(0, 2 * S + 2);
  return a > b ? a : b;
}
static_assert(k4_need(5) >= k4_pos(9, 0) && k4_need(7) >= k4_pos(5, 2) && k4_need(12) >= k4_issued(10) - 1, "");
constexpr int K5_SLOT = 17408, K5_BASE = 0, K5_P = 4;
constexpr unsigned k5_so(int v) { return K5_BASE + ((v + 7) & 7) * K5_SLOT; }
DI int k5_u(int v, int nt) {
  if (v < 32) { const int q = ((v >> 2) + nt) & 3; return 18 * (q >> 1) + 8 * (q & 1) + 4 * ((v >> 1) & 1) + (v & 1) + 2 * (v >> 4); }
  const int q = (v + nt) & 3;
  return 18 * (q >> 1) + 16 + (q & 1);
}
DI unsigned k5_mv(int rho, int sp, int nt, int t16) {
  const int rr = sp ? ((rho & 7) | ((rho & 8) << 1) | ((rho & 16) >> 1)) : rho;
  return rr * 1152 + 256 * nt + 32 * ((t16 >> 1) ^ (rho & 7)) + 16 * (t16 & 1);
}
DI unsigned k5_fl(int i, int sg, int mt5) { return (((((mt5 >> 1) * 4 + ((i >> 2) & 3)) * 8 + 2 * sg + ((i >> 4) & 1)) * 8) + 4 * (mt5 & 1) + (i & 3)) * 64; }

DI void p4(const Ctx& c) {
  char* ws = c.ws;
  const int w = c.w, r = c.r, xcd = c.xcd, lane = fresh_lane();
  const int mt = r >> 2, Qd = xcd, j = r & 3;
  const unsigned lds0 = c.lds0;
  const int g = lane >> 4;
  const int T0 = 16 * mt + 2 * w;
  const rsrc_t rwm = mk_rsrc(w < 4 ? c.wg : c.wu, D_ * F_ * 2), rhf = mk_rsrc(ws + WS_HF, 128 * 18 * 1024), rwd = mk_rsrc(c.wd, F_ * D_ * 2);
  const rsrc_t rhh = mk_rsrc(ws + WS_HH, 128 * 144 * 1024);
  const unsigned f4 = FL4 + ((r * 8 + xcd) * 8 + w) * 64;
  const int sg = xcd >> 1, mt5 = 8 * (xcd & 1) + (r >> 2);
  STAMP_DECL; STAMP(ws, 4, 0);
  const unsigned wv = k4_wv(lane, w, Qd, j);
  const unsigned wdst = lds0 + K4_RING + w * 1024, l16 = lane * 16;
  poll(c, min(lane & 15, 8) * 64 + (lane & 16) * 64, FL3 + T0 * 1024);
  bf16x8 act[18][2];
  u32x2 pf;
  auto pf_off = [&] { const int z = opq(l16) >> 4; const unsigned a = opq(FL4 + k5_fl(z, sg, mt5)), b = opq(FLS + (40 + 18 * sg + min(z - 32, 17)) * 64); return z < 32 ? a : b; };
  auto doop = [&](auto KIND, auto IDX) {
    constexpr int kind = KIND.value, u = IDX.value;
    if constexpr (kind == 0) glds16(wdst + k4_so(u), wv, rwm, (u % 18) * 32 * 9216 + (u / 18) * 128);
    else if constexpr (kind <= 2) bload16c(act[u][kind - 1], l16, rhf, (T0 * 18 + 18 * (kind - 1) + u) * 1024);
    else if constexpr (kind == 3) {
      const int pc = min(w + 8 * u, 35);
      glds16c(lds0 + K4_LG + pc * 1024, l16, mk_rsrc(ws + WS_LG, 8 * 4 * 36864), (Qd * 4 + j) * 36864 + pc * 1024);
    }
    else if constexpr (kind == 4) warm(lds0 + LDS_SCR, l16 * 8, rwd, sg * 1327104 + min(r * 8 + w, 161) * 8192);
    else if constexpr (kind == 5) glds16c(lds0 + K4_STP + w * 3072 + u * 1024, l16, mk_rsrc(ws + WS_ST, 9 * 2048 * 8), T0 * 1152 + u * 1024);
    else if constexpr (kind == 6) {
      const unsigned z = opq(l16), f = (576 * Qd + (z < 512 ? 128 * j : 384 + 16 * j)) * 4 + z;
      glds16c(lds0 + K4_CS + (w & 1) * 1024, z < 576 ? f : OOB, mk_rsrc(ws + WS_CS, 2 * 4608 * 4), (w & 1) * 4608 * 4);
    } else if constexpr (kind == 7) set_flag(c, f4);
    else if constexpr (kind == 8) bload8c(pf, pf_off(), fl_rsrc(c), 0);
    else bload8c(pf, min(opq(l16), 13u * 16) * 32, fl_rsrc(c), FLS + xcd * 64);
  };
  sfor<4>([&](auto U) { doop(ic<1>{}, U); doop(ic<2>{}, U); });
  if (w < 4) cnt_add(c, 4);
  cnt_wait(c, 4, 4);
  unsigned wa[4];
  {
    const int r2 = 4 * g + ((lane >> 2) & 3);
#pragma unroll
    for (int i = 0; i < 4; ++i) wa[i] = lds0 + K4_RING + r2 * 128 + 32 * (i ^ ((r2 >> 1) & 3)) + 8 * (lane & 3);
  }
  f32x4 ag[4][2], au[4][2], cg, cu;
  bf16x4 fr[4][2];
  unsigned hd[2][2], lga, csa;
  f32x2 nmu = {0.f, 0.f};
  auto rd1 = [&](auto H, auto I) {
    constexpr int n = H.value >> 1, m = H.value & 1, i = I.value;
    if constexpr (n < 144) fr[H.value & 3][i] = trh(wa[n % 4] + k4_so(n / 4) + 4096 * m + 2048 * i);
    else if constexpr (n < 162) fr[H.value & 3][i] = trh(lga + (n - 144) * 1024 + m * 18432 + i * 512);
  };
  auto rd = [&](auto H) { rd1(H, ic<0>{}); rd1(H, ic<1>{}); };
  auto mm = [&](auto N, auto KS, f32x4& g0, f32x4& g1, f32x4& u0, f32x4& u1, auto&& hook) {
    constexpr int n = N.value, ks = KS.value;
    constexpr int h0 = n == 15 ? 999 : 2 * n + 3, h1 = (n == 14 || n == 15) ? 999 : 2 * n + 4;
    const bf16x8 fg = cat(fr[(2 * n) & 3][0], fr[(2 * n) & 3][1]);
    if constexpr (ks == 0) mfma16_z(g0, fg, act[ks][0]); else mfma16_ip(g0, fg, act[ks][0]);
    SBAR(); rd1(ic<h0>{}, ic<0>{}); SBAR(); hook(ic<0>{}); SBAR();
    if constexpr (ks == 0) mfma16_z(g1, fg, act[ks][1]); else mfma16_ip(g1, fg, act[ks][1]);
    SBAR(); rd1(ic<h0>{}, ic<1>{}); SBAR(); hook(ic<1>{}); SBAR();
    const bf16x8 fu = cat(fr[(2 * n + 1) & 3][0], fr[(2 * n + 1) & 3][1]);
    if constexpr (ks == 0) mfma16_z(u1, fu, act[ks][1]); else mfma16_ip(u1, fu, act[ks][1]);
    SBAR(); rd1(ic<h1>{}, ic<0>{}); SBAR(); hook(ic<2>{}); SBAR();
    if constexpr (ks == 0) mfma16_z(u0, fu, act[ks][0]); else mfma16_ip(u0, fu, act[ks][0]);
    SBAR(); rd1(ic<h1>{}, ic<1>{}); SBAR(); hook(ic<3>{}); SBAR();
  };
  auto ldcs = [&](int e) { cg = ldsr<f32x4>(csa + 64 * e); cu = ldsr<f32x4>(csa + 1024 + 64 * e); };
  auto epi = [&](auto E, auto I) {
    constexpr int grp = E.value >> 2, cc = E.value & 3, i = I.value, T = 1 - (i >> 1);
    if constexpr ((i & 1) == 0) {
      ag[cc][T] += cg * nmu[T]; au[cc][T] += cu * nmu[T]; PIN(ag[cc][T]); PIN(au[cc][T]);
      if constexpr (i == 2 && E.value != 3) { SBAR(); ldcs(E.value + 1); }
    }
    else {
      const f32x4 v = ag[cc][T] * au[cc][T];
      const unsigned d0 = pk2(v[0], v[1]), d1 = pk2(v[2], v[3]);
      if constexpr ((cc & 1) == 0) { hd[T][0] = d0; hd[T][1] = d1; PIN(hd[T][0]); PIN(hd[T][1]); }
      else bstore16<16>((u32x4){hd[T][0], hd[T][1], d0, d1}, rhh, l16, ((T0 + T) * 144 + 18 * Qd + 4 * j + 2 * grp + (cc >> 1)) * 1024);
    }
  };
  f32x2 sp[3];
  float rs0;
  auto stats = [&](auto TT, auto P) {
    constexpr int T = TT.value, p = P.value;
    if constexpr (p == 0) {
      const unsigned sa = lds0 + K4_STP + w * 3072 + T * 1152 + (opq(l16) >> 1);
      sp[0] = ldsr<f32x2>(sa); sp[1] = ldsr<f32x2>(sa + 512); sp[2] = ldsr<f32x2>(sa + 1024);
    } else if constexpr (p == 4) { sp[0] = sp[0] + sp[1] + (opq(l16) < 256 ? sp[2] : (f32x2){0.f, 0.f}); PIN(sp[0]); }
    else if constexpr (p == 5) { sp[0][0] = sum_x16(sp[0][0]); PIN(sp[0]); }
    else if constexpr (p == 6) { sp[0][1] = sum_x16(sp[0][1]); PIN(sp[0]); }
    else if constexpr (p == 7) { sp[0][0] = sum_x32(sp[0][0]); PIN(sp[0]); }
    else if constexpr (p == 8) { sp[0][1] = sum_x32(sp[0][1]); PIN(sp[0]); }
    else if constexpr (p == 9) {
      nmu[T] = sp[0][0] * (-1.f / D_);
      const float rs = __builtin_amdgcn_rcpf(fmaxf(sp[0][1] * (1.f / D_) - nmu[T] * nmu[T], 0.f) + 1e-5f);
      PIN(nmu);
      if constexpr (T == 0) { rs0 = rs; PIN(rs0); }
      else {
        const unsigned z = opq(l16);
        bstore4<16>(__float_as_uint((z & 256) ? rs : rs0), mk_rsrc(ws + WS_RS + (sg * 2048 + 16 * T0) * 4, (Qd & 1) == 0 && j == 0 ? 128 : 0), z >> 2, 0);
      }
    }
  };
  unsigned mv5;
  auto pf5 = [&](auto N) {
    constexpr int n = N.value, v = (n - 1) >> 1;
    if constexpr (n == 0) {
      const int vv = min(w, K5_P - 1), uu = k5_u(vv, j);
      glds16c(lds0 + K5_BASE + ((vv + 7) & 7) * K5_SLOT + 16384, l16, mk_rsrc(ws + WS_WT, 4 * 4608 * 32), (j * 144 + 36 * sg + uu) * 1024);
      const unsigned z = opq(l16);
      mv5 = k5_mv(4 * w + (z >> 8), 0, j, (z >> 4) & 15);
    } else if constexpr (n & 1) glds16(lds0 + k5_so(v) + 8192 + w * 1024, mv5, rwd, sg * 1152 * 1152 + k5_u(v, j) * 32 * 1152);
    else glds16c(lds0 + k5_so(v) + w * 1024, l16, rhh, ((8 * mt5 + w) * 144 + 36 * sg + k5_u(v, j)) * 1024);
  };
  sfor<18>([&](auto SS) {
    constexpr int S = SS.value;
    constexpr int nw = k4_issued(S) - k4_need(S) - 1;
    static_assert(nw >= 0 && nw < 64, "");
    if constexpr (S == 5) asm volatile("s_waitcnt vmcnt(%5)" : "+v"(act[2 * S][0]), "+v"(act[2 * S][1]), "+v"(act[2 * S + 1][0]), "+v"(act[2 * S + 1][1]), "+v"(pf) : "i"(nw) : "memory");
    else if constexpr (S < 9) asm volatile("s_waitcnt vmcnt(%4)" : "+v"(act[2 * S][0]), "+v"(act[2 * S][1]), "+v"(act[2 * S + 1][0]), "+v"(act[2 * S + 1][1]) : "i"(nw) : "memory");
    else if constexpr (S == 16) asm volatile("s_waitcnt vmcnt(%1)" : "+v"(pf) : "i"(nw) : "memory");
    else wait_vm<nw>();
    if constexpr (S == 16) { if (__builtin_expect(!tag_ok(c, pf), 0)) poll(c, pf_off(), 0); }
    if constexpr (S == 5) { if (__builtin_expect(!tag_ok(c, pf), 0)) poll(c, min(opq(l16), 13u * 16) * 32, FLS + xcd * 64); }
    if constexpr (S >= 2) bar();
    if constexpr (S == 0) STAMP(ws, 4, 1);
    if constexpr (S == 2) STAMP(ws, 4, 7);
    if constexpr (S == 4) STAMP(ws, 4, 2);
    if constexpr (S == 9) STAMP(ws, 4, 3);
    if constexpr (S == 17) STAMP(ws, 4, 4);
    if constexpr (S == 0 || S == 2) { rd(ic<16 * S>{}); rd(ic<16 * S + 1>{}); rd(ic<16 * S + 2>{}); }
    sfor<8>([&](auto L) {
      constexpr int l = L.value, n = 8 * S + l, t = n / 4, cc = n % 4, ks = t % 18, no = k4_ops(S).n;
      mm(ic<n>{}, ic<ks>{}, ag[cc][0], ag[cc][1], au[cc][0], au[cc][1], [&](auto I) {
        constexpr int i = I.value;
        sfor<no>([&](auto O) {
          constexpr int nd = no ? no : 1, at = O.value * 32 / nd + 16 / nd;
          if constexpr (at == 4 * l + i) doop(ic<k4_ops(S).op[O.value].kind>{}, ic<k4_ops(S).op[O.value].idx>{});
        });
        if constexpr (S == 7) stats(ic<(l >> 2)>{}, ic<4 * (l & 3) + i>{});
        if constexpr ((S == 8 || S == 17) && l == 3 && i == 0) { csa = lds0 + K4_CS + ((opq(l16) >> 4) & 48); PIN(csa); }
        if constexpr ((S == 8 || S == 17) && l == 4 && i == 0) ldcs(S == 8 ? 0 : 4);
        if constexpr (S == 17 && l == 0 && i == 0) { const unsigned z = opq(l16); lga = lds0 + K4_LG + (z >> 6) * 32 + ((z >> 1) & 24); PIN(lga); }
        if constexpr (S == 17 && l < 5 && i == 1) pf5(L);
        if constexpr (ks == 17 && cc >= 1) epi(ic<4 * (t / 18) + cc - 1>{}, I);
        if constexpr (t == 18 && cc == 0) epi(ic<3>{}, I);
      });
    });
  });
  STAMP(ws, 4, 5);
  {
    f32x4 g0, g1, u0, u1;
    sfor<18>([&](auto K) {
      constexpr int ks = K.value, n = 144 + ks;
      mm(ic<n>{}, ic<ks>{}, g0, g1, u0, u1, [&](auto I) {
        if constexpr (ks == 0) epi(ic<7>{}, I);
        if constexpr (ks >= 1 && ks <= 4 && I.value == 1) pf5(ic<4 + ks>{});
        if constexpr (ks == 10 && I.value == 1) { wait_vm<4>(); set_flag(c, f4 + 2048 * 64); }
      });
    });
    asm volatile("s_nop 9" : "+v"(g0), "+v"(g1), "+v"(u0), "+v"(u1));
    const f32x4 v0 = (g0 + cg * nmu[0]) * (u0 + cu * nmu[0]), v1 = (g1 + cg * nmu[1]) * (u1 + cu * nmu[1]);
    const unsigned a0 = pk2(v0[0], v0[1]), a1 = pk2(v0[2], v0[3]), b0 = pk2(v1[0], v1[1]), b1 = pk2(v1[2], v1[3]);
    auto s0 = __builtin_amdgcn_permlane32_swap(a0, b0, false, false);
    auto s1 = __builtin_amdgcn_permlane32_swap(a1, b1, false, false);
    const unsigned z = opq(l16);
    bstore16<16>((u32x4){s0[0], s1[0], s0[1], s1[1]}, rhh, (z >> 9) * 144 * 1024 + (z & 511) + 512 * (j & 1), (T0 * 144 + 18 * Qd + 16 + (j >> 1)) * 1024);
    STAMP(ws, 4, 6);
    STAMP_FLUSH(ws, 4);
  }
}

constexpr int k5_E(int i) { int e = 36; if (K5_P + 2 * (i + 1) < e) e = K5_P + 2 * (i + 1); if (i >= 0 && i + 8 < e) e = i + 8; return e; }
static_assert(k5_E(6) <= 16 && k5_E(20) <= 32, "");

DI void p5(const Ctx& c) {
  char* ws = c.ws;
  const int w = c.w, r = c.r, xcd = c.xcd, lane = fresh_lane();
  const int sg = xcd >> 1, mt = 8 * (xcd & 1) + (r >> 2), nt = r & 3;
  const unsigned lds0 = c.lds0;
  const int t16 = lane & 15, g = lane >> 4;
  const int tile = 4 * mt + nt;
  const rsrc_t rpt = mk_rsrc(ws + WS_PT, 64 * 8 * 4 * PT_REC);
  const unsigned f4 = FL4 + (4096 + (r * 8 + xcd) * 8 + w) * 64;
  STAMP_DECL; STAMP(ws, 5, 0);
  if (w >= 4) {
    const int l = w - 4;
    const rsrc_t rwd = mk_rsrc(c.wd, F_ * D_ * 2), rhh = mk_rsrc(ws + WS_HH, 128 * 144 * 1024);
    const rsrc_t rwt = mk_rsrc(ws + WS_WT, 4 * 4608 * 32);
    unsigned mv[2][2];
#pragma unroll
    for (int sp = 0; sp < 2; ++sp)
#pragma unroll
      for (int i = 0; i < 2; ++i) mv[sp][i] = k5_mv(4 * (2 * l + i) + g, sp, nt, t16);
    const unsigned hs = ((8 * mt + 2 * l) * 144 + 36 * sg) * 1024, wsb = sg * 1152 * 1152, dl = lds0 + l * 2048;
    auto issue = [&](auto V) {
      constexpr int v = V.value, sp = v >= 32;
      const int u = k5_u(v, nt);
      constexpr unsigned so = k5_so(v);
      glds16(dl + so + 8192, mv[sp][0], rwd, wsb + u * 32 * 1152);
      glds16c(dl + so, lane * 16, rhh, hs + u * 1024);
      glds16(dl + so + 8192 + 1024, mv[sp][1], rwd, wsb + u * 32 * 1152);
      glds16c(dl + so + 1024, lane * 16, rhh, hs + (144 + u) * 1024);
      if ((v & 3) == l) glds16c(lds0 + so + 16384, lane * 16, rwt, (nt * 144 + 36 * sg + u) * 1024);
    };
    sfor<36>([&](auto I) {
      constexpr int i = I.value, y = k5_E(i - 1) - (i + 2 < 36 ? i + 2 : 36), yy = k5_E(i - 1) - K5_P;
      if constexpr (i == 0) wait_vm<2>();
      else if constexpr (i == 2) { wait_vm<4 * yy + yy / 4>(); set_flag(c, f4); }
      else if constexpr (i + 2 > K5_P) wait_vm<4 * y + y / 4>();
      bar();
      if constexpr (i == 0) STAMP(ws, 5, 1);
      if constexpr (i == 18) STAMP(ws, 5, 2);
      sfor<k5_E(i) - k5_E(i - 1)>([&](auto U) { issue(ic<k5_E(i - 1) + U.value>{}); });
    });
    bar();
    STAMP(ws, 5, 3);
    bar();
  } else {
    const int r2 = 4 * g + ((lane >> 2) & 3);
    unsigned wa[2][10];
#pragma unroll
    for (int i = 0; i < 8; ++i) wa[0][i] = lds0 + K5_BASE + 8192 + r2 * 256 + 32 * (i ^ (r2 & 7)) + 8 * (lane & 3);
    wa[0][8] = lds0 + K5_BASE + 16384 + r2 * 32 + 8 * (lane & 3);
    wa[0][9] = lds0 + K5_BASE + w * 2048 + lane * 16;
#pragma unroll
    for (int i = 0; i < 10; ++i) { wa[1][i] = wa[0][i] + 4 * K5_SLOT; PIN(wa[0][i]); PIN(wa[1][i]); }
    const unsigned fo = k5_fl(lane & 31, sg, mt);
    f32x4 acc[9][2];
    bf16x4 fr[3][3][2];
    bf16x8 hb[2][2];
    u32x2 pf;
    auto rd1 = [&](auto N, auto I) {
      constexpr int n = N.value, u = n / 3, i = I.value;
      if constexpr (u < 36) {
        constexpr int pos = (u + 7) & 7, hi = pos >> 2, cc = 3 * (n % 3) + i / 2;
        constexpr unsigned so = (pos & 3) * K5_SLOT;
        if constexpr (i >= 6) hb[u & 1][i - 6] = ldsr<bf16x8>(wa[hi][9] + so + (i - 6) * 1024);
        else fr[n % 3][i / 2][i & 1] = trh(wa[hi][cc] + so + (i & 1) * (cc < 8 ? 4096 : 512));
      }
    };
    sfor<36>([&](auto U) {
      constexpr int u = U.value;
      if constexpr (u == 0) wait_vm<2>();
      if constexpr (u == 4) { wait_vm<0>(); set_flag(c, f4); }
      if constexpr (u == 4 || u == 18) bload8c(pf, fo, fl_rsrc(c), FL4 + (u == 4 ? 1 : 2) * 2048 * 64);
      if constexpr (u == 7 || u == 21) {
        asm volatile("s_waitcnt vmcnt(0)" : "+v"(pf)::"memory");
        if (__builtin_expect(!tag_ok(c, pf), 0)) poll(c, fo, FL4 + (u == 7 ? 1 : 2) * 2048 * 64);
      }
      bar();
      if constexpr (u == 0) STAMP(ws, 5, 1);
      if constexpr (u == 18) STAMP(ws, 5, 2);
      if constexpr (u == 0) { rd1(ic<0>{}, ic<6>{}); rd1(ic<0>{}, ic<7>{}); sfor<6>([&](auto I) { rd1(ic<0>{}, I); }); sfor<6>([&](auto I) { rd1(ic<1>{}, I); }); }
      sfor<18>([&](auto M) {
        constexpr int mi = M.value, v = mi / 6, n = 3 * u + v, cc = mi / 2;
        if constexpr (u == 0) mfma16_z(acc[cc][mi & 1], cat(fr[n % 3][cc % 3][0], fr[n % 3][cc % 3][1]), hb[u & 1][mi & 1]);
        else mfma16_ip(acc[cc][mi & 1], cat(fr[n % 3][cc % 3][0], fr[n % 3][cc % 3][1]), hb[u & 1][mi & 1]);
        SBAR();
        if constexpr (mi == 6 || mi == 8) rd1(ic<n + 2>{}, ic<6 + (mi - 6) / 2>{});
        rd1(ic<n + 2>{}, ic<mi % 6>{});
        SBAR();
      });
    });
    asm volatile("s_nop 9" : "+v"(acc[7][0]), "+v"(acc[7][1]), "+v"(acc[8][0]), "+v"(acc[8][1]));
#pragma unroll
    for (int i = 0; i < 7; ++i) asm volatile("" : "+v"(acc[i][0]), "+v"(acc[i][1]));
    bar();
    STAMP(ws, 5, 3);
    if (w == sg) {
      sfor<2>([&](auto T) { sfor<9>([&](auto C) { ldsw<f32x4>(lds0 + (T.value * 9 + C.value) * 1024 + lane * 16, acc[C.value][T.value]); }); });
      lds_bar();
    } else {
      sfor<2>([&](auto T) {
        const unsigned pb = (((tile * 4 + w) * 4 + sg) * 2 + T.value) * PT_REC;
        sfor<4>([&](auto I) {
          constexpr int i = I.value;
          const f32x4 a = acc[2 * i][T.value], b = acc[2 * i + 1][T.value];
          bstore16<16>((u32x4){pk2(a[0], a[1]), pk2(a[2], a[3]), pk2(b[0], b[1]), pk2(b[2], b[3])}, rpt, lane * 16 + i * 1024, pb);
        });
        const f32x4 a = acc[8][T.value];
        bstore8<16>((u32x2){pk2(a[0], a[1]), pk2(a[2], a[3])}, rpt, lane * 8 + 4096, pb);
      });
      bar();
      wait_vm<0>();
      set_flag(c, FL5 + ((tile * 4 + w) * 4 + sg) * 64);
    }
  }
  const int T = w >> 2, pi = w & 3, tt = 8 * mt + 2 * sg + T;
  const rsrc_t rhf = mk_rsrc(ws + WS_HF, 128 * 18 * 1024);
  u32x4 hv, pv[3];
  u32x2 hl, pl[3];
  unsigned rs2u;
  STAMP(ws, 5, 4);
  bload16c(hv, lane * 16, rhf, (tt * 18 + 4 * nt + pi) * 1024);
  bload8c(hl, lane * 16 + 8 * (nt & 1), rhf, (tt * 18 + 16 + (nt >> 1)) * 1024);
  bload4c(rs2u, t16 * 4, mk_rsrc(ws + WS_RS, 4 * 2048 * 4), (sg * 2048 + 16 * tt) * 4);
  poll(c, ((sg + 1 + min(lane, 2)) & 3) * 64, FL5 + (tile * 4 + sg) * 256);
  STAMP(ws, 5, 5);
#pragma unroll
  for (int o = 0; o < 3; ++o) {
    const unsigned so = (((tile * 4 + sg) * 4 + ((sg + 1 + o) & 3)) * 2 + T) * PT_REC;
    bload16c(pv[o], lane * 16 + pi * 1024, rpt, so);
    bload8c(pl[o], pi == 0 ? lane * 8 + 4096 : OOB, rpt, so);
  }
  const unsigned orow = (16 * tt + t16) * 1152, own = lds0 + T * 9216 + lane * 16;
  const f32x4 o0 = ldsr<f32x4>(own + pi * 2048), o1 = ldsr<f32x4>(own + pi * 2048 + 1024), o2 = ldsr<f32x4>(own + 8192);
  asm volatile("s_waitcnt vmcnt(0)" : "+v"(hv), "+v"(hl), "+v"(rs2u), "+v"(pv[0]), "+v"(pv[1]), "+v"(pv[2]), "+v"(pl[0]), "+v"(pl[1]), "+v"(pl[2])::"memory");
  STAMP(ws, 5, 6);
  const float rs2 = __uint_as_float(rs2u);
  const rsrc_t rout = mk_rsrc(c.out, S_ * D_ * 2);
  auto fin = [&](const f32x4& ow, unsigned a0, unsigned a1, unsigned b0, unsigned b1, unsigned c0, unsigned c1, unsigned h0, unsigned h1, unsigned& d0, unsigned& d1) {
    const float v0 = ((ow[0] + bflo(b0)) + (bflo(a0) + bflo(c0))) * rs2 + bflo(h0);
    const float v1 = ((ow[1] + bfhi(b0)) + (bfhi(a0) + bfhi(c0))) * rs2 + bfhi(h0);
    const float v2 = ((ow[2] + bflo(b1)) + (bflo(a1) + bflo(c1))) * rs2 + bflo(h1);
    const float v3 = ((ow[3] + bfhi(b1)) + (bfhi(a1) + bfhi(c1))) * rs2 + bfhi(h1);
    d0 = pk2(v0, v1); d1 = pk2(v2, v3);
  };
  {
    unsigned a0, a1, b0, b1;
    fin(o0, pv[0][0], pv[0][1], pv[1][0], pv[1][1], pv[2][0], pv[2][1], hv[0], hv[1], a0, a1);
    fin(o1, pv[0][2], pv[0][3], pv[1][2], pv[1][3], pv[2][2], pv[2][3], hv[2], hv[3], b0, b1);
    auto s0 = __builtin_amdgcn_permlane16_swap(a0, b0, false, false);
    auto s1 = __builtin_amdgcn_permlane16_swap(a1, b1, false, false);
    bstore16<0>((u32x4){s0[0], s1[0], s0[1], s1[1]}, rout, orow + 256 * nt + 64 * pi + 32 * (g & 1) + 16 * (g >> 1), 0);
  }
  {
    unsigned e0, e1;
    fin(o2, pl[0][0], pl[0][1], pl[1][0], pl[1][1], pl[2][0], pl[2][1], hl[0], hl[1], e0, e1);
    bstore8<0>((u32x2){e0, e1}, rout, pi == 0 ? orow + 1024 + 32 * nt + 8 * g : OOB, 0);
  }
  STAMP(ws, 5, 7);
  STAMP_FLUSH(ws, 5);
}

extern "C" __global__ __launch_bounds__(512, 2) void k1_all(const void* x, const void* wqkv, const void* wo, const void* wg, const void* wu, const void* wd, char* ws, void* out) {
  Ctx c = {x, wqkv, wo, wg, wu, wd, ws, out, (int)rfl(threadIdx.x >> 6), (int)(blockIdx.x & 7), (int)(blockIdx.x >> 3), (unsigned)(size_t)smem, 0u, 0u};
  if (c.w == 7) {
    unsigned long long pc; unsigned back, size;
    asm volatile("s_getpc_b64 %0\n.Lk1pc%=:\n\ts_mov_b32 %1, .Lk1pc%=-k1_all\n\ts_mov_b32 %2, k9_end-k1_all" : "=s"(pc), "=s"(back), "=s"(size));
    const int lane = fresh_lane();
    warm(c.lds0 + LDS_SCR, lane < 24 ? (c.r * 24 + lane) * 128 : OOB, mk_rsrc((const void*)(pc - back), size), 0);
  }
  {
    const int mt = 4 * (c.xcd & 3) + (c.r & 3), nt = 8 * (c.xcd >> 2) + (c.r >> 2);
    if (c.w < 4) p1_body<0>(c, mt, nt); else p1_body<1>(c, mt, nt);
  }
  p2(c);
  if (c.w >= 4 || c.r >= 18) cnt_wait(c, 1, 8);
  if (c.w >= 4) k4_first<4>(c, fresh_lane());
  if (c.r < 18) p3_gemm(c); else p3_side(c);
  if (c.w < 4) k4_first<0>(c, fresh_lane());
  p4(c);
  p5(c);
  STAMP_OUT();
}

extern "C" __global__ void k9_end() {}

extern "C" void kernel_launch(void* const* d_in, const int* in_sizes, int n_in, void* d_out, int out_size, void* d_ws, size_t ws_size,
                              hipStream_t stream) {
  (void)hipFuncSetAttribute((const void*)k1_all, hipFuncAttributeMaxDynamicSharedMemorySize, LDS_ALL);
  k1_all<<<256, 512, LDS_ALL, stream>>>(d_in[0], d_in[1], d_in[2], d_in[4], d_in[5], d_in[6], (char*)d_ws, d_out);
}
```

```cpp
#define STAMPS
#define KB 1
#define IB 2
#define MB 1
#define CONSTD 0
#include <hip/hip_runtime.h>
#include <stdint.h>

typedef short bf16x8 __attribute__((ext_vector_type(8)));
typedef short bf16x4 __attribute__((ext_vector_type(4)));
typedef float f32x2 __attribute__((ext_vector_type(2)));
typedef float f32x4 __attribute__((ext_vector_type(4)));
typedef float f32x16 __attribute__((ext_vector_type(16)));
typedef unsigned u32x2 __attribute__((ext_vector_type(2)));
typedef unsigned u32x4 __attribute__((ext_vector_type(4)));
typedef __bf16 bf2_t __attribute__((ext_vector_type(2)));
typedef __amdgpu_buffer_rsrc_t rsrc_t;
#define LAS __attribute__((address_space(3)))
#define GAS __attribute__((address_space(1)))
#define DI __device__ __forceinline__

constexpr int S_ = 2048, D_ = 576, F_ = 4608;
constexpr float QSCALE = 0.23570226039551584f * 1.4426950408889634f;
constexpr unsigned ONES2 = 0x3f803f80u;

constexpr size_t QREC = 1280, KVREC = 2560;
constexpr unsigned OOB = 0x7fffff00u;
constexpr unsigned FL1 = 128;
constexpr unsigned FL2 = FL1 + 256 * 64;
constexpr unsigned FL3 = FL2 + 2048 * 64;
constexpr unsigned FLS = FL3 + 2048 * 64;
constexpr unsigned FL4 = FLS + 128 * 64;
constexpr unsigned FL5 = FL4 + 3 * 2048 * 64;
constexpr unsigned FL_END = FL5 + 1024 * 64;
constexpr size_t WS_QF = 1u << 20;
constexpr size_t WS_KV = WS_QF + 32 * 64 * QREC;
constexpr size_t WS_OF = WS_KV + 32 * 64 * KVREC;
constexpr size_t WS_HF = WS_OF + 128 * 18 * 1024;
constexpr size_t WS_ST = WS_HF + 128 * 18 * 1024;
constexpr size_t WS_CS = WS_ST + 9 * 2048 * 8;
constexpr size_t WS_RS = WS_CS + 2 * 4608 * 4;
constexpr size_t WS_LG = WS_RS + 4 * 2048 * 4;
constexpr size_t WS_WT = WS_LG + 8 * 4 * 36864;
constexpr size_t WS_HH = WS_WT + 4 * 4608 * 32;
constexpr size_t PT_REC = 4608;
constexpr size_t WS_PT = WS_HH + (size_t)128 * 144 * 1024;
constexpr size_t WS_END = WS_PT + (size_t)64 * 8 * 4 * PT_REC;

DI unsigned rfl(unsigned v) { return __builtin_amdgcn_readfirstlane(v); }
DI rsrc_t mk_rsrc(const void* p, unsigned bytes) { return __builtin_amdgcn_make_buffer_rsrc((void*)p, (short)0, (int)bytes, 0x00020000); }
DI void glds16(unsigned lds, unsigned voff, rsrc_t r, unsigned soff) {
  asm volatile("s_mov_b32 m0, %0\n\ts_nop 0\n\tbuffer_load_dwordx4 %1, %2, %3 offen lds" ::"s"(lds), "v"(voff), "s"(r), "s"(soff) : "memory", "m0");
}
DI void glds16c(unsigned lds, unsigned voff, rsrc_t r, unsigned soff) {
  asm volatile("s_mov_b32 m0, %0\n\ts_nop 0\n\tbuffer_load_dwordx4 %1, %2, %3 offen sc1 lds" ::"s"(lds), "v"(voff), "s"(r), "s"(soff) : "memory", "m0");
}
template <class T> DI void bload16c(T& d, unsigned voff, rsrc_t r, unsigned soff) {
  asm volatile("buffer_load_dwordx4 %0, %1, %2, %3 offen sc1" : "=v"(d) : "v"(voff), "s"(r), "s"(soff) : "memory");
}
DI void bload8c(u32x2& d, unsigned voff, rsrc_t r, unsigned soff) {
  asm volatile("buffer_load_dwordx2 %0, %1, %2, %3 offen sc1" : "=v"(d) : "v"(voff), "s"(r), "s"(soff) : "memory");
}
DI void bload4c(unsigned& d, unsigned voff, rsrc_t r, unsigned soff) {
  asm volatile("buffer_load_dword %0, %1, %2, %3 offen sc1" : "=v"(d) : "v"(voff), "s"(r), "s"(soff) : "memory");
}
DI void bload8(u32x2& d, unsigned voff, rsrc_t r, unsigned soff) {
  asm volatile("buffer_load_dwordx2 %0, %1, %2, %3 offen" : "=v"(d) : "v"(voff), "s"(r), "s"(soff) : "memory");
}
DI void warm(unsigned lds, unsigned voff, rsrc_t r, unsigned soff) {
  asm volatile("s_mov_b32 m0, %0\n\ts_nop 0\n\tbuffer_load_dword %1, %2, %3 offen lds" ::"s"(lds), "v"(voff), "s"(r), "s"(soff) : "memory", "m0");
}
template <int N> DI void wait_vm() { asm volatile("s_waitcnt vmcnt(%0)" ::"i"(N) : "memory"); }
#define PIN(x) asm volatile("" : "+v"(x))
#define SBAR() __builtin_amdgcn_sched_barrier(0)
DI void bar() { SBAR(); asm volatile("s_barrier" ::: "memory"); SBAR(); }
DI void lds_bar() { SBAR(); asm volatile("s_waitcnt lgkmcnt(0)\n\ts_barrier" ::: "memory"); SBAR(); }
DI bf16x8 trfrag(unsigned a0, unsigned a1) {
  bf16x4 lo = __builtin_amdgcn_ds_read_tr16_b64_v4i16((LAS bf16x4*)(size_t)a0);
  bf16x4 hi = __builtin_amdgcn_ds_read_tr16_b64_v4i16((LAS bf16x4*)(size_t)a1);
  return __builtin_shufflevector(lo, hi, 0, 1, 2, 3, 4, 5, 6, 7);
}
DI bf16x4 trh(unsigned a) { return __builtin_amdgcn_ds_read_tr16_b64_v4i16((LAS bf16x4*)(size_t)a); }
DI bf16x8 cat(bf16x4 lo, bf16x4 hi) { return __builtin_shufflevector(lo, hi, 0, 1, 2, 3, 4, 5, 6, 7); }
template <class T> DI T ldsr(unsigned a) { return *(LAS T*)(size_t)a; }
template <class T> DI void ldsw(unsigned a, T v) { *(LAS T*)(size_t)a = v; }
DI unsigned pk2(float a, float b) { f32x2 v = {a, b}; return __builtin_bit_cast(unsigned, __builtin_convertvector(v, bf2_t)); }
DI float bflo(unsigned u) { return __uint_as_float(u << 16); }
DI float bfhi(unsigned u) { return __uint_as_float(u & 0xffff0000u); }
DI f32x4 mfma16(bf16x8 a, bf16x8 b, f32x4 c) { return __builtin_amdgcn_mfma_f32_16x16x32_bf16(a, b, c, 0, 0, 0); }
DI void mfma16_ip(f32x4& c, bf16x8 a, bf16x8 b) { asm volatile("v_mfma_f32_16x16x32_bf16 %0, %1, %2, %0" : "+v"(c) : "v"(a), "v"(b)); }
DI void mfma16_z(f32x4& c, bf16x8 a, bf16x8 b) { asm volatile("v_mfma_f32_16x16x32_bf16 %0, %1, %2, 0" : "=&v"(c) : "v"(a), "v"(b)); }
DI f32x16 mfma32(bf16x8 a, bf16x8 b, f32x16 c) { return __builtin_amdgcn_mfma_f32_32x32x16_bf16(a, b, c, 0, 0, 0); }
DI float xor32(float v) { auto r = __builtin_amdgcn_permlane32_swap(__float_as_uint(v), __float_as_uint(v), false, false); return __uint_as_float((threadIdx.x & 32) ? r[0] : r[1]); }
DI float sum_x32(float v) { auto r = __builtin_amdgcn_permlane32_swap(__float_as_uint(v), __float_as_uint(v), false, false); return __uint_as_float(r[0]) + __uint_as_float(r[1]); }
DI float max_x32(float v) { auto r = __builtin_amdgcn_permlane32_swap(__float_as_uint(v), __float_as_uint(v), false, false); return fmaxf(__uint_as_float(r[0]), __uint_as_float(r[1])); }
DI float sum_x16(float v) { auto r = __builtin_amdgcn_permlane16_swap(__float_as_uint(v), __float_as_uint(v), false, false); return __uint_as_float(r[0]) + __uint_as_float(r[1]); }
template <int AUX> DI void bstore16(u32x4 v, rsrc_t r, unsigned voff, unsigned soff) {
  if constexpr (AUX == 16) asm volatile("buffer_store_dwordx4 %0, %1, %2, %3 offen sc1\n\ts_nop 1" ::"v"(v), "v"(voff), "s"(r), "s"(soff) : "memory");
  else asm volatile("buffer_store_dwordx4 %0, %1, %2, %3 offen\n\ts_nop 1" ::"v"(v), "v"(voff), "s"(r), "s"(soff) : "memory");
}
template <int AUX> DI void bstore8(u32x2 v, rsrc_t r, unsigned voff, unsigned soff) { __builtin_amdgcn_raw_buffer_store_b64(v, r, voff, soff, AUX); }
template <int AUX> DI void bstore4(unsigned v, rsrc_t r, unsigned voff, unsigned soff) { __builtin_amdgcn_raw_buffer_store_b32(v, r, voff, soff, AUX); }

template <int... I> struct iseq {};
template <int N, int... I> struct mkseq : mkseq<N - 1, N - 1, I...> {};
template <int... I> struct mkseq<0, I...> { typedef iseq<I...> type; };
template <int V> struct ic { static constexpr int value = V; };
template <class Fn, int... I> DI void sfor_(Fn&& f, iseq<I...>) { (f(ic<I>{}), ...); }
template <int N, class Fn> DI void sfor(Fn&& f) { sfor_(f, typename mkseq<N>::type{}); }

extern __shared__ __attribute__((aligned(16))) char smem[];
constexpr size_t WS_DBG = 64u << 20;
constexpr int LDS_DBG = 156 * 1024;
#define STAMP(ws, kid, i) do { SBAR(); unsigned long long t_, c_; asm volatile("s_memrealtime %0\n\ts_memtime %1\n\ts_waitcnt lgkmcnt(0)" : "=s"(t_), "=s"(c_) :: "memory"); \
  asm volatile("v_writelane_b32 %0, %1, %3\n\tv_writelane_b32 %0, %2, %4" : "+v"(stamp_v) : "s"((unsigned)t_), "s"((unsigned)c_), "i"(i), "i"(8 + i)); SBAR(); } while (0)
#define STAMP_DECL unsigned stamp_v = 0
#define STAMP_FLUSH(ws, kid) do { const int l_ = fresh_lane(); if (l_ < 16) ldsw<unsigned>(c.lds0 + LDS_DBG + ((c.w * 7 + kid) * 16 + l_) * 4, stamp_v); } while (0)
#define STAMP_OUT() do { const int l_ = fresh_lane(); if (l_ < 16) for (int k_ = 1; k_ < 7; ++k_) if ((k_ == 3) == (c.r < 18) || (k_ != 3 && k_ != 6)) \
  *(GAS unsigned*)(c.ws + WS_DBG + ((((size_t)k_ * 256 + blockIdx.x) * 8 + c.w) * 16 + l_) * 4) = ldsr<unsigned>(c.lds0 + LDS_DBG + ((c.w * 7 + k_) * 16 + l_) * 4); } while (0)

struct Ctx {
  const void *x, *wqkv, *wo, *wg, *wu, *wd;
  char* ws; void* out;
  int w, xcd, r;
  unsigned lds0, t0, t1;
};
constexpr int LDS_ALL = 160 * 1024, LDS_SCR = LDS_ALL - 256;
DI int fresh_lane() { int l; asm volatile("v_mbcnt_lo_u32_b32 %0, -1, 0\n\tv_mbcnt_hi_u32_b32 %0, -1, %0" : "=v"(l)); __builtin_assume(l >= 0 && l < 64); return l; }
DI unsigned opq(unsigned v) { PIN(v); return v; }
constexpr int LDS_CNT = 147456;
DI void cnt_add(const Ctx& c, int i) {
  asm volatile("s_mov_b64 exec, 1\n\tds_add_u32 %0, %1\n\ts_mov_b64 exec, -1" ::"v"(c.lds0 + LDS_CNT + 4 * i), "v"(1u) : "memory");
}
DI unsigned cnt_get(const Ctx& c, int i) {
  unsigned v;
  asm volatile("ds_read_b32 %0, %1\n\ts_waitcnt lgkmcnt(0)" : "=v"(v) : "v"(c.lds0 + LDS_CNT + 4 * i) : "memory");
  return rfl(v);
}
DI void cnt_wait(const Ctx& c, int i, unsigned n) {
  if (__builtin_expect(cnt_get(c, i) < n, 0))
    for (int spin = 0; spin < (1 << 22) && cnt_get(c, i) < n; ++spin) __builtin_amdgcn_s_sleep(1);
}
DI rsrc_t fl_rsrc(const Ctx& c) { return mk_rsrc(c.ws, FL_END); }
DI void set_flag(const Ctx& c, unsigned off) {
  u32x2 t;
  asm volatile("v_mov_b32 %0, %1" : "=v"(t[0]) : "s"(c.t0));
  asm volatile("v_mov_b32 %0, %1" : "=v"(t[1]) : "s"(c.t1));
  asm volatile("s_mov_b64 exec, 1\n\tbuffer_store_dwordx2 %0, off, %1, %2 sc1\n\ts_mov_b64 exec, -1" ::"v"(t), "s"(fl_rsrc(c)), "s"(off) : "memory");
}
DI bool tag_ok(const Ctx& c, u32x2 v) { return __builtin_amdgcn_ballot_w64(v[0] != c.t0 || v[1] != c.t1) == 0; }
DI void poll(const Ctx& c, unsigned voff, unsigned soff) {
  for (int spin = 0; spin < (1 << 20); ++spin) {
    u32x2 v;
    asm volatile("buffer_load_dwordx2 %0, %1, %2, %3 offen sc1\n\ts_waitcnt vmcnt(0)" : "=&v"(v) : "v"(voff), "s"(fl_rsrc(c)), "s"(soff) : "memory");
    if (tag_ok(c, v)) break;
  }
}

constexpr int K1_SLOT = 32768, K1_TBL = K1_SLOT * 4;
extern "C" __global__ void k9_end();

template <int NN> DI void p1_body(Ctx& c, int mt, int nt) {
  char* ws = c.ws;
  const int w = c.w, lane = fresh_lane();
  const unsigned lds0 = c.lds0;
  const rsrc_t rx = mk_rsrc(c.x, S_ * D_ * 2), rw = mk_rsrc(c.wqkv, D_ * 3 * D_ * 2);
  const int t16 = lane & 15, g = lane >> 4, q4 = (lane >> 2) & 3, p = lane & 3, m = w & 3;
  constexpr int NT = NN ? 3 : 4;
  unsigned xv[2], wv[2];
#pragma unroll
  for (int i = 0; i < 2; ++i) {
    const int row = 8 * (2 * w + i) + (lane >> 3);
    xv[i] = (128 * mt + row) * 1152 + 16 * ((lane & 7) ^ ((row >> 1) & 7));
    int li = 64 * (2 * w + i) + lane;
    if (li >= 896) li -= 128;
    const int r64 = (li * 4682) >> 16, cc = li - 14 * r64;
    const int rho = r64 & 31, k = (r64 & 32) + 8 * ((rho >> 2) & 3) + 4 * (rho >> 4) + (rho & 3);
    wv[i] = k * 3456 + ((216 * nt) & ~15) + 16 * cc;
  }
  STAMP_DECL; STAMP(ws, 1, 0);
  auto issue1 = [&](int slab, int i) {
    const unsigned dst = lds0 + (slab & 3) * K1_SLOT + w * 2048 + (i >> 1) * 1024;
    if (i & 1) glds16(dst + 16384, wv[i >> 1], rw, slab * 64 * 3456);
    else glds16(dst, xv[i >> 1], rx, slab * 128);
  };
#pragma unroll
  for (int sl = 0; sl < 2; ++sl)
#pragma unroll
    for (int i = 0; i < 4; ++i) issue1(sl, i);
  u32x2 ev;
  bload8c(ev, 0u, fl_rsrc(c), 0);
  STAMP(ws, 1, 1);
  const unsigned wa = lds0 + 16384 + (4 * g + q4) * 224 + (nt & 1) * 8;
  unsigned ad[2][5];
  ad[0][0] = wa + 8 * p + 144 * NN;
  ad[0][1] = wa + 72 * (p < 3 ? p : 2) + 32;
  ad[0][2] = NN && m >= 2 ? ad[0][1] : ad[0][0] + 72 * (m >> 1) + 40 * (m & 1);
  ad[0][3] = lds0 + (32 * m + t16) * 128 + 16 * (g ^ ((t16 >> 1) & 7));
  ad[0][4] = ad[0][3] ^ 64;
#pragma unroll
  for (int i = 0; i < 5; ++i) { ad[1][i] = ad[0][i] + 2 * K1_SLOT; PIN(ad[1][i]); }
  f32x4 acc[4][2] = {}, cs = {};
  bf16x8 ones = __builtin_bit_cast(bf16x8, (u32x4){ONES2, ONES2, ONES2, ONES2});
  PIN(ones);
  bf16x8 fb[2][2];
  bf16x4 fa[2][NT + 1][2];
  constexpr int NR = 2 * NT + 4;
  auto rd1 = [&](auto KS, auto I) {
    constexpr int ks = KS.value, i = I.value, sp = ks & 1, sl = (ks >> 1) & 3, ti = (i - 2) >> 1;
    constexpr unsigned so = (sl & 1) * K1_SLOT;
    if constexpr (ks < 18 && i < NR) {
      if constexpr (i < 2) fb[sp][i] = ldsr<bf16x8>(ad[sl >> 1][3 + sp] + so + i * 2048);
      else {
        constexpr int ai = ti == NT ? 2 : (NN && ti == 2) ? 1 : 0;
        constexpr unsigned co = ai ? 0 : 72 * (ti >> 1) + 40 * (ti & 1);
        fa[sp][ti][i & 1] = trh(ad[sl >> 1][ai] + so + co + sp * 32 * 224 + (i & 1) * 16 * 224);
      }
    }
  };
  sfor<9>([&](auto SL) {
    constexpr int sl = SL.value;
    constexpr int iss = sl == 0 ? 2 : (sl + 3 < 9 ? sl + 3 : 9);
    constexpr int need = sl == 0 ? 0 : (sl + 1 < 9 ? sl + 1 : 8);
    if constexpr (sl == 1) asm volatile("s_waitcnt vmcnt(%1)" : "+v"(ev) : "i"(4 * (iss - need - 1)) : "memory");
    else wait_vm<4 * (iss - need - 1) + (sl == 0)>();
    bar();
    if constexpr (sl == 0) STAMP(ws, 1, 2);
    if constexpr (sl == 4) STAMP(ws, 1, 3);
    if constexpr (sl <= 1) sfor<NR>([&](auto I) { rd1(ic<2 * sl>{}, I); });
    if constexpr (sl == 1) {
      const unsigned long long e = (((unsigned long long)rfl(ev[1]) << 32) | rfl(ev[0])) + 1ull;
      c.t0 = (unsigned)e; c.t1 = (unsigned)(e >> 32);
    }
    sfor<2>([&](auto SP) {
      constexpr int sp = SP.value, ks = 2 * sl + sp;
      bf16x8 tl;
      if constexpr (NN) tl = t16 >= 12 ? ones : cat(fa[sp][2][0], fa[sp][2][1]);
      sfor<9>([&](auto M) {
        constexpr int mi = M.value, ti = mi >> 1;
        if constexpr (mi == 8) cs = mfma16(cat(fa[sp][NT][0], fa[sp][NT][1]), ones, cs);
        else if constexpr (NN && ti == 3) acc[3][mi & 1] = mfma16(fb[sp][mi & 1], fb[sp][mi & 1], acc[3][mi & 1]);
        else if constexpr (NN && ti == 2) acc[2][mi & 1] = mfma16(tl, fb[sp][mi & 1], acc[2][mi & 1]);
        else acc[ti][mi & 1] = mfma16(cat(fa[sp][ti][0], fa[sp][ti][1]), fb[sp][mi & 1], acc[ti][mi & 1]);
        SBAR();
        if constexpr (ks != 1) {
          if constexpr (mi < 3) { rd1(ic<ks + 1>{}, ic<2 * mi>{}); rd1(ic<ks + 1>{}, ic<2 * mi + 1>{}); }
          else rd1(ic<ks + 1>{}, ic<3 + mi>{});
        }
        if constexpr (sl == 0) { if constexpr (mi & 1) issue1(2 + sp, mi >> 1); }
        else if constexpr (sl + 3 < 9 && (mi == 2 + 2 * NN || mi == 6 + 2 * NN)) issue1(sl + 3, 2 * sp + (mi >= 6));
        SBAR();
      });
    });
  });
  STAMP(ws, 1, 4);
  const unsigned tb = lds0 + K1_TBL, cb = tb + 1024, dummy = cb + 512;
  ldsw<f32x4>(t16 == 0 ? cb + (w * 16 + 4 * g) * 4 : dummy, cs);
  if constexpr (NN) sfor<2>([&](auto T) {
    const unsigned a = tb + (32 * m + 16 * T.value + t16) * 8;
    const f32x4 gr = acc[3][T.value];
    const float d01 = (p & 1) ? gr[1] : gr[0], d23 = (p & 1) ? gr[3] : gr[2];
    ldsw<float>(g == 3 ? a : dummy, acc[2][T.value][0]);
    ldsw<float>(g == q4 ? a + 4 : dummy, (p & 2) ? d23 : d01);
  });
  lds_bar();
  f32x2 st[2];
  sfor<2>([&](auto T) {
    const f32x2 v = ldsr<f32x2>(tb + (32 * m + 16 * T.value + t16) * 8);
    const float mu = v[0] * (1.f / D_), rs = __builtin_amdgcn_rsqf(fmaxf(v[1] * (1.f / D_) - mu * mu, 0.f) + 1e-5f);
    st[T.value] = (f32x2){rs, -rs * mu};
  });
  const rsrc_t ro = mk_rsrc(ws + WS_QF, 32 * 64 * (QREC + KVREC));
  constexpr unsigned KV0 = WS_KV - WS_QF;
  const int tile32 = 4 * mt + m, key = 16 * (g >> 1) + t16;
  const unsigned lo_qk = (g & 1) * 512 + key * 16, lo_v = 1280 + key * 32 + (g & 1) * 16;
  sfor<(NN ? 2 : 4)>([&](auto TI) {
    constexpr int ti = TI.value, tile = 4 * NN + ti;
    const int pg = 3 * nt + (tile >> 1), type = pg >> 4, rec = (2 * (pg & 15) + (tile & 1)) * 64 + tile32;
    const f32x4 cc = ldsr<f32x4>(cb + (tile * 16 + 4 * g) * 4);
    const float sc = type == 0 ? QSCALE : 1.f;
    const f32x4 v0 = acc[ti][0] * (st[0][0] * sc) + cc * (st[0][1] * sc), v1 = acc[ti][1] * (st[1][0] * sc) + cc * (st[1][1] * sc);
    auto s0 = __builtin_amdgcn_permlane32_swap(pk2(v0[0], v0[1]), pk2(v1[0], v1[1]), false, false);
    auto s1 = __builtin_amdgcn_permlane32_swap(pk2(v0[2], v0[3]), pk2(v1[2], v1[3]), false, false);
    bstore16<16>((u32x4){s0[0], s1[0], s0[1], s1[1]}, ro, type == 2 ? lo_v : lo_qk, type == 0 ? rec * QREC : KV0 + rec * KVREC);
  });
  if constexpr (NN) {
    const int pg = 3 * nt + g, type = pg >> 4, rec = 2 * (pg & 15) * 64 + tile32;
    const f32x4 cc = ldsr<f32x4>(cb + (96 + 4 * g) * 4);
    const float sc = type == 0 ? QSCALE : 1.f;
    const unsigned rb = type == 0 ? rec * QREC : KV0 + rec * KVREC, hs = type == 0 ? 64 * QREC : 64 * KVREC;
    const unsigned oq = g < 3 ? rb + (type == 2 ? 2304 : 1024) + t16 * 8 : OOB, one = type == 1 ? 0u : 0x00003f80u;
    sfor<2>([&](auto T) {
      const f32x4 v = acc[2][T.value] * (st[T.value][0] * sc) + cc * (st[T.value][1] * sc);
      const unsigned d0 = pk2(v[0], v[1]), d1 = pk2(v[2], v[3]);
      bstore8<16>((u32x2){d0, one}, ro, oq, T.value * 128); bstore8<16>((u32x2){d1, one}, ro, oq + hs, T.value * 128);
    });
  }
  STAMP(ws, 1, 5);
  wait_vm<0>();
  STAMP(ws, 1, 6);
  bar();
  if (w == 0) set_flag(c, FL1 + (mt * 16 + nt) * 64);
  STAMP(ws, 1, 7);
  STAMP_FLUSH(ws, 1);
}

constexpr int P3_WO = 73728;
constexpr int K4_RING = 8192;
constexpr unsigned k4_so(int u) { return ((((u >> 1) + 2) & 3) * 2 + (u & 1)) * 8192; }
DI unsigned k4_wv(int lane, int w, int Qd, int j) {
  const int rho = 8 * (w & 3) + (lane >> 3);
  return rho * 9216 + 1152 * Qd + 256 * j + 32 * (((lane & 7) >> 1) ^ ((rho >> 1) & 3)) + 16 * (lane & 1);
}

template <int U0> DI void k4_first(const Ctx& c, int lane) {
  const rsrc_t rg = mk_rsrc(c.wg, D_ * F_ * 2), ru = mk_rsrc(c.wu, D_ * F_ * 2);
  const unsigned wv4 = k4_wv(lane, c.w, c.xcd, c.r & 3), dst = c.lds0 + K4_RING + (c.w & 3) * 1024;
  sfor<4>([&](auto U) {
    glds16(dst + k4_so(U0 + U.value), wv4, rg, (U0 + U.value) * 32 * 9216);
    glds16(dst + 4096 + k4_so(U0 + U.value), wv4, ru, (U0 + U.value) * 32 * 9216);
  });
}
DI void p2_ahead(const Ctx& c) {
  const int w = c.w, r = c.r, lane = fresh_lane();
  const rsrc_t rwo = mk_rsrc(c.wo, D_ * D_ * 2);
  const int nt = r >= 9 ? r - 9 : r;
  const int rho = 8 * w + (lane >> 3), rg = (rho >> 2) & 3, rj = 4 * (rho >> 4) + (rho & 3);
  const unsigned cbyte = 128 * nt + 32 * (((lane & 7) >> 1) ^ ((rho >> 1) & 3)) + 16 * (lane & 1);
  const int th = 4 * rg + (rj >> 1);
  const unsigned wv = (20 * (rg >> 1) + 8 * (rg & 1) + rj) * 1152 + cbyte, wvt = (18 * th + ((th & 1) ? 0 : 16) + (rj & 1)) * 1152 + cbyte;
  sfor<16>([&](auto K) { glds16(c.lds0 + P3_WO + (w + 4 * K.value) * 1024, wv, rwo, K.value * 36 * 1152); });
  sfor<2>([&](auto K) { glds16(c.lds0 + P3_WO + (w + 64 + 4 * K.value) * 1024, wvt, rwo, K.value * 288 * 1152); });
}

DI void p2(const Ctx& c) {
  const int head = 4 * c.xcd + (c.r & 3), qb = c.r >> 2;
  char* ws = c.ws;
  const int w = c.w, lane = fresh_lane();
  const unsigned lds0 = c.lds0;
  const int q = lane & 31, h = lane >> 5, G = lane >> 4;
  const int qt = 8 * qb + w;
  const rsrc_t rq = mk_rsrc(ws + WS_QF, 32 * 64 * QREC), rkv = mk_rsrc(ws + WS_KV, 32 * 64 * KVREC);
  STAMP_DECL; STAMP(ws, 2, 0);
  unsigned kb = lds0 + w * KVREC + lane * 16, ktb = lds0 + w * KVREC + 1024 + q * 8;
  unsigned vb[4];
#pragma unroll
  for (int i = 0; i < 4; ++i) { const int row = 8 * i + 4 * (G >> 1) + ((lane >> 2) & 3); vb[i] = lds0 + w * KVREC + ((G & 1) ? 2304 + row * 8 : 1280 + row * 32 + 8 * (lane & 3)); PIN(vb[i]); }
  unsigned mf[16], mk[8];
#pragma unroll
  for (int e = 0; e < 16; ++e) { mf[e] = ((e & 3) + 8 * (e >> 2) <= q - 4 * h) ? ~0u : 0u; PIN(mf[e]); }
#pragma unroll
  for (int e = 0; e < 8; ++e) { mk[e] = (mf[2 * e] & 0xffffu) | (mf[2 * e + 1] & 0xffff0000u); PIN(mk[e]); }
  const unsigned tt = 2 * qt + (q >> 4);
  unsigned om_o = (tt * 18 + (head >> 1)) * 1024 + ((q & 15) + 16 * (2 * (head & 1) + h)) * 16;
  unsigned ot_o = h ? OOB : (tt * 18 + 16 + (head >> 4)) * 1024 + ((q & 15) + 16 * ((head >> 2) & 3)) * 16 + (head & 3) * 4;
  unsigned l16 = lane * 16, qto = h ? OOB : 1024 + q * 8, wmo;
  const int wmv = c.r * 8 + w;
  {
    const int l = (wmv >= 162 ? wmv - 162 : wmv >= 81 ? wmv - 81 : wmv) * 64 + lane, row = (l * 7282) >> 16;
    wmo = wmv >= 162 ? l * 128 : row * 9216 + c.xcd * 1152 + (l - 9 * row) * 128;
    PIN(wmo);
  }
  PIN(kb); PIN(ktb); PIN(om_o); PIN(ot_o); PIN(l16); PIN(qto);
  if (qb == 0) {
#pragma unroll
    for (int i = 0; i < 3; ++i) {
      const unsigned o = min(w * 64 + lane + 512 * i, 1279) * 16, m = (o % KVREC - 1024 < 256) ? 0x0000f149u : 0u;
      ldsw<u32x4>(lds0 + o, (u32x4){0u, m, 0u, m});
    }
  }
  if (w == 0) {
    const int ty = lane < 4 ? 1 : lane < 8 ? 2 : 0, pm = max(2 * qb + (ty ? (lane & 3) - 2 : (lane & 1)), 0);
    ldsw<unsigned>(lds0 + LDS_CNT + 4 * (lane & 7), 0u);
    poll(c, (pm * 16 + (((16 * ty + (head >> 1)) * 43) >> 7)) * 64, FL1);
  }
  lds_bar();
  STAMP(ws, 2, 7);
  bf16x8 qm; u32x2 qt2;
#pragma unroll
  for (int i = 0; i < 5; ++i) { const int pc = max(w + 8 * i, qb ? 0 : 20 + w); glds16c(lds0 + pc * 1024, l16, rkv, (head * 64 + 8 * qb - 8) * KVREC + pc * 1024); }
  bload16c(qm, l16, rq, (head * 64 + qt) * QREC);
  bload8c(qt2, qto, rq, (head * 64 + qt) * QREC);
  auto toff = [&](int i) { return (unsigned)(i * KVREC); };
  STAMP(ws, 2, 1);
  asm volatile("s_waitcnt vmcnt(0)" : "+v"(qm), "+v"(qt2)::"memory");
  bar();
  STAMP(ws, 2, 2);
  const bf16x4 qt4 = __builtin_bit_cast(bf16x4, qt2);
  bf16x8 km[9]; bf16x4 kt[9];
  constexpr int ord[9] = {0, 8, 1, 2, 3, 4, 5, 6, 7};
  sfor<9>([&](auto I) { constexpr int i = ord[I.value]; km[i] = ldsr<bf16x8>(kb + toff(i)); kt[i] = ldsr<bf16x4>(ktb + toff(i)); });
  SBAR();
  f32x16 s[9];
  float m4[4];
  auto qk = [&](auto I, auto J) {
    constexpr int i = I.value, j = J.value;
    f32x16 a = mfma32(km[i], qm, (f32x16){}), b;
    if constexpr (j < 9) b = mfma32(km[j], qm, (f32x16){});
    s[i] = __builtin_amdgcn_mfma_f32_32x32x8bf16_1k(kt[i], qt4, a, 0, 0, 0);
    if constexpr (j < 9) s[j] = __builtin_amdgcn_mfma_f32_32x32x8bf16_1k(kt[j], qt4, b, 0, 0, 0);
    SBAR();
  };
  auto mx1 = [&](auto I) {
    constexpr int i = I.value;
#pragma unroll
    for (int e = 0; e < 8; ++e) m4[e & 3] = fmaxf(fmaxf(m4[e & 3], s[i][2 * e]), s[i][2 * e + 1]);
  };
  bf16x4 vf[2][4];
  auto rdv = [&](int set, unsigned off) {
#pragma unroll
    for (int i = 0; i < 4; ++i) vf[set][i] = trh(vb[i] + off);
  };
  qk(ic<0>{}, ic<8>{});
  qk(ic<1>{}, ic<2>{});
  {
    size_t p0 = (size_t)c.wg, p1 = (size_t)c.wu, p2 = (size_t)c.wo;
    asm("" : "+s"(p0), "+s"(p1), "+s"(p2));
    p0 = wmv >= 81 ? p1 : p0;
    warm(lds0 + LDS_SCR, wmo, mk_rsrc((const void*)(wmv >= 162 ? p2 : p0), wmv >= 162 ? D_ * D_ * 2 : D_ * F_ * 2), 0);
  }
  SBAR();
#pragma unroll
  for (int e = 0; e < 16; ++e) s[8][e] = __uint_as_float((__float_as_uint(s[8][e]) & mf[e]) | (__float_as_uint(s[0][e]) & ~mf[e]));
#pragma unroll
  for (int e = 0; e < 4; ++e) asm("v_max_f32 %0, %1, %2\n\tv_max3_f32 %0, %0, %3, %4" : "=&v"(m4[e]) : "v"(s[8][4 * e]), "v"(s[8][4 * e + 1]), "v"(s[8][4 * e + 2]), "v"(s[8][4 * e + 3]));
  SBAR();
  qk(ic<3>{}, ic<4>{});
  mx1(ic<1>{}); mx1(ic<2>{}); SBAR();
  qk(ic<5>{}, ic<6>{});
  mx1(ic<3>{}); mx1(ic<4>{}); SBAR();
  qk(ic<7>{}, ic<9>{});
  rdv(1, toff(1));
  mx1(ic<5>{}); mx1(ic<6>{}); SBAR();
  mx1(ic<7>{});
  float mx = max_x32(fmaxf(fmaxf(m4[0], m4[1]), fmaxf(m4[2], m4[3])));
  STAMP(ws, 2, 3);
  f32x16 o = {};
  u32x4 pp;
  sfor<16>([&](auto H) {
    constexpr int hh = H.value, i = 1 + hh / 2, ks = hh & 1;
    f32x2 dd[4];
#pragma unroll
    for (int e = 0; e < 4; ++e) dd[e] = (f32x2){s[i][8 * ks + 2 * e], s[i][8 * ks + 2 * e + 1]} - (f32x2){mx, mx};
    SBAR();
    if constexpr (hh > 0 && hh < 15) o = mfma32(cat(vf[(1 + (hh - 1) / 2) & 1][2 * (ks ^ 1)], vf[(1 + (hh - 1) / 2) & 1][2 * (ks ^ 1) + 1]), __builtin_bit_cast(bf16x8, pp), o);
    if constexpr (hh == 15) {
      const u32x4 pd = {pp[0] & mk[0], pp[1] & mk[1], pp[2] & mk[2], pp[3] & mk[3]};
      o = mfma32(cat(vf[0][0], vf[0][1]), __builtin_bit_cast(bf16x8, pd), o);
      o = mfma32(cat(vf[1][0], vf[1][1]), __builtin_bit_cast(bf16x8, pp ^ pd), o);
    }
    SBAR();
    if constexpr (ks == 0) rdv((i + 1) & 1, toff(i < 8 ? i + 1 : 0));
    SBAR();
    float pv[8];
#pragma unroll
    for (int e = 0; e < 4; ++e) { pv[2 * e] = __builtin_amdgcn_exp2f(dd[e][0]); pv[2 * e + 1] = __builtin_amdgcn_exp2f(dd[e][1]); }
    SBAR();
    pp = (u32x4){pk2(pv[0], pv[1]), pk2(pv[2], pv[3]), pk2(pv[4], pv[5]), pk2(pv[6], pv[7])};
    SBAR();
  });
  {
    const u32x4 pd = {pp[0] & mk[4], pp[1] & mk[5], pp[2] & mk[6], pp[3] & mk[7]};
    o = mfma32(cat(vf[0][2], vf[0][3]), __builtin_bit_cast(bf16x8, pd), o);
    o = mfma32(cat(vf[1][2], vf[1][3]), __builtin_bit_cast(bf16x8, pp ^ pd), o);
  }
  PIN(o);
  STAMP(ws, 2, 4);
  auto lr = __builtin_amdgcn_permlane32_swap(__float_as_uint(o[10]), __float_as_uint(o[10]), false, false);
  const float inv = __builtin_amdgcn_rcpf(__uint_as_float(lr[0]));
  const rsrc_t ro = mk_rsrc(ws + WS_OF, 128 * 18 * 1024);
  const u32x4 om = {pk2(o[0] * inv, o[1] * inv), pk2(o[2] * inv, o[3] * inv), pk2(o[4] * inv, o[5] * inv), pk2(o[6] * inv, o[7] * inv)};
  bstore16<16>(om, ro, om_o, 0);
  bstore4<16>(pk2(o[8] * inv, o[9] * inv), ro, ot_o, 0);
  STAMP(ws, 2, 5);
  cnt_add(c, 1);
  if (w < 4 && c.r < 18) { p2_ahead(c); wait_vm<18>(); } else wait_vm<0>();
  set_flag(c, FL2 + (qt * 32 + head) * 64);
  STAMP(ws, 2, 6);
  STAMP_FLUSH(ws, 2);
}

DI void p3_side(const Ctx& c) {
  char* ws = c.ws;
  const int w = c.w, xcd = c.xcd, lane = fresh_lane();
  const unsigned lds0 = c.lds0;
  const int sb = c.r - 18, blk = sb * 8 + xcd;
  const rsrc_t rwt = mk_rsrc(ws + WS_WT, 4 * 4608 * 32), rlg = mk_rsrc(ws + WS_LG, 8 * 4 * 36864), rcs = mk_rsrc(ws + WS_CS, 2 * 4608 * 4);
  STAMP_DECL; STAMP(ws, 3, 0);
  const int mat = sb >= 5, un = sb < 5 ? 2 * sb : sb - 5;
  const rsrc_t rw = mk_rsrc(mat ? c.wu : c.wg, D_ * F_ * 2);
  const unsigned vo = (lane & 7) * 16 + (72 * w + (lane >> 3)) * 9216;
  u32x4 v[2][9];
#pragma unroll
  for (int i = 0; i < 9; ++i) v[0][i] = __builtin_amdgcn_raw_buffer_load_b128(rw, vo, (9 * xcd + un) * 128 + i * 8 * 9216, 0);
#pragma unroll
  for (int i = 0; i < 9; ++i) v[1][i] = __builtin_amdgcn_raw_buffer_load_b128(rw, sb < 4 ? vo : OOB, (9 * xcd + un + 1) * 128 + i * 8 * 9216, 0);
  const int drow = 64 * (blk - 40) + 8 * w + (lane >> 3);
  const u32x4 dv = __builtin_amdgcn_raw_buffer_load_b128(mk_rsrc(c.wd, F_ * D_ * 2), mat ? drow * 1152 + 1024 + (lane & 7) * 16 : OOB, 0, 0);
  if (un == 8) {
    const unsigned lo = ((lane & 7) >> 1) * 36864 + (72 * w + (lane >> 3)) * 32 + (lane & 1) * 16;
#pragma unroll
    for (int i = 0; i < 9; ++i) bstore16<16>(v[0][i], rlg, lo, (xcd * 8 + (sb >> 3)) * 18432 + i * 256);
  }
  const int pr = (drow % 576) >= 512 ? ((drow & ~24) | ((drow & 8) << 1) | ((drow & 16) >> 1)) : drow;
  bstore16<16>(dv, rwt, mat ? (((lane & 7) >> 1) * 4608 + pr) * 32 + (lane & 1) * 16 : OOB, 0);
  float a[2][8] = {};
#pragma unroll
  for (int k = 0; k < 2; ++k) {
#pragma unroll
    for (int i = 0; i < 9; ++i)
#pragma unroll
      for (int e = 0; e < 4; ++e) { a[k][2 * e] += bflo(v[k][i][e]); a[k][2 * e + 1] += bfhi(v[k][i][e]); }
#pragma unroll
    for (int e = 0; e < 8; ++e) a[k][e] = sum_x32(sum_x16(a[k][e]));
  }
  if (lane < 16) {
#pragma unroll
    for (int k = 0; k < 2; ++k) {
      ldsw<f32x4>(lds0 + k * 4096 + w * 512 + lane * 32, (f32x4){a[k][0], a[k][1], a[k][2], a[k][3]});
      ldsw<f32x4>(lds0 + k * 4096 + w * 512 + lane * 32 + 16, (f32x4){a[k][4], a[k][5], a[k][6], a[k][7]});
    }
  }
  lds_bar();
  if (w < 2) {
    float t = 0.f;
#pragma unroll
    for (int i = 0; i < 16; ++i) t += ldsr<float>(lds0 + w * 4096 + i * 256 + lane * 4);
    bstore4<16>(__float_as_uint(t), rcs, w == 0 || sb < 4 ? lane * 4 : OOB, ((sb >= 5 ? 4608 : 0) + (9 * xcd + un + w) * 64) * 4);
  }
  STAMP(ws, 3, 5);
  wait_vm<0>();
  bar();
  if (w == 0) set_flag(c, FLS + blk * 64);
  STAMP(ws, 3, 6);
  if (w == 5 && blk == 48) {
    for (int i = 0; i < 4; ++i) poll(c, lane * 64, FL1 + i * 4096);
    set_flag(c, 0);
  }
  STAMP_FLUSH(ws, 6);
}

DI void p3_gemm(const Ctx& c) {
  char* ws = c.ws;
  const int w = c.w, lane = fresh_lane();
  const unsigned lds0 = c.lds0;
  const int t16 = lane & 15, g = lane >> 4;
  const int nt = c.r >= 9 ? c.r - 9 : c.r;
  const int T = 16 * c.xcd + 8 * (w >> 2) + 4 * (c.r >= 9) + (w & 3);
  const rsrc_t rof = mk_rsrc(ws + WS_OF, 128 * 18 * 1024);
  STAMP_DECL; STAMP(ws, 3, 0);
  unsigned wa[4];
  {
    const int r2 = 4 * g + ((lane >> 2) & 3);
#pragma unroll
    for (int i = 0; i < 4; ++i) wa[i] = lds0 + P3_WO + r2 * 128 + 32 * (i ^ ((r2 >> 1) & 3)) + 8 * (lane & 3);
  }
  u32x2 xr[4];
  sfor<4>([&](auto I) { bload8(xr[I.value], (16 * T + t16) * 1152 + 128 * nt + 8 * g, mk_rsrc(c.x, S_ * D_ * 2), 32 * I.value); });
  if (w < 4) { wait_vm<5>(); cnt_add(c, 0); }
  cnt_wait(c, 0, 4);
  STAMP(ws, 3, 1);
  bf16x8 of[18];
  f32x4 acc[4] = {};
  bf16x4 fr[2][4][2];
  auto rd1 = [&](auto KS, auto I) { constexpr int ks = KS.value, i = I.value; fr[ks & 1][i >> 1][i & 1] = trh(wa[i >> 1] + (ks & 15) * 4096 + (i & 1) * 2048); };
  sfor<8>([&](auto I) { rd1(ic<0>{}, I); });
  poll(c, (lane & 31) * 64, FL2 + (T >> 1) * 2048);
  asm volatile("" : "+v"(xr[0]), "+v"(xr[1]), "+v"(xr[2]), "+v"(xr[3]));
  STAMP(ws, 3, 2);
#define P3_PRO 18
  constexpr int PRO = P3_PRO;
  auto ld = [&](auto K) { if constexpr (K.value < 18) bload16c(of[K.value], lane * 16, rof, (T * 18 + K.value) * 1024); };
  sfor<PRO>(ld);
  sfor<18>([&](auto KS) {
    constexpr int ks = KS.value, iss = PRO + ks < 18 ? PRO + ks : 18;
    asm volatile("s_waitcnt vmcnt(%1)" : "+v"(of[ks]) : "i"(iss - ks - 1) : "memory");
    if constexpr (ks == 0) STAMP(ws, 3, 3);
    if constexpr (ks == 9) STAMP(ws, 3, 4);
    if constexpr (ks == 15) sfor<4>([&](auto I) { wa[I.value] += 65536; PIN(wa[I.value]); });
    sfor<4>([&](auto C) {
      acc[C.value] = mfma16(cat(fr[ks & 1][C.value][0], fr[ks & 1][C.value][1]), of[ks], acc[C.value]);
      SBAR();
      if constexpr (ks + 1 < 18) { rd1(ic<ks + 1>{}, ic<2 * C.value>{}); rd1(ic<ks + 1>{}, ic<2 * C.value + 1>{}); }
      if constexpr (C.value == 1) ld(ic<PRO + ks>{});
      SBAR();
    });
  });
  unsigned d[4][2];
  float s1 = 0.f, s2 = 0.f;
#pragma unroll
  for (int i = 0; i < 4; ++i) {
    d[i][0] = pk2(acc[i][0] + bflo(xr[i][0]), acc[i][1] + bfhi(xr[i][0]));
    d[i][1] = pk2(acc[i][2] + bflo(xr[i][1]), acc[i][3] + bfhi(xr[i][1]));
  }
  const rsrc_t rhf = mk_rsrc(ws + WS_HF, 128 * 18 * 1024);
  bstore16<16>((u32x4){d[0][0], d[0][1], d[1][0], d[1][1]}, rhf, lane * 16, (T * 18 + 2 * nt) * 1024);
  bstore16<16>((u32x4){d[2][0], d[2][1], d[3][0], d[3][1]}, rhf, lane * 16, (T * 18 + 2 * nt + 1) * 1024);
#pragma unroll
  for (int i = 0; i < 4; ++i)
#pragma unroll
    for (int e = 0; e < 2; ++e) { const float a = bflo(d[i][e]), b = bfhi(d[i][e]); s1 += a + b; s2 += a * a + b * b; }
  s1 = sum_x32(sum_x16(s1)); s2 = sum_x32(sum_x16(s2));
  bstore8<16>((u32x2){__float_as_uint(s1), __float_as_uint(s2)}, mk_rsrc(ws + WS_ST, 9 * 2048 * 8), g == 0 ? t16 * 8 : OOB, (T * 9 + nt) * 128);
  STAMP(ws, 3, 5);
  wait_vm<0>();
  set_flag(c, FL3 + (T * 16 + nt) * 64);
  STAMP(ws, 3, 6);
  STAMP_FLUSH(ws, 3);
}

constexpr int K4_LG = P3_WO;
constexpr int K4_CS = K4_LG + 36864;
constexpr int K4_STP = K4_CS + 4096;
struct K4Op { int kind, idx; };
struct K4Ops { int n; K4Op op[12]; };
constexpr K4Ops k4_ops(int S) {
  K4Ops o = {};
  auto add = [&](int k, int i) { o.op[o.n++] = {k, i}; };
  if (S == 3) add(9, 0);
  if (S == 0) for (int u = 4; u < 8; ++u) { add(1, u); add(2, u); }
  else for (int u = 2 * S + 6; u < 2 * S + 8 && u < 36; ++u) {
    if (S == 2) { add(0, 2 * u - 12); add(0, 2 * u - 11); }
    if (S > 2) add(0, u);
    if (u < 18) { add(1, u); add(2, u); }
  }
  if (S == 4) { add(5, 0); add(5, 1); add(5, 2); }
  if (S == 6) add(6, 0);
  if (S >= 10 && S < 15) add(3, S - 10);
  if (S == 12) add(7, 0);
  if (S == 14) add(8, 0);
  if (S == 16) add(4, 0);
  return o;
}
constexpr int k4_nst(int S) { return (S == 8 || S == 9) ? 2 : S == 7 ? 1 : 0; }
constexpr int k4_issued(int S) { int n = 8; for (int s = 0; s < S; ++s) n += k4_ops(s).n + k4_nst(s); return n; }
constexpr int k4_pos(int kind, int idx) {
  if (kind == 0 && idx < 8) return -1;
  if ((kind == 1 || kind == 2) && idx < 4) return 2 * idx + kind - 1;
  for (int S = 0;; ++S) for (int i = 0; i < k4_ops(S).n; ++i) if (k4_ops(S).op[i].kind == kind && k4_ops(S).op[i].idx == idx) return k4_issued(S) + k4_nst(S) + i;
}
constexpr int k4_need(int S) {
  if (S == 8) return k4_issued(7) - 1;
  if (S >= 16) return k4_issued(15) - 1;
  const int a = S < 9 ? k4_pos(2, 2 * S + 1) : -1, b = k4_pos(0, 2 * S + 2);
  return a > b ? a : b;
}
static_assert(k4_need(5) >= k4_pos(9, 0) && k4_need(7) >= k4_pos(5, 2) && k4_need(12) >= k4_issued(10) - 1, "");
constexpr int K5_SLOT = 17408, K5_BASE = 0, K5_P = 4;
constexpr unsigned k5_so(int v) { return K5_BASE + ((v + 7) & 7) * K5_SLOT; }
DI int k5_u(int v, int nt) {
  if (v < 32) { const int q = ((v >> 2) + nt) & 3; return 18 * (q >> 1) + 8 * (q & 1) + 4 * ((v >> 1) & 1) + (v & 1) + 2 * (v >> 4); }
  const int q = (v + nt) & 3;
  return 18 * (q >> 1) + 16 + (q & 1);
}
DI unsigned k5_mv(int rho, int sp, int nt, int t16) {
  const int rr = sp ? ((rho & 7) | ((rho & 8) << 1) | ((rho & 16) >> 1)) : rho;
  return rr * 1152 + 256 * nt + 32 * ((t16 >> 1) ^ (rho & 7)) + 16 * (t16 & 1);
}
DI unsigned k5_fl(int i, int sg, int mt5) { return (((((mt5 >> 1) * 4 + ((i >> 2) & 3)) * 8 + 2 * sg + ((i >> 4) & 1)) * 8) + 4 * (mt5 & 1) + (i & 3)) * 64; }

DI void p4(const Ctx& c) {
  char* ws = c.ws;
  const int w = c.w, r = c.r, xcd = c.xcd, lane = fresh_lane();
  const int mt = r >> 2, Qd = xcd, j = r & 3;
  const unsigned lds0 = c.lds0;
  const int g = lane >> 4;
  const int T0 = 16 * mt + 2 * w;
  const rsrc_t rwm = mk_rsrc(w < 4 ? c.wg : c.wu, D_ * F_ * 2), rhf = mk_rsrc(ws + WS_HF, 128 * 18 * 1024), rwd = mk_rsrc(c.wd, F_ * D_ * 2);
  const rsrc_t rhh = mk_rsrc(ws + WS_HH, 128 * 144 * 1024);
  const unsigned f4 = FL4 + ((r * 8 + xcd) * 8 + w) * 64;
  const int sg = xcd >> 1, mt5 = 8 * (xcd & 1) + (r >> 2);
  STAMP_DECL; STAMP(ws, 4, 0);
  const unsigned wv = k4_wv(lane, w, Qd, j);
  const unsigned wdst = lds0 + K4_RING + w * 1024, l16 = lane * 16;
  poll(c, min(lane & 15, 8) * 64 + (lane & 16) * 64, FL3 + T0 * 1024);
  bf16x8 act[18][2];
  u32x2 pf;
  auto pf_off = [&] { const int z = opq(l16) >> 4; const unsigned a = opq(FL4 + k5_fl(z, sg, mt5)), b = opq(FLS + (40 + 18 * sg + min(z - 32, 17)) * 64); return z < 32 ? a : b; };
  auto doop = [&](auto KIND, auto IDX) {
    constexpr int kind = KIND.value, u = IDX.value;
    if constexpr (kind == 0) glds16(wdst + k4_so(u), wv, rwm, (u % 18) * 32 * 9216 + (u / 18) * 128);
    else if constexpr (kind <= 2) bload16c(act[u][kind - 1], l16, rhf, (T0 * 18 + 18 * (kind - 1) + u) * 1024);
    else if constexpr (kind == 3) {
      const int pc = min(w + 8 * u, 35);
      glds16c(lds0 + K4_LG + pc * 1024, l16, mk_rsrc(ws + WS_LG, 8 * 4 * 36864), (Qd * 4 + j) * 36864 + pc * 1024);
    }
    else if constexpr (kind == 4) warm(lds0 + LDS_SCR, l16 * 8, rwd, sg * 1327104 + min(r * 8 + w, 161) * 8192);
    else if constexpr (kind == 5) glds16c(lds0 + K4_STP + w * 3072 + u * 1024, l16, mk_rsrc(ws + WS_ST, 9 * 2048 * 8), T0 * 1152 + u * 1024);
    else if constexpr (kind == 6) {
      const unsigned z = opq(l16), f = (576 * Qd + (z < 512 ? 128 * j : 384 + 16 * j)) * 4 + z;
      glds16c(lds0 + K4_CS + (w & 1) * 1024, z < 576 ? f : OOB, mk_rsrc(ws + WS_CS, 2 * 4608 * 4), (w & 1) * 4608 * 4);
    } else if constexpr (kind == 7) set_flag(c, f4);
    else if constexpr (kind == 8) bload8c(pf, pf_off(), fl_rsrc(c), 0);
    else bload8c(pf, min(opq(l16), 13u * 16) * 32, fl_rsrc(c), FLS + xcd * 64);
  };
  sfor<4>([&](auto U) { doop(ic<1>{}, U); doop(ic<2>{}, U); });
  if (w < 4) cnt_add(c, 4);
  cnt_wait(c, 4, 4);
  unsigned wa[4];
  {
    const int r2 = 4 * g + ((lane >> 2) & 3);
#pragma unroll
    for (int i = 0; i < 4; ++i) wa[i] = lds0 + K4_RING + r2 * 128 + 32 * (i ^ ((r2 >> 1) & 3)) + 8 * (lane & 3);
  }
  f32x4 ag[4][2], au[4][2], cg, cu;
  bf16x4 fr[4][2];
  unsigned hd[2][2], lga, csa;
  f32x2 nmu = {0.f, 0.f};
  auto rd1 = [&](auto H, auto I) {
    constexpr int n = H.value >> 1, m = H.value & 1, i = I.value;
    if constexpr (n < 144) fr[H.value & 3][i] = trh(wa[n % 4] + k4_so(n / 4) + 4096 * m + 2048 * i);
    else if constexpr (n < 162) fr[H.value & 3][i] = trh(lga + (n - 144) * 1024 + m * 18432 + i * 512);
  };
  auto rd = [&](auto H) { rd1(H, ic<0>{}); rd1(H, ic<1>{}); };
  auto mm = [&](auto N, auto KS, f32x4& g0, f32x4& g1, f32x4& u0, f32x4& u1, auto&& hook) {
    constexpr int n = N.value, ks = KS.value;
    constexpr int h0 = n == 15 ? 999 : 2 * n + 3, h1 = (n == 14 || n == 15) ? 999 : 2 * n + 4;
    const bf16x8 fg = cat(fr[(2 * n) & 3][0], fr[(2 * n) & 3][1]);
    if constexpr (ks == 0) mfma16_z(g0, fg, act[ks][0]); else mfma16_ip(g0, fg, act[ks][0]);
    SBAR(); rd1(ic<h0>{}, ic<0>{}); SBAR(); hook(ic<0>{}); SBAR();
    if constexpr (ks == 0) mfma16_z(g1, fg, act[ks][1]); else mfma16_ip(g1, fg, act[ks][1]);
    SBAR(); rd1(ic<h0>{}, ic<1>{}); SBAR(); hook(ic<1>{}); SBAR();
    const bf16x8 fu = cat(fr[(2 * n + 1) & 3][0], fr[(2 * n + 1) & 3][1]);
    if constexpr (ks == 0) mfma16_z(u1, fu, act[ks][1]); else mfma16_ip(u1, fu, act[ks][1]);
    SBAR(); rd1(ic<h1>{}, ic<0>{}); SBAR(); hook(ic<2>{}); SBAR();
    if constexpr (ks == 0) mfma16_z(u0, fu, act[ks][0]); else mfma16_ip(u0, fu, act[ks][0]);
    SBAR(); rd1(ic<h1>{}, ic<1>{}); SBAR(); hook(ic<3>{}); SBAR();
  };
  auto ldcs = [&](int e) { cg = ldsr<f32x4>(csa + 64 * e); cu = ldsr<f32x4>(csa + 1024 + 64 * e); };
  auto epi = [&](auto E, auto I) {
    constexpr int grp = E.value >> 2, cc = E.value & 3, i = I.value, T = 1 - (i >> 1);
    if constexpr ((i & 1) == 0) {
      ag[cc][T] += cg * nmu[T]; au[cc][T] += cu * nmu[T]; PIN(ag[cc][T]); PIN(au[cc][T]);
      if constexpr (i == 2 && E.value != 3) { SBAR(); ldcs(E.value + 1); }
    }
    else {
      const f32x4 v = ag[cc][T] * au[cc][T];
      const unsigned d0 = pk2(v[0], v[1]), d1 = pk2(v[2], v[3]);
      if constexpr ((cc & 1) == 0) { hd[T][0] = d0; hd[T][1] = d1; PIN(hd[T][0]); PIN(hd[T][1]); }
      else bstore16<16>((u32x4){hd[T][0], hd[T][1], d0, d1}, rhh, l16, ((T0 + T) * 144 + 18 * Qd + 4 * j + 2 * grp + (cc >> 1)) * 1024);
    }
  };
  f32x2 sp[3];
  float rs0;
  auto stats = [&](auto TT, auto P) {
    constexpr int T = TT.value, p = P.value;
    if constexpr (p == 0) {
      const unsigned sa = lds0 + K4_STP + w * 3072 + T * 1152 + (opq(l16) >> 1);
      sp[0] = ldsr<f32x2>(sa); sp[1] = ldsr<f32x2>(sa + 512); sp[2] = ldsr<f32x2>(sa + 1024);
    } else if constexpr (p == 4) { sp[0] = sp[0] + sp[1] + (opq(l16) < 256 ? sp[2] : (f32x2){0.f, 0.f}); PIN(sp[0]); }
    else if constexpr (p == 5) { sp[0][0] = sum_x16(sp[0][0]); PIN(sp[0]); }
    else if constexpr (p == 6) { sp[0][1] = sum_x16(sp[0][1]); PIN(sp[0]); }
    else if constexpr (p == 7) { sp[0][0] = sum_x32(sp[0][0]); PIN(sp[0]); }
    else if constexpr (p == 8) { sp[0][1] = sum_x32(sp[0][1]); PIN(sp[0]); }
    else if constexpr (p == 9) {
      nmu[T] = sp[0][0] * (-1.f / D_);
      const float rs = __builtin_amdgcn_rcpf(fmaxf(sp[0][1] * (1.f / D_) - nmu[T] * nmu[T], 0.f) + 1e-5f);
      PIN(nmu);
      if constexpr (T == 0) { rs0 = rs; PIN(rs0); }
      else {
        const unsigned z = opq(l16);
        bstore4<16>(__float_as_uint((z & 256) ? rs : rs0), mk_rsrc(ws + WS_RS + (sg * 2048 + 16 * T0) * 4, (Qd & 1) == 0 && j == 0 ? 128 : 0), z >> 2, 0);
      }
    }
  };
  unsigned mv5;
  auto pf5 = [&](auto N) {
    constexpr int n = N.value, v = (n - 1) >> 1;
    if constexpr (n == 0) {
      const int vv = min(w, K5_P - 1), uu = k5_u(vv, j);
      glds16c(lds0 + K5_BASE + ((vv + 7) & 7) * K5_SLOT + 16384, l16, mk_rsrc(ws + WS_WT, 4 * 4608 * 32), (j * 144 + 36 * sg + uu) * 1024);
      const unsigned z = opq(l16);
      mv5 = k5_mv(4 * w + (z >> 8), 0, j, (z >> 4) & 15);
    } else if constexpr (n & 1) glds16(lds0 + k5_so(v) + 8192 + w * 1024, mv5, rwd, sg * 1152 * 1152 + k5_u(v, j) * 32 * 1152);
    else glds16c(lds0 + k5_so(v) + w * 1024, l16, rhh, ((8 * mt5 + w) * 144 + 36 * sg + k5_u(v, j)) * 1024);
  };
  sfor<18>([&](auto SS) {
    constexpr int S = SS.value;
    constexpr int nw = k4_issued(S) - k4_need(S) - 1;
    static_assert(nw >= 0 && nw < 64, "");
    if constexpr (S == 5) asm volatile("s_waitcnt vmcnt(%5)" : "+v"(act[2 * S][0]), "+v"(act[2 * S][1]), "+v"(act[2 * S + 1][0]), "+v"(act[2 * S + 1][1]), "+v"(pf) : "i"(nw) : "memory");
    else if constexpr (S < 9) asm volatile("s_waitcnt vmcnt(%4)" : "+v"(act[2 * S][0]), "+v"(act[2 * S][1]), "+v"(act[2 * S + 1][0]), "+v"(act[2 * S + 1][1]) : "i"(nw) : "memory");
    else if constexpr (S == 16) asm volatile("s_waitcnt vmcnt(%1)" : "+v"(pf) : "i"(nw) : "memory");
    else wait_vm<nw>();
    if constexpr (S == 16) { if (__builtin_expect(!tag_ok(c, pf), 0)) poll(c, pf_off(), 0); }
    if constexpr (S == 5) { if (__builtin_expect(!tag_ok(c, pf), 0)) poll(c, min(opq(l16), 13u * 16) * 32, FLS + xcd * 64); }
    if constexpr (S >= 2) bar();
    if constexpr (S == 0) STAMP(ws, 4, 1);
    if constexpr (S == 2) STAMP(ws, 4, 7);
    if constexpr (S == 4) STAMP(ws, 4, 2);
    if constexpr (S == 9) STAMP(ws, 4, 3);
    if constexpr (S == 17) STAMP(ws, 4, 4);
    if constexpr (S == 0 || S == 2) { rd(ic<16 * S>{}); rd(ic<16 * S + 1>{}); rd(ic<16 * S + 2>{}); }
    sfor<8>([&](auto L) {
      constexpr int l = L.value, n = 8 * S + l, t = n / 4, cc = n % 4, ks = t % 18, no = k4_ops(S).n;
      mm(ic<n>{}, ic<ks>{}, ag[cc][0], ag[cc][1], au[cc][0], au[cc][1], [&](auto I) {
        constexpr int i = I.value;
        sfor<no>([&](auto O) {
          constexpr int nd = no ? no : 1, at = O.value * 32 / nd + 16 / nd;
          if constexpr (at == 4 * l + i) doop(ic<k4_ops(S).op[O.value].kind>{}, ic<k4_ops(S).op[O.value].idx>{});
        });
        if constexpr (S == 7) stats(ic<(l >> 2)>{}, ic<4 * (l & 3) + i>{});
        if constexpr ((S == 8 || S == 17) && l == 3 && i == 0) { csa = lds0 + K4_CS + ((opq(l16) >> 4) & 48); PIN(csa); }
        if constexpr ((S == 8 || S == 17) && l == 4 && i == 0) ldcs(S == 8 ? 0 : 4);
        if constexpr (S == 17 && l == 0 && i == 0) { const unsigned z = opq(l16); lga = lds0 + K4_LG + (z >> 6) * 32 + ((z >> 1) & 24); PIN(lga); }
        if constexpr (S == 17 && l < 5 && i == 1) pf5(L);
        if constexpr (ks == 17 && cc >= 1) epi(ic<4 * (t / 18) + cc - 1>{}, I);
        if constexpr (t == 18 && cc == 0) epi(ic<3>{}, I);
      });
    });
  });
  STAMP(ws, 4, 5);
  {
    f32x4 g0, g1, u0, u1;
    sfor<18>([&](auto K) {
      constexpr int ks = K.value, n = 144 + ks;
      mm(ic<n>{}, ic<ks>{}, g0, g1, u0, u1, [&](auto I) {
        if constexpr (ks == 0) epi(ic<7>{}, I);
        if constexpr (ks >= 1 && ks <= 4 && I.value == 1) pf5(ic<4 + ks>{});
        if constexpr (ks == 10 && I.value == 1) { wait_vm<4>(); set_flag(c, f4 + 2048 * 64); }
      });
    });
    asm volatile("s_nop 9" : "+v"(g0), "+v"(g1), "+v"(u0), "+v"(u1));
    const f32x4 v0 = (g0 + cg * nmu[0]) * (u0 + cu * nmu[0]), v1 = (g1 + cg * nmu[1]) * (u1 + cu * nmu[1]);
    const unsigned a0 = pk2(v0[0], v0[1]), a1 = pk2(v0[2], v0[3]), b0 = pk2(v1[0], v1[1]), b1 = pk2(v1[2], v1[3]);
    auto s0 = __builtin_amdgcn_permlane32_swap(a0, b0, false, false);
    auto s1 = __builtin_amdgcn_permlane32_swap(a1, b1, false, false);
    const unsigned z = opq(l16);
    bstore16<16>((u32x4){s0[0], s1[0], s0[1], s1[1]}, rhh, (z >> 9) * 144 * 1024 + (z & 511) + 512 * (j & 1), (T0 * 144 + 18 * Qd + 16 + (j >> 1)) * 1024);
    STAMP(ws, 4, 6);
    STAMP_FLUSH(ws, 4);
  }
}

constexpr int k5_E(int i) { int e = 36; if (K5_P + 2 * (i + 1) < e) e = K5_P + 2 * (i + 1); if (i >= 0 && i + 8 < e) e = i + 8; return e; }
static_assert(k5_E(6) <= 16 && k5_E(20) <= 32, "");

DI void p5(const Ctx& c) {
  char* ws = c.ws;
  const int w = c.w, r = c.r, xcd = c.xcd, lane = fresh_lane();
  const int sg = xcd >> 1, mt = 8 * (xcd & 1) + (r >> 2), nt = r & 3;
  const unsigned lds0 = c.lds0;
  const int t16 = lane & 15, g = lane >> 4;
  const int tile = 4 * mt + nt;
  const rsrc_t rpt = mk_rsrc(ws + WS_PT, 64 * 8 * 4 * PT_REC);
  const unsigned f4 = FL4 + (4096 + (r * 8 + xcd) * 8 + w) * 64;
  STAMP_DECL; STAMP(ws, 5, 0);
  if (w >= 4) {
    const int l = w - 4;
    const rsrc_t rwd = mk_rsrc(c.wd, F_ * D_ * 2), rhh = mk_rsrc(ws + WS_HH, 128 * 144 * 1024);
    const rsrc_t rwt = mk_rsrc(ws + WS_WT, 4 * 4608 * 32);
    unsigned mv[2][2];
#pragma unroll
    for (int sp = 0; sp < 2; ++sp)
#pragma unroll
      for (int i = 0; i < 2; ++i) mv[sp][i] = k5_mv(4 * (2 * l + i) + g, sp, nt, t16);
    const unsigned hs = ((8 * mt + 2 * l) * 144 + 36 * sg) * 1024, wsb = sg * 1152 * 1152, dl = lds0 + l * 2048;
    auto issue = [&](auto V) {
      constexpr int v = V.value, sp = v >= 32;
      const int u = k5_u(v, nt);
      constexpr unsigned so = k5_so(v);
      glds16(dl + so + 8192, mv[sp][0], rwd, wsb + u * 32 * 1152);
      glds16c(dl + so, lane * 16, rhh, hs + u * 1024);
      glds16(dl + so + 8192 + 1024, mv[sp][1], rwd, wsb + u * 32 * 1152);
      glds16c(dl + so + 1024, lane * 16, rhh, hs + (144 + u) * 1024);
      if ((v & 3) == l) glds16c(lds0 + so + 16384, lane * 16, rwt, (nt * 144 + 36 * sg + u) * 1024);
    };
    sfor<36>([&](auto I) {
      constexpr int i = I.value, y = k5_E(i - 1) - (i + 2 < 36 ? i + 2 : 36), yy = k5_E(i - 1) - K5_P;
      if constexpr (i == 0) wait_vm<2>();
      else if constexpr (i == 2) { wait_vm<4 * yy + yy / 4>(); set_flag(c, f4); }
      else if constexpr (i + 2 > K5_P) wait_vm<4 * y + y / 4>();
      bar();
      if constexpr (i == 0) STAMP(ws, 5, 1);
      if constexpr (i == 18) STAMP(ws, 5, 2);
      sfor<k5_E(i) - k5_E(i - 1)>([&](auto U) { issue(ic<k5_E(i - 1) + U.value>{}); });
    });
    bar();
    STAMP(ws, 5, 3);
    bar();
  } else {
    const int r2 = 4 * g + ((lane >> 2) & 3);
    unsigned wa[2][10];
#pragma unroll
    for (int i = 0; i < 8; ++i) wa[0][i] = lds0 + K5_BASE + 8192 + r2 * 256 + 32 * (i ^ (r2 & 7)) + 8 * (lane & 3);
    wa[0][8] = lds0 + K5_BASE + 16384 + r2 * 32 + 8 * (lane & 3);
    wa[0][9] = lds0 + K5_BASE + w * 2048 + lane * 16;
#pragma unroll
    for (int i = 0; i < 10; ++i) { wa[1][i] = wa[0][i] + 4 * K5_SLOT; PIN(wa[0][i]); PIN(wa[1][i]); }
    const unsigned fo = k5_fl(lane & 31, sg, mt);
    f32x4 acc[9][2];
    bf16x4 fr[3][3][2];
    bf16x8 hb[2][2];
    u32x2 pf;
    auto rd1 = [&](auto N, auto I) {
      constexpr int n = N.value, u = n / 3, i = I.value;
      if constexpr (u < 36) {
        constexpr int pos = (u + 7) & 7, hi = pos >> 2, cc = 3 * (n % 3) + i / 2;
        constexpr unsigned so = (pos & 3) * K5_SLOT;
        if constexpr (i >= 6) hb[u & 1][i - 6] = ldsr<bf16x8>(wa[hi][9] + so + (i - 6) * 1024);
        else fr[n % 3][i / 2][i & 1] = trh(wa[hi][cc] + so + (i & 1) * (cc < 8 ? 4096 : 512));
      }
    };
    sfor<36>([&](auto U) {
      constexpr int u = U.value;
      if constexpr (u == 0) wait_vm<2>();
      if constexpr (u == 4) { wait_vm<0>(); set_flag(c, f4); }
      if constexpr (u == 4 || u == 18) bload8c(pf, fo, fl_rsrc(c), FL4 + (u == 4 ? 1 : 2) * 2048 * 64);
      if constexpr (u == 7 || u == 21) {
        asm volatile("s_waitcnt vmcnt(0)" : "+v"(pf)::"memory");
        if (__builtin_expect(!tag_ok(c, pf), 0)) poll(c, fo, FL4 + (u == 7 ? 1 : 2) * 2048 * 64);
      }
      bar();
      if constexpr (u == 0) STAMP(ws, 5, 1);
      if constexpr (u == 18) STAMP(ws, 5, 2);
      if constexpr (u == 0) { rd1(ic<0>{}, ic<6>{}); rd1(ic<0>{}, ic<7>{}); sfor<6>([&](auto I) { rd1(ic<0>{}, I); }); sfor<6>([&](auto I) { rd1(ic<1>{}, I); }); }
      sfor<18>([&](auto M) {
        constexpr int mi = M.value, v = mi / 6, n = 3 * u + v, cc = mi / 2;
        if constexpr (u == 0) mfma16_z(acc[cc][mi & 1], cat(fr[n % 3][cc % 3][0], fr[n % 3][cc % 3][1]), hb[u & 1][mi & 1]);
        else mfma16_ip(acc[cc][mi & 1], cat(fr[n % 3][cc % 3][0], fr[n % 3][cc % 3][1]), hb[u & 1][mi & 1]);
        SBAR();
        if constexpr (mi == 6 || mi == 8) rd1(ic<n + 2>{}, ic<6 + (mi - 6) / 2>{});
        rd1(ic<n + 2>{}, ic<mi % 6>{});
        SBAR();
      });
    });
    asm volatile("s_nop 9" : "+v"(acc[7][0]), "+v"(acc[7][1]), "+v"(acc[8][0]), "+v"(acc[8][1]));
#pragma unroll
    for (int i = 0; i < 7; ++i) asm volatile("" : "+v"(acc[i][0]), "+v"(acc[i][1]));
    bar();
    STAMP(ws, 5, 3);
    if (w == sg) {
      sfor<2>([&](auto T) { sfor<9>([&](auto C) { ldsw<f32x4>(lds0 + (T.value * 9 + C.value) * 1024 + lane * 16, acc[C.value][T.value]); }); });
      lds_bar();
    } else {
      sfor<2>([&](auto T) {
        const unsigned pb = (((tile * 4 + w) * 4 + sg) * 2 + T.value) * PT_REC;
        sfor<4>([&](auto I) {
          constexpr int i = I.value;
          const f32x4 a = acc[2 * i][T.value], b = acc[2 * i + 1][T.value];
          bstore16<16>((u32x4){pk2(a[0], a[1]), pk2(a[2], a[3]), pk2(b[0], b[1]), pk2(b[2], b[3])}, rpt, lane * 16 + i * 1024, pb);
        });
        const f32x4 a = acc[8][T.value];
        bstore8<16>((u32x2){pk2(a[0], a[1]), pk2(a[2], a[3])}, rpt, lane * 8 + 4096, pb);
      });
      bar();
      wait_vm<0>();
      set_flag(c, FL5 + ((tile * 4 + w) * 4 + sg) * 64);
    }
  }
  const int T = w >> 2, pi = w & 3, tt = 8 * mt + 2 * sg + T;
  const rsrc_t rhf = mk_rsrc(ws + WS_HF, 128 * 18 * 1024);
  u32x4 hv, pv[3];
  u32x2 hl, pl[3];
  unsigned rs2u;
  STAMP(ws, 5, 4);
  bload16c(hv, lane * 16, rhf, (tt * 18 + 4 * nt + pi) * 1024);
  bload8c(hl, lane * 16 + 8 * (nt & 1), rhf, (tt * 18 + 16 + (nt >> 1)) * 1024);
  bload4c(rs2u, t16 * 4, mk_rsrc(ws + WS_RS, 4 * 2048 * 4), (sg * 2048 + 16 * tt) * 4);
  poll(c, ((sg + 1 + min(lane, 2)) & 3) * 64, FL5 + (tile * 4 + sg) * 256);
  STAMP(ws, 5, 5);
#pragma unroll
  for (int o = 0; o < 3; ++o) {
    const unsigned so = (((tile * 4 + sg) * 4 + ((sg + 1 + o) & 3)) * 2 + T) * PT_REC;
    bload16c(pv[o], lane * 16 + pi * 1024, rpt, so);
    bload8c(pl[o], pi == 0 ? lane * 8 + 4096 : OOB, rpt, so);
  }
  const unsigned orow = (16 * tt + t16) * 1152, own = lds0 + T * 9216 + lane * 16;
  const f32x4 o0 = ldsr<f32x4>(own + pi * 2048), o1 = ldsr<f32x4>(own + pi * 2048 + 1024), o2 = ldsr<f32x4>(own + 8192);
  asm volatile("s_waitcnt vmcnt(0)" : "+v"(hv), "+v"(hl), "+v"(rs2u), "+v"(pv[0]), "+v"(pv[1]), "+v"(pv[2]), "+v"(pl[0]), "+v"(pl[1]), "+v"(pl[2])::"memory");
  STAMP(ws, 5, 6);
  const float rs2 = __uint_as_float(rs2u);
  const rsrc_t rout = mk_rsrc(c.out, S_ * D_ * 2);
  auto fin = [&](const f32x4& ow, unsigned a0, unsigned a1, unsigned b0, unsigned b1, unsigned c0, unsigned c1, unsigned h0, unsigned h1, unsigned& d0, unsigned& d1) {
    const float v0 = ((ow[0] + bflo(b0)) + (bflo(a0) + bflo(c0))) * rs2 + bflo(h0);
    const float v1 = ((ow[1] + bfhi(b0)) + (bfhi(a0) + bfhi(c0))) * rs2 + bfhi(h0);
    const float v2 = ((ow[2] + bflo(b1)) + (bflo(a1) + bflo(c1))) * rs2 + bflo(h1);
    const float v3 = ((ow[3] + bfhi(b1)) + (bfhi(a1) + bfhi(c1))) * rs2 + bfhi(h1);
    d0 = pk2(v0, v1); d1 = pk2(v2, v3);
  };
  {
    unsigned a0, a1, b0, b1;
    fin(o0, pv[0][0], pv[0][1], pv[1][0], pv[1][1], pv[2][0], pv[2][1], hv[0], hv[1], a0, a1);
    fin(o1, pv[0][2], pv[0][3], pv[1][2], pv[1][3], pv[2][2], pv[2][3], hv[2], hv[3], b0, b1);
    auto s0 = __builtin_amdgcn_permlane16_swap(a0, b0, false, false);
    auto s1 = __builtin_amdgcn_permlane16_swap(a1, b1, false, false);
    bstore16<0>((u32x4){s0[0], s1[0], s0[1], s1[1]}, rout, orow + 256 * nt + 64 * pi + 32 * (g & 1) + 16 * (g >> 1), 0);
  }
  {
    unsigned e0, e1;
    fin(o2, pl[0][0], pl[0][1], pl[1][0], pl[1][1], pl[2][0], pl[2][1], hl[0], hl[1], e0, e1);
    bstore8<0>((u32x2){e0, e1}, rout, pi == 0 ? orow + 1024 + 32 * nt + 8 * g : OOB, 0);
  }
  wait_vm<0>();
  STAMP(ws, 5, 7);
  STAMP_FLUSH(ws, 5);
}

extern "C" __global__ __launch_bounds__(512, 2) void k1_all(const void* x, const void* wqkv, const void* wo, const void* wg, const void* wu, const void* wd, char* ws, void* out) {
  Ctx c = {x, wqkv, wo, wg, wu, wd, ws, out, (int)rfl(threadIdx.x >> 6), (int)(blockIdx.x & 7), (int)(blockIdx.x >> 3), (unsigned)(size_t)smem, 0u, 0u};
  if (c.w == 7) {
    unsigned long long pc; unsigned back, size;
    asm volatile("s_getpc_b64 %0\n.Lk1pc%=:\n\ts_mov_b32 %1, .Lk1pc%=-k1_all\n\ts_mov_b32 %2, k9_end-k1_all" : "=s"(pc), "=s"(back), "=s"(size));
    const int lane = fresh_lane();
    warm(c.lds0 + LDS_SCR, lane < 24 ? (c.r * 24 + lane) * 128 : OOB, mk_rsrc((const void*)(pc - back), size), 0);
  }
  {
    const int mt = 4 * (c.xcd & 3) + (c.r & 3), nt = 8 * (c.xcd >> 2) + (c.r >> 2);
    if (c.w < 4) p1_body<0>(c, mt, nt); else p1_body<1>(c, mt, nt);
  }
  p2(c);
  if (c.w >= 4 || c.r >= 18) cnt_wait(c, 1, 8);
  if (c.w >= 4) k4_first<4>(c, fresh_lane());
  if (c.r < 18) p3_gemm(c); else p3_side(c);
  if (c.w < 4) k4_first<0>(c, fresh_lane());
  p4(c);
  p5(c);
  STAMP_OUT();
}

extern "C" __global__ void k9_end() {}

extern "C" __global__ __launch_bounds__(256) void k6_diag(char* ws) {
  __shared__ unsigned ra[256], rb[256];
  const int b = threadIdx.x;
  unsigned long long t0 = __builtin_amdgcn_s_memrealtime();
  const unsigned ref = *(GAS unsigned*)(ws + WS_DBG + ((size_t)1 * 256 * 8 * 16) * 4) - 100000u;
  unsigned a = ~0u, c = MB ? 0u : ~0u;
  const int nbb = KB == 3 ? 144 : 256;
  for (int w = 0; w < 8; ++w) {
    { unsigned v = *(GAS unsigned*)(ws + WS_DBG + ((((size_t)1 * 256 + b) * 8 + w) * 16) * 4) - ref; a = v < a ? v : a; }
    if (b < nbb) { unsigned v = *(GAS unsigned*)(ws + WS_DBG + ((((size_t)KB * 256 + b) * 8 + w) * 16 + IB) * 4) - ref; c = MB ? (v > c ? v : c) : (v < c ? v : c); }
  }
  ra[b] = a; rb[b] = c;
  __syncthreads();
  if (b == 0) {
    for (int i = 1; i < 256; ++i) { a = ra[i] < a ? ra[i] : a; c = MB ? (rb[i] > c ? rb[i] : c) : (rb[i] < c ? rb[i] : c); }
    unsigned long long d = c - a;
    if (d > 10000) d = 10000;
    if (CONSTD) d = 200;
    d *= 10;
    while (__builtin_amdgcn_s_memrealtime() - t0 < d + 300) __builtin_amdgcn_s_sleep(2);
  }
}

extern "C" void kernel_launch(void* const* d_in, const int* in_sizes, int n_in, void* d_out, int out_size, void* d_ws, size_t ws_size,
                              hipStream_t stream) {
  (void)hipFuncSetAttribute((const void*)k1_all, hipFuncAttributeMaxDynamicSharedMemorySize, LDS_ALL);
  k1_all<<<256, 512, LDS_ALL, stream>>>(d_in[0], d_in[1], d_in[2], d_in[4], d_in[5], d_in[6], (char*)d_ws, d_out);
  k6_diag<<<1, 256, 0, stream>>>((char*)d_ws);
}
```

```cpp
#define PF_SL 99
#define WD_S 10
#include <hip/hip_runtime.h>
#include <stdint.h>

typedef short bf16x8 __attribute__((ext_vector_type(8)));
typedef short bf16x4 __attribute__((ext_vector_type(4)));
typedef float f32x2 __attribute__((ext_vector_type(2)));
typedef float f32x4 __attribute__((ext_vector_type(4)));
typedef float f32x16 __attribute__((ext_vector_type(16)));
typedef unsigned u32x2 __attribute__((ext_vector_type(2)));
typedef unsigned u32x4 __attribute__((ext_vector_type(4)));
typedef __bf16 bf2_t __attribute__((ext_vector_type(2)));
typedef __amdgpu_buffer_rsrc_t rsrc_t;
#define LAS __attribute__((address_space(3)))
#define GAS __attribute__((address_space(1)))
#define DI __device__ __forceinline__

constexpr int S_ = 2048, D_ = 576, F_ = 4608;
constexpr float QSCALE = 0.23570226039551584f * 1.4426950408889634f;
constexpr unsigned ONES2 = 0x3f803f80u;

constexpr size_t QREC = 1280, KVREC = 2560;
constexpr unsigned OOB = 0x7fffff00u;
constexpr unsigned FL1 = 128;
constexpr unsigned FL2 = FL1 + 256 * 64;
constexpr unsigned FL3 = FL2 + 2048 * 64;
constexpr unsigned FLS = FL3 + 2048 * 64;
constexpr unsigned FL4 = FLS + 128 * 64;
constexpr unsigned FL5 = FL4 + 3 * 2048 * 64;
constexpr unsigned FL_END = FL5 + 1024 * 64;
constexpr size_t WS_QF = 1u << 20;
constexpr size_t WS_KV = WS_QF + 32 * 64 * QREC;
constexpr size_t WS_OF = WS_KV + 32 * 64 * KVREC;
constexpr size_t WS_HF = WS_OF + 128 * 18 * 1024;
constexpr size_t WS_ST = WS_HF + 128 * 18 * 1024;
constexpr size_t WS_CS = WS_ST + 9 * 2048 * 8;
constexpr size_t WS_RS = WS_CS + 2 * 4608 * 4;
constexpr size_t WS_LG = WS_RS + 4 * 2048 * 4;
constexpr size_t WS_WT = WS_LG + 8 * 4 * 36864;
constexpr size_t WS_HH = WS_WT + 4 * 4608 * 32;
constexpr size_t PT_REC = 4608;
constexpr size_t WS_PT = WS_HH + (size_t)128 * 144 * 1024;
constexpr size_t WS_END = WS_PT + (size_t)64 * 8 * 4 * PT_REC;

DI unsigned rfl(unsigned v) { return __builtin_amdgcn_readfirstlane(v); }
DI rsrc_t mk_rsrc(const void* p, unsigned bytes) { return __builtin_amdgcn_make_buffer_rsrc((void*)p, (short)0, (int)bytes, 0x00020000); }
DI void glds16(unsigned lds, unsigned voff, rsrc_t r, unsigned soff) {
  asm volatile("s_mov_b32 m0, %0\n\ts_nop 0\n\tbuffer_load_dwordx4 %1, %2, %3 offen lds" ::"s"(lds), "v"(voff), "s"(r), "s"(soff) : "memory", "m0");
}
DI void glds16c(unsigned lds, unsigned voff, rsrc_t r, unsigned soff) {
  asm volatile("s_mov_b32 m0, %0\n\ts_nop 0\n\tbuffer_load_dwordx4 %1, %2, %3 offen sc1 lds" ::"s"(lds), "v"(voff), "s"(r), "s"(soff) : "memory", "m0");
}
template <class T> DI void bload16c(T& d, unsigned voff, rsrc_t r, unsigned soff) {
  asm volatile("buffer_load_dwordx4 %0, %1, %2, %3 offen sc1" : "=v"(d) : "v"(voff), "s"(r), "s"(soff) : "memory");
}
DI void bload8c(u32x2& d, unsigned voff, rsrc_t r, unsigned soff) {
  asm volatile("buffer_load_dwordx2 %0, %1, %2, %3 offen sc1" : "=v"(d) : "v"(voff), "s"(r), "s"(soff) : "memory");
}
DI void bload4c(unsigned& d, unsigned voff, rsrc_t r, unsigned soff) {
  asm volatile("buffer_load_dword %0, %1, %2, %3 offen sc1" : "=v"(d) : "v"(voff), "s"(r), "s"(soff) : "memory");
}
DI void bload8(u32x2& d, unsigned voff, rsrc_t r, unsigned soff) {
  asm volatile("buffer_load_dwordx2 %0, %1, %2, %3 offen" : "=v"(d) : "v"(voff), "s"(r), "s"(soff) : "memory");
}
DI void warm(unsigned lds, unsigned voff, rsrc_t r, unsigned soff) {
  asm volatile("s_mov_b32 m0, %0\n\ts_nop 0\n\tbuffer_load_dword %1, %2, %3 offen lds" ::"s"(lds), "v"(voff), "s"(r), "s"(soff) : "memory", "m0");
}
template <int N> DI void wait_vm() { asm volatile("s_waitcnt vmcnt(%0)" ::"i"(N) : "memory"); }
#define PIN(x) asm volatile("" : "+v"(x))
#define SBAR() __builtin_amdgcn_sched_barrier(0)
DI void bar() { SBAR(); asm volatile("s_barrier" ::: "memory"); SBAR(); }
DI void lds_bar() { SBAR(); asm volatile("s_waitcnt lgkmcnt(0)\n\ts_barrier" ::: "memory"); SBAR(); }
DI bf16x8 trfrag(unsigned a0, unsigned a1) {
  bf16x4 lo = __builtin_amdgcn_ds_read_tr16_b64_v4i16((LAS bf16x4*)(size_t)a0);
  bf16x4 hi = __builtin_amdgcn_ds_read_tr16_b64_v4i16((LAS bf16x4*)(size_t)a1);
  return __builtin_shufflevector(lo, hi, 0, 1, 2, 3, 4, 5, 6, 7);
}
DI bf16x4 trh(unsigned a) { return __builtin_amdgcn_ds_read_tr16_b64_v4i16((LAS bf16x4*)(size_t)a); }
DI bf16x8 cat(bf16x4 lo, bf16x4 hi) { return __builtin_shufflevector(lo, hi, 0, 1, 2, 3, 4, 5, 6, 7); }
template <class T> DI T ldsr(unsigned a) { return *(LAS T*)(size_t)a; }
template <class T> DI void ldsw(unsigned a, T v) { *(LAS T*)(size_t)a = v; }
DI unsigned pk2(float a, float b) { f32x2 v = {a, b}; return __builtin_bit_cast(unsigned, __builtin_convertvector(v, bf2_t)); }
DI float bflo(unsigned u) { return __uint_as_float(u << 16); }
DI float bfhi(unsigned u) { return __uint_as_float(u & 0xffff0000u); }
DI f32x4 mfma16(bf16x8 a, bf16x8 b, f32x4 c) { return __builtin_amdgcn_mfma_f32_16x16x32_bf16(a, b, c, 0, 0, 0); }
DI void mfma16_ip(f32x4& c, bf16x8 a, bf16x8 b) { asm volatile("v_mfma_f32_16x16x32_bf16 %0, %1, %2, %0" : "+v"(c) : "v"(a), "v"(b)); }
DI void mfma16_z(f32x4& c, bf16x8 a, bf16x8 b) { asm volatile("v_mfma_f32_16x16x32_bf16 %0, %1, %2, 0" : "=&v"(c) : "v"(a), "v"(b)); }
DI f32x16 mfma32(bf16x8 a, bf16x8 b, f32x16 c) { return __builtin_amdgcn_mfma_f32_32x32x16_bf16(a, b, c, 0, 0, 0); }
DI float xor32(float v) { auto r = __builtin_amdgcn_permlane32_swap(__float_as_uint(v), __float_as_uint(v), false, false); return __uint_as_float((threadIdx.x & 32) ? r[0] : r[1]); }
DI float sum_x32(float v) { auto r = __builtin_amdgcn_permlane32_swap(__float_as_uint(v), __float_as_uint(v), false, false); return __uint_as_float(r[0]) + __uint_as_float(r[1]); }
DI float max_x32(float v) { auto r = __builtin_amdgcn_permlane32_swap(__float_as_uint(v), __float_as_uint(v), false, false); return fmaxf(__uint_as_float(r[0]), __uint_as_float(r[1])); }
DI float sum_x16(float v) { auto r = __builtin_amdgcn_permlane16_swap(__float_as_uint(v), __float_as_uint(v), false, false); return __uint_as_float(r[0]) + __uint_as_float(r[1]); }
template <int AUX> DI void bstore16(u32x4 v, rsrc_t r, unsigned voff, unsigned soff) {
  if constexpr (AUX == 16) asm volatile("buffer_store_dwordx4 %0, %1, %2, %3 offen sc1\n\ts_nop 1" ::"v"(v), "v"(voff), "s"(r), "s"(soff) : "memory");
  else asm volatile("buffer_store_dwordx4 %0, %1, %2, %3 offen\n\ts_nop 1" ::"v"(v), "v"(voff), "s"(r), "s"(soff) : "memory");
}
template <int AUX> DI void bstore8(u32x2 v, rsrc_t r, unsigned voff, unsigned soff) { __builtin_amdgcn_raw_buffer_store_b64(v, r, voff, soff, AUX); }
template <int AUX> DI void bstore4(unsigned v, rsrc_t r, unsigned voff, unsigned soff) { __builtin_amdgcn_raw_buffer_store_b32(v, r, voff, soff, AUX); }

template <int... I> struct iseq {};
template <int N, int... I> struct mkseq : mkseq<N - 1, N - 1, I...> {};
template <int... I> struct mkseq<0, I...> { typedef iseq<I...> type; };
template <int V> struct ic { static constexpr int value = V; };
template <class Fn, int... I> DI void sfor_(Fn&& f, iseq<I...>) { (f(ic<I>{}), ...); }
template <int N, class Fn> DI void sfor(Fn&& f) { sfor_(f, typename mkseq<N>::type{}); }

extern __shared__ __attribute__((aligned(16))) char smem[];
#define STAMP(ws, kid, i)
#define STAMP_DECL
#define STAMP_FLUSH(ws, kid)
#define STAMP_OUT()

struct Ctx {
  const void *x, *wqkv, *wo, *wg, *wu, *wd;
  char* ws; void* out;
  int w, xcd, r;
  unsigned lds0, t0, t1;
};
constexpr int LDS_ALL = 160 * 1024, LDS_SCR = LDS_ALL - 256;
DI int fresh_lane() { int l; asm volatile("v_mbcnt_lo_u32_b32 %0, -1, 0\n\tv_mbcnt_hi_u32_b32 %0, -1, %0" : "=v"(l)); __builtin_assume(l >= 0 && l < 64); return l; }
DI unsigned opq(unsigned v) { PIN(v); return v; }
constexpr int LDS_CNT = 147456;
DI void cnt_add(const Ctx& c, int i) {
  asm volatile("s_mov_b64 exec, 1\n\tds_add_u32 %0, %1\n\ts_mov_b64 exec, -1" ::"v"(c.lds0 + LDS_CNT + 4 * i), "v"(1u) : "memory");
}
DI unsigned cnt_get(const Ctx& c, int i) {
  unsigned v;
  asm volatile("ds_read_b32 %0, %1\n\ts_waitcnt lgkmcnt(0)" : "=v"(v) : "v"(c.lds0 + LDS_CNT + 4 * i) : "memory");
  return rfl(v);
}
DI void cnt_wait(const Ctx& c, int i, unsigned n) {
  if (__builtin_expect(cnt_get(c, i) < n, 0))
    for (int spin = 0; spin < (1 << 22) && cnt_get(c, i) < n; ++spin) __builtin_amdgcn_s_sleep(1);
}
DI rsrc_t fl_rsrc(const Ctx& c) { return mk_rsrc(c.ws, FL_END); }
DI void set_flag(const Ctx& c, unsigned off) {
  u32x2 t;
  asm volatile("v_mov_b32 %0, %1" : "=v"(t[0]) : "s"(c.t0));
  asm volatile("v_mov_b32 %0, %1" : "=v"(t[1]) : "s"(c.t1));
  asm volatile("s_mov_b64 exec, 1\n\tbuffer_store_dwordx2 %0, off, %1, %2 sc1\n\ts_mov_b64 exec, -1" ::"v"(t), "s"(fl_rsrc(c)), "s"(off) : "memory");
}
DI bool tag_ok(const Ctx& c, u32x2 v) { return __builtin_amdgcn_ballot_w64(v[0] != c.t0 || v[1] != c.t1) == 0; }
DI void poll(const Ctx& c, unsigned voff, unsigned soff) {
  for (int spin = 0; spin < (1 << 20); ++spin) {
    u32x2 v;
    asm volatile("buffer_load_dwordx2 %0, %1, %2, %3 offen sc1\n\ts_waitcnt vmcnt(0)" : "=&v"(v) : "v"(voff), "s"(fl_rsrc(c)), "s"(soff) : "memory");
    if (tag_ok(c, v)) break;
  }
}

constexpr int K1_SLOT = 32768, K1_TBL = K1_SLOT * 4;
extern "C" __global__ void k9_end();

template <int NN> DI void p1_body(Ctx& c, int mt, int nt) {
  char* ws = c.ws;
  const int w = c.w, lane = fresh_lane();
  const unsigned lds0 = c.lds0;
  const rsrc_t rx = mk_rsrc(c.x, S_ * D_ * 2), rw = mk_rsrc(c.wqkv, D_ * 3 * D_ * 2);
  const int t16 = lane & 15, g = lane >> 4, q4 = (lane >> 2) & 3, p = lane & 3, m = w & 3;
  constexpr int NT = NN ? 3 : 4;
  unsigned xv[2], wv[2];
#pragma unroll
  for (int i = 0; i < 2; ++i) {
    const int row = 8 * (2 * w + i) + (lane >> 3);
    xv[i] = (128 * mt + row) * 1152 + 16 * ((lane & 7) ^ ((row >> 1) & 7));
    int li = 64 * (2 * w + i) + lane;
    if (li >= 896) li -= 128;
    const int r64 = (li * 4682) >> 16, cc = li - 14 * r64;
    const int rho = r64 & 31, k = (r64 & 32) + 8 * ((rho >> 2) & 3) + 4 * (rho >> 4) + (rho & 3);
    wv[i] = k * 3456 + ((216 * nt) & ~15) + 16 * cc;
  }
  STAMP_DECL; STAMP(ws, 1, 0);
  auto issue1 = [&](int slab, int i) {
    const unsigned dst = lds0 + (slab & 3) * K1_SLOT + w * 2048 + (i >> 1) * 1024;
    if (i & 1) glds16(dst + 16384, wv[i >> 1], rw, slab * 64 * 3456);
    else glds16(dst, xv[i >> 1], rx, slab * 128);
  };
#pragma unroll
  for (int sl = 0; sl < 2; ++sl)
#pragma unroll
    for (int i = 0; i < 4; ++i) issue1(sl, i);
  u32x2 ev;
  bload8c(ev, 0u, fl_rsrc(c), 0);
  STAMP(ws, 1, 1);
  const unsigned wa = lds0 + 16384 + (4 * g + q4) * 224 + (nt & 1) * 8;
  unsigned ad[2][5];
  ad[0][0] = wa + 8 * p + 144 * NN;
  ad[0][1] = wa + 72 * (p < 3 ? p : 2) + 32;
  ad[0][2] = NN && m >= 2 ? ad[0][1] : ad[0][0] + 72 * (m >> 1) + 40 * (m & 1);
  ad[0][3] = lds0 + (32 * m + t16) * 128 + 16 * (g ^ ((t16 >> 1) & 7));
  ad[0][4] = ad[0][3] ^ 64;
#pragma unroll
  for (int i = 0; i < 5; ++i) { ad[1][i] = ad[0][i] + 2 * K1_SLOT; PIN(ad[1][i]); }
  f32x4 acc[4][2] = {}, cs = {};
  bf16x8 ones = __builtin_bit_cast(bf16x8, (u32x4){ONES2, ONES2, ONES2, ONES2});
  PIN(ones);
  bf16x8 fb[2][2];
  bf16x4 fa[2][NT + 1][2];
  constexpr int NR = 2 * NT + 4;
  auto rd1 = [&](auto KS, auto I) {
    constexpr int ks = KS.value, i = I.value, sp = ks & 1, sl = (ks >> 1) & 3, ti = (i - 2) >> 1;
    constexpr unsigned so = (sl & 1) * K1_SLOT;
    if constexpr (ks < 18 && i < NR) {
      if constexpr (i < 2) fb[sp][i] = ldsr<bf16x8>(ad[sl >> 1][3 + sp] + so + i * 2048);
      else {
        constexpr int ai = ti == NT ? 2 : (NN && ti == 2) ? 1 : 0;
        constexpr unsigned co = ai ? 0 : 72 * (ti >> 1) + 40 * (ti & 1);
        fa[sp][ti][i & 1] = trh(ad[sl >> 1][ai] + so + co + sp * 32 * 224 + (i & 1) * 16 * 224);
      }
    }
  };
  auto pfw = [&] {
    const int v = c.r * 8 + w, vi = v >= 243 ? v - 243 : v >= 162 ? v - 162 : v >= 81 ? v - 81 : v;
    const int l = vi * 64 + fresh_lane(), row = (l * 7282) >> 16;
    size_t p0 = (size_t)c.wg, p1 = (size_t)c.wu, p2 = (size_t)c.wd, p3 = (size_t)c.wo;
    asm("" : "+s"(p0), "+s"(p1), "+s"(p2), "+s"(p3));
    p0 = v >= 81 ? p1 : p0; p2 = v >= 243 ? p3 : p2;
    warm(lds0 + LDS_SCR, v >= 162 ? l * 128 : row * 9216 + (l - 9 * row) * 128, mk_rsrc((const void*)(v >= 162 ? p2 : p0), v >= 243 ? D_ * D_ * 2 : D_ * F_ * 2),
         c.xcd * (v >= 243 ? 82944 : v >= 162 ? 663552 : 1152));
  };
  sfor<9>([&](auto SL) {
    constexpr int sl = SL.value;
    constexpr int iss = sl == 0 ? 2 : (sl + 3 < 9 ? sl + 3 : 9);
    constexpr int need = sl == 0 ? 0 : (sl + 1 < 9 ? sl + 1 : 8);
    if constexpr (sl == 1) asm volatile("s_waitcnt vmcnt(%1)" : "+v"(ev) : "i"(4 * (iss - need - 1)) : "memory");
    else wait_vm<4 * (iss - need - 1) + (sl == 0)>();
    bar();
    if constexpr (sl == 0) STAMP(ws, 1, 2);
    if constexpr (sl == 4) STAMP(ws, 1, 3);
    if constexpr (sl <= 1) sfor<NR>([&](auto I) { rd1(ic<2 * sl>{}, I); });
    if constexpr (sl == 1) {
      const unsigned long long e = (((unsigned long long)rfl(ev[1]) << 32) | rfl(ev[0])) + 1ull;
      c.t0 = (unsigned)e; c.t1 = (unsigned)(e >> 32);
    }
    sfor<2>([&](auto SP) {
      constexpr int sp = SP.value, ks = 2 * sl + sp;
      bf16x8 tl;
      if constexpr (NN) tl = t16 >= 12 ? ones : cat(fa[sp][2][0], fa[sp][2][1]);
      sfor<9>([&](auto M) {
        constexpr int mi = M.value, ti = mi >> 1;
        if constexpr (mi == 8) cs = mfma16(cat(fa[sp][NT][0], fa[sp][NT][1]), ones, cs);
        else if constexpr (NN && ti == 3) acc[3][mi & 1] = mfma16(fb[sp][mi & 1], fb[sp][mi & 1], acc[3][mi & 1]);
        else if constexpr (NN && ti == 2) acc[2][mi & 1] = mfma16(tl, fb[sp][mi & 1], acc[2][mi & 1]);
        else acc[ti][mi & 1] = mfma16(cat(fa[sp][ti][0], fa[sp][ti][1]), fb[sp][mi & 1], acc[ti][mi & 1]);
        SBAR();
        if constexpr (ks != 1) {
          if constexpr (mi < 3) { rd1(ic<ks + 1>{}, ic<2 * mi>{}); rd1(ic<ks + 1>{}, ic<2 * mi + 1>{}); }
          else rd1(ic<ks + 1>{}, ic<3 + mi>{});
        }
        if constexpr (sl == 0) { if constexpr (mi & 1) issue1(2 + sp, mi >> 1); }
        else if constexpr (sl + 3 < 9 && (mi == 2 + 2 * NN || mi == 6 + 2 * NN)) issue1(sl + 3, 2 * sp + (mi >= 6));
        if constexpr (sl == PF_SL && sp == 0 && mi == 0) pfw();
        SBAR();
      });
    });
  });
  STAMP(ws, 1, 4);
  const unsigned tb = lds0 + K1_TBL, cb = tb + 1024, dummy = cb + 512;
  ldsw<f32x4>(t16 == 0 ? cb + (w * 16 + 4 * g) * 4 : dummy, cs);
  if constexpr (NN) sfor<2>([&](auto T) {
    const unsigned a = tb + (32 * m + 16 * T.value + t16) * 8;
    const f32x4 gr = acc[3][T.value];
    const float d01 = (p & 1) ? gr[1] : gr[0], d23 = (p & 1) ? gr[3] : gr[2];
    ldsw<float>(g == 3 ? a : dummy, acc[2][T.value][0]);
    ldsw<float>(g == q4 ? a + 4 : dummy, (p & 2) ? d23 : d01);
  });
  lds_bar();
  f32x2 st[2];
  sfor<2>([&](auto T) {
    const f32x2 v = ldsr<f32x2>(tb + (32 * m + 16 * T.value + t16) * 8);
    const float mu = v[0] * (1.f / D_), rs = __builtin_amdgcn_rsqf(fmaxf(v[1] * (1.f / D_) - mu * mu, 0.f) + 1e-5f);
    st[T.value] = (f32x2){rs, -rs * mu};
  });
  const rsrc_t ro = mk_rsrc(ws + WS_QF, 32 * 64 * (QREC + KVREC));
  constexpr unsigned KV0 = WS_KV - WS_QF;
  const int tile32 = 4 * mt + m, key = 16 * (g >> 1) + t16;
  const unsigned lo_qk = (g & 1) * 512 + key * 16, lo_v = 1280 + key * 32 + (g & 1) * 16;
  sfor<(NN ? 2 : 4)>([&](auto TI) {
    constexpr int ti = TI.value, tile = 4 * NN + ti;
    const int pg = 3 * nt + (tile >> 1), type = pg >> 4, rec = (2 * (pg & 15) + (tile & 1)) * 64 + tile32;
    const f32x4 cc = ldsr<f32x4>(cb + (tile * 16 + 4 * g) * 4);
    const float sc = type == 0 ? QSCALE : 1.f;
    const f32x4 v0 = acc[ti][0] * (st[0][0] * sc) + cc * (st[0][1] * sc), v1 = acc[ti][1] * (st[1][0] * sc) + cc * (st[1][1] * sc);
    auto s0 = __builtin_amdgcn_permlane32_swap(pk2(v0[0], v0[1]), pk2(v1[0], v1[1]), false, false);
    auto s1 = __builtin_amdgcn_permlane32_swap(pk2(v0[2], v0[3]), pk2(v1[2], v1[3]), false, false);
    bstore16<16>((u32x4){s0[0], s1[0], s0[1], s1[1]}, ro, type == 2 ? lo_v : lo_qk, type == 0 ? rec * QREC : KV0 + rec * KVREC);
  });
  if constexpr (NN) {
    const int pg = 3 * nt + g, type = pg >> 4, rec = 2 * (pg & 15) * 64 + tile32;
    const f32x4 cc = ldsr<f32x4>(cb + (96 + 4 * g) * 4);
    const float sc = type == 0 ? QSCALE : 1.f;
    const unsigned rb = type == 0 ? rec * QREC : KV0 + rec * KVREC, hs = type == 0 ? 64 * QREC : 64 * KVREC;
    const unsigned oq = g < 3 ? rb + (type == 2 ? 2304 : 1024) + t16 * 8 : OOB, one = type == 1 ? 0u : 0x00003f80u;
    sfor<2>([&](auto T) {
      const f32x4 v = acc[2][T.value] * (st[T.value][0] * sc) + cc * (st[T.value][1] * sc);
      const unsigned d0 = pk2(v[0], v[1]), d1 = pk2(v[2], v[3]);
      bstore8<16>((u32x2){d0, one}, ro, oq, T.value * 128); bstore8<16>((u32x2){d1, one}, ro, oq + hs, T.value * 128);
    });
  }
  STAMP(ws, 1, 5);
  wait_vm<0>();
  STAMP(ws, 1, 6);
  bar();
  if (w == 0) set_flag(c, FL1 + (mt * 16 + nt) * 64);
  STAMP(ws, 1, 7);
  STAMP_FLUSH(ws, 1);
}

constexpr int P3_WO = 73728;
constexpr int K4_RING = 8192;
constexpr unsigned k4_so(int u) { return ((((u >> 1) + 2) & 3) * 2 + (u & 1)) * 8192; }
DI unsigned k4_wv(int lane, int w, int Qd, int j) {
  const int rho = 8 * (w & 3) + (lane >> 3);
  return rho * 9216 + 1152 * Qd + 256 * j + 32 * (((lane & 7) >> 1) ^ ((rho >> 1) & 3)) + 16 * (lane & 1);
}

template <int U0> DI void k4_first(const Ctx& c, int lane) {
  const rsrc_t rg = mk_rsrc(c.wg, D_ * F_ * 2), ru = mk_rsrc(c.wu, D_ * F_ * 2);
  const unsigned wv4 = k4_wv(lane, c.w, c.xcd, c.r & 3), dst = c.lds0 + K4_RING + (c.w & 3) * 1024;
  sfor<4>([&](auto U) {
    glds16(dst + k4_so(U0 + U.value), wv4, rg, (U0 + U.value) * 32 * 9216);
    glds16(dst + 4096 + k4_so(U0 + U.value), wv4, ru, (U0 + U.value) * 32 * 9216);
  });
}
DI void p2_ahead(const Ctx& c) {
  const int w = c.w, r = c.r, lane = fresh_lane();
  const rsrc_t rwo = mk_rsrc(c.wo, D_ * D_ * 2);
  const int nt = r >= 9 ? r - 9 : r;
  const int rho = 8 * w + (lane >> 3), rg = (rho >> 2) & 3, rj = 4 * (rho >> 4) + (rho & 3);
  const unsigned cbyte = 128 * nt + 32 * (((lane & 7) >> 1) ^ ((rho >> 1) & 3)) + 16 * (lane & 1);
  const int th = 4 * rg + (rj >> 1);
  const unsigned wv = (20 * (rg >> 1) + 8 * (rg & 1) + rj) * 1152 + cbyte, wvt = (18 * th + ((th & 1) ? 0 : 16) + (rj & 1)) * 1152 + cbyte;
  sfor<16>([&](auto K) { glds16(c.lds0 + P3_WO + (w + 4 * K.value) * 1024, wv, rwo, K.value * 36 * 1152); });
  sfor<2>([&](auto K) { glds16(c.lds0 + P3_WO + (w + 64 + 4 * K.value) * 1024, wvt, rwo, K.value * 288 * 1152); });
}

DI void p2(const Ctx& c) {
  const int head = 4 * c.xcd + (c.r & 3), qb = c.r >> 2;
  char* ws = c.ws;
  const int w = c.w, lane = fresh_lane();
  const unsigned lds0 = c.lds0;
  const int q = lane & 31, h = lane >> 5, G = lane >> 4;
  const int qt = 8 * qb + w;
  const rsrc_t rq = mk_rsrc(ws + WS_QF, 32 * 64 * QREC), rkv = mk_rsrc(ws + WS_KV, 32 * 64 * KVREC);
  STAMP_DECL; STAMP(ws, 2, 0);
  unsigned kb = lds0 + w * KVREC + lane * 16, ktb = lds0 + w * KVREC + 1024 + q * 8;
  unsigned vb[4];
#pragma unroll
  for (int i = 0; i < 4; ++i) { const int row = 8 * i + 4 * (G >> 1) + ((lane >> 2) & 3); vb[i] = lds0 + w * KVREC + ((G & 1) ? 2304 + row * 8 : 1280 + row * 32 + 8 * (lane & 3)); PIN(vb[i]); }
  unsigned mf[16], mk[8];
#pragma unroll
  for (int e = 0; e < 16; ++e) { mf[e] = ((e & 3) + 8 * (e >> 2) <= q - 4 * h) ? ~0u : 0u; PIN(mf[e]); }
#pragma unroll
  for (int e = 0; e < 8; ++e) { mk[e] = (mf[2 * e] & 0xffffu) | (mf[2 * e + 1] & 0xffff0000u); PIN(mk[e]); }
  const unsigned tt = 2 * qt + (q >> 4);
  unsigned om_o = (tt * 18 + (head >> 1)) * 1024 + ((q & 15) + 16 * (2 * (head & 1) + h)) * 16;
  unsigned ot_o = h ? OOB : (tt * 18 + 16 + (head >> 4)) * 1024 + ((q & 15) + 16 * ((head >> 2) & 3)) * 16 + (head & 3) * 4;
  unsigned l16 = lane * 16, qto = h ? OOB : 1024 + q * 8, wmo;
  const int wmv = c.r * 8 + w;
  {
    const int l = (wmv >= 162 ? wmv - 162 : wmv >= 81 ? wmv - 81 : wmv) * 64 + lane, row = (l * 7282) >> 16;
    wmo = wmv >= 162 ? l * 128 : row * 9216 + c.xcd * 1152 + (l - 9 * row) * 128;
    PIN(wmo);
  }
  PIN(kb); PIN(ktb); PIN(om_o); PIN(ot_o); PIN(l16); PIN(qto);
  if (qb == 0) {
#pragma unroll
    for (int i = 0; i < 3; ++i) {
      const unsigned o = min(w * 64 + lane + 512 * i, 1279) * 16, m = (o % KVREC - 1024 < 256) ? 0x0000f149u : 0u;
      ldsw<u32x4>(lds0 + o, (u32x4){0u, m, 0u, m});
    }
  }
  if (w == 0) {
    const int ty = lane < 4 ? 1 : lane < 8 ? 2 : 0, pm = max(2 * qb + (ty ? (lane & 3) - 2 : (lane & 1)), 0);
    ldsw<unsigned>(lds0 + LDS_CNT + 4 * (lane & 7), 0u);
    poll(c, (pm * 16 + (((16 * ty + (head >> 1)) * 43) >> 7)) * 64, FL1);
  }
  lds_bar();
  STAMP(ws, 2, 7);
  bf16x8 qm; u32x2 qt2;
#pragma unroll
  for (int i = 0; i < 5; ++i) { const int pc = max(w + 8 * i, qb ? 0 : 20 + w); glds16c(lds0 + pc * 1024, l16, rkv, (head * 64 + 8 * qb - 8) * KVREC + pc * 1024); }
  bload16c(qm, l16, rq, (head * 64 + qt) * QREC);
  bload8c(qt2, qto, rq, (head * 64 + qt) * QREC);
  auto toff = [&](int i) { return (unsigned)(i * KVREC); };
  STAMP(ws, 2, 1);
  asm volatile("s_waitcnt vmcnt(0)" : "+v"(qm), "+v"(qt2)::"memory");
  bar();
  STAMP(ws, 2, 2);
  const bf16x4 qt4 = __builtin_bit_cast(bf16x4, qt2);
  bf16x8 km[9]; bf16x4 kt[9];
  constexpr int ord[9] = {0, 8, 1, 2, 3, 4, 5, 6, 7};
  sfor<9>([&](auto I) { constexpr int i = ord[I.value]; km[i] = ldsr<bf16x8>(kb + toff(i)); kt[i] = ldsr<bf16x4>(ktb + toff(i)); });
  SBAR();
  f32x16 s[9];
  float m4[4];
  auto qk = [&](auto I, auto J) {
    constexpr int i = I.value, j = J.value;
    f32x16 a = mfma32(km[i], qm, (f32x16){}), b;
    if constexpr (j < 9) b = mfma32(km[j], qm, (f32x16){});
    s[i] = __builtin_amdgcn_mfma_f32_32x32x8bf16_1k(kt[i], qt4, a, 0, 0, 0);
    if constexpr (j < 9) s[j] = __builtin_amdgcn_mfma_f32_32x32x8bf16_1k(kt[j], qt4, b, 0, 0, 0);
    SBAR();
  };
  auto mx1 = [&](auto I) {
    constexpr int i = I.value;
#pragma unroll
    for (int e = 0; e < 8; ++e) m4[e & 3] = fmaxf(fmaxf(m4[e & 3], s[i][2 * e]), s[i][2 * e + 1]);
  };
  bf16x4 vf[2][4];
  auto rdv = [&](int set, unsigned off) {
#pragma unroll
    for (int i = 0; i < 4; ++i) vf[set][i] = trh(vb[i] + off);
  };
  qk(ic<0>{}, ic<8>{});
  qk(ic<1>{}, ic<2>{});
  {
    size_t p0 = (size_t)c.wg, p1 = (size_t)c.wu, p2 = (size_t)c.wo;
    asm("" : "+s"(p0), "+s"(p1), "+s"(p2));
    p0 = wmv >= 81 ? p1 : p0;
    warm(lds0 + LDS_SCR, wmo, mk_rsrc((const void*)(wmv >= 162 ? p2 : p0), wmv >= 162 ? D_ * D_ * 2 : D_ * F_ * 2), 0);
  }
  SBAR();
#pragma unroll
  for (int e = 0; e < 16; ++e) s[8][e] = __uint_as_float((__float_as_uint(s[8][e]) & mf[e]) | (__float_as_uint(s[0][e]) & ~mf[e]));
#pragma unroll
  for (int e = 0; e < 4; ++e) asm("v_max_f32 %0, %1, %2\n\tv_max3_f32 %0, %0, %3, %4" : "=&v"(m4[e]) : "v"(s[8][4 * e]), "v"(s[8][4 * e + 1]), "v"(s[8][4 * e + 2]), "v"(s[8][4 * e + 3]));
  SBAR();
  qk(ic<3>{}, ic<4>{});
  mx1(ic<1>{}); mx1(ic<2>{}); SBAR();
  qk(ic<5>{}, ic<6>{});
  mx1(ic<3>{}); mx1(ic<4>{}); SBAR();
  qk(ic<7>{}, ic<9>{});
  rdv(1, toff(1));
  mx1(ic<5>{}); mx1(ic<6>{}); SBAR();
  mx1(ic<7>{});
  float mx = max_x32(fmaxf(fmaxf(m4[0], m4[1]), fmaxf(m4[2], m4[3])));
  STAMP(ws, 2, 3);
  f32x16 o = {};
  u32x4 pp;
  sfor<16>([&](auto H) {
    constexpr int hh = H.value, i = 1 + hh / 2, ks = hh & 1;
    f32x2 dd[4];
#pragma unroll
    for (int e = 0; e < 4; ++e) dd[e] = (f32x2){s[i][8 * ks + 2 * e], s[i][8 * ks + 2 * e + 1]} - (f32x2){mx, mx};
    SBAR();
    if constexpr (hh > 0 && hh < 15) o = mfma32(cat(vf[(1 + (hh - 1) / 2) & 1][2 * (ks ^ 1)], vf[(1 + (hh - 1) / 2) & 1][2 * (ks ^ 1) + 1]), __builtin_bit_cast(bf16x8, pp), o);
    if constexpr (hh == 15) {
      const u32x4 pd = {pp[0] & mk[0], pp[1] & mk[1], pp[2] & mk[2], pp[3] & mk[3]};
      o = mfma32(cat(vf[0][0], vf[0][1]), __builtin_bit_cast(bf16x8, pd), o);
      o = mfma32(cat(vf[1][0], vf[1][1]), __builtin_bit_cast(bf16x8, pp ^ pd), o);
    }
    SBAR();
    if constexpr (ks == 0) rdv((i + 1) & 1, toff(i < 8 ? i + 1 : 0));
    SBAR();
    float pv[8];
#pragma unroll
    for (int e = 0; e < 4; ++e) { pv[2 * e] = __builtin_amdgcn_exp2f(dd[e][0]); pv[2 * e + 1] = __builtin_amdgcn_exp2f(dd[e][1]); }
    SBAR();
    pp = (u32x4){pk2(pv[0], pv[1]), pk2(pv[2], pv[3]), pk2(pv[4], pv[5]), pk2(pv[6], pv[7])};
    SBAR();
  });
  {
    const u32x4 pd = {pp[0] & mk[4], pp[1] & mk[5], pp[2] & mk[6], pp[3] & mk[7]};
    o = mfma32(cat(vf[0][2], vf[0][3]), __builtin_bit_cast(bf16x8, pd), o);
    o = mfma32(cat(vf[1][2], vf[1][3]), __builtin_bit_cast(bf16x8, pp ^ pd), o);
  }
  STAMP(ws, 2, 4);
  auto lr = __builtin_amdgcn_permlane32_swap(__float_as_uint(o[10]), __float_as_uint(o[10]), false, false);
  const float inv = __builtin_amdgcn_rcpf(__uint_as_float(lr[0]));
  const rsrc_t ro = mk_rsrc(ws + WS_OF, 128 * 18 * 1024);
  const u32x4 om = {pk2(o[0] * inv, o[1] * inv), pk2(o[2] * inv, o[3] * inv), pk2(o[4] * inv, o[5] * inv), pk2(o[6] * inv, o[7] * inv)};
  bstore16<16>(om, ro, om_o, 0);
  bstore4<16>(pk2(o[8] * inv, o[9] * inv), ro, ot_o, 0);
  STAMP(ws, 2, 5);
  cnt_add(c, 1);
  if (w < 4 && c.r < 18) { p2_ahead(c); wait_vm<18>(); } else wait_vm<0>();
  set_flag(c, FL2 + (qt * 32 + head) * 64);
  STAMP(ws, 2, 6);
  STAMP_FLUSH(ws, 2);
}

DI void p3_side(const Ctx& c) {
  char* ws = c.ws;
  const int w = c.w, xcd = c.xcd, lane = fresh_lane();
  const unsigned lds0 = c.lds0;
  const int sb = c.r - 18, blk = sb * 8 + xcd;
  const rsrc_t rwt = mk_rsrc(ws + WS_WT, 4 * 4608 * 32), rlg = mk_rsrc(ws + WS_LG, 8 * 4 * 36864), rcs = mk_rsrc(ws + WS_CS, 2 * 4608 * 4);
  STAMP_DECL; STAMP(ws, 3, 0);
  const int mat = sb >= 5, un = sb < 5 ? 2 * sb : sb - 5;
  const rsrc_t rw = mk_rsrc(mat ? c.wu : c.wg, D_ * F_ * 2);
  const unsigned vo = (lane & 7) * 16 + (72 * w + (lane >> 3)) * 9216;
  u32x4 v[2][9];
#pragma unroll
  for (int i = 0; i < 9; ++i) v[0][i] = __builtin_amdgcn_raw_buffer_load_b128(rw, vo, (9 * xcd + un) * 128 + i * 8 * 9216, 0);
#pragma unroll
  for (int i = 0; i < 9; ++i) v[1][i] = __builtin_amdgcn_raw_buffer_load_b128(rw, sb < 4 ? vo : OOB, (9 * xcd + un + 1) * 128 + i * 8 * 9216, 0);
  const int drow = 64 * (blk - 40) + 8 * w + (lane >> 3);
  const u32x4 dv = __builtin_amdgcn_raw_buffer_load_b128(mk_rsrc(c.wd, F_ * D_ * 2), mat ? drow * 1152 + 1024 + (lane & 7) * 16 : OOB, 0, 0);
  if (un == 8) {
    const unsigned lo = ((lane & 7) >> 1) * 36864 + (72 * w + (lane >> 3)) * 32 + (lane & 1) * 16;
#pragma unroll
    for (int i = 0; i < 9; ++i) bstore16<16>(v[0][i], rlg, lo, (xcd * 8 + (sb >> 3)) * 18432 + i * 256);
  }
  const int pr = (drow % 576) >= 512 ? ((drow & ~24) | ((drow & 8) << 1) | ((drow & 16) >> 1)) : drow;
  bstore16<16>(dv, rwt, mat ? (((lane & 7) >> 1) * 4608 + pr) * 32 + (lane & 1) * 16 : OOB, 0);
  float a[2][8] = {};
#pragma unroll
  for (int k = 0; k < 2; ++k) {
#pragma unroll
    for (int i = 0; i < 9; ++i)
#pragma unroll
      for (int e = 0; e < 4; ++e) { a[k][2 * e] += bflo(v[k][i][e]); a[k][2 * e + 1] += bfhi(v[k][i][e]); }
#pragma unroll
    for (int e = 0; e < 8; ++e) a[k][e] = sum_x32(sum_x16(a[k][e]));
  }
  if (lane < 16) {
#pragma unroll
    for (int k = 0; k < 2; ++k) {
      ldsw<f32x4>(lds0 + k * 4096 + w * 512 + lane * 32, (f32x4){a[k][0], a[k][1], a[k][2], a[k][3]});
      ldsw<f32x4>(lds0 + k * 4096 + w * 512 + lane * 32 + 16, (f32x4){a[k][4], a[k][5], a[k][6], a[k][7]});
    }
  }
  lds_bar();
  if (w < 2) {
    float t = 0.f;
#pragma unroll
    for (int i = 0; i < 16; ++i) t += ldsr<float>(lds0 + w * 4096 + i * 256 + lane * 4);
    bstore4<16>(__float_as_uint(t), rcs, w == 0 || sb < 4 ? lane * 4 : OOB, ((sb >= 5 ? 4608 : 0) + (9 * xcd + un + w) * 64) * 4);
  }
  STAMP(ws, 3, 5);
  wait_vm<0>();
  bar();
  if (w == 0) set_flag(c, FLS + blk * 64);
  STAMP(ws, 3, 6);
  if (w == 5 && blk == 48) {
    for (int i = 0; i < 4; ++i) poll(c, lane * 64, FL1 + i * 4096);
    set_flag(c, 0);
  }
  STAMP_FLUSH(ws, 6);
}

DI void p3_gemm(const Ctx& c) {
  char* ws = c.ws;
  const int w = c.w, lane = fresh_lane();
  const unsigned lds0 = c.lds0;
  const int t16 = lane & 15, g = lane >> 4;
  const int nt = c.r >= 9 ? c.r - 9 : c.r;
  const int T = 16 * c.xcd + 8 * (w >> 2) + 4 * (c.r >= 9) + (w & 3);
  const rsrc_t rof = mk_rsrc(ws + WS_OF, 128 * 18 * 1024);
  STAMP_DECL; STAMP(ws, 3, 0);
  unsigned wa[4];
  {
    const int r2 = 4 * g + ((lane >> 2) & 3);
#pragma unroll
    for (int i = 0; i < 4; ++i) wa[i] = lds0 + P3_WO + r2 * 128 + 32 * (i ^ ((r2 >> 1) & 3)) + 8 * (lane & 3);
  }
  u32x2 xr[4];
  sfor<4>([&](auto I) { bload8(xr[I.value], (16 * T + t16) * 1152 + 128 * nt + 8 * g, mk_rsrc(c.x, S_ * D_ * 2), 32 * I.value); });
  if (w < 4) { wait_vm<5>(); cnt_add(c, 0); }
  cnt_wait(c, 0, 4);
  STAMP(ws, 3, 1);
  bf16x8 of[18];
  f32x4 acc[4] = {};
  bf16x4 fr[2][4][2];
  auto rd1 = [&](auto KS, auto I) { constexpr int ks = KS.value, i = I.value; fr[ks & 1][i >> 1][i & 1] = trh(wa[i >> 1] + (ks & 15) * 4096 + (i & 1) * 2048); };
  sfor<8>([&](auto I) { rd1(ic<0>{}, I); });
  poll(c, (lane & 31) * 64, FL2 + (T >> 1) * 2048);
  asm volatile("" : "+v"(xr[0]), "+v"(xr[1]), "+v"(xr[2]), "+v"(xr[3]));
  STAMP(ws, 3, 2);
#define P3_PRO 18
  constexpr int PRO = P3_PRO;
  auto ld = [&](auto K) { if constexpr (K.value < 18) bload16c(of[K.value], lane * 16, rof, (T * 18 + K.value) * 1024); };
  sfor<PRO>(ld);
  sfor<18>([&](auto KS) {
    constexpr int ks = KS.value, iss = PRO + ks < 18 ? PRO + ks : 18;
    asm volatile("s_waitcnt vmcnt(%1)" : "+v"(of[ks]) : "i"(iss - ks - 1) : "memory");
    if constexpr (ks == 0) STAMP(ws, 3, 3);
    if constexpr (ks == 9) STAMP(ws, 3, 4);
    if constexpr (ks == 15) sfor<4>([&](auto I) { wa[I.value] += 65536; PIN(wa[I.value]); });
    sfor<4>([&](auto C) {
      acc[C.value] = mfma16(cat(fr[ks & 1][C.value][0], fr[ks & 1][C.value][1]), of[ks], acc[C.value]);
      SBAR();
      if constexpr (ks + 1 < 18) { rd1(ic<ks + 1>{}, ic<2 * C.value>{}); rd1(ic<ks + 1>{}, ic<2 * C.value + 1>{}); }
      if constexpr (C.value == 1) ld(ic<PRO + ks>{});
      SBAR();
    });
  });
  unsigned d[4][2];
  float s1 = 0.f, s2 = 0.f;
#pragma unroll
  for (int i = 0; i < 4; ++i) {
    d[i][0] = pk2(acc[i][0] + bflo(xr[i][0]), acc[i][1] + bfhi(xr[i][0]));
    d[i][1] = pk2(acc[i][2] + bflo(xr[i][1]), acc[i][3] + bfhi(xr[i][1]));
  }
  const rsrc_t rhf = mk_rsrc(ws + WS_HF, 128 * 18 * 1024);
  bstore16<16>((u32x4){d[0][0], d[0][1], d[1][0], d[1][1]}, rhf, lane * 16, (T * 18 + 2 * nt) * 1024);
  bstore16<16>((u32x4){d[2][0], d[2][1], d[3][0], d[3][1]}, rhf, lane * 16, (T * 18 + 2 * nt + 1) * 1024);
#pragma unroll
  for (int i = 0; i < 4; ++i)
#pragma unroll
    for (int e = 0; e < 2; ++e) { const float a = bflo(d[i][e]), b = bfhi(d[i][e]); s1 += a + b; s2 += a * a + b * b; }
  s1 = sum_x32(sum_x16(s1)); s2 = sum_x32(sum_x16(s2));
  bstore8<16>((u32x2){__float_as_uint(s1), __float_as_uint(s2)}, mk_rsrc(ws + WS_ST, 9 * 2048 * 8), g == 0 ? t16 * 8 : OOB, (T * 9 + nt) * 128);
  STAMP(ws, 3, 5);
  wait_vm<0>();
  set_flag(c, FL3 + (T * 16 + nt) * 64);
  STAMP(ws, 3, 6);
  STAMP_FLUSH(ws, 3);
}

constexpr int K4_LG = P3_WO;
constexpr int K4_CS = K4_LG + 36864;
constexpr int K4_STP = K4_CS + 4096;
struct K4Op { int kind, idx; };
struct K4Ops { int n; K4Op op[12]; };
constexpr K4Ops k4_ops(int S) {
  K4Ops o = {};
  auto add = [&](int k, int i) { o.op[o.n++] = {k, i}; };
  if (S == 3) add(9, 0);
  if (S == 0) for (int u = 4; u < 8; ++u) { add(1, u); add(2, u); }
  else for (int u = 2 * S + 6; u < 2 * S + 8 && u < 36; ++u) {
    if (S == 2) { add(0, 2 * u - 12); add(0, 2 * u - 11); }
    if (S > 2) add(0, u);
    if (u < 18) { add(1, u); add(2, u); }
  }
  if (S == 4) { add(5, 0); add(5, 1); add(5, 2); }
  if (S == 6) add(6, 0);
  if (S >= 10 && S < 15) add(3, S - 10);
  if (S == 12) add(7, 0);
  if (S == 14) add(8, 0);
  if (S == WD_S) add(4, 0);
  return o;
}
constexpr int k4_nst(int S) { return (S == 8 || S == 9) ? 2 : S == 7 ? 1 : 0; }
constexpr int k4_issued(int S) { int n = 8; for (int s = 0; s < S; ++s) n += k4_ops(s).n + k4_nst(s); return n; }
constexpr int k4_pos(int kind, int idx) {
  if (kind == 0 && idx < 8) return -1;
  if ((kind == 1 || kind == 2) && idx < 4) return 2 * idx + kind - 1;
  for (int S = 0;; ++S) for (int i = 0; i < k4_ops(S).n; ++i) if (k4_ops(S).op[i].kind == kind && k4_ops(S).op[i].idx == idx) return k4_issued(S) + k4_nst(S) + i;
}
constexpr int k4_need(int S) {
  if (S == 8) return k4_issued(7) - 1;
  if (S >= 16) return k4_issued(15) - 1;
  const int a = S < 9 ? k4_pos(2, 2 * S + 1) : -1, b = k4_pos(0, 2 * S + 2);
  return a > b ? a : b;
}
static_assert(k4_need(5) >= k4_pos(9, 0) && k4_need(7) >= k4_pos(5, 2) && k4_need(12) >= k4_issued(10) - 1, "");
constexpr int K5_SLOT = 17408, K5_BASE = 0, K5_P = 4;
constexpr unsigned k5_so(int v) { return K5_BASE + ((v + 7) & 7) * K5_SLOT; }
DI int k5_u(int v, int nt) {
  if (v < 32) { const int q = ((v >> 2) + nt) & 3; return 18 * (q >> 1) + 8 * (q & 1) + 4 * ((v >> 1) & 1) + (v & 1) + 2 * (v >> 4); }
  const int q = (v + nt) & 3;
  return 18 * (q >> 1) + 16 + (q & 1);
}
DI unsigned k5_mv(int rho, int sp, int nt, int t16) {
  const int rr = sp ? ((rho & 7) | ((rho & 8) << 1) | ((rho & 16) >> 1)) : rho;
  return rr * 1152 + 256 * nt + 32 * ((t16 >> 1) ^ (rho & 7)) + 16 * (t16 & 1);
}
DI unsigned k5_fl(int i, int sg, int mt5) { return (((((mt5 >> 1) * 4 + ((i >> 2) & 3)) * 8 + 2 * sg + ((i >> 4) & 1)) * 8) + 4 * (mt5 & 1) + (i & 3)) * 64; }

DI void p4(const Ctx& c) {
  char* ws = c.ws;
  const int w = c.w, r = c.r, xcd = c.xcd, lane = fresh_lane();
  const int mt = r >> 2, Qd = xcd, j = r & 3;
  const unsigned lds0 = c.lds0;
  const int g = lane >> 4;
  const int T0 = 16 * mt + 2 * w;
  const rsrc_t rwm = mk_rsrc(w < 4 ? c.wg : c.wu, D_ * F_ * 2), rhf = mk_rsrc(ws + WS_HF, 128 * 18 * 1024), rwd = mk_rsrc(c.wd, F_ * D_ * 2);
  const rsrc_t rhh = mk_rsrc(ws + WS_HH, 128 * 144 * 1024);
  const unsigned f4 = FL4 + ((r * 8 + xcd) * 8 + w) * 64;
  const int sg = xcd >> 1, mt5 = 8 * (xcd & 1) + (r >> 2);
  STAMP_DECL; STAMP(ws, 4, 0);
  const unsigned wv = k4_wv(lane, w, Qd, j);
  const unsigned wdst = lds0 + K4_RING + w * 1024, l16 = lane * 16;
  poll(c, min(lane & 15, 8) * 64 + (lane & 16) * 64, FL3 + T0 * 1024);
  bf16x8 act[18][2];
  u32x2 pf;
  auto pf_off = [&] { const int z = opq(l16) >> 4; const unsigned a = opq(FL4 + k5_fl(z, sg, mt5)), b = opq(FLS + (40 + 18 * sg + min(z - 32, 17)) * 64); return z < 32 ? a : b; };
  auto doop = [&](auto KIND, auto IDX) {
    constexpr int kind = KIND.value, u = IDX.value;
    if constexpr (kind == 0) glds16(wdst + k4_so(u), wv, rwm, (u % 18) * 32 * 9216 + (u / 18) * 128);
    else if constexpr (kind <= 2) bload16c(act[u][kind - 1], l16, rhf, (T0 * 18 + 18 * (kind - 1) + u) * 1024);
    else if constexpr (kind == 3) {
      const int pc = min(w + 8 * u, 35);
      glds16c(lds0 + K4_LG + pc * 1024, l16, mk_rsrc(ws + WS_LG, 8 * 4 * 36864), (Qd * 4 + j) * 36864 + pc * 1024);
    }
    else if constexpr (kind == 4) warm(lds0 + LDS_SCR, l16 * 8, rwd, sg * 1327104 + min(r * 8 + w, 161) * 8192);
    else if constexpr (kind == 5) glds16c(lds0 + K4_STP + w * 3072 + u * 1024, l16, mk_rsrc(ws + WS_ST, 9 * 2048 * 8), T0 * 1152 + u * 1024);
    else if constexpr (kind == 6) {
      const unsigned z = opq(l16), f = (576 * Qd + (z < 512 ? 128 * j : 384 + 16 * j)) * 4 + z;
      glds16c(lds0 + K4_CS + (w & 1) * 1024, z < 576 ? f : OOB, mk_rsrc(ws + WS_CS, 2 * 4608 * 4), (w & 1) * 4608 * 4);
    } else if constexpr (kind == 7) set_flag(c, f4);
    else if constexpr (kind == 8) bload8c(pf, pf_off(), fl_rsrc(c), 0);
    else bload8c(pf, min(opq(l16), 13u * 16) * 32, fl_rsrc(c), FLS + xcd * 64);
  };
  sfor<4>([&](auto U) { doop(ic<1>{}, U); doop(ic<2>{}, U); });
  if (w < 4) cnt_add(c, 4);
  cnt_wait(c, 4, 4);
  unsigned wa[4];
  {
    const int r2 = 4 * g + ((lane >> 2) & 3);
#pragma unroll
    for (int i = 0; i < 4; ++i) wa[i] = lds0 + K4_RING + r2 * 128 + 32 * (i ^ ((r2 >> 1) & 3)) + 8 * (lane & 3);
  }
  f32x4 ag[4][2], au[4][2], cg, cu;
  bf16x4 fr[4][2];
  unsigned hd[2][2], lga, csa;
  f32x2 nmu = {0.f, 0.f};
  auto rd1 = [&](auto H, auto I) {
    constexpr int n = H.value >> 1, m = H.value & 1, i = I.value;
    if constexpr (n < 144) fr[H.value & 3][i] = trh(wa[n % 4] + k4_so(n / 4) + 4096 * m + 2048 * i);
    else if constexpr (n < 162) fr[H.value & 3][i] = trh(lga + (n - 144) * 1024 + m * 18432 + i * 512);
  };
  auto rd = [&](auto H) { rd1(H, ic<0>{}); rd1(H, ic<1>{}); };
  auto mm = [&](auto N, auto KS, f32x4& g0, f32x4& g1, f32x4& u0, f32x4& u1, auto&& hook) {
    constexpr int n = N.value, ks = KS.value;
    constexpr int h0 = n == 15 ? 999 : 2 * n + 3, h1 = (n == 14 || n == 15) ? 999 : 2 * n + 4;
    const bf16x8 fg = cat(fr[(2 * n) & 3][0], fr[(2 * n) & 3][1]);
    if constexpr (ks == 0) mfma16_z(g0, fg, act[ks][0]); else mfma16_ip(g0, fg, act[ks][0]);
    SBAR(); rd1(ic<h0>{}, ic<0>{}); SBAR(); hook(ic<0>{}); SBAR();
    if constexpr (ks == 0) mfma16_z(g1, fg, act[ks][1]); else mfma16_ip(g1, fg, act[ks][1]);
    SBAR(); rd1(ic<h0>{}, ic<1>{}); SBAR(); hook(ic<1>{}); SBAR();
    const bf16x8 fu = cat(fr[(2 * n + 1) & 3][0], fr[(2 * n + 1) & 3][1]);
    if constexpr (ks == 0) mfma16_z(u1, fu, act[ks][1]); else mfma16_ip(u1, fu, act[ks][1]);
    SBAR(); rd1(ic<h1>{}, ic<0>{}); SBAR(); hook(ic<2>{}); SBAR();
    if constexpr (ks == 0) mfma16_z(u0, fu, act[ks][0]); else mfma16_ip(u0, fu, act[ks][0]);
    SBAR(); rd1(ic<h1>{}, ic<1>{}); SBAR(); hook(ic<3>{}); SBAR();
  };
  auto ldcs = [&](int e) { cg = ldsr<f32x4>(csa + 64 * e); cu = ldsr<f32x4>(csa + 1024 + 64 * e); };
  auto epi = [&](auto E, auto I) {
    constexpr int grp = E.value >> 2, cc = E.value & 3, i = I.value, T = 1 - (i >> 1);
    if constexpr ((i & 1) == 0) {
      ag[cc][T] += cg * nmu[T]; au[cc][T] += cu * nmu[T]; PIN(ag[cc][T]); PIN(au[cc][T]);
      if constexpr (i == 2 && E.value != 3) { SBAR(); ldcs(E.value + 1); }
    }
    else {
      const f32x4 v = ag[cc][T] * au[cc][T];
      const unsigned d0 = pk2(v[0], v[1]), d1 = pk2(v[2], v[3]);
      if constexpr ((cc & 1) == 0) { hd[T][0] = d0; hd[T][1] = d1; PIN(hd[T][0]); PIN(hd[T][1]); }
      else bstore16<16>((u32x4){hd[T][0], hd[T][1], d0, d1}, rhh, l16, ((T0 + T) * 144 + 18 * Qd + 4 * j + 2 * grp + (cc >> 1)) * 1024);
    }
  };
  f32x2 sp[3];
  float rs0;
  auto stats = [&](auto TT, auto P) {
    constexpr int T = TT.value, p = P.value;
    if constexpr (p == 0) {
      const unsigned sa = lds0 + K4_STP + w * 3072 + T * 1152 + (opq(l16) >> 1);
      sp[0] = ldsr<f32x2>(sa); sp[1] = ldsr<f32x2>(sa + 512); sp[2] = ldsr<f32x2>(sa + 1024);
    } else if constexpr (p == 4) { sp[0] = sp[0] + sp[1] + (opq(l16) < 256 ? sp[2] : (f32x2){0.f, 0.f}); PIN(sp[0]); }
    else if constexpr (p == 5) { sp[0][0] = sum_x16(sp[0][0]); PIN(sp[0]); }
    else if constexpr (p == 6) { sp[0][1] = sum_x16(sp[0][1]); PIN(sp[0]); }
    else if constexpr (p == 7) { sp[0][0] = sum_x32(sp[0][0]); PIN(sp[0]); }
    else if constexpr (p == 8) { sp[0][1] = sum_x32(sp[0][1]); PIN(sp[0]); }
    else if constexpr (p == 9) {
      nmu[T] = sp[0][0] * (-1.f / D_);
      const float rs = __builtin_amdgcn_rcpf(fmaxf(sp[0][1] * (1.f / D_) - nmu[T] * nmu[T], 0.f) + 1e-5f);
      PIN(nmu);
      if constexpr (T == 0) { rs0 = rs; PIN(rs0); }
      else {
        const unsigned z = opq(l16);
        bstore4<16>(__float_as_uint((z & 256) ? rs : rs0), mk_rsrc(ws + WS_RS + (sg * 2048 + 16 * T0) * 4, (Qd & 1) == 0 && j == 0 ? 128 : 0), z >> 2, 0);
      }
    }
  };
  unsigned mv5;
  auto pf5 = [&](auto N) {
    constexpr int n = N.value, v = (n - 1) >> 1;
    if constexpr (n == 0) {
      const int vv = min(w, K5_P - 1), uu = k5_u(vv, j);
      glds16c(lds0 + K5_BASE + ((vv + 7) & 7) * K5_SLOT + 16384, l16, mk_rsrc(ws + WS_WT, 4 * 4608 * 32), (j * 144 + 36 * sg + uu) * 1024);
      const unsigned z = opq(l16);
      mv5 = k5_mv(4 * w + (z >> 8), 0, j, (z >> 4) & 15);
    } else if constexpr (n & 1) glds16(lds0 + k5_so(v) + 8192 + w * 1024, mv5, rwd, sg * 1152 * 1152 + k5_u(v, j) * 32 * 1152);
    else glds16c(lds0 + k5_so(v) + w * 1024, l16, rhh, ((8 * mt5 + w) * 144 + 36 * sg + k5_u(v, j)) * 1024);
  };
  sfor<18>([&](auto SS) {
    constexpr int S = SS.value;
    constexpr int nw = k4_issued(S) - k4_need(S) - 1;
    static_assert(nw >= 0 && nw < 64, "");
    if constexpr (S == 5) asm volatile("s_waitcnt vmcnt(%5)" : "+v"(act[2 * S][0]), "+v"(act[2 * S][1]), "+v"(act[2 * S + 1][0]), "+v"(act[2 * S + 1][1]), "+v"(pf) : "i"(nw) : "memory");
    else if constexpr (S < 9) asm volatile("s_waitcnt vmcnt(%4)" : "+v"(act[2 * S][0]), "+v"(act[2 * S][1]), "+v"(act[2 * S + 1][0]), "+v"(act[2 * S + 1][1]) : "i"(nw) : "memory");
    else if constexpr (S == 16) asm volatile("s_waitcnt vmcnt(%1)" : "+v"(pf) : "i"(nw) : "memory");
    else wait_vm<nw>();
    if constexpr (S == 16) { if (__builtin_expect(!tag_ok(c, pf), 0)) poll(c, pf_off(), 0); }
    if constexpr (S == 5) { if (__builtin_expect(!tag_ok(c, pf), 0)) poll(c, min(opq(l16), 13u * 16) * 32, FLS + xcd * 64); }
    if constexpr (S >= 2) bar();
    if constexpr (S == 0) STAMP(ws, 4, 1);
    if constexpr (S == 2) STAMP(ws, 4, 7);
    if constexpr (S == 4) STAMP(ws, 4, 2);
    if constexpr (S == 9) STAMP(ws, 4, 3);
    if constexpr (S == 17) STAMP(ws, 4, 4);
    if constexpr (S == 0 || S == 2) { rd(ic<16 * S>{}); rd(ic<16 * S + 1>{}); rd(ic<16 * S + 2>{}); }
    sfor<8>([&](auto L) {
      constexpr int l = L.value, n = 8 * S + l, t = n / 4, cc = n % 4, ks = t % 18, no = k4_ops(S).n;
      mm(ic<n>{}, ic<ks>{}, ag[cc][0], ag[cc][1], au[cc][0], au[cc][1], [&](auto I) {
        constexpr int i = I.value;
        sfor<no>([&](auto O) {
          constexpr int nd = no ? no : 1, at = O.value * 32 / nd + 16 / nd;
          if constexpr (at == 4 * l + i) doop(ic<k4_ops(S).op[O.value].kind>{}, ic<k4_ops(S).op[O.value].idx>{});
        });
        if constexpr (S == 7) stats(ic<(l >> 2)>{}, ic<4 * (l & 3) + i>{});
        if constexpr ((S == 8 || S == 17) && l == 3 && i == 0) { csa = lds0 + K4_CS + ((opq(l16) >> 4) & 48); PIN(csa); }
        if constexpr ((S == 8 || S == 17) && l == 4 && i == 0) ldcs(S == 8 ? 0 : 4);
        if constexpr (S == 17 && l == 0 && i == 0) { const unsigned z = opq(l16); lga = lds0 + K4_LG + (z >> 6) * 32 + ((z >> 1) & 24); PIN(lga); }
        if constexpr (S == 17 && l < 5 && i == 1) pf5(L);
        if constexpr (ks == 17 && cc >= 1) epi(ic<4 * (t / 18) + cc - 1>{}, I);
        if constexpr (t == 18 && cc == 0) epi(ic<3>{}, I);
      });
    });
  });
  STAMP(ws, 4, 5);
  {
    f32x4 g0, g1, u0, u1;
    sfor<18>([&](auto K) {
      constexpr int ks = K.value, n = 144 + ks;
      mm(ic<n>{}, ic<ks>{}, g0, g1, u0, u1, [&](auto I) {
        if constexpr (ks == 0) epi(ic<7>{}, I);
        if constexpr (ks >= 1 && ks <= 4 && I.value == 1) pf5(ic<4 + ks>{});
        if constexpr (ks == 10 && I.value == 1) { wait_vm<4>(); set_flag(c, f4 + 2048 * 64); }
      });
    });
    asm volatile("s_nop 9" : "+v"(g0), "+v"(g1), "+v"(u0), "+v"(u1));
    const f32x4 v0 = (g0 + cg * nmu[0]) * (u0 + cu * nmu[0]), v1 = (g1 + cg * nmu[1]) * (u1 + cu * nmu[1]);
    const unsigned a0 = pk2(v0[0], v0[1]), a1 = pk2(v0[2], v0[3]), b0 = pk2(v1[0], v1[1]), b1 = pk2(v1[2], v1[3]);
    auto s0 = __builtin_amdgcn_permlane32_swap(a0, b0, false, false);
    auto s1 = __builtin_amdgcn_permlane32_swap(a1, b1, false, false);
    const unsigned z = opq(l16);
    bstore16<16>((u32x4){s0[0], s1[0], s0[1], s1[1]}, rhh, (z >> 9) * 144 * 1024 + (z & 511) + 512 * (j & 1), (T0 * 144 + 18 * Qd + 16 + (j >> 1)) * 1024);
    STAMP(ws, 4, 6);
    STAMP_FLUSH(ws, 4);
  }
}

constexpr int k5_E(int i) { int e = 36; if (K5_P + 2 * (i + 1) < e) e = K5_P + 2 * (i + 1); if (i >= 0 && i + 8 < e) e = i + 8; return e; }
static_assert(k5_E(6) <= 16 && k5_E(20) <= 32, "");

DI void p5(const Ctx& c) {
  char* ws = c.ws;
  const int w = c.w, r = c.r, xcd = c.xcd, lane = fresh_lane();
  const int sg = xcd >> 1, mt = 8 * (xcd & 1) + (r >> 2), nt = r & 3;
  const unsigned lds0 = c.lds0;
  const int t16 = lane & 15, g = lane >> 4;
  const int tile = 4 * mt + nt;
  const rsrc_t rpt = mk_rsrc(ws + WS_PT, 64 * 8 * 4 * PT_REC);
  const unsigned f4 = FL4 + (4096 + (r * 8 + xcd) * 8 + w) * 64;
  STAMP_DECL; STAMP(ws, 5, 0);
  if (w >= 4) {
    const int l = w - 4;
    const rsrc_t rwd = mk_rsrc(c.wd, F_ * D_ * 2), rhh = mk_rsrc(ws + WS_HH, 128 * 144 * 1024);
    const rsrc_t rwt = mk_rsrc(ws + WS_WT, 4 * 4608 * 32);
    unsigned mv[2][2];
#pragma unroll
    for (int sp = 0; sp < 2; ++sp)
#pragma unroll
      for (int i = 0; i < 2; ++i) mv[sp][i] = k5_mv(4 * (2 * l + i) + g, sp, nt, t16);
    const unsigned hs = ((8 * mt + 2 * l) * 144 + 36 * sg) * 1024, wsb = sg * 1152 * 1152, dl = lds0 + l * 2048;
    auto issue = [&](auto V) {
      constexpr int v = V.value, sp = v >= 32;
      const int u = k5_u(v, nt);
      constexpr unsigned so = k5_so(v);
      glds16(dl + so + 8192, mv[sp][0], rwd, wsb + u * 32 * 1152);
      glds16c(dl + so, lane * 16, rhh, hs + u * 1024);
      glds16(dl + so + 8192 + 1024, mv[sp][1], rwd, wsb + u * 32 * 1152);
      glds16c(dl + so + 1024, lane * 16, rhh, hs + (144 + u) * 1024);
      if ((v & 3) == l) glds16c(lds0 + so + 16384, lane * 16, rwt, (nt * 144 + 36 * sg + u) * 1024);
    };
    sfor<36>([&](auto I) {
      constexpr int i = I.value, y = k5_E(i - 1) - (i + 2 < 36 ? i + 2 : 36), yy = k5_E(i - 1) - K5_P;
      if constexpr (i == 0) wait_vm<2>();
      else if constexpr (i == 2) { wait_vm<4 * yy + yy / 4>(); set_flag(c, f4); }
      else if constexpr (i + 2 > K5_P) wait_vm<4 * y + y / 4>();
      bar();
      if constexpr (i == 0) STAMP(ws, 5, 1);
      if constexpr (i == 18) STAMP(ws, 5, 2);
      sfor<k5_E(i) - k5_E(i - 1)>([&](auto U) { issue(ic<k5_E(i - 1) + U.value>{}); });
    });
    bar();
    STAMP(ws, 5, 3);
    bar();
  } else {
    const int r2 = 4 * g + ((lane >> 2) & 3);
    unsigned wa[2][10];
#pragma unroll
    for (int i = 0; i < 8; ++i) wa[0][i] = lds0 + K5_BASE + 8192 + r2 * 256 + 32 * (i ^ (r2 & 7)) + 8 * (lane & 3);
    wa[0][8] = lds0 + K5_BASE + 16384 + r2 * 32 + 8 * (lane & 3);
    wa[0][9] = lds0 + K5_BASE + w * 2048 + lane * 16;
#pragma unroll
    for (int i = 0; i < 10; ++i) { wa[1][i] = wa[0][i] + 4 * K5_SLOT; PIN(wa[0][i]); PIN(wa[1][i]); }
    const unsigned fo = k5_fl(lane & 31, sg, mt);
    f32x4 acc[9][2];
    bf16x4 fr[3][3][2];
    bf16x8 hb[2][2];
    u32x2 pf;
    auto rd1 = [&](auto N, auto I) {
      constexpr int n = N.value, u = n / 3, i = I.value;
      if constexpr (u < 36) {
        constexpr int pos = (u + 7) & 7, hi = pos >> 2, cc = 3 * (n % 3) + i / 2;
        constexpr unsigned so = (pos & 3) * K5_SLOT;
        if constexpr (i >= 6) hb[u & 1][i - 6] = ldsr<bf16x8>(wa[hi][9] + so + (i - 6) * 1024);
        else fr[n % 3][i / 2][i & 1] = trh(wa[hi][cc] + so + (i & 1) * (cc < 8 ? 4096 : 512));
      }
    };
    sfor<36>([&](auto U) {
      constexpr int u = U.value;
      if constexpr (u == 0) wait_vm<2>();
      if constexpr (u == 4) { wait_vm<0>(); set_flag(c, f4); }
      if constexpr (u == 4 || u == 18) bload8c(pf, fo, fl_rsrc(c), FL4 + (u == 4 ? 1 : 2) * 2048 * 64);
      if constexpr (u == 7 || u == 21) {
        asm volatile("s_waitcnt vmcnt(0)" : "+v"(pf)::"memory");
        if (__builtin_expect(!tag_ok(c, pf), 0)) poll(c, fo, FL4 + (u == 7 ? 1 : 2) * 2048 * 64);
      }
      bar();
      if constexpr (u == 0) STAMP(ws, 5, 1);
      if constexpr (u == 18) STAMP(ws, 5, 2);
      if constexpr (u == 0) { rd1(ic<0>{}, ic<6>{}); rd1(ic<0>{}, ic<7>{}); sfor<6>([&](auto I) { rd1(ic<0>{}, I); }); sfor<6>([&](auto I) { rd1(ic<1>{}, I); }); }
      sfor<18>([&](auto M) {
        constexpr int mi = M.value, v = mi / 6, n = 3 * u + v, cc = mi / 2;
        if constexpr (u == 0) mfma16_z(acc[cc][mi & 1], cat(fr[n % 3][cc % 3][0], fr[n % 3][cc % 3][1]), hb[u & 1][mi & 1]);
        else mfma16_ip(acc[cc][mi & 1], cat(fr[n % 3][cc % 3][0], fr[n % 3][cc % 3][1]), hb[u & 1][mi & 1]);
        SBAR();
        if constexpr (mi == 6 || mi == 8) rd1(ic<n + 2>{}, ic<6 + (mi - 6) / 2>{});
        rd1(ic<n + 2>{}, ic<mi % 6>{});
        SBAR();
      });
    });
    asm volatile("s_nop 9" : "+v"(acc[7][0]), "+v"(acc[7][1]), "+v"(acc[8][0]), "+v"(acc[8][1]));
#pragma unroll
    for (int i = 0; i < 7; ++i) asm volatile("" : "+v"(acc[i][0]), "+v"(acc[i][1]));
    bar();
    STAMP(ws, 5, 3);
    if (w == sg) {
      sfor<2>([&](auto T) { sfor<9>([&](auto C) { ldsw<f32x4>(lds0 + (T.value * 9 + C.value) * 1024 + lane * 16, acc[C.value][T.value]); }); });
      lds_bar();
    } else {
      sfor<2>([&](auto T) {
        const unsigned pb = (((tile * 4 + w) * 4 + sg) * 2 + T.value) * PT_REC;
        sfor<4>([&](auto I) {
          constexpr int i = I.value;
          const f32x4 a = acc[2 * i][T.value], b = acc[2 * i + 1][T.value];
          bstore16<16>((u32x4){pk2(a[0], a[1]), pk2(a[2], a[3]), pk2(b[0], b[1]), pk2(b[2], b[3])}, rpt, lane * 16 + i * 1024, pb);
        });
        const f32x4 a = acc[8][T.value];
        bstore8<16>((u32x2){pk2(a[0], a[1]), pk2(a[2], a[3])}, rpt, lane * 8 + 4096, pb);
      });
      bar();
      wait_vm<0>();
      set_flag(c, FL5 + ((tile * 4 + w) * 4 + sg) * 64);
    }
  }
  const int T = w >> 2, pi = w & 3, tt = 8 * mt + 2 * sg + T;
  const rsrc_t rhf = mk_rsrc(ws + WS_HF, 128 * 18 * 1024);
  u32x4 hv, pv[3];
  u32x2 hl, pl[3];
  unsigned rs2u;
  STAMP(ws, 5, 4);
  bload16c(hv, lane * 16, rhf, (tt * 18 + 4 * nt + pi) * 1024);
  bload8c(hl, lane * 16 + 8 * (nt & 1), rhf, (tt * 18 + 16 + (nt >> 1)) * 1024);
  bload4c(rs2u, t16 * 4, mk_rsrc(ws + WS_RS, 4 * 2048 * 4), (sg * 2048 + 16 * tt) * 4);
  poll(c, ((sg + 1 + min(lane, 2)) & 3) * 64, FL5 + (tile * 4 + sg) * 256);
  STAMP(ws, 5, 5);
#pragma unroll
  for (int o = 0; o < 3; ++o) {
    const unsigned so = (((tile * 4 + sg) * 4 + ((sg + 1 + o) & 3)) * 2 + T) * PT_REC;
    bload16c(pv[o], lane * 16 + pi * 1024, rpt, so);
    bload8c(pl[o], pi == 0 ? lane * 8 + 4096 : OOB, rpt, so);
  }
  const unsigned orow = (16 * tt + t16) * 1152, own = lds0 + T * 9216 + lane * 16;
  const f32x4 o0 = ldsr<f32x4>(own + pi * 2048), o1 = ldsr<f32x4>(own + pi * 2048 + 1024), o2 = ldsr<f32x4>(own + 8192);
  asm volatile("s_waitcnt vmcnt(0)" : "+v"(hv), "+v"(hl), "+v"(rs2u), "+v"(pv[0]), "+v"(pv[1]), "+v"(pv[2]), "+v"(pl[0]), "+v"(pl[1]), "+v"(pl[2])::"memory");
  STAMP(ws, 5, 6);
  const float rs2 = __uint_as_float(rs2u);
  const rsrc_t rout = mk_rsrc(c.out, S_ * D_ * 2);
  auto fin = [&](const f32x4& ow, unsigned a0, unsigned a1, unsigned b0, unsigned b1, unsigned c0, unsigned c1, unsigned h0, unsigned h1, unsigned& d0, unsigned& d1) {
    const float v0 = ((ow[0] + bflo(b0)) + (bflo(a0) + bflo(c0))) * rs2 + bflo(h0);
    const float v1 = ((ow[1] + bfhi(b0)) + (bfhi(a0) + bfhi(c0))) * rs2 + bfhi(h0);
    const float v2 = ((ow[2] + bflo(b1)) + (bflo(a1) + bflo(c1))) * rs2 + bflo(h1);
    const float v3 = ((ow[3] + bfhi(b1)) + (bfhi(a1) + bfhi(c1))) * rs2 + bfhi(h1);
    d0 = pk2(v0, v1); d1 = pk2(v2, v3);
  };
  {
    unsigned a0, a1, b0, b1;
    fin(o0, pv[0][0], pv[0][1], pv[1][0], pv[1][1], pv[2][0], pv[2][1], hv[0], hv[1], a0, a1);
    fin(o1, pv[0][2], pv[0][3], pv[1][2], pv[1][3], pv[2][2], pv[2][3], hv[2], hv[3], b0, b1);
    auto s0 = __builtin_amdgcn_permlane16_swap(a0, b0, false, false);
    auto s1 = __builtin_amdgcn_permlane16_swap(a1, b1, false, false);
    bstore16<0>((u32x4){s0[0], s1[0], s0[1], s1[1]}, rout, orow + 256 * nt + 64 * pi + 32 * (g & 1) + 16 * (g >> 1), 0);
  }
  {
    unsigned e0, e1;
    fin(o2, pl[0][0], pl[0][1], pl[1][0], pl[1][1], pl[2][0], pl[2][1], hl[0], hl[1], e0, e1);
    bstore8<0>((u32x2){e0, e1}, rout, pi == 0 ? orow + 1024 + 32 * nt + 8 * g : OOB, 0);
  }
  STAMP(ws, 5, 7);
  STAMP_FLUSH(ws, 5);
}

extern "C" __global__ __launch_bounds__(512, 2) void k1_all(const void* x, const void* wqkv, const void* wo, const void* wg, const void* wu, const void* wd, char* ws, void* out) {
  Ctx c = {x, wqkv, wo, wg, wu, wd, ws, out, (int)rfl(threadIdx.x >> 6), (int)(blockIdx.x & 7), (int)(blockIdx.x >> 3), (unsigned)(size_t)smem, 0u, 0u};
  if (c.w == 7) {
    unsigned long long pc; unsigned back, size;
    asm volatile("s_getpc_b64 %0\n.Lk1pc%=:\n\ts_mov_b32 %1, .Lk1pc%=-k1_all\n\ts_mov_b32 %2, k9_end-k1_all" : "=s"(pc), "=s"(back), "=s"(size));
    const int lane = fresh_lane();
    warm(c.lds0 + LDS_SCR, lane < 24 ? (c.r * 24 + lane) * 128 : OOB, mk_rsrc((const void*)(pc - back), size), 0);
  }
  {
    const int mt = 4 * (c.xcd & 3) + (c.r & 3), nt = 8 * (c.xcd >> 2) + (c.r >> 2);
    if (c.w < 4) p1_body<0>(c, mt, nt); else p1_body<1>(c, mt, nt);
  }
  p2(c);
  if (c.w >= 4 || c.r >= 18) cnt_wait(c, 1, 8);
  if (c.w >= 4) k4_first<4>(c, fresh_lane());
  if (c.r < 18) p3_gemm(c); else p3_side(c);
  if (c.w < 4) k4_first<0>(c, fresh_lane());
  p4(c);
  p5(c);
  STAMP_OUT();
}

extern "C" __global__ void k9_end() {}

extern "C" void kernel_launch(void* const* d_in, const int* in_sizes, int n_in, void* d_out, int out_size, void* d_ws, size_t ws_size,
                              hipStream_t stream) {
  (void)hipFuncSetAttribute((const void*)k1_all, hipFuncAttributeMaxDynamicSharedMemorySize, LDS_ALL);
  k1_all<<<256, 512, LDS_ALL, stream>>>(d_in[0], d_in[1], d_in[2], d_in[4], d_in[5], d_in[6], (char*)d_ws, d_out);
}
```

```cpp
#define WD_S 15
#include <hip/hip_runtime.h>
#include <stdint.h>

typedef short bf16x8 __attribute__((ext_vector_type(8)));
typedef short bf16x4 __attribute__((ext_vector_type(4)));
typedef float f32x2 __attribute__((ext_vector_type(2)));
typedef float f32x4 __attribute__((ext_vector_type(4)));
typedef float f32x16 __attribute__((ext_vector_type(16)));
typedef unsigned u32x2 __attribute__((ext_vector_type(2)));
typedef unsigned u32x4 __attribute__((ext_vector_type(4)));
typedef __bf16 bf2_t __attribute__((ext_vector_type(2)));
typedef __amdgpu_buffer_rsrc_t rsrc_t;
#define LAS __attribute__((address_space(3)))
#define GAS __attribute__((address_space(1)))
#define DI __device__ __forceinline__

constexpr int S_ = 2048, D_ = 576, F_ = 4608;
constexpr float QSCALE = 0.23570226039551584f * 1.4426950408889634f;
constexpr unsigned ONES2 = 0x3f803f80u;

constexpr size_t QREC = 1280, KVREC = 2560;
constexpr unsigned OOB = 0x7fffff00u;
#define FS1 64
#define FS2 64
#define FS3 64
#define FS5 64
constexpr unsigned FL1 = 128;
constexpr unsigned FL2 = FL1 + 256 * 64;
constexpr unsigned FL3 = FL2 + 2048 * 64;
constexpr unsigned FLS = FL3 + 2048 * 64;
constexpr unsigned FL4 = FLS + 128 * 64;
constexpr unsigned FL5 = FL4 + 3 * 2048 * 64;
constexpr unsigned FL_END = FL5 + 1024 * 64;
constexpr size_t WS_QF = 1u << 20;
constexpr size_t WS_KV = WS_QF + 32 * 64 * QREC;
constexpr size_t WS_OF = WS_KV + 32 * 64 * KVREC;
constexpr size_t WS_HF = WS_OF + 128 * 18 * 1024;
constexpr size_t WS_ST = WS_HF + 128 * 18 * 1024;
constexpr size_t WS_CS = WS_ST + 9 * 2048 * 8;
constexpr size_t WS_RS = WS_CS + 2 * 4608 * 4;
constexpr size_t WS_LG = WS_RS + 4 * 2048 * 4;
constexpr size_t WS_WT = WS_LG + 8 * 4 * 36864;
constexpr size_t WS_HH = WS_WT + 4 * 4608 * 32;
constexpr size_t PT_REC = 4608;
constexpr size_t WS_PT = WS_HH + (size_t)128 * 144 * 1024;
constexpr size_t WS_END = WS_PT + (size_t)64 * 8 * 4 * PT_REC;

DI unsigned rfl(unsigned v) { return __builtin_amdgcn_readfirstlane(v); }
DI rsrc_t mk_rsrc(const void* p, unsigned bytes) { return __builtin_amdgcn_make_buffer_rsrc((void*)p, (short)0, (int)bytes, 0x00020000); }
DI void glds16(unsigned lds, unsigned voff, rsrc_t r, unsigned soff) {
  asm volatile("s_mov_b32 m0, %0\n\ts_nop 0\n\tbuffer_load_dwordx4 %1, %2, %3 offen lds" ::"s"(lds), "v"(voff), "s"(r), "s"(soff) : "memory", "m0");
}
DI void glds16c(unsigned lds, unsigned voff, rsrc_t r, unsigned soff) {
  asm volatile("s_mov_b32 m0, %0\n\ts_nop 0\n\tbuffer_load_dwordx4 %1, %2, %3 offen sc1 lds" ::"s"(lds), "v"(voff), "s"(r), "s"(soff) : "memory", "m0");
}
template <class T> DI void bload16c(T& d, unsigned voff, rsrc_t r, unsigned soff) {
  asm volatile("buffer_load_dwordx4 %0, %1, %2, %3 offen sc1" : "=v"(d) : "v"(voff), "s"(r), "s"(soff) : "memory");
}
DI void bload8c(u32x2& d, unsigned voff, rsrc_t r, unsigned soff) {
  asm volatile("buffer_load_dwordx2 %0, %1, %2, %3 offen sc1" : "=v"(d) : "v"(voff), "s"(r), "s"(soff) : "memory");
}
DI void bload4c(unsigned& d, unsigned voff, rsrc_t r, unsigned soff) {
  asm volatile("buffer_load_dword %0, %1, %2, %3 offen sc1" : "=v"(d) : "v"(voff), "s"(r), "s"(soff) : "memory");
}
DI void bload8(u32x2& d, unsigned voff, rsrc_t r, unsigned soff) {
  asm volatile("buffer_load_dwordx2 %0, %1, %2, %3 offen" : "=v"(d) : "v"(voff), "s"(r), "s"(soff) : "memory");
}
DI void warm(unsigned lds, unsigned voff, rsrc_t r, unsigned soff) {
  asm volatile("s_mov_b32 m0, %0\n\ts_nop 0\n\tbuffer_load_dword %1, %2, %3 offen lds" ::"s"(lds), "v"(voff), "s"(r), "s"(soff) : "memory", "m0");
}
template <int N> DI void wait_vm() { asm volatile("s_waitcnt vmcnt(%0)" ::"i"(N) : "memory"); }
#define PIN(x) asm volatile("" : "+v"(x))
#define SBAR() __builtin_amdgcn_sched_barrier(0)
DI void bar() { SBAR(); asm volatile("s_barrier" ::: "memory"); SBAR(); }
DI void lds_bar() { SBAR(); asm volatile("s_waitcnt lgkmcnt(0)\n\ts_barrier" ::: "memory"); SBAR(); }
DI bf16x8 trfrag(unsigned a0, unsigned a1) {
  bf16x4 lo = __builtin_amdgcn_ds_read_tr16_b64_v4i16((LAS bf16x4*)(size_t)a0);
  bf16x4 hi = __builtin_amdgcn_ds_read_tr16_b64_v4i16((LAS bf16x4*)(size_t)a1);
  return __builtin_shufflevector(lo, hi, 0, 1, 2, 3, 4, 5, 6, 7);
}
DI bf16x4 trh(unsigned a) { return __builtin_amdgcn_ds_read_tr16_b64_v4i16((LAS bf16x4*)(size_t)a); }
DI bf16x8 cat(bf16x4 lo, bf16x4 hi) { return __builtin_shufflevector(lo, hi, 0, 1, 2, 3, 4, 5, 6, 7); }
template <class T> DI T ldsr(unsigned a) { return *(LAS T*)(size_t)a; }
template <class T> DI void ldsw(unsigned a, T v) { *(LAS T*)(size_t)a = v; }
DI unsigned pk2(float a, float b) { f32x2 v = {a, b}; return __builtin_bit_cast(unsigned, __builtin_convertvector(v, bf2_t)); }
DI float bflo(unsigned u) { return __uint_as_float(u << 16); }
DI float bfhi(unsigned u) { return __uint_as_float(u & 0xffff0000u); }
DI f32x4 mfma16(bf16x8 a, bf16x8 b, f32x4 c) { return __builtin_amdgcn_mfma_f32_16x16x32_bf16(a, b, c, 0, 0, 0); }
DI void mfma16_ip(f32x4& c, bf16x8 a, bf16x8 b) { asm volatile("v_mfma_f32_16x16x32_bf16 %0, %1, %2, %0" : "+v"(c) : "v"(a), "v"(b)); }
DI void mfma16_z(f32x4& c, bf16x8 a, bf16x8 b) { asm volatile("v_mfma_f32_16x16x32_bf16 %0, %1, %2, 0" : "=&v"(c) : "v"(a), "v"(b)); }
DI f32x16 mfma32(bf16x8 a, bf16x8 b, f32x16 c) { return __builtin_amdgcn_mfma_f32_32x32x16_bf16(a, b, c, 0, 0, 0); }
DI float xor32(float v) { auto r = __builtin_amdgcn_permlane32_swap(__float_as_uint(v), __float_as_uint(v), false, false); return __uint_as_float((threadIdx.x & 32) ? r[0] : r[1]); }
DI float sum_x32(float v) { auto r = __builtin_amdgcn_permlane32_swap(__float_as_uint(v), __float_as_uint(v), false, false); return __uint_as_float(r[0]) + __uint_as_float(r[1]); }
DI float max_x32(float v) { auto r = __builtin_amdgcn_permlane32_swap(__float_as_uint(v), __float_as_uint(v), false, false); return fmaxf(__uint_as_float(r[0]), __uint_as_float(r[1])); }
DI float sum_x16(float v) { auto r = __builtin_amdgcn_permlane16_swap(__float_as_uint(v), __float_as_uint(v), false, false); return __uint_as_float(r[0]) + __uint_as_float(r[1]); }
template <int AUX> DI void bstore16(u32x4 v, rsrc_t r, unsigned voff, unsigned soff) {
  if constexpr (AUX == 16) asm volatile("buffer_store_dwordx4 %0, %1, %2, %3 offen sc1\n\ts_nop 1" ::"v"(v), "v"(voff), "s"(r), "s"(soff) : "memory");
  else asm volatile("buffer_store_dwordx4 %0, %1, %2, %3 offen\n\ts_nop 1" ::"v"(v), "v"(voff), "s"(r), "s"(soff) : "memory");
}
template <int AUX> DI void bstore8(u32x2 v, rsrc_t r, unsigned voff, unsigned soff) { __builtin_amdgcn_raw_buffer_store_b64(v, r, voff, soff, AUX); }
template <int AUX> DI void bstore4(unsigned v, rsrc_t r, unsigned voff, unsigned soff) { __builtin_amdgcn_raw_buffer_store_b32(v, r, voff, soff, AUX); }

template <int... I> struct iseq {};
template <int N, int... I> struct mkseq : mkseq<N - 1, N - 1, I...> {};
template <int... I> struct mkseq<0, I...> { typedef iseq<I...> type; };
template <int V> struct ic { static constexpr int value = V; };
template <class Fn, int... I> DI void sfor_(Fn&& f, iseq<I...>) { (f(ic<I>{}), ...); }
template <int N, class Fn> DI void sfor(Fn&& f) { sfor_(f, typename mkseq<N>::type{}); }

extern __shared__ __attribute__((aligned(16))) char smem[];
#define STAMP(ws, kid, i)
#define STAMP_DECL
#define STAMP_FLUSH(ws, kid)
#define STAMP_OUT()

struct Ctx {
  const void *x, *wqkv, *wo, *wg, *wu, *wd;
  char* ws; void* out;
  int w, xcd, r;
  unsigned lds0, t0, t1;
};
constexpr int LDS_ALL = 160 * 1024, LDS_SCR = LDS_ALL - 256;
DI int fresh_lane() { int l; asm volatile("v_mbcnt_lo_u32_b32 %0, -1, 0\n\tv_mbcnt_hi_u32_b32 %0, -1, %0" : "=v"(l)); __builtin_assume(l >= 0 && l < 64); return l; }
DI unsigned opq(unsigned v) { PIN(v); return v; }
constexpr int LDS_CNT = 147456;
DI void cnt_add(const Ctx& c, int i) {
  asm volatile("s_mov_b64 exec, 1\n\tds_add_u32 %0, %1\n\ts_mov_b64 exec, -1" ::"v"(c.lds0 + LDS_CNT + 4 * i), "v"(1u) : "memory");
}
DI unsigned cnt_get(const Ctx& c, int i) {
  unsigned v;
  asm volatile("ds_read_b32 %0, %1\n\ts_waitcnt lgkmcnt(0)" : "=v"(v) : "v"(c.lds0 + LDS_CNT + 4 * i) : "memory");
  return rfl(v);
}
DI void cnt_wait(const Ctx& c, int i, unsigned n) {
  if (__builtin_expect(cnt_get(c, i) < n, 0))
    for (int spin = 0; spin < (1 << 22) && cnt_get(c, i) < n; ++spin) __builtin_amdgcn_s_sleep(1);
}
DI rsrc_t fl_rsrc(const Ctx& c) { return mk_rsrc(c.ws, FL_END); }
DI void set_flag(const Ctx& c, unsigned off) {
  u32x2 t;
  asm volatile("v_mov_b32 %0, %1" : "=v"(t[0]) : "s"(c.t0));
  asm volatile("v_mov_b32 %0, %1" : "=v"(t[1]) : "s"(c.t1));
  asm volatile("s_mov_b64 exec, 1\n\tbuffer_store_dwordx2 %0, off, %1, %2 sc1\n\ts_mov_b64 exec, -1" ::"v"(t), "s"(fl_rsrc(c)), "s"(off) : "memory");
}
DI bool tag_ok(const Ctx& c, u32x2 v) { return __builtin_amdgcn_ballot_w64(v[0] != c.t0 || v[1] != c.t1) == 0; }
DI void poll(const Ctx& c, unsigned voff, unsigned soff) {
  for (int spin = 0; spin < (1 << 20); ++spin) {
    u32x2 v;
    asm volatile("buffer_load_dwordx2 %0, %1, %2, %3 offen sc1\n\ts_waitcnt vmcnt(0)" : "=&v"(v) : "v"(voff), "s"(fl_rsrc(c)), "s"(soff) : "memory");
    if (tag_ok(c, v)) break;
  }
}

constexpr int K1_SLOT = 32768, K1_TBL = K1_SLOT * 4;
extern "C" __global__ void k9_end();

template <int NN> DI void p1_body(Ctx& c, int mt, int nt) {
  char* ws = c.ws;
  const int w = c.w, lane = fresh_lane();
  const unsigned lds0 = c.lds0;
  const rsrc_t rx = mk_rsrc(c.x, S_ * D_ * 2), rw = mk_rsrc(c.wqkv, D_ * 3 * D_ * 2);
  const int t16 = lane & 15, g = lane >> 4, q4 = (lane >> 2) & 3, p = lane & 3, m = w & 3;
  constexpr int NT = NN ? 3 : 4;
  unsigned xv[2], wv[2];
#pragma unroll
  for (int i = 0; i < 2; ++i) {
    const int row = 8 * (2 * w + i) + (lane >> 3);
    xv[i] = (128 * mt + row) * 1152 + 16 * ((lane & 7) ^ ((row >> 1) & 7));
    int li = 64 * (2 * w + i) + lane;
    if (li >= 896) li -= 128;
    const int r64 = (li * 4682) >> 16, cc = li - 14 * r64;
    const int rho = r64 & 31, k = (r64 & 32) + 8 * ((rho >> 2) & 3) + 4 * (rho >> 4) + (rho & 3);
    wv[i] = k * 3456 + ((216 * nt) & ~15) + 16 * cc;
  }
  STAMP_DECL; STAMP(ws, 1, 0);
  auto issue1 = [&](int slab, int i) {
    const unsigned dst = lds0 + (slab & 3) * K1_SLOT + w * 2048 + (i >> 1) * 1024;
    if (i & 1) glds16(dst + 16384, wv[i >> 1], rw, slab * 64 * 3456);
    else glds16(dst, xv[i >> 1], rx, slab * 128);
  };
#pragma unroll
  for (int sl = 0; sl < 2; ++sl)
#pragma unroll
    for (int i = 0; i < 4; ++i) issue1(sl, i);
  u32x2 ev;
  bload8c(ev, 0u, fl_rsrc(c), 0);
  STAMP(ws, 1, 1);
  const unsigned wa = lds0 + 16384 + (4 * g + q4) * 224 + (nt & 1) * 8;
  unsigned ad[2][5];
  ad[0][0] = wa + 8 * p + 144 * NN;
  ad[0][1] = wa + 72 * (p < 3 ? p : 2) + 32;
  ad[0][2] = NN && m >= 2 ? ad[0][1] : ad[0][0] + 72 * (m >> 1) + 40 * (m & 1);
  ad[0][3] = lds0 + (32 * m + t16) * 128 + 16 * (g ^ ((t16 >> 1) & 7));
  ad[0][4] = ad[0][3] ^ 64;
#pragma unroll
  for (int i = 0; i < 5; ++i) { ad[1][i] = ad[0][i] + 2 * K1_SLOT; PIN(ad[1][i]); }
  const unsigned tb = lds0 + K1_TBL, cb = tb + 1024, dummy = cb + 512;
  unsigned e_cs = t16 == 0 ? cb + (w * 16 + 4 * g) * 4 : dummy, e_st = tb + (32 * m + t16) * 8, e_cc = cb + (64 * NN + 4 * g) * 4;
  unsigned e_s1 = g == 3 ? e_st : dummy, e_s2 = g == q4 ? e_st + 4 : dummy, e_vo[2], e_so[4], e_t2 = 0, e_one = 0;
  unsigned e_sc[2];
  float e_tsc = 0.f;
  {
    constexpr unsigned KV0 = WS_KV - WS_QF;
    const int tile32 = 4 * mt + m, key = 16 * (g >> 1) + t16;
    const unsigned lo_qk = (g & 1) * 512 + key * 16, lo_v = 1280 + key * 32 + (g & 1) * 16;
#pragma unroll
    for (int i = 0; i < 2 - NN; ++i) {
      const int pg = 3 * nt + 2 * NN + i, type = pg >> 4, rl = type ? KVREC : QREC;
      e_so[2 * i] = (type ? KV0 : 0) + (2 * (pg & 15) * 64 + tile32) * rl;
      e_so[2 * i + 1] = e_so[2 * i] + 64 * rl;
      e_vo[i] = type == 2 ? lo_v : lo_qk;
      e_sc[i] = type ? 0x3f800000u : __builtin_bit_cast(unsigned, QSCALE);
      asm volatile("" : "+s"(e_so[2 * i]), "+s"(e_so[2 * i + 1]), "+s"(e_sc[i]), "+v"(e_vo[i]));
    }
    if constexpr (NN) {
      const int pg = 3 * nt + g, type = pg >> 4, rl = type ? KVREC : QREC;
      e_vo[1] = g < 3 ? (type ? KV0 : 0) + (2 * (pg & 15) * 64 + tile32) * rl + (type == 2 ? 2304 : 1024) + t16 * 8 : OOB;
      e_t2 = e_vo[1] + 64 * rl;
      e_one = type == 1 ? 0u : 0x00003f80u;
      e_tsc = type ? 1.f : QSCALE;
      PIN(e_vo[1]); PIN(e_t2); PIN(e_one); PIN(e_tsc); PIN(e_s1); PIN(e_s2);
    }
    PIN(e_cs); PIN(e_st); PIN(e_cc);
  }
  f32x4 acc[4][2] = {}, cs = {};
  bf16x8 ones = __builtin_bit_cast(bf16x8, (u32x4){ONES2, ONES2, ONES2, ONES2});
  PIN(ones);
  bf16x8 fb[2][2];
  bf16x4 fa[2][NT + 1][2];
  constexpr int NR = 2 * NT + 4;
  auto rd1 = [&](auto KS, auto I) {
    constexpr int ks = KS.value, i = I.value, sp = ks & 1, sl = (ks >> 1) & 3, ti = (i - 2) >> 1;
    constexpr unsigned so = (sl & 1) * K1_SLOT;
    if constexpr (ks < 18 && i < NR) {
      if constexpr (i < 2) fb[sp][i] = ldsr<bf16x8>(ad[sl >> 1][3 + sp] + so + i * 2048);
      else {
        constexpr int ai = ti == NT ? 2 : (NN && ti == 2) ? 1 : 0;
        constexpr unsigned co = ai ? 0 : 72 * (ti >> 1) + 40 * (ti & 1);
        fa[sp][ti][i & 1] = trh(ad[sl >> 1][ai] + so + co + sp * 32 * 224 + (i & 1) * 16 * 224);
      }
    }
  };
  sfor<9>([&](auto SL) {
    constexpr int sl = SL.value;
    constexpr int iss = sl == 0 ? 2 : (sl + 3 < 9 ? sl + 3 : 9);
    constexpr int need = sl == 0 ? 0 : (sl + 1 < 9 ? sl + 1 : 8);
    if constexpr (sl == 1) asm volatile("s_waitcnt vmcnt(%1)" : "+v"(ev) : "i"(4 * (iss - need - 1)) : "memory");
    else wait_vm<4 * (iss - need - 1) + (sl == 0)>();
    bar();
    if constexpr (sl == 0) STAMP(ws, 1, 2);
    if constexpr (sl == 4) STAMP(ws, 1, 3);
    if constexpr (sl <= 1) sfor<NR>([&](auto I) { rd1(ic<2 * sl>{}, I); });
    if constexpr (sl == 1) {
      const unsigned long long e = (((unsigned long long)rfl(ev[1]) << 32) | rfl(ev[0])) + 1ull;
      c.t0 = (unsigned)e; c.t1 = (unsigned)(e >> 32);
    }
    sfor<2>([&](auto SP) {
      constexpr int sp = SP.value, ks = 2 * sl + sp;
      bf16x8 tl;
      if constexpr (NN) tl = t16 >= 12 ? ones : cat(fa[sp][2][0], fa[sp][2][1]);
      sfor<9>([&](auto M) {
        constexpr int mi = M.value, ti = mi >> 1;
        if constexpr (mi == 8) cs = mfma16(cat(fa[sp][NT][0], fa[sp][NT][1]), ones, cs);
        else if constexpr (NN && ti == 3) acc[3][mi & 1] = mfma16(fb[sp][mi & 1], fb[sp][mi & 1], acc[3][mi & 1]);
        else if constexpr (NN && ti == 2) acc[2][mi & 1] = mfma16(tl, fb[sp][mi & 1], acc[2][mi & 1]);
        else acc[ti][mi & 1] = mfma16(cat(fa[sp][ti][0], fa[sp][ti][1]), fb[sp][mi & 1], acc[ti][mi & 1]);
        SBAR();
        if constexpr (ks != 1) {
          if constexpr (mi < 3) { rd1(ic<ks + 1>{}, ic<2 * mi>{}); rd1(ic<ks + 1>{}, ic<2 * mi + 1>{}); }
          else rd1(ic<ks + 1>{}, ic<3 + mi>{});
        }
        if constexpr (sl == 0) { if constexpr (mi & 1) issue1(2 + sp, mi >> 1); }
        else if constexpr (sl + 3 < 9 && (mi == 2 + 2 * NN || mi == 6 + 2 * NN)) issue1(sl + 3, 2 * sp + (mi >= 6));
        SBAR();
      });
    });
  });
  STAMP(ws, 1, 4);
  ldsw<f32x4>(e_cs, cs);
  if constexpr (NN) sfor<2>([&](auto T) {
    const f32x4 gr = acc[3][T.value];
    const float d01 = (p & 1) ? gr[1] : gr[0], d23 = (p & 1) ? gr[3] : gr[2];
    ldsw<float>(e_s1 + 128 * T.value, acc[2][T.value][0]);
    ldsw<float>(e_s2 + 128 * T.value, (p & 2) ? d23 : d01);
  });
  lds_bar();
  f32x2 sv[2];
  f32x4 cc[4];
  sfor<2>([&](auto T) { sv[T.value] = ldsr<f32x2>(e_st + 128 * T.value); });
  sfor<NT>([&](auto I) { cc[I.value] = ldsr<f32x4>(e_cc + 64 * I.value); });
  SBAR();
  float nmu[2], rs[2];
  sfor<2>([&](auto T) {
    nmu[T.value] = sv[T.value][0] * (-1.f / D_);
    rs[T.value] = __builtin_amdgcn_rsqf(fmaxf(sv[T.value][1] * (1.f / D_) - nmu[T.value] * nmu[T.value], 0.f) + 1e-5f);
  });
  const rsrc_t ro = mk_rsrc(ws + WS_QF, 32 * 64 * (QREC + KVREC));
  sfor<(NN ? 2 : 4)>([&](auto TI) {
    constexpr int ti = TI.value;
    const f32x4 v0 = (acc[ti][0] + cc[ti] * nmu[0]) * (rs[0] * __uint_as_float(e_sc[ti >> 1])), v1 = (acc[ti][1] + cc[ti] * nmu[1]) * (rs[1] * __uint_as_float(e_sc[ti >> 1]));
    auto s0 = __builtin_amdgcn_permlane32_swap(pk2(v0[0], v0[1]), pk2(v1[0], v1[1]), false, false);
    auto s1 = __builtin_amdgcn_permlane32_swap(pk2(v0[2], v0[3]), pk2(v1[2], v1[3]), false, false);
    bstore16<16>((u32x4){s0[0], s1[0], s0[1], s1[1]}, ro, e_vo[ti >> 1], e_so[ti]);
  });
  if constexpr (NN) sfor<2>([&](auto T) {
    const f32x4 v = (acc[2][T.value] + cc[2] * nmu[T.value]) * (rs[T.value] * e_tsc);
    bstore8<16>((u32x2){pk2(v[0], v[1]), e_one}, ro, e_vo[1], T.value * 128);
    bstore8<16>((u32x2){pk2(v[2], v[3]), e_one}, ro, e_t2, T.value * 128);
  });
  STAMP(ws, 1, 5);
  wait_vm<0>();
  STAMP(ws, 1, 6);
  bar();
  if (w == 0) set_flag(c, FL1 + (mt * 16 + nt) * FS1);
  STAMP(ws, 1, 7);
  STAMP_FLUSH(ws, 1);
}

constexpr int P3_WO = 73728;
constexpr int K4_RING = 8192;
constexpr unsigned k4_so(int u) { return ((((u >> 1) + 2) & 3) * 2 + (u & 1)) * 8192; }
DI unsigned k4_wv(int lane, int w, int Qd, int j) {
  const int rho = 8 * (w & 3) + (lane >> 3);
  return rho * 9216 + 1152 * Qd + 256 * j + 32 * (((lane & 7) >> 1) ^ ((rho >> 1) & 3)) + 16 * (lane & 1);
}

template <int U0> DI void k4_first(const Ctx& c, int lane) {
  const rsrc_t rg = mk_rsrc(c.wg, D_ * F_ * 2), ru = mk_rsrc(c.wu, D_ * F_ * 2);
  const unsigned wv4 = k4_wv(lane, c.w, c.xcd, c.r & 3), dst = c.lds0 + K4_RING + (c.w & 3) * 1024;
  sfor<4>([&](auto U) {
    glds16(dst + k4_so(U0 + U.value), wv4, rg, (U0 + U.value) * 32 * 9216);
    glds16(dst + 4096 + k4_so(U0 + U.value), wv4, ru, (U0 + U.value) * 32 * 9216);
  });
}
DI void p2_ahead(const Ctx& c) {
  const int w = c.w, r = c.r, lane = fresh_lane();
  const rsrc_t rwo = mk_rsrc(c.wo, D_ * D_ * 2);
  const int nt = r >= 9 ? r - 9 : r;
  const int rho = 8 * w + (lane >> 3), rg = (rho >> 2) & 3, rj = 4 * (rho >> 4) + (rho & 3);
  const unsigned cbyte = 128 * nt + 32 * (((lane & 7) >> 1) ^ ((rho >> 1) & 3)) + 16 * (lane & 1);
  const int th = 4 * rg + (rj >> 1);
  const unsigned wv = (20 * (rg >> 1) + 8 * (rg & 1) + rj) * 1152 + cbyte, wvt = (18 * th + ((th & 1) ? 0 : 16) + (rj & 1)) * 1152 + cbyte;
  sfor<16>([&](auto K) { glds16(c.lds0 + P3_WO + (w + 4 * K.value) * 1024, wv, rwo, K.value * 36 * 1152); });
  sfor<2>([&](auto K) { glds16(c.lds0 + P3_WO + (w + 64 + 4 * K.value) * 1024, wvt, rwo, K.value * 288 * 1152); });
}

DI void p2(const Ctx& c) {
  const int head = 4 * c.xcd + (c.r & 3), qb = c.r >> 2;
  char* ws = c.ws;
  const int w = c.w, lane = fresh_lane();
  const unsigned lds0 = c.lds0;
  const int q = lane & 31, h = lane >> 5, G = lane >> 4;
  const int qt = 8 * qb + w;
  const rsrc_t rq = mk_rsrc(ws + WS_QF, 32 * 64 * QREC), rkv = mk_rsrc(ws + WS_KV, 32 * 64 * KVREC);
  STAMP_DECL; STAMP(ws, 2, 0);
  unsigned kb = lds0 + w * KVREC + lane * 16, ktb = lds0 + w * KVREC + 1024 + q * 8;
  unsigned vb[4];
#pragma unroll
  for (int i = 0; i < 4; ++i) { const int row = 8 * i + 4 * (G >> 1) + ((lane >> 2) & 3); vb[i] = lds0 + w * KVREC + ((G & 1) ? 2304 + row * 8 : 1280 + row * 32 + 8 * (lane & 3)); PIN(vb[i]); }
  unsigned mf[16], mk[8];
#pragma unroll
  for (int e = 0; e < 16; ++e) { mf[e] = ((e & 3) + 8 * (e >> 2) <= q - 4 * h) ? ~0u : 0u; PIN(mf[e]); }
#pragma unroll
  for (int e = 0; e < 8; ++e) { mk[e] = (mf[2 * e] & 0xffffu) | (mf[2 * e + 1] & 0xffff0000u); PIN(mk[e]); }
  const unsigned tt = 2 * qt + (q >> 4);
  unsigned om_o = (tt * 18 + (head >> 1)) * 1024 + ((q & 15) + 16 * (2 * (head & 1) + h)) * 16;
  unsigned ot_o = h ? OOB : (tt * 18 + 16 + (head >> 4)) * 1024 + ((q & 15) + 16 * ((head >> 2) & 3)) * 16 + (head & 3) * 4;
  unsigned l16 = lane * 16, qto = h ? OOB : 1024 + q * 8, wmo;
  const int wmv = c.r * 8 + w;
  {
    const int l = (wmv >= 162 ? wmv - 162 : wmv >= 81 ? wmv - 81 : wmv) * 64 + lane, row = (l * 7282) >> 16;
    wmo = wmv >= 162 ? l * 128 : row * 9216 + c.xcd * 1152 + (l - 9 * row) * 128;
    PIN(wmo);
  }
  PIN(kb); PIN(ktb); PIN(om_o); PIN(ot_o); PIN(l16); PIN(qto);
  if (qb == 0) {
#pragma unroll
    for (int i = 0; i < 3; ++i) {
      const unsigned o = min(w * 64 + lane + 512 * i, 1279) * 16, m = (o % KVREC - 1024 < 256) ? 0x0000f149u : 0u;
      ldsw<u32x4>(lds0 + o, (u32x4){0u, m, 0u, m});
    }
  }
#define TSL 0
  if (TSL && w) __builtin_amdgcn_s_sleep(TSL);
  if (w >= 1 && w <= 3) warm(lds0 + LDS_SCR, (w * 64 + lane < 224 ? (w * 64 + lane - 64) * 128 : OOB), rkv, (head * 64 + 8 * qb) * KVREC);
  if (w == 4 || w == 5) warm(lds0 + LDS_SCR, (w * 64 + lane < 336 ? (w * 64 + lane - 256) * 128 : OOB), rq, (head * 64 + 8 * qb) * QREC);
  if (w == 0) {
    const int ty = lane < 4 ? 1 : lane < 8 ? 2 : 0, pm = max(2 * qb + (ty ? (lane & 3) - 2 : (lane & 1)), 0);
    ldsw<unsigned>(lds0 + LDS_CNT + 4 * (lane & 7), 0u);
    poll(c, (pm * 16 + (((16 * ty + (head >> 1)) * 43) >> 7)) * FS1, FL1);
  }
  lds_bar();
  STAMP(ws, 2, 7);
  bf16x8 qm; u32x2 qt2;
#pragma unroll
  for (int i = 0; i < 5; ++i) { const int pc = max(w + 8 * i, qb ? 0 : 20 + w); glds16c(lds0 + pc * 1024, l16, rkv, (head * 64 + 8 * qb - 8) * KVREC + pc * 1024); }
  bload16c(qm, l16, rq, (head * 64 + qt) * QREC);
  bload8c(qt2, qto, rq, (head * 64 + qt) * QREC);
  auto toff = [&](int i) { return (unsigned)(i * KVREC); };
  STAMP(ws, 2, 1);
  asm volatile("s_waitcnt vmcnt(0)" : "+v"(qm), "+v"(qt2)::"memory");
  bar();
  STAMP(ws, 2, 2);
  const bf16x4 qt4 = __builtin_bit_cast(bf16x4, qt2);
  bf16x8 km[9]; bf16x4 kt[9];
  constexpr int ord[9] = {0, 8, 1, 2, 3, 4, 5, 6, 7};
  sfor<9>([&](auto I) { constexpr int i = ord[I.value]; km[i] = ldsr<bf16x8>(kb + toff(i)); kt[i] = ldsr<bf16x4>(ktb + toff(i)); });
  SBAR();
  f32x16 s[9];
  float m4[4];
  auto qk = [&](auto I, auto J) {
    constexpr int i = I.value, j = J.value;
    f32x16 a = mfma32(km[i], qm, (f32x16){}), b;
    if constexpr (j < 9) b = mfma32(km[j], qm, (f32x16){});
    s[i] = __builtin_amdgcn_mfma_f32_32x32x8bf16_1k(kt[i], qt4, a, 0, 0, 0);
    if constexpr (j < 9) s[j] = __builtin_amdgcn_mfma_f32_32x32x8bf16_1k(kt[j], qt4, b, 0, 0, 0);
    SBAR();
  };
  auto mx1 = [&](auto I) {
    constexpr int i = I.value;
#pragma unroll
    for (int e = 0; e < 8; ++e) m4[e & 3] = fmaxf(fmaxf(m4[e & 3], s[i][2 * e]), s[i][2 * e + 1]);
  };
  bf16x4 vf[2][4];
  auto rdv = [&](int set, unsigned off) {
#pragma unroll
    for (int i = 0; i < 4; ++i) vf[set][i] = trh(vb[i] + off);
  };
  qk(ic<0>{}, ic<8>{});
  qk(ic<1>{}, ic<2>{});
  {
    size_t p0 = (size_t)c.wg, p1 = (size_t)c.wu, p2 = (size_t)c.wo;
    asm("" : "+s"(p0), "+s"(p1), "+s"(p2));
    p0 = wmv >= 81 ? p1 : p0;
    warm(lds0 + LDS_SCR, wmo, mk_rsrc((const void*)(wmv >= 162 ? p2 : p0), wmv >= 162 ? D_ * D_ * 2 : D_ * F_ * 2), 0);
  }
  SBAR();
#pragma unroll
  for (int e = 0; e < 16; ++e) s[8][e] = __uint_as_float((__float_as_uint(s[8][e]) & mf[e]) | (__float_as_uint(s[0][e]) & ~mf[e]));
#pragma unroll
  for (int e = 0; e < 4; ++e) asm("v_max_f32 %0, %1, %2\n\tv_max3_f32 %0, %0, %3, %4" : "=&v"(m4[e]) : "v"(s[8][4 * e]), "v"(s[8][4 * e + 1]), "v"(s[8][4 * e + 2]), "v"(s[8][4 * e + 3]));
  SBAR();
  qk(ic<3>{}, ic<4>{});
  mx1(ic<1>{}); mx1(ic<2>{}); SBAR();
  qk(ic<5>{}, ic<6>{});
  mx1(ic<3>{}); mx1(ic<4>{}); SBAR();
  qk(ic<7>{}, ic<9>{});
  rdv(1, toff(1));
  mx1(ic<5>{}); mx1(ic<6>{}); SBAR();
  mx1(ic<7>{});
  float mx = max_x32(fmaxf(fmaxf(m4[0], m4[1]), fmaxf(m4[2], m4[3])));
  STAMP(ws, 2, 3);
  f32x16 o = {};
  u32x4 pp;
  sfor<16>([&](auto H) {
    constexpr int hh = H.value, i = 1 + hh / 2, ks = hh & 1;
    f32x2 dd[4];
#pragma unroll
    for (int e = 0; e < 4; ++e) dd[e] = (f32x2){s[i][8 * ks + 2 * e], s[i][8 * ks + 2 * e + 1]} - (f32x2){mx, mx};
    SBAR();
    if constexpr (hh > 0 && hh < 15) o = mfma32(cat(vf[(1 + (hh - 1) / 2) & 1][2 * (ks ^ 1)], vf[(1 + (hh - 1) / 2) & 1][2 * (ks ^ 1) + 1]), __builtin_bit_cast(bf16x8, pp), o);
    if constexpr (hh == 15) {
      const u32x4 pd = {pp[0] & mk[0], pp[1] & mk[1], pp[2] & mk[2], pp[3] & mk[3]};
      o = mfma32(cat(vf[0][0], vf[0][1]), __builtin_bit_cast(bf16x8, pd), o);
      o = mfma32(cat(vf[1][0], vf[1][1]), __builtin_bit_cast(bf16x8, pp ^ pd), o);
    }
    SBAR();
    if constexpr (ks == 0) rdv((i + 1) & 1, toff(i < 8 ? i + 1 : 0));
    SBAR();
    float pv[8];
#pragma unroll
    for (int e = 0; e < 4; ++e) { pv[2 * e] = __builtin_amdgcn_exp2f(dd[e][0]); pv[2 * e + 1] = __builtin_amdgcn_exp2f(dd[e][1]); }
    SBAR();
    pp = (u32x4){pk2(pv[0], pv[1]), pk2(pv[2], pv[3]), pk2(pv[4], pv[5]), pk2(pv[6], pv[7])};
    SBAR();
  });
  {
    const u32x4 pd = {pp[0] & mk[4], pp[1] & mk[5], pp[2] & mk[6], pp[3] & mk[7]};
    o = mfma32(cat(vf[0][2], vf[0][3]), __builtin_bit_cast(bf16x8, pd), o);
    o = mfma32(cat(vf[1][2], vf[1][3]), __builtin_bit_cast(bf16x8, pp ^ pd), o);
  }
  STAMP(ws, 2, 4);
  auto lr = __builtin_amdgcn_permlane32_swap(__float_as_uint(o[10]), __float_as_uint(o[10]), false, false);
  const float inv = __builtin_amdgcn_rcpf(__uint_as_float(lr[0]));
  const rsrc_t ro = mk_rsrc(ws + WS_OF, 128 * 18 * 1024);
  const u32x4 om = {pk2(o[0] * inv, o[1] * inv), pk2(o[2] * inv, o[3] * inv), pk2(o[4] * inv, o[5] * inv), pk2(o[6] * inv, o[7] * inv)};
  bstore16<16>(om, ro, om_o, 0);
  bstore4<16>(pk2(o[8] * inv, o[9] * inv), ro, ot_o, 0);
  STAMP(ws, 2, 5);
  cnt_add(c, 1);
  if (w < 4 && c.r < 18) { p2_ahead(c); wait_vm<18>(); } else wait_vm<0>();
  set_flag(c, FL2 + (qt * 32 + head) * FS2);
  STAMP(ws, 2, 6);
  STAMP_FLUSH(ws, 2);
}

DI void p3_side(const Ctx& c) {
  char* ws = c.ws;
  const int w = c.w, xcd = c.xcd, lane = fresh_lane();
  const unsigned lds0 = c.lds0;
  const int sb = c.r - 18, blk = sb * 8 + xcd;
  const rsrc_t rwt = mk_rsrc(ws + WS_WT, 4 * 4608 * 32), rlg = mk_rsrc(ws + WS_LG, 8 * 4 * 36864), rcs = mk_rsrc(ws + WS_CS, 2 * 4608 * 4);
  STAMP_DECL; STAMP(ws, 3, 0);
  const int mat = sb >= 5, un = sb < 5 ? 2 * sb : sb - 5;
  const rsrc_t rw = mk_rsrc(mat ? c.wu : c.wg, D_ * F_ * 2);
  const unsigned vo = (lane & 7) * 16 + (72 * w + (lane >> 3)) * 9216;
  u32x4 v[2][9];
#pragma unroll
  for (int i = 0; i < 9; ++i) v[0][i] = __builtin_amdgcn_raw_buffer_load_b128(rw, vo, (9 * xcd + un) * 128 + i * 8 * 9216, 0);
#pragma unroll
  for (int i = 0; i < 9; ++i) v[1][i] = __builtin_amdgcn_raw_buffer_load_b128(rw, sb < 4 ? vo : OOB, (9 * xcd + un + 1) * 128 + i * 8 * 9216, 0);
  const int drow = 64 * (blk - 40) + 8 * w + (lane >> 3);
  const u32x4 dv = __builtin_amdgcn_raw_buffer_load_b128(mk_rsrc(c.wd, F_ * D_ * 2), mat ? drow * 1152 + 1024 + (lane & 7) * 16 : OOB, 0, 0);
  if (un == 8) {
    const unsigned lo = ((lane & 7) >> 1) * 36864 + (72 * w + (lane >> 3)) * 32 + (lane & 1) * 16;
#pragma unroll
    for (int i = 0; i < 9; ++i) bstore16<16>(v[0][i], rlg, lo, (xcd * 8 + (sb >> 3)) * 18432 + i * 256);
  }
  const int pr = (drow % 576) >= 512 ? ((drow & ~24) | ((drow & 8) << 1) | ((drow & 16) >> 1)) : drow;
  bstore16<16>(dv, rwt, mat ? (((lane & 7) >> 1) * 4608 + pr) * 32 + (lane & 1) * 16 : OOB, 0);
  float a[2][8] = {};
#pragma unroll
  for (int k = 0; k < 2; ++k) {
#pragma unroll
    for (int i = 0; i < 9; ++i)
#pragma unroll
      for (int e = 0; e < 4; ++e) { a[k][2 * e] += bflo(v[k][i][e]); a[k][2 * e + 1] += bfhi(v[k][i][e]); }
#pragma unroll
    for (int e = 0; e < 8; ++e) a[k][e] = sum_x32(sum_x16(a[k][e]));
  }
  if (lane < 16) {
#pragma unroll
    for (int k = 0; k < 2; ++k) {
      ldsw<f32x4>(lds0 + k * 4096 + w * 512 + lane * 32, (f32x4){a[k][0], a[k][1], a[k][2], a[k][3]});
      ldsw<f32x4>(lds0 + k * 4096 + w * 512 + lane * 32 + 16, (f32x4){a[k][4], a[k][5], a[k][6], a[k][7]});
    }
  }
  lds_bar();
  if (w < 2) {
    float t = 0.f;
#pragma unroll
    for (int i = 0; i < 16; ++i) t += ldsr<float>(lds0 + w * 4096 + i * 256 + lane * 4);
    bstore4<16>(__float_as_uint(t), rcs, w == 0 || sb < 4 ? lane * 4 : OOB, ((sb >= 5 ? 4608 : 0) + (9 * xcd + un + w) * 64) * 4);
  }
  STAMP(ws, 3, 5);
  wait_vm<0>();
  bar();
  if (w == 0) set_flag(c, FLS + blk * 64);
  sfor<2>([&](auto I) { warm(lds0 + LDS_SCR, lane * 128, mk_rsrc(c.wd, F_ * D_ * 2), (xcd >> 1) * 1327104 + min(sb * 16 + w * 2 + I.value, 161) * 8192); });
  STAMP(ws, 3, 6);
  if (w == 5 && blk == 48) {
    for (int i = 0; i < 4; ++i) poll(c, lane * FS1, FL1 + i * 64 * FS1);
    set_flag(c, 0);
  }
  STAMP_FLUSH(ws, 6);
}

DI void p3_gemm(const Ctx& c) {
  char* ws = c.ws;
  const int w = c.w, lane = fresh_lane();
  const unsigned lds0 = c.lds0;
  const int t16 = lane & 15, g = lane >> 4;
  const int nt = c.r >= 9 ? c.r - 9 : c.r;
  const int T = 16 * c.xcd + 8 * (w >> 2) + 4 * (c.r >= 9) + (w & 3);
  const rsrc_t rof = mk_rsrc(ws + WS_OF, 128 * 18 * 1024);
  STAMP_DECL; STAMP(ws, 3, 0);
  unsigned wa[4];
  {
    const int r2 = 4 * g + ((lane >> 2) & 3);
#pragma unroll
    for (int i = 0; i < 4; ++i) wa[i] = lds0 + P3_WO + r2 * 128 + 32 * (i ^ ((r2 >> 1) & 3)) + 8 * (lane & 3);
  }
  unsigned xo = (16 * T + t16) * 1152 + 128 * nt + 8 * g;
  PIN(xo);
  if (w < 4) { wait_vm<1>(); cnt_add(c, 0); }
  cnt_wait(c, 0, 4);
  STAMP(ws, 3, 1);
  bf16x8 of[18];
  f32x4 acc[4] = {};
  bf16x4 fr[2][4][2];
  auto rd1 = [&](auto KS, auto I) { constexpr int ks = KS.value, i = I.value; fr[ks & 1][i >> 1][i & 1] = trh(wa[i >> 1] + (ks & 15) * 4096 + (i & 1) * 2048); };
  sfor<8>([&](auto I) { rd1(ic<0>{}, I); });
  poll(c, (lane & 31) * FS2, FL2 + (T >> 1) * 32 * FS2);
  STAMP(ws, 3, 2);
    u32x2 xr[4];
  sfor<18>([&](auto K) { bload16c(of[K.value], lane * 16, rof, (T * 18 + K.value) * 1024); });
  sfor<4>([&](auto I) { bload8(xr[I.value], xo, mk_rsrc(c.x, S_ * D_ * 2), 32 * I.value); });
  sfor<18>([&](auto KS) {
    constexpr int ks = KS.value;
    asm volatile("s_waitcnt vmcnt(%1)" : "+v"(of[ks]) : "i"(21 - ks) : "memory");
    if constexpr (ks == 0) STAMP(ws, 3, 3);
    if constexpr (ks == 9) STAMP(ws, 3, 4);
    if constexpr (ks == 15) sfor<4>([&](auto I) { wa[I.value] += 65536; PIN(wa[I.value]); });
    sfor<4>([&](auto C) {
      acc[C.value] = mfma16(cat(fr[ks & 1][C.value][0], fr[ks & 1][C.value][1]), of[ks], acc[C.value]);
      SBAR();
      if constexpr (ks + 1 < 18) { rd1(ic<ks + 1>{}, ic<2 * C.value>{}); rd1(ic<ks + 1>{}, ic<2 * C.value + 1>{}); }
      SBAR();
    });
  });
  asm volatile("s_waitcnt vmcnt(0)" : "+v"(xr[0]), "+v"(xr[1]), "+v"(xr[2]), "+v"(xr[3])::"memory");
  unsigned d[4][2];
  float s1 = 0.f, s2 = 0.f;
#pragma unroll
  for (int i = 0; i < 4; ++i) {
    d[i][0] = pk2(acc[i][0] + bflo(xr[i][0]), acc[i][1] + bfhi(xr[i][0]));
    d[i][1] = pk2(acc[i][2] + bflo(xr[i][1]), acc[i][3] + bfhi(xr[i][1]));
  }
  const rsrc_t rhf = mk_rsrc(ws + WS_HF, 128 * 18 * 1024);
  bstore16<16>((u32x4){d[0][0], d[0][1], d[1][0], d[1][1]}, rhf, lane * 16, (T * 18 + 2 * nt) * 1024);
  bstore16<16>((u32x4){d[2][0], d[2][1], d[3][0], d[3][1]}, rhf, lane * 16, (T * 18 + 2 * nt + 1) * 1024);
#pragma unroll
  for (int i = 0; i < 4; ++i)
#pragma unroll
    for (int e = 0; e < 2; ++e) { const float a = bflo(d[i][e]), b = bfhi(d[i][e]); s1 += a + b; s2 += a * a + b * b; }
  s1 = sum_x32(sum_x16(s1)); s2 = sum_x32(sum_x16(s2));
  bstore8<16>((u32x2){__float_as_uint(s1), __float_as_uint(s2)}, mk_rsrc(ws + WS_ST, 9 * 2048 * 8), g == 0 ? t16 * 8 : OOB, (T * 9 + nt) * 128);
  STAMP(ws, 3, 5);
  wait_vm<0>();
  set_flag(c, FL3 + (T * 16 + nt) * FS3);
  STAMP(ws, 3, 6);
  STAMP_FLUSH(ws, 3);
}

constexpr int K4_LG = P3_WO;
constexpr int K4_CS = K4_LG + 36864;
constexpr int K4_STP = K4_CS + 4096;
struct K4Op { int kind, idx; };
struct K4Ops { int n; K4Op op[12]; };
constexpr K4Ops k4_ops(int S) {
  K4Ops o = {};
  auto add = [&](int k, int i) { o.op[o.n++] = {k, i}; };
  if (S == 3) add(9, 0);
  if (S == 0) for (int u = 4; u < 8; ++u) { add(1, u); add(2, u); }
  else for (int u = 2 * S + 6; u < 2 * S + 8 && u < 36; ++u) {
    if (S == 2) { add(0, 2 * u - 12); add(0, 2 * u - 11); }
    if (S > 2) add(0, u);
    if (u < 18) { add(1, u); add(2, u); }
  }
  if (S == 4) { add(5, 0); add(5, 1); add(5, 2); }
  if (S == 6) add(6, 0);
  if (S >= 10 && S < 15) add(3, S - 10);
  if (S == 12) add(7, 0);
  if (S == 14) add(8, 0);
  if (S == WD_S) add(4, 0);
  return o;
}
constexpr int k4_nst(int S) { return (S == 8 || S == 9) ? 2 : S == 7 ? 1 : 0; }
constexpr int k4_issued(int S) { int n = 8; for (int s = 0; s < S; ++s) n += k4_ops(s).n + k4_nst(s); return n; }
constexpr int k4_pos(int kind, int idx) {
  if (kind == 0 && idx < 8) return -1;
  if ((kind == 1 || kind == 2) && idx < 4) return 2 * idx + kind - 1;
  for (int S = 0;; ++S) for (int i = 0; i < k4_ops(S).n; ++i) if (k4_ops(S).op[i].kind == kind && k4_ops(S).op[i].idx == idx) return k4_issued(S) + k4_nst(S) + i;
}
constexpr int k4_need(int S) {
  if (S == 8) return k4_issued(7) - 1;
  if (S >= 16) return k4_issued(15) - 1;
  const int a = S < 9 ? k4_pos(2, 2 * S + 1) : -1, b = k4_pos(0, 2 * S + 2);
  return a > b ? a : b;
}
static_assert(k4_need(5) >= k4_pos(9, 0) && k4_need(7) >= k4_pos(5, 2) && k4_need(12) >= k4_issued(10) - 1, "");
constexpr int K5_SLOT = 17408, K5_BASE = 0, K5_P = 4;
constexpr unsigned k5_so(int v) { return K5_BASE + ((v + 7) & 7) * K5_SLOT; }
DI int k5_u(int v, int nt) {
  if (v < 32) { const int q = ((v >> 2) + nt) & 3; return 18 * (q >> 1) + 8 * (q & 1) + 4 * ((v >> 1) & 1) + (v & 1) + 2 * (v >> 4); }
  const int q = (v + nt) & 3;
  return 18 * (q >> 1) + 16 + (q & 1);
}
DI unsigned k5_mv(int rho, int sp, int nt, int t16) {
  const int rr = sp ? ((rho & 7) | ((rho & 8) << 1) | ((rho & 16) >> 1)) : rho;
  return rr * 1152 + 256 * nt + 32 * ((t16 >> 1) ^ (rho & 7)) + 16 * (t16 & 1);
}
DI unsigned k5_fl(int i, int sg, int mt5) { return (((((mt5 >> 1) * 4 + ((i >> 2) & 3)) * 8 + 2 * sg + ((i >> 4) & 1)) * 8) + 4 * (mt5 & 1) + (i & 3)) * 64; }

DI void p4(const Ctx& c) {
  char* ws = c.ws;
  const int w = c.w, r = c.r, xcd = c.xcd, lane = fresh_lane();
  const int mt = r >> 2, Qd = xcd, j = r & 3;
  const unsigned lds0 = c.lds0;
  const int g = lane >> 4;
  const int T0 = 16 * mt + 2 * w;
  const rsrc_t rwm = mk_rsrc(w < 4 ? c.wg : c.wu, D_ * F_ * 2), rhf = mk_rsrc(ws + WS_HF, 128 * 18 * 1024), rwd = mk_rsrc(c.wd, F_ * D_ * 2);
  const rsrc_t rhh = mk_rsrc(ws + WS_HH, 128 * 144 * 1024);
  const unsigned f4 = FL4 + ((r * 8 + xcd) * 8 + w) * 64;
  const int sg = xcd >> 1, mt5 = 8 * (xcd & 1) + (r >> 2);
  STAMP_DECL; STAMP(ws, 4, 0);
  const unsigned wv = k4_wv(lane, w, Qd, j);
  const unsigned wdst = lds0 + K4_RING + w * 1024, l16 = lane * 16;
  poll(c, (min(lane & 15, 8) + (lane & 16)) * FS3, FL3 + T0 * 16 * FS3);
  bf16x8 act[18][2];
  u32x2 pf;
  auto pf_off = [&] { const int z = opq(l16) >> 4; const unsigned a = opq(FL4 + k5_fl(z, sg, mt5)), b = opq(FLS + (40 + 18 * sg + min(z - 32, 17)) * 64); return z < 32 ? a : b; };
  auto doop = [&](auto KIND, auto IDX) {
    constexpr int kind = KIND.value, u = IDX.value;
    if constexpr (kind == 0) glds16(wdst + k4_so(u), wv, rwm, (u % 18) * 32 * 9216 + (u / 18) * 128);
    else if constexpr (kind <= 2) bload16c(act[u][kind - 1], l16, rhf, (T0 * 18 + 18 * (kind - 1) + u) * 1024);
    else if constexpr (kind == 3) {
      const int pc = min(w + 8 * u, 35);
      glds16c(lds0 + K4_LG + pc * 1024, l16, mk_rsrc(ws + WS_LG, 8 * 4 * 36864), (Qd * 4 + j) * 36864 + pc * 1024);
    }
    else if constexpr (kind == 4) warm(lds0 + LDS_SCR, l16 * 8, rwd, sg * 1327104 + min(r * 8 + w, 161) * 8192);
    else if constexpr (kind == 5) glds16c(lds0 + K4_STP + w * 3072 + u * 1024, l16, mk_rsrc(ws + WS_ST, 9 * 2048 * 8), T0 * 1152 + u * 1024);
    else if constexpr (kind == 6) {
      const unsigned z = opq(l16), f = (576 * Qd + (z < 512 ? 128 * j : 384 + 16 * j)) * 4 + z;
      glds16c(lds0 + K4_CS + (w & 1) * 1024, z < 576 ? f : OOB, mk_rsrc(ws + WS_CS, 2 * 4608 * 4), (w & 1) * 4608 * 4);
    } else if constexpr (kind == 7) set_flag(c, f4);
    else if constexpr (kind == 8) bload8c(pf, pf_off(), fl_rsrc(c), 0);
    else bload8c(pf, min(opq(l16), 13u * 16) * 32, fl_rsrc(c), FLS + xcd * 64);
  };
  sfor<4>([&](auto U) { doop(ic<1>{}, U); doop(ic<2>{}, U); });
  if (w < 4) cnt_add(c, 4);
  cnt_wait(c, 4, 4);
  unsigned wa[4];
  {
    const int r2 = 4 * g + ((lane >> 2) & 3);
#pragma unroll
    for (int i = 0; i < 4; ++i) wa[i] = lds0 + K4_RING + r2 * 128 + 32 * (i ^ ((r2 >> 1) & 3)) + 8 * (lane & 3);
  }
  f32x4 ag[4][2], au[4][2], cg, cu;
  bf16x4 fr[4][2];
  unsigned hd[2][2], lga, csa;
  f32x2 nmu = {0.f, 0.f};
  auto rd1 = [&](auto H, auto I) {
    constexpr int n = H.value >> 1, m = H.value & 1, i = I.value;
    if constexpr (n < 144) fr[H.value & 3][i] = trh(wa[n % 4] + k4_so(n / 4) + 4096 * m + 2048 * i);
    else if constexpr (n < 162) fr[H.value & 3][i] = trh(lga + (n - 144) * 1024 + m * 18432 + i * 512);
  };
  auto rd = [&](auto H) { rd1(H, ic<0>{}); rd1(H, ic<1>{}); };
  auto mm = [&](auto N, auto KS, f32x4& g0, f32x4& g1, f32x4& u0, f32x4& u1, auto&& hook) {
    constexpr int n = N.value, ks = KS.value;
    constexpr int h0 = n == 15 ? 999 : 2 * n + 3, h1 = (n == 14 || n == 15) ? 999 : 2 * n + 4;
    const bf16x8 fg = cat(fr[(2 * n) & 3][0], fr[(2 * n) & 3][1]);
    if constexpr (ks == 0) mfma16_z(g0, fg, act[ks][0]); else mfma16_ip(g0, fg, act[ks][0]);
    SBAR(); rd1(ic<h0>{}, ic<0>{}); SBAR(); hook(ic<0>{}); SBAR();
    if constexpr (ks == 0) mfma16_z(g1, fg, act[ks][1]); else mfma16_ip(g1, fg, act[ks][1]);
    SBAR(); rd1(ic<h0>{}, ic<1>{}); SBAR(); hook(ic<1>{}); SBAR();
    const bf16x8 fu = cat(fr[(2 * n + 1) & 3][0], fr[(2 * n + 1) & 3][1]);
    if constexpr (ks == 0) mfma16_z(u1, fu, act[ks][1]); else mfma16_ip(u1, fu, act[ks][1]);
    SBAR(); rd1(ic<h1>{}, ic<0>{}); SBAR(); hook(ic<2>{}); SBAR();
    if constexpr (ks == 0) mfma16_z(u0, fu, act[ks][0]); else mfma16_ip(u0, fu, act[ks][0]);
    SBAR(); rd1(ic<h1>{}, ic<1>{}); SBAR(); hook(ic<3>{}); SBAR();
  };
  auto ldcs = [&](int e) { cg = ldsr<f32x4>(csa + 64 * e); cu = ldsr<f32x4>(csa + 1024 + 64 * e); };
  auto epi = [&](auto E, auto I) {
    constexpr int grp = E.value >> 2, cc = E.value & 3, i = I.value, T = 1 - (i >> 1);
    if constexpr ((i & 1) == 0) {
      ag[cc][T] += cg * nmu[T]; au[cc][T] += cu * nmu[T]; PIN(ag[cc][T]); PIN(au[cc][T]);
      if constexpr (i == 2 && E.value != 3) { SBAR(); ldcs(E.value + 1); }
    }
    else {
      const f32x4 v = ag[cc][T] * au[cc][T];
      const unsigned d0 = pk2(v[0], v[1]), d1 = pk2(v[2], v[3]);
      if constexpr ((cc & 1) == 0) { hd[T][0] = d0; hd[T][1] = d1; PIN(hd[T][0]); PIN(hd[T][1]); }
      else bstore16<16>((u32x4){hd[T][0], hd[T][1], d0, d1}, rhh, l16, ((T0 + T) * 144 + 18 * Qd + 4 * j + 2 * grp + (cc >> 1)) * 1024);
    }
  };
  f32x2 sp[3];
  float rs0;
  auto stats = [&](auto TT, auto P) {
    constexpr int T = TT.value, p = P.value;
    if constexpr (p == 0) {
      const unsigned sa = lds0 + K4_STP + w * 3072 + T * 1152 + (opq(l16) >> 1);
      sp[0] = ldsr<f32x2>(sa); sp[1] = ldsr<f32x2>(sa + 512); sp[2] = ldsr<f32x2>(sa + 1024);
    } else if constexpr (p == 4) { sp[0] = sp[0] + sp[1] + (opq(l16) < 256 ? sp[2] : (f32x2){0.f, 0.f}); PIN(sp[0]); }
    else if constexpr (p == 5) { sp[0][0] = sum_x16(sp[0][0]); PIN(sp[0]); }
    else if constexpr (p == 6) { sp[0][1] = sum_x16(sp[0][1]); PIN(sp[0]); }
    else if constexpr (p == 7) { sp[0][0] = sum_x32(sp[0][0]); PIN(sp[0]); }
    else if constexpr (p == 8) { sp[0][1] = sum_x32(sp[0][1]); PIN(sp[0]); }
    else if constexpr (p == 9) {
      nmu[T] = sp[0][0] * (-1.f / D_);
      const float rs = __builtin_amdgcn_rcpf(fmaxf(sp[0][1] * (1.f / D_) - nmu[T] * nmu[T], 0.f) + 1e-5f);
      PIN(nmu);
      if constexpr (T == 0) { rs0 = rs; PIN(rs0); }
      else {
        const unsigned z = opq(l16);
        bstore4<16>(__float_as_uint((z & 256) ? rs : rs0), mk_rsrc(ws + WS_RS + (sg * 2048 + 16 * T0) * 4, (Qd & 1) == 0 && j == 0 ? 128 : 0), z >> 2, 0);
      }
    }
  };
  unsigned mv5;
  auto pf5 = [&](auto N) {
    constexpr int n = N.value, v = (n - 1) >> 1;
    if constexpr (n == 0) {
      const int vv = min(w, K5_P - 1), uu = k5_u(vv, j);
      glds16c(lds0 + K5_BASE + ((vv + 7) & 7) * K5_SLOT + 16384, l16, mk_rsrc(ws + WS_WT, 4 * 4608 * 32), (j * 144 + 36 * sg + uu) * 1024);
      const unsigned z = opq(l16);
      mv5 = k5_mv(4 * w + (z >> 8), 0, j, (z >> 4) & 15);
    } else if constexpr (n & 1) glds16(lds0 + k5_so(v) + 8192 + w * 1024, mv5, rwd, sg * 1152 * 1152 + k5_u(v, j) * 32 * 1152);
    else glds16c(lds0 + k5_so(v) + w * 1024, l16, rhh, ((8 * mt5 + w) * 144 + 36 * sg + k5_u(v, j)) * 1024);
  };
  sfor<18>([&](auto SS) {
    constexpr int S = SS.value;
    constexpr int nw = k4_issued(S) - k4_need(S) - 1;
    static_assert(nw >= 0 && nw < 64, "");
    if constexpr (S == 5) asm volatile("s_waitcnt vmcnt(%5)" : "+v"(act[2 * S][0]), "+v"(act[2 * S][1]), "+v"(act[2 * S + 1][0]), "+v"(act[2 * S + 1][1]), "+v"(pf) : "i"(nw) : "memory");
    else if constexpr (S < 9) asm volatile("s_waitcnt vmcnt(%4)" : "+v"(act[2 * S][0]), "+v"(act[2 * S][1]), "+v"(act[2 * S + 1][0]), "+v"(act[2 * S + 1][1]) : "i"(nw) : "memory");
    else if constexpr (S == 16) asm volatile("s_waitcnt vmcnt(%1)" : "+v"(pf) : "i"(nw) : "memory");
    else wait_vm<nw>();
    if constexpr (S == 16) { if (__builtin_expect(!tag_ok(c, pf), 0)) poll(c, pf_off(), 0); }
    if constexpr (S == 5) { if (__builtin_expect(!tag_ok(c, pf), 0)) poll(c, min(opq(l16), 13u * 16) * 32, FLS + xcd * 64); }
    if constexpr (S >= 2) bar();
    if constexpr (S == 0) STAMP(ws, 4, 1);
    if constexpr (S == 2) STAMP(ws, 4, 7);
    if constexpr (S == 4) STAMP(ws, 4, 2);
    if constexpr (S == 9) STAMP(ws, 4, 3);
    if constexpr (S == 17) STAMP(ws, 4, 4);
    if constexpr (S == 0 || S == 2) { rd(ic<16 * S>{}); rd(ic<16 * S + 1>{}); rd(ic<16 * S + 2>{}); }
    sfor<8>([&](auto L) {
      constexpr int l = L.value, n = 8 * S + l, t = n / 4, cc = n % 4, ks = t % 18, no = k4_ops(S).n;
      mm(ic<n>{}, ic<ks>{}, ag[cc][0], ag[cc][1], au[cc][0], au[cc][1], [&](auto I) {
        constexpr int i = I.value;
        sfor<no>([&](auto O) {
          constexpr int nd = no ? no : 1, at = O.value * 32 / nd + 16 / nd;
          if constexpr (at == 4 * l + i) doop(ic<k4_ops(S).op[O.value].kind>{}, ic<k4_ops(S).op[O.value].idx>{});
        });
        if constexpr (S == 7) stats(ic<(l >> 2)>{}, ic<4 * (l & 3) + i>{});
        if constexpr ((S == 8 || S == 17) && l == 3 && i == 0) { csa = lds0 + K4_CS + ((opq(l16) >> 4) & 48); PIN(csa); }
        if constexpr ((S == 8 || S == 17) && l == 4 && i == 0) ldcs(S == 8 ? 0 : 4);
        if constexpr (S == 17 && l == 0 && i == 0) { const unsigned z = opq(l16); lga = lds0 + K4_LG + (z >> 6) * 32 + ((z >> 1) & 24); PIN(lga); }
        if constexpr (S == 17 && l < 5 && i == 1) pf5(L);
        if constexpr (ks == 17 && cc >= 1) epi(ic<4 * (t / 18) + cc - 1>{}, I);
        if constexpr (t == 18 && cc == 0) epi(ic<3>{}, I);
      });
    });
  });
  STAMP(ws, 4, 5);
  {
    f32x4 g0, g1, u0, u1;
    sfor<18>([&](auto K) {
      constexpr int ks = K.value, n = 144 + ks;
      mm(ic<n>{}, ic<ks>{}, g0, g1, u0, u1, [&](auto I) {
        if constexpr (ks == 0) epi(ic<7>{}, I);
        if constexpr (ks >= 1 && ks <= 4 && I.value == 1) pf5(ic<4 + ks>{});
        if constexpr (ks == 10 && I.value == 1) { wait_vm<4>(); set_flag(c, f4 + 2048 * 64); }
      });
    });
    asm volatile("s_nop 9" : "+v"(g0), "+v"(g1), "+v"(u0), "+v"(u1));
    const f32x4 v0 = (g0 + cg * nmu[0]) * (u0 + cu * nmu[0]), v1 = (g1 + cg * nmu[1]) * (u1 + cu * nmu[1]);
    const unsigned a0 = pk2(v0[0], v0[1]), a1 = pk2(v0[2], v0[3]), b0 = pk2(v1[0], v1[1]), b1 = pk2(v1[2], v1[3]);
    auto s0 = __builtin_amdgcn_permlane32_swap(a0, b0, false, false);
    auto s1 = __builtin_amdgcn_permlane32_swap(a1, b1, false, false);
    const unsigned z = opq(l16);
    bstore16<16>((u32x4){s0[0], s1[0], s0[1], s1[1]}, rhh, (z >> 9) * 144 * 1024 + (z & 511) + 512 * (j & 1), (T0 * 144 + 18 * Qd + 16 + (j >> 1)) * 1024);
    STAMP(ws, 4, 6);
    STAMP_FLUSH(ws, 4);
  }
}

constexpr int k5_E(int i) { int e = 36; if (K5_P + 2 * (i + 1) < e) e = K5_P + 2 * (i + 1); if (i >= 0 && i + 8 < e) e = i + 8; return e; }
static_assert(k5_E(6) <= 16 && k5_E(20) <= 32, "");

DI void p5(const Ctx& c) {
  char* ws = c.ws;
  const int w = c.w, r = c.r, xcd = c.xcd, lane = fresh_lane();
  const int sg = xcd >> 1, mt = 8 * (xcd & 1) + (r >> 2), nt = r & 3;
  const unsigned lds0 = c.lds0;
  const int t16 = lane & 15, g = lane >> 4;
  const int tile = 4 * mt + nt;
  const rsrc_t rpt = mk_rsrc(ws + WS_PT, 64 * 8 * 4 * PT_REC);
  const unsigned f4 = FL4 + (4096 + (r * 8 + xcd) * 8 + w) * 64;
  STAMP_DECL; STAMP(ws, 5, 0);
  if (w >= 4) {
    const int l = w - 4;
    const rsrc_t rwd = mk_rsrc(c.wd, F_ * D_ * 2), rhh = mk_rsrc(ws + WS_HH, 128 * 144 * 1024);
    const rsrc_t rwt = mk_rsrc(ws + WS_WT, 4 * 4608 * 32);
    unsigned mv[2][2];
#pragma unroll
    for (int sp = 0; sp < 2; ++sp)
#pragma unroll
      for (int i = 0; i < 2; ++i) mv[sp][i] = k5_mv(4 * (2 * l + i) + g, sp, nt, t16);
    const unsigned hs = ((8 * mt + 2 * l) * 144 + 36 * sg) * 1024, wsb = sg * 1152 * 1152, dl = lds0 + l * 2048;
    auto issue = [&](auto V) {
      constexpr int v = V.value, sp = v >= 32;
      const int u = k5_u(v, nt);
      constexpr unsigned so = k5_so(v);
      glds16(dl + so + 8192, mv[sp][0], rwd, wsb + u * 32 * 1152);
      glds16c(dl + so, lane * 16, rhh, hs + u * 1024);
      glds16(dl + so + 8192 + 1024, mv[sp][1], rwd, wsb + u * 32 * 1152);
      glds16c(dl + so + 1024, lane * 16, rhh, hs + (144 + u) * 1024);
      if ((v & 3) == l) glds16c(lds0 + so + 16384, lane * 16, rwt, (nt * 144 + 36 * sg + u) * 1024);
    };
    sfor<36>([&](auto I) {
      constexpr int i = I.value, y = k5_E(i - 1) - (i + 2 < 36 ? i + 2 : 36), yy = k5_E(i - 1) - K5_P;
      if constexpr (i == 0) wait_vm<2>();
      else if constexpr (i == 2) { wait_vm<4 * yy + yy / 4>(); set_flag(c, f4); }
      else if constexpr (i + 2 > K5_P) wait_vm<4 * y + y / 4>();
      bar();
      if constexpr (i == 0) STAMP(ws, 5, 1);
      if constexpr (i == 18) STAMP(ws, 5, 2);
      sfor<k5_E(i) - k5_E(i - 1)>([&](auto U) { issue(ic<k5_E(i - 1) + U.value>{}); });
    });
    bar();
    STAMP(ws, 5, 3);
    bar();
  } else {
    const int r2 = 4 * g + ((lane >> 2) & 3);
    unsigned wa[2][10];
#pragma unroll
    for (int i = 0; i < 8; ++i) wa[0][i] = lds0 + K5_BASE + 8192 + r2 * 256 + 32 * (i ^ (r2 & 7)) + 8 * (lane & 3);
    wa[0][8] = lds0 + K5_BASE + 16384 + r2 * 32 + 8 * (lane & 3);
    wa[0][9] = lds0 + K5_BASE + w * 2048 + lane * 16;
#pragma unroll
    for (int i = 0; i < 10; ++i) { wa[1][i] = wa[0][i] + 4 * K5_SLOT; PIN(wa[0][i]); PIN(wa[1][i]); }
    const unsigned fo = k5_fl(lane & 31, sg, mt);
    f32x4 acc[9][2];
    bf16x4 fr[3][3][2];
    bf16x8 hb[2][2];
    u32x2 pf;
    auto rd1 = [&](auto N, auto I) {
      constexpr int n = N.value, u = n / 3, i = I.value;
      if constexpr (u < 36) {
        constexpr int pos = (u + 7) & 7, hi = pos >> 2, cc = 3 * (n % 3) + i / 2;
        constexpr unsigned so = (pos & 3) * K5_SLOT;
        if constexpr (i >= 6) hb[u & 1][i - 6] = ldsr<bf16x8>(wa[hi][9] + so + (i - 6) * 1024);
        else fr[n % 3][i / 2][i & 1] = trh(wa[hi][cc] + so + (i & 1) * (cc < 8 ? 4096 : 512));
      }
    };
    sfor<36>([&](auto U) {
      constexpr int u = U.value;
      if constexpr (u == 0) wait_vm<2>();
      if constexpr (u == 4) { wait_vm<0>(); set_flag(c, f4); }
      if constexpr (u == 4 || u == 18) bload8c(pf, fo, fl_rsrc(c), FL4 + (u == 4 ? 1 : 2) * 2048 * 64);
      if constexpr (u == 7 || u == 21) {
        asm volatile("s_waitcnt vmcnt(0)" : "+v"(pf)::"memory");
        if (__builtin_expect(!tag_ok(c, pf), 0)) poll(c, fo, FL4 + (u == 7 ? 1 : 2) * 2048 * 64);
      }
      bar();
      if constexpr (u == 0) STAMP(ws, 5, 1);
      if constexpr (u == 18) STAMP(ws, 5, 2);
      if constexpr (u == 0) { rd1(ic<0>{}, ic<6>{}); rd1(ic<0>{}, ic<7>{}); sfor<6>([&](auto I) { rd1(ic<0>{}, I); }); sfor<6>([&](auto I) { rd1(ic<1>{}, I); }); }
      sfor<18>([&](auto M) {
        constexpr int mi = M.value, v = mi / 6, n = 3 * u + v, cc = mi / 2;
        if constexpr (u == 0) mfma16_z(acc[cc][mi & 1], cat(fr[n % 3][cc % 3][0], fr[n % 3][cc % 3][1]), hb[u & 1][mi & 1]);
        else mfma16_ip(acc[cc][mi & 1], cat(fr[n % 3][cc % 3][0], fr[n % 3][cc % 3][1]), hb[u & 1][mi & 1]);
        SBAR();
        if constexpr (mi == 6 || mi == 8) rd1(ic<n + 2>{}, ic<6 + (mi - 6) / 2>{});
        rd1(ic<n + 2>{}, ic<mi % 6>{});
        SBAR();
      });
    });
    asm volatile("s_nop 9" : "+v"(acc[7][0]), "+v"(acc[7][1]), "+v"(acc[8][0]), "+v"(acc[8][1]));
#pragma unroll
    for (int i = 0; i < 7; ++i) asm volatile("" : "+v"(acc[i][0]), "+v"(acc[i][1]));
    bar();
    STAMP(ws, 5, 3);
    if (w == sg) {
      sfor<2>([&](auto T) { sfor<9>([&](auto C) { ldsw<f32x4>(lds0 + (T.value * 9 + C.value) * 1024 + lane * 16, acc[C.value][T.value]); }); });
      lds_bar();
    } else {
      sfor<2>([&](auto T) {
        const unsigned pb = (((tile * 4 + w) * 4 + sg) * 2 + T.value) * PT_REC;
        sfor<4>([&](auto I) {
          constexpr int i = I.value;
          const f32x4 a = acc[2 * i][T.value], b = acc[2 * i + 1][T.value];
          bstore16<16>((u32x4){pk2(a[0], a[1]), pk2(a[2], a[3]), pk2(b[0], b[1]), pk2(b[2], b[3])}, rpt, lane * 16 + i * 1024, pb);
        });
        const f32x4 a = acc[8][T.value];
        bstore8<16>((u32x2){pk2(a[0], a[1]), pk2(a[2], a[3])}, rpt, lane * 8 + 4096, pb);
      });
      bar();
      wait_vm<0>();
      set_flag(c, FL5 + (tile * 4 + w) * 256 + sg * FS5);
    }
  }
  const int T = w >> 2, pi = w & 3, tt = 8 * mt + 2 * sg + T;
  const rsrc_t rhf = mk_rsrc(ws + WS_HF, 128 * 18 * 1024);
  u32x4 hv, pv[3];
  u32x2 hl, pl[3];
  unsigned rs2u;
  STAMP(ws, 5, 4);
  bload16c(hv, lane * 16, rhf, (tt * 18 + 4 * nt + pi) * 1024);
  bload8c(hl, lane * 16 + 8 * (nt & 1), rhf, (tt * 18 + 16 + (nt >> 1)) * 1024);
  bload4c(rs2u, t16 * 4, mk_rsrc(ws + WS_RS, 4 * 2048 * 4), (sg * 2048 + 16 * tt) * 4);
  poll(c, ((sg + 1 + min(lane, 2)) & 3) * FS5, FL5 + (tile * 4 + sg) * 256);
  STAMP(ws, 5, 5);
#pragma unroll
  for (int o = 0; o < 3; ++o) {
    const unsigned so = (((tile * 4 + sg) * 4 + ((sg + 1 + o) & 3)) * 2 + T) * PT_REC;
    bload16c(pv[o], lane * 16 + pi * 1024, rpt, so);
    bload8c(pl[o], pi == 0 ? lane * 8 + 4096 : OOB, rpt, so);
  }
  const unsigned orow = (16 * tt + t16) * 1152, own = lds0 + T * 9216 + lane * 16;
  const f32x4 o0 = ldsr<f32x4>(own + pi * 2048), o1 = ldsr<f32x4>(own + pi * 2048 + 1024), o2 = ldsr<f32x4>(own + 8192);
  asm volatile("s_waitcnt vmcnt(0)" : "+v"(hv), "+v"(hl), "+v"(rs2u), "+v"(pv[0]), "+v"(pv[1]), "+v"(pv[2]), "+v"(pl[0]), "+v"(pl[1]), "+v"(pl[2])::"memory");
  STAMP(ws, 5, 6);
  const float rs2 = __uint_as_float(rs2u);
  const rsrc_t rout = mk_rsrc(c.out, S_ * D_ * 2);
  auto fin = [&](const f32x4& ow, unsigned a0, unsigned a1, unsigned b0, unsigned b1, unsigned c0, unsigned c1, unsigned h0, unsigned h1, unsigned& d0, unsigned& d1) {
    const float v0 = ((ow[0] + bflo(b0)) + (bflo(a0) + bflo(c0))) * rs2 + bflo(h0);
    const float v1 = ((ow[1] + bfhi(b0)) + (bfhi(a0) + bfhi(c0))) * rs2 + bfhi(h0);
    const float v2 = ((ow[2] + bflo(b1)) + (bflo(a1) + bflo(c1))) * rs2 + bflo(h1);
    const float v3 = ((ow[3] + bfhi(b1)) + (bfhi(a1) + bfhi(c1))) * rs2 + bfhi(h1);
    d0 = pk2(v0, v1); d1 = pk2(v2, v3);
  };
  {
    unsigned a0, a1, b0, b1;
    fin(o0, pv[0][0], pv[0][1], pv[1][0], pv[1][1], pv[2][0], pv[2][1], hv[0], hv[1], a0, a1);
    fin(o1, pv[0][2], pv[0][3], pv[1][2], pv[1][3], pv[2][2], pv[2][3], hv[2], hv[3], b0, b1);
    auto s0 = __builtin_amdgcn_permlane16_swap(a0, b0, false, false);
    auto s1 = __builtin_amdgcn_permlane16_swap(a1, b1, false, false);
    bstore16<0>((u32x4){s0[0], s1[0], s0[1], s1[1]}, rout, orow + 256 * nt + 64 * pi + 32 * (g & 1) + 16 * (g >> 1), 0);
  }
  {
    unsigned e0, e1;
    fin(o2, pl[0][0], pl[0][1], pl[1][0], pl[1][1], pl[2][0], pl[2][1], hl[0], hl[1], e0, e1);
    bstore8<0>((u32x2){e0, e1}, rout, pi == 0 ? orow + 1024 + 32 * nt + 8 * g : OOB, 0);
  }
  STAMP(ws, 5, 7);
  STAMP_FLUSH(ws, 5);
}

extern "C" __global__ __launch_bounds__(512, 2) void k1_all(const void* x, const void* wqkv, const void* wo, const void* wg, const void* wu, const void* wd, char* ws, void* out) {
  Ctx c = {x, wqkv, wo, wg, wu, wd, ws, out, (int)rfl(threadIdx.x >> 6), (int)(blockIdx.x & 7), (int)(blockIdx.x >> 3), (unsigned)(size_t)smem, 0u, 0u};
  if (c.w == 7) {
    unsigned long long pc; unsigned back, size;
    asm volatile("s_getpc_b64 %0\n.Lk1pc%=:\n\ts_mov_b32 %1, .Lk1pc%=-k1_all\n\ts_mov_b32 %2, k9_end-k1_all" : "=s"(pc), "=s"(back), "=s"(size));
    const int lane = fresh_lane();
    warm(c.lds0 + LDS_SCR, lane < 24 ? (c.r * 24 + lane) * 128 : OOB, mk_rsrc((const void*)(pc - back), size), 0);
  }
  {
    const int mt = 4 * (c.xcd & 3) + (c.r & 3), nt = 8 * (c.xcd >> 2) + (c.r >> 2);
    if (c.w < 4) p1_body<0>(c, mt, nt); else p1_body<1>(c, mt, nt);
  }
  p2(c);
  if (c.r < 18) p3_gemm(c); else { cnt_wait(c, 1, 8); p3_side(c); }
  if (c.w < 4) k4_first<0>(c, fresh_lane());
  else { cnt_wait(c, 1, 8); k4_first<4>(c, fresh_lane()); }
  p4(c);
  p5(c);
  STAMP_OUT();
}

extern "C" __global__ void k9_end() {}

extern "C" void kernel_launch(void* const* d_in, const int* in_sizes, int n_in, void* d_out, int out_size, void* d_ws, size_t ws_size,
                              hipStream_t stream) {
  (void)hipFuncSetAttribute((const void*)k1_all, hipFuncAttributeMaxDynamicSharedMemorySize, LDS_ALL);
  k1_all<<<256, 512, LDS_ALL, stream>>>(d_in[0], d_in[1], d_in[2], d_in[4], d_in[5], d_in[6], (char*)d_ws, d_out);
}
```

```cpp
#define STAMPS
#define KB 1
#define IB 0
#define MB 1
#define CONSTD 0
#include <hip/hip_runtime.h>
#include <stdint.h>

typedef short bf16x8 __attribute__((ext_vector_type(8)));
typedef short bf16x4 __attribute__((ext_vector_type(4)));
typedef float f32x2 __attribute__((ext_vector_type(2)));
typedef float f32x4 __attribute__((ext_vector_type(4)));
typedef float f32x16 __attribute__((ext_vector_type(16)));
typedef unsigned u32x2 __attribute__((ext_vector_type(2)));
typedef unsigned u32x4 __attribute__((ext_vector_type(4)));
typedef __bf16 bf2_t __attribute__((ext_vector_type(2)));
typedef __amdgpu_buffer_rsrc_t rsrc_t;
#define LAS __attribute__((address_space(3)))
#define GAS __attribute__((address_space(1)))
#define DI __device__ __forceinline__

constexpr int S_ = 2048, D_ = 576, F_ = 4608;
constexpr float QSCALE = 0.23570226039551584f * 1.4426950408889634f;
constexpr unsigned ONES2 = 0x3f803f80u;

constexpr size_t QREC = 1280, KVREC = 2560;
constexpr unsigned OOB = 0x7fffff00u;
#define FS1 64
#define FS2 64
#define FS3 64
#define FS5 64
constexpr unsigned FL1 = 128;
constexpr unsigned FL2 = FL1 + 256 * 64;
constexpr unsigned FL3 = FL2 + 2048 * 64;
constexpr unsigned FLS = FL3 + 2048 * 64;
constexpr unsigned FL4 = FLS + 128 * 64;
constexpr unsigned FL5 = FL4 + 3 * 2048 * 64;
constexpr unsigned FL_END = FL5 + 1024 * 64;
constexpr size_t WS_QF = 1u << 20;
constexpr size_t WS_KV = WS_QF + 32 * 64 * QREC;
constexpr size_t WS_OF = WS_KV + 32 * 64 * KVREC;
constexpr size_t WS_HF = WS_OF + 128 * 18 * 1024;
constexpr size_t WS_ST = WS_HF + 128 * 18 * 1024;
constexpr size_t WS_CS = WS_ST + 9 * 2048 * 8;
constexpr size_t WS_RS = WS_CS + 2 * 4608 * 4;
constexpr size_t WS_LG = WS_RS + 4 * 2048 * 4;
constexpr size_t WS_WT = WS_LG + 8 * 4 * 36864;
constexpr size_t WS_HH = WS_WT + 4 * 4608 * 32;
constexpr size_t PT_REC = 4608;
constexpr size_t WS_PT = WS_HH + (size_t)128 * 144 * 1024;
constexpr size_t WS_END = WS_PT + (size_t)64 * 8 * 4 * PT_REC;

DI unsigned rfl(unsigned v) { return __builtin_amdgcn_readfirstlane(v); }
DI rsrc_t mk_rsrc(const void* p, unsigned bytes) { return __builtin_amdgcn_make_buffer_rsrc((void*)p, (short)0, (int)bytes, 0x00020000); }
DI void glds16(unsigned lds, unsigned voff, rsrc_t r, unsigned soff) {
  asm volatile("s_mov_b32 m0, %0\n\ts_nop 0\n\tbuffer_load_dwordx4 %1, %2, %3 offen lds" ::"s"(lds), "v"(voff), "s"(r), "s"(soff) : "memory", "m0");
}
DI void glds16c(unsigned lds, unsigned voff, rsrc_t r, unsigned soff) {
  asm volatile("s_mov_b32 m0, %0\n\ts_nop 0\n\tbuffer_load_dwordx4 %1, %2, %3 offen sc1 lds" ::"s"(lds), "v"(voff), "s"(r), "s"(soff) : "memory", "m0");
}
template <class T> DI void bload16c(T& d, unsigned voff, rsrc_t r, unsigned soff) {
  asm volatile("buffer_load_dwordx4 %0, %1, %2, %3 offen sc1" : "=v"(d) : "v"(voff), "s"(r), "s"(soff) : "memory");
}
DI void bload8c(u32x2& d, unsigned voff, rsrc_t r, unsigned soff) {
  asm volatile("buffer_load_dwordx2 %0, %1, %2, %3 offen sc1" : "=v"(d) : "v"(voff), "s"(r), "s"(soff) : "memory");
}
DI void bload4c(unsigned& d, unsigned voff, rsrc_t r, unsigned soff) {
  asm volatile("buffer_load_dword %0, %1, %2, %3 offen sc1" : "=v"(d) : "v"(voff), "s"(r), "s"(soff) : "memory");
}
DI void bload8(u32x2& d, unsigned voff, rsrc_t r, unsigned soff) {
  asm volatile("buffer_load_dwordx2 %0, %1, %2, %3 offen" : "=v"(d) : "v"(voff), "s"(r), "s"(soff) : "memory");
}
DI void warm(unsigned lds, unsigned voff, rsrc_t r, unsigned soff) {
  asm volatile("s_mov_b32 m0, %0\n\ts_nop 0\n\tbuffer_load_dword %1, %2, %3 offen lds" ::"s"(lds), "v"(voff), "s"(r), "s"(soff) : "memory", "m0");
}
template <int N> DI void wait_vm() { asm volatile("s_waitcnt vmcnt(%0)" ::"i"(N) : "memory"); }
#define PIN(x) asm volatile("" : "+v"(x))
#define SBAR() __builtin_amdgcn_sched_barrier(0)
DI void bar() { SBAR(); asm volatile("s_barrier" ::: "memory"); SBAR(); }
DI void lds_bar() { SBAR(); asm volatile("s_waitcnt lgkmcnt(0)\n\ts_barrier" ::: "memory"); SBAR(); }
DI bf16x8 trfrag(unsigned a0, unsigned a1) {
  bf16x4 lo = __builtin_amdgcn_ds_read_tr16_b64_v4i16((LAS bf16x4*)(size_t)a0);
  bf16x4 hi = __builtin_amdgcn_ds_read_tr16_b64_v4i16((LAS bf16x4*)(size_t)a1);
  return __builtin_shufflevector(lo, hi, 0, 1, 2, 3, 4, 5, 6, 7);
}
DI bf16x4 trh(unsigned a) { return __builtin_amdgcn_ds_read_tr16_b64_v4i16((LAS bf16x4*)(size_t)a); }
DI bf16x8 cat(bf16x4 lo, bf16x4 hi) { return __builtin_shufflevector(lo, hi, 0, 1, 2, 3, 4, 5, 6, 7); }
template <class T> DI T ldsr(unsigned a) { return *(LAS T*)(size_t)a; }
template <class T> DI void ldsw(unsigned a, T v) { *(LAS T*)(size_t)a = v; }
DI unsigned pk2(float a, float b) { f32x2 v = {a, b}; return __builtin_bit_cast(unsigned, __builtin_convertvector(v, bf2_t)); }
DI float bflo(unsigned u) { return __uint_as_float(u << 16); }
DI float bfhi(unsigned u) { return __uint_as_float(u & 0xffff0000u); }
DI f32x4 mfma16(bf16x8 a, bf16x8 b, f32x4 c) { return __builtin_amdgcn_mfma_f32_16x16x32_bf16(a, b, c, 0, 0, 0); }
DI void mfma16_ip(f32x4& c, bf16x8 a, bf16x8 b) { asm volatile("v_mfma_f32_16x16x32_bf16 %0, %1, %2, %0" : "+v"(c) : "v"(a), "v"(b)); }
DI void mfma16_z(f32x4& c, bf16x8 a, bf16x8 b) { asm volatile("v_mfma_f32_16x16x32_bf16 %0, %1, %2, 0" : "=&v"(c) : "v"(a), "v"(b)); }
DI f32x16 mfma32(bf16x8 a, bf16x8 b, f32x16 c) { return __builtin_amdgcn_mfma_f32_32x32x16_bf16(a, b, c, 0, 0, 0); }
DI float xor32(float v) { auto r = __builtin_amdgcn_permlane32_swap(__float_as_uint(v), __float_as_uint(v), false, false); return __uint_as_float((threadIdx.x & 32) ? r[0] : r[1]); }
DI float sum_x32(float v) { auto r = __builtin_amdgcn_permlane32_swap(__float_as_uint(v), __float_as_uint(v), false, false); return __uint_as_float(r[0]) + __uint_as_float(r[1]); }
DI float max_x32(float v) { auto r = __builtin_amdgcn_permlane32_swap(__float_as_uint(v), __float_as_uint(v), false, false); return fmaxf(__uint_as_float(r[0]), __uint_as_float(r[1])); }
DI float sum_x16(float v) { auto r = __builtin_amdgcn_permlane16_swap(__float_as_uint(v), __float_as_uint(v), false, false); return __uint_as_float(r[0]) + __uint_as_float(r[1]); }
template <int AUX> DI void bstore16(u32x4 v, rsrc_t r, unsigned voff, unsigned soff) {
  if constexpr (AUX == 16) asm volatile("buffer_store_dwordx4 %0, %1, %2, %3 offen sc1\n\ts_nop 1" ::"v"(v), "v"(voff), "s"(r), "s"(soff) : "memory");
  else asm volatile("buffer_store_dwordx4 %0, %1, %2, %3 offen\n\ts_nop 1" ::"v"(v), "v"(voff), "s"(r), "s"(soff) : "memory");
}
template <int AUX> DI void bstore8(u32x2 v, rsrc_t r, unsigned voff, unsigned soff) { __builtin_amdgcn_raw_buffer_store_b64(v, r, voff, soff, AUX); }
template <int AUX> DI void bstore4(unsigned v, rsrc_t r, unsigned voff, unsigned soff) { __builtin_amdgcn_raw_buffer_store_b32(v, r, voff, soff, AUX); }

template <int... I> struct iseq {};
template <int N, int... I> struct mkseq : mkseq<N - 1, N - 1, I...> {};
template <int... I> struct mkseq<0, I...> { typedef iseq<I...> type; };
template <int V> struct ic { static constexpr int value = V; };
template <class Fn, int... I> DI void sfor_(Fn&& f, iseq<I...>) { (f(ic<I>{}), ...); }
template <int N, class Fn> DI void sfor(Fn&& f) { sfor_(f, typename mkseq<N>::type{}); }

extern __shared__ __attribute__((aligned(16))) char smem[];
constexpr size_t WS_DBG = 64u << 20;
constexpr int LDS_DBG = 156 * 1024;
#define STAMP(ws, kid, i) do { SBAR(); unsigned long long t_, c_; asm volatile("s_memrealtime %0\n\ts_memtime %1\n\ts_waitcnt lgkmcnt(0)" : "=s"(t_), "=s"(c_) :: "memory"); \
  asm volatile("v_writelane_b32 %0, %1, %3\n\tv_writelane_b32 %0, %2, %4" : "+v"(stamp_v) : "s"((unsigned)t_), "s"((unsigned)c_), "i"(i), "i"(8 + i)); SBAR(); } while (0)
#define STAMP_DECL unsigned stamp_v = 0
#define STAMP_FLUSH(ws, kid) do { const int l_ = fresh_lane(); if (l_ < 16) ldsw<unsigned>(c.lds0 + LDS_DBG + ((c.w * 7 + kid) * 16 + l_) * 4, stamp_v); } while (0)
#define STAMP_OUT() do { const int l_ = fresh_lane(); if (l_ < 16) for (int k_ = 1; k_ < 7; ++k_) if ((k_ == 3) == (c.r < 18) || (k_ != 3 && k_ != 6)) \
  *(GAS unsigned*)(c.ws + WS_DBG + ((((size_t)k_ * 256 + blockIdx.x) * 8 + c.w) * 16 + l_) * 4) = ldsr<unsigned>(c.lds0 + LDS_DBG + ((c.w * 7 + k_) * 16 + l_) * 4); } while (0)

struct Ctx {
  const void *x, *wqkv, *wo, *wg, *wu, *wd;
  char* ws; void* out;
  int w, xcd, r;
  unsigned lds0, t0, t1;
  unsigned long long te;
};
constexpr int LDS_ALL = 160 * 1024, LDS_SCR = LDS_ALL - 256;
DI int fresh_lane() { int l; asm volatile("v_mbcnt_lo_u32_b32 %0, -1, 0\n\tv_mbcnt_hi_u32_b32 %0, -1, %0" : "=v"(l)); __builtin_assume(l >= 0 && l < 64); return l; }
DI unsigned opq(unsigned v) { PIN(v); return v; }
constexpr int LDS_CNT = 147456;
DI void cnt_add(const Ctx& c, int i) {
  asm volatile("s_mov_b64 exec, 1\n\tds_add_u32 %0, %1\n\ts_mov_b64 exec, -1" ::"v"(c.lds0 + LDS_CNT + 4 * i), "v"(1u) : "memory");
}
DI unsigned cnt_get(const Ctx& c, int i) {
  unsigned v;
  asm volatile("ds_read_b32 %0, %1\n\ts_waitcnt lgkmcnt(0)" : "=v"(v) : "v"(c.lds0 + LDS_CNT + 4 * i) : "memory");
  return rfl(v);
}
DI void cnt_wait(const Ctx& c, int i, unsigned n) {
  if (__builtin_expect(cnt_get(c, i) < n, 0))
    for (int spin = 0; spin < (1 << 22) && cnt_get(c, i) < n; ++spin) __builtin_amdgcn_s_sleep(1);
}
DI rsrc_t fl_rsrc(const Ctx& c) { return mk_rsrc(c.ws, FL_END); }
DI void set_flag(const Ctx& c, unsigned off) {
  u32x2 t;
  asm volatile("v_mov_b32 %0, %1" : "=v"(t[0]) : "s"(c.t0));
  asm volatile("v_mov_b32 %0, %1" : "=v"(t[1]) : "s"(c.t1));
  asm volatile("s_mov_b64 exec, 1\n\tbuffer_store_dwordx2 %0, off, %1, %2 sc1\n\ts_mov_b64 exec, -1" ::"v"(t), "s"(fl_rsrc(c)), "s"(off) : "memory");
}
DI bool tag_ok(const Ctx& c, u32x2 v) { return __builtin_amdgcn_ballot_w64(v[0] != c.t0 || v[1] != c.t1) == 0; }
DI void poll(const Ctx& c, unsigned voff, unsigned soff) {
  for (int spin = 0; spin < (1 << 20); ++spin) {
    u32x2 v;
    asm volatile("buffer_load_dwordx2 %0, %1, %2, %3 offen sc1\n\ts_waitcnt vmcnt(0)" : "=&v"(v) : "v"(voff), "s"(fl_rsrc(c)), "s"(soff) : "memory");
    if (tag_ok(c, v)) break;
  }
}

DI bool poll_first(const Ctx& c, unsigned voff, unsigned soff, unsigned long long first) {
  unsigned long long m = 0;
  for (int spin = 0; spin < (1 << 20); ++spin) {
    u32x2 v;
    asm volatile("buffer_load_dwordx2 %0, %1, %2, %3 offen sc1\n\ts_waitcnt vmcnt(0)" : "=&v"(v) : "v"(voff), "s"(fl_rsrc(c)), "s"(soff) : "memory");
    m = __builtin_amdgcn_ballot_w64(v[0] != c.t0 || v[1] != c.t1);
    if ((m & first) == 0) break;
  }
  return m == 0;
}

constexpr int K1_SLOT = 32768, K1_TBL = K1_SLOT * 4;
extern "C" __global__ void k9_end();

template <int NN> DI void p1_body(Ctx& c, int mt, int nt) {
  char* ws = c.ws;
  const int w = c.w, lane = fresh_lane();
  const unsigned lds0 = c.lds0;
  const rsrc_t rx = mk_rsrc(c.x, S_ * D_ * 2), rw = mk_rsrc(c.wqkv, D_ * 3 * D_ * 2);
  const int t16 = lane & 15, g = lane >> 4, q4 = (lane >> 2) & 3, p = lane & 3, m = w & 3;
  constexpr int NT = NN ? 3 : 4;
  unsigned xv[2], wv[2];
#pragma unroll
  for (int i = 0; i < 2; ++i) {
    const int row = 8 * (2 * w + i) + (lane >> 3);
    xv[i] = (128 * mt + row) * 1152 + 16 * ((lane & 7) ^ ((row >> 1) & 7));
    int li = 64 * (2 * w + i) + lane;
    if (li >= 896) li -= 128;
    const int r64 = (li * 4682) >> 16, cc = li - 14 * r64;
    const int rho = r64 & 31, k = (r64 & 32) + 8 * ((rho >> 2) & 3) + 4 * (rho >> 4) + (rho & 3);
    wv[i] = k * 3456 + ((216 * nt) & ~15) + 16 * cc;
  }
  STAMP_DECL; STAMP(ws, 1, 3);
  asm volatile("v_writelane_b32 %0, %1, 0" : "+v"(stamp_v) : "s"((unsigned)c.te));

  auto issue1 = [&](int slab, int i) {
    const unsigned dst = lds0 + (slab & 3) * K1_SLOT + w * 2048 + (i >> 1) * 1024;
    if (i & 1) glds16(dst + 16384, wv[i >> 1], rw, slab * 64 * 3456);
    else glds16(dst, xv[i >> 1], rx, slab * 128);
  };
#pragma unroll
  for (int sl = 0; sl < 2; ++sl)
#pragma unroll
    for (int i = 0; i < 4; ++i) issue1(sl, i);
  u32x2 ev;
  bload8c(ev, 0u, fl_rsrc(c), 0);
  STAMP(ws, 1, 1);
  const unsigned wa = lds0 + 16384 + (4 * g + q4) * 224 + (nt & 1) * 8;
  unsigned ad[2][5];
  ad[0][0] = wa + 8 * p + 144 * NN;
  ad[0][1] = wa + 72 * (p < 3 ? p : 2) + 32;
  ad[0][2] = NN && m >= 2 ? ad[0][1] : ad[0][0] + 72 * (m >> 1) + 40 * (m & 1);
  ad[0][3] = lds0 + (32 * m + t16) * 128 + 16 * (g ^ ((t16 >> 1) & 7));
  ad[0][4] = ad[0][3] ^ 64;
#pragma unroll
  for (int i = 0; i < 5; ++i) { ad[1][i] = ad[0][i] + 2 * K1_SLOT; PIN(ad[1][i]); }
  const unsigned tb = lds0 + K1_TBL, cb = tb + 1024, dummy = cb + 512;
  unsigned e_cs = t16 == 0 ? cb + (w * 16 + 4 * g) * 4 : dummy, e_st = tb + (32 * m + t16) * 8, e_cc = cb + (64 * NN + 4 * g) * 4;
  unsigned e_s1 = g == 3 ? e_st : dummy, e_s2 = g == q4 ? e_st + 4 : dummy, e_vo[2], e_so[4], e_t2 = 0, e_one = 0;
  unsigned e_sc[2];
  float e_tsc = 0.f;
  {
    constexpr unsigned KV0 = WS_KV - WS_QF;
    const int tile32 = 4 * mt + m, key = 16 * (g >> 1) + t16;
    const unsigned lo_qk = (g & 1) * 512 + key * 16, lo_v = 1280 + key * 32 + (g & 1) * 16;
#pragma unroll
    for (int i = 0; i < 2 - NN; ++i) {
      const int pg = 3 * nt + 2 * NN + i, type = pg >> 4, rl = type ? KVREC : QREC;
      e_so[2 * i] = (type ? KV0 : 0) + (2 * (pg & 15) * 64 + tile32) * rl;
      e_so[2 * i + 1] = e_so[2 * i] + 64 * rl;
      e_vo[i] = type == 2 ? lo_v : lo_qk;
      e_sc[i] = type ? 0x3f800000u : __builtin_bit_cast(unsigned, QSCALE);
      asm volatile("" : "+s"(e_so[2 * i]), "+s"(e_so[2 * i + 1]), "+s"(e_sc[i]), "+v"(e_vo[i]));
    }
    if constexpr (NN) {
      const int pg = 3 * nt + g, type = pg >> 4, rl = type ? KVREC : QREC;
      e_vo[1] = g < 3 ? (type ? KV0 : 0) + (2 * (pg & 15) * 64 + tile32) * rl + (type == 2 ? 2304 : 1024) + t16 * 8 : OOB;
      e_t2 = e_vo[1] + 64 * rl;
      e_one = type == 1 ? 0u : 0x00003f80u;
      e_tsc = type ? 1.f : QSCALE;
      PIN(e_vo[1]); PIN(e_t2); PIN(e_one); PIN(e_tsc); PIN(e_s1); PIN(e_s2);
    }
    PIN(e_cs); PIN(e_st); PIN(e_cc);
  }
  f32x4 acc[4][2] = {}, cs = {};
  bf16x8 ones = __builtin_bit_cast(bf16x8, (u32x4){ONES2, ONES2, ONES2, ONES2});
  PIN(ones);
  bf16x8 fb[2][2];
  bf16x4 fa[2][NT + 1][2];
  constexpr int NR = 2 * NT + 4;
  auto rd1 = [&](auto KS, auto I) {
    constexpr int ks = KS.value, i = I.value, sp = ks & 1, sl = (ks >> 1) & 3, ti = (i - 2) >> 1;
    constexpr unsigned so = (sl & 1) * K1_SLOT;
    if constexpr (ks < 18 && i < NR) {
      if constexpr (i < 2) fb[sp][i] = ldsr<bf16x8>(ad[sl >> 1][3 + sp] + so + i * 2048);
      else {
        constexpr int ai = ti == NT ? 2 : (NN && ti == 2) ? 1 : 0;
        constexpr unsigned co = ai ? 0 : 72 * (ti >> 1) + 40 * (ti & 1);
        fa[sp][ti][i & 1] = trh(ad[sl >> 1][ai] + so + co + sp * 32 * 224 + (i & 1) * 16 * 224);
      }
    }
  };
  sfor<9>([&](auto SL) {
    constexpr int sl = SL.value;
    constexpr int iss = sl == 0 ? 2 : (sl + 3 < 9 ? sl + 3 : 9);
    constexpr int need = sl == 0 ? 0 : (sl + 1 < 9 ? sl + 1 : 8);
    if constexpr (sl == 1) asm volatile("s_waitcnt vmcnt(%1)" : "+v"(ev) : "i"(4 * (iss - need - 1)) : "memory");
    else wait_vm<4 * (iss - need - 1) + (sl == 0)>();
    bar();
    if constexpr (sl == 0) STAMP(ws, 1, 2);
    if constexpr (sl <= 1) sfor<NR>([&](auto I) { rd1(ic<2 * sl>{}, I); });
    if constexpr (sl == 1) {
      const unsigned long long e = (((unsigned long long)rfl(ev[1]) << 32) | rfl(ev[0])) + 1ull;
      c.t0 = (unsigned)e; c.t1 = (unsigned)(e >> 32);
    }
    sfor<2>([&](auto SP) {
      constexpr int sp = SP.value, ks = 2 * sl + sp;
      bf16x8 tl;
      if constexpr (NN) tl = t16 >= 12 ? ones : cat(fa[sp][2][0], fa[sp][2][1]);
      sfor<9>([&](auto M) {
        constexpr int mi = M.value, ti = mi >> 1;
        if constexpr (mi == 8) cs = mfma16(cat(fa[sp][NT][0], fa[sp][NT][1]), ones, cs);
        else if constexpr (NN && ti == 3) acc[3][mi & 1] = mfma16(fb[sp][mi & 1], fb[sp][mi & 1], acc[3][mi & 1]);
        else if constexpr (NN && ti == 2) acc[2][mi & 1] = mfma16(tl, fb[sp][mi & 1], acc[2][mi & 1]);
        else acc[ti][mi & 1] = mfma16(cat(fa[sp][ti][0], fa[sp][ti][1]), fb[sp][mi & 1], acc[ti][mi & 1]);
        SBAR();
        if constexpr (ks != 1) {
          if constexpr (mi < 3) { rd1(ic<ks + 1>{}, ic<2 * mi>{}); rd1(ic<ks + 1>{}, ic<2 * mi + 1>{}); }
          else rd1(ic<ks + 1>{}, ic<3 + mi>{});
        }
        if constexpr (sl == 0) { if constexpr (mi & 1) issue1(2 + sp, mi >> 1); }
        else if constexpr (sl + 3 < 9 && (mi == 2 + 2 * NN || mi == 6 + 2 * NN)) issue1(sl + 3, 2 * sp + (mi >= 6));
        SBAR();
      });
    });
  });
  STAMP(ws, 1, 4);
  ldsw<f32x4>(e_cs, cs);
  if constexpr (NN) sfor<2>([&](auto T) {
    const f32x4 gr = acc[3][T.value];
    const float d01 = (p & 1) ? gr[1] : gr[0], d23 = (p & 1) ? gr[3] : gr[2];
    ldsw<float>(e_s1 + 128 * T.value, acc[2][T.value][0]);
    ldsw<float>(e_s2 + 128 * T.value, (p & 2) ? d23 : d01);
  });
  lds_bar();
  f32x2 sv[2];
  f32x4 cc[4];
  sfor<2>([&](auto T) { sv[T.value] = ldsr<f32x2>(e_st + 128 * T.value); });
  sfor<NT>([&](auto I) { cc[I.value] = ldsr<f32x4>(e_cc + 64 * I.value); });
  SBAR();
  float nmu[2], rs[2];
  sfor<2>([&](auto T) {
    nmu[T.value] = sv[T.value][0] * (-1.f / D_);
    rs[T.value] = __builtin_amdgcn_rsqf(fmaxf(sv[T.value][1] * (1.f / D_) - nmu[T.value] * nmu[T.value], 0.f) + 1e-5f);
  });
  const rsrc_t ro = mk_rsrc(ws + WS_QF, 32 * 64 * (QREC + KVREC));
  sfor<(NN ? 2 : 4)>([&](auto TI) {
    constexpr int ti = TI.value;
    const f32x4 v0 = (acc[ti][0] + cc[ti] * nmu[0]) * (rs[0] * __uint_as_float(e_sc[ti >> 1])), v1 = (acc[ti][1] + cc[ti] * nmu[1]) * (rs[1] * __uint_as_float(e_sc[ti >> 1]));
    auto s0 = __builtin_amdgcn_permlane32_swap(pk2(v0[0], v0[1]), pk2(v1[0], v1[1]), false, false);
    auto s1 = __builtin_amdgcn_permlane32_swap(pk2(v0[2], v0[3]), pk2(v1[2], v1[3]), false, false);
    bstore16<16>((u32x4){s0[0], s1[0], s0[1], s1[1]}, ro, e_vo[ti >> 1], e_so[ti]);
  });
  if constexpr (NN) sfor<2>([&](auto T) {
    const f32x4 v = (acc[2][T.value] + cc[2] * nmu[T.value]) * (rs[T.value] * e_tsc);
    bstore8<16>((u32x2){pk2(v[0], v[1]), e_one}, ro, e_vo[1], T.value * 128);
    bstore8<16>((u32x2){pk2(v[2], v[3]), e_one}, ro, e_t2, T.value * 128);
  });
  STAMP(ws, 1, 5);
  wait_vm<0>();
  STAMP(ws, 1, 6);
  bar();
  if (w == 0) set_flag(c, FL1 + (mt * 16 + nt) * FS1);
  STAMP(ws, 1, 7);
  STAMP_FLUSH(ws, 1);
}

constexpr int P3_WO = 73728;
constexpr int K4_RING = 8192;
constexpr unsigned k4_so(int u) { return ((((u >> 1) + 2) & 3) * 2 + (u & 1)) * 8192; }
DI unsigned k4_wv(int lane, int w, int Qd, int j) {
  const int rho = 8 * (w & 3) + (lane >> 3);
  return rho * 9216 + 1152 * Qd + 256 * j + 32 * (((lane & 7) >> 1) ^ ((rho >> 1) & 3)) + 16 * (lane & 1);
}

template <int U0> DI void k4_first(const Ctx& c, int lane) {
  const rsrc_t rg = mk_rsrc(c.wg, D_ * F_ * 2), ru = mk_rsrc(c.wu, D_ * F_ * 2);
  const unsigned wv4 = k4_wv(lane, c.w, c.xcd, c.r & 3), dst = c.lds0 + K4_RING + (c.w & 3) * 1024;
  sfor<4>([&](auto U) {
    glds16(dst + k4_so(U0 + U.value), wv4, rg, (U0 + U.value) * 32 * 9216);
    glds16(dst + 4096 + k4_so(U0 + U.value), wv4, ru, (U0 + U.value) * 32 * 9216);
  });
}
DI void p2_ahead(const Ctx& c, unsigned flag) {
  const int w = c.w, r = c.r, lane = fresh_lane();
  const rsrc_t rwo = mk_rsrc(c.wo, D_ * D_ * 2);
  const int nt = r >= 9 ? r - 9 : r;
  const int rho = 8 * w + (lane >> 3), rg = (rho >> 2) & 3, rj = 4 * (rho >> 4) + (rho & 3);
  const unsigned cbyte = 128 * nt + 32 * (((lane & 7) >> 1) ^ ((rho >> 1) & 3)) + 16 * (lane & 1);
  const int th = 4 * rg + (rj >> 1);
  const unsigned wv = (20 * (rg >> 1) + 8 * (rg & 1) + rj) * 1152 + cbyte, wvt = (18 * th + ((th & 1) ? 0 : 16) + (rj & 1)) * 1152 + cbyte;
  sfor<16>([&](auto K) {
    glds16(c.lds0 + P3_WO + (w + 4 * K.value) * 1024, wv, rwo, K.value * 36 * 1152);
    if constexpr (K.value == 7) { wait_vm<8>(); set_flag(c, flag); }
  });
  sfor<2>([&](auto K) { glds16(c.lds0 + P3_WO + (w + 64 + 4 * K.value) * 1024, wvt, rwo, K.value * 288 * 1152); });
}

DI void p2(const Ctx& c) {
  const int head = 4 * c.xcd + (c.r & 3), qb = c.r >> 2;
  char* ws = c.ws;
  const int w = c.w, lane = fresh_lane();
  const unsigned lds0 = c.lds0;
  const int q = lane & 31, h = lane >> 5, G = lane >> 4;
  const int qt = 8 * qb + w;
  const rsrc_t rq = mk_rsrc(ws + WS_QF, 32 * 64 * QREC), rkv = mk_rsrc(ws + WS_KV, 32 * 64 * KVREC);
  STAMP_DECL; STAMP(ws, 2, 0);
  unsigned kb = lds0 + w * KVREC + lane * 16, ktb = lds0 + w * KVREC + 1024 + q * 8;
  unsigned vb[4];
#pragma unroll
  for (int i = 0; i < 4; ++i) { const int row = 8 * i + 4 * (G >> 1) + ((lane >> 2) & 3); vb[i] = lds0 + w * KVREC + ((G & 1) ? 2304 + row * 8 : 1280 + row * 32 + 8 * (lane & 3)); PIN(vb[i]); }
  unsigned mf[16], mk[8];
#pragma unroll
  for (int e = 0; e < 16; ++e) { mf[e] = ((e & 3) + 8 * (e >> 2) <= q - 4 * h) ? ~0u : 0u; PIN(mf[e]); }
#pragma unroll
  for (int e = 0; e < 8; ++e) { mk[e] = (mf[2 * e] & 0xffffu) | (mf[2 * e + 1] & 0xffff0000u); PIN(mk[e]); }
  const unsigned tt = 2 * qt + (q >> 4);
  unsigned om_o = (tt * 18 + (head >> 1)) * 1024 + ((q & 15) + 16 * (2 * (head & 1) + h)) * 16;
  unsigned ot_o = h ? OOB : (tt * 18 + 16 + (head >> 4)) * 1024 + ((q & 15) + 16 * ((head >> 2) & 3)) * 16 + (head & 3) * 4;
  unsigned l16 = lane * 16, qto = h ? OOB : 1024 + q * 8, wmo;
  const int wmv = c.r * 8 + w;
  {
    const int l = (wmv >= 162 ? wmv - 162 : wmv >= 81 ? wmv - 81 : wmv) * 64 + lane, row = (l * 7282) >> 16;
    wmo = wmv >= 162 ? l * 128 : row * 9216 + c.xcd * 1152 + (l - 9 * row) * 128;
    PIN(wmo);
  }
  PIN(kb); PIN(ktb); PIN(om_o); PIN(ot_o); PIN(l16); PIN(qto);
  if (qb == 0) {
#pragma unroll
    for (int i = 0; i < 3; ++i) {
      const unsigned o = min(w * 64 + lane + 512 * i, 1279) * 16, m = (o % KVREC - 1024 < 256) ? 0x0000f149u : 0u;
      ldsw<u32x4>(lds0 + o, (u32x4){0u, m, 0u, m});
    }
  }
#define TSL 0
  if (TSL && w) __builtin_amdgcn_s_sleep(TSL);
  if (w >= 1 && w <= 3) warm(lds0 + LDS_SCR, (w * 64 + lane < 224 ? (w * 64 + lane - 64) * 128 : OOB), rkv, (head * 64 + 8 * qb) * KVREC);
  if (w == 4 || w == 5) warm(lds0 + LDS_SCR, (w * 64 + lane < 336 ? (w * 64 + lane - 256) * 128 : OOB), rq, (head * 64 + 8 * qb) * QREC);
  if (w == 0) {
    const int ty = lane < 4 ? 1 : lane < 8 ? 2 : 0, pm = max(2 * qb + (ty ? (lane & 3) - 2 : (lane & 1)), 0);
    ldsw<unsigned>(lds0 + LDS_CNT + 4 * (lane & 7), 0u);
    poll(c, (pm * 16 + (((16 * ty + (head >> 1)) * 43) >> 7)) * FS1, FL1);
  }
  lds_bar();
  STAMP(ws, 2, 7);
  bf16x8 qm; u32x2 qt2;
#pragma unroll
  for (int i = 0; i < 5; ++i) { const int pc = max(w + 8 * i, qb ? 0 : 20 + w); glds16c(lds0 + pc * 1024, l16, rkv, (head * 64 + 8 * qb - 8) * KVREC + pc * 1024); }
  bload16c(qm, l16, rq, (head * 64 + qt) * QREC);
  bload8c(qt2, qto, rq, (head * 64 + qt) * QREC);
  auto toff = [&](int i) { return (unsigned)(i * KVREC); };
  STAMP(ws, 2, 1);
  asm volatile("s_waitcnt vmcnt(0)" : "+v"(qm), "+v"(qt2)::"memory");
  bar();
  STAMP(ws, 2, 2);
  const bf16x4 qt4 = __builtin_bit_cast(bf16x4, qt2);
  bf16x8 km[9]; bf16x4 kt[9];
  constexpr int ord[9] = {0, 8, 1, 2, 3, 4, 5, 6, 7};
  sfor<9>([&](auto I) { constexpr int i = ord[I.value]; km[i] = ldsr<bf16x8>(kb + toff(i)); kt[i] = ldsr<bf16x4>(ktb + toff(i)); });
  SBAR();
  f32x16 s[9];
  float m4[4];
  auto qk = [&](auto I, auto J) {
    constexpr int i = I.value, j = J.value;
    f32x16 a = mfma32(km[i], qm, (f32x16){}), b;
    if constexpr (j < 9) b = mfma32(km[j], qm, (f32x16){});
    s[i] = __builtin_amdgcn_mfma_f32_32x32x8bf16_1k(kt[i], qt4, a, 0, 0, 0);
    if constexpr (j < 9) s[j] = __builtin_amdgcn_mfma_f32_32x32x8bf16_1k(kt[j], qt4, b, 0, 0, 0);
    SBAR();
  };
  auto mx1 = [&](auto I) {
    constexpr int i = I.value;
#pragma unroll
    for (int e = 0; e < 8; ++e) m4[e & 3] = fmaxf(fmaxf(m4[e & 3], s[i][2 * e]), s[i][2 * e + 1]);
  };
  bf16x4 vf[2][4];
  auto rdv = [&](int set, unsigned off) {
#pragma unroll
    for (int i = 0; i < 4; ++i) vf[set][i] = trh(vb[i] + off);
  };
  qk(ic<0>{}, ic<8>{});
  qk(ic<1>{}, ic<2>{});
  {
    size_t p0 = (size_t)c.wg, p1 = (size_t)c.wu, p2 = (size_t)c.wo;
    asm("" : "+s"(p0), "+s"(p1), "+s"(p2));
    p0 = wmv >= 81 ? p1 : p0;
    warm(lds0 + LDS_SCR, wmo, mk_rsrc((const void*)(wmv >= 162 ? p2 : p0), wmv >= 162 ? D_ * D_ * 2 : D_ * F_ * 2), 0);
  }
  SBAR();
#pragma unroll
  for (int e = 0; e < 16; ++e) s[8][e] = __uint_as_float((__float_as_uint(s[8][e]) & mf[e]) | (__float_as_uint(s[0][e]) & ~mf[e]));
#pragma unroll
  for (int e = 0; e < 4; ++e) asm("v_max_f32 %0, %1, %2\n\tv_max3_f32 %0, %0, %3, %4" : "=&v"(m4[e]) : "v"(s[8][4 * e]), "v"(s[8][4 * e + 1]), "v"(s[8][4 * e + 2]), "v"(s[8][4 * e + 3]));
  SBAR();
  qk(ic<3>{}, ic<4>{});
  mx1(ic<1>{}); mx1(ic<2>{}); SBAR();
  qk(ic<5>{}, ic<6>{});
  mx1(ic<3>{}); mx1(ic<4>{}); SBAR();
  qk(ic<7>{}, ic<9>{});
  rdv(1, toff(1));
  mx1(ic<5>{}); mx1(ic<6>{}); SBAR();
  mx1(ic<7>{});
  float mx = max_x32(fmaxf(fmaxf(m4[0], m4[1]), fmaxf(m4[2], m4[3])));
  STAMP(ws, 2, 3);
  f32x16 o = {};
  u32x4 pp;
  sfor<16>([&](auto H) {
    constexpr int hh = H.value, i = 1 + hh / 2, ks = hh & 1;
    f32x2 dd[4];
#pragma unroll
    for (int e = 0; e < 4; ++e) dd[e] = (f32x2){s[i][8 * ks + 2 * e], s[i][8 * ks + 2 * e + 1]} - (f32x2){mx, mx};
    SBAR();
    if constexpr (hh > 0 && hh < 15) o = mfma32(cat(vf[(1 + (hh - 1) / 2) & 1][2 * (ks ^ 1)], vf[(1 + (hh - 1) / 2) & 1][2 * (ks ^ 1) + 1]), __builtin_bit_cast(bf16x8, pp), o);
    if constexpr (hh == 15) {
      const u32x4 pd = {pp[0] & mk[0], pp[1] & mk[1], pp[2] & mk[2], pp[3] & mk[3]};
      o = mfma32(cat(vf[0][0], vf[0][1]), __builtin_bit_cast(bf16x8, pd), o);
      o = mfma32(cat(vf[1][0], vf[1][1]), __builtin_bit_cast(bf16x8, pp ^ pd), o);
    }
    SBAR();
    if constexpr (ks == 0) rdv((i + 1) & 1, toff(i < 8 ? i + 1 : 0));
    SBAR();
    float pv[8];
#pragma unroll
    for (int e = 0; e < 4; ++e) { pv[2 * e] = __builtin_amdgcn_exp2f(dd[e][0]); pv[2 * e + 1] = __builtin_amdgcn_exp2f(dd[e][1]); }
    SBAR();
    pp = (u32x4){pk2(pv[0], pv[1]), pk2(pv[2], pv[3]), pk2(pv[4], pv[5]), pk2(pv[6], pv[7])};
    SBAR();
  });
  {
    const u32x4 pd = {pp[0] & mk[4], pp[1] & mk[5], pp[2] & mk[6], pp[3] & mk[7]};
    o = mfma32(cat(vf[0][2], vf[0][3]), __builtin_bit_cast(bf16x8, pd), o);
    o = mfma32(cat(vf[1][2], vf[1][3]), __builtin_bit_cast(bf16x8, pp ^ pd), o);
  }
  PIN(o);
  STAMP(ws, 2, 4);
  auto lr = __builtin_amdgcn_permlane32_swap(__float_as_uint(o[10]), __float_as_uint(o[10]), false, false);
  const float inv = __builtin_amdgcn_rcpf(__uint_as_float(lr[0]));
  const rsrc_t ro = mk_rsrc(ws + WS_OF, 128 * 18 * 1024);
  const u32x4 om = {pk2(o[0] * inv, o[1] * inv), pk2(o[2] * inv, o[3] * inv), pk2(o[4] * inv, o[5] * inv), pk2(o[6] * inv, o[7] * inv)};
  bstore16<16>(om, ro, om_o, 0);
  bstore4<16>(pk2(o[8] * inv, o[9] * inv), ro, ot_o, 0);
  STAMP(ws, 2, 5);
  cnt_add(c, 1);
  if (w < 4 && c.r < 18) p2_ahead(c, FL2 + (qt * 32 + head) * FS2);
  else { wait_vm<0>(); set_flag(c, FL2 + (qt * 32 + head) * FS2); }
  STAMP(ws, 2, 6);
  STAMP_FLUSH(ws, 2);
}

DI void p3_side(const Ctx& c) {
  char* ws = c.ws;
  const int w = c.w, xcd = c.xcd, lane = fresh_lane();
  const unsigned lds0 = c.lds0;
  const int sb = c.r - 18, blk = sb * 8 + xcd;
  const rsrc_t rwt = mk_rsrc(ws + WS_WT, 4 * 4608 * 32), rlg = mk_rsrc(ws + WS_LG, 8 * 4 * 36864), rcs = mk_rsrc(ws + WS_CS, 2 * 4608 * 4);
  STAMP_DECL; STAMP(ws, 3, 0);
  const int mat = sb >= 5, un = sb < 5 ? 2 * sb : sb - 5;
  const rsrc_t rw = mk_rsrc(mat ? c.wu : c.wg, D_ * F_ * 2);
  const unsigned vo = (lane & 7) * 16 + (72 * w + (lane >> 3)) * 9216;
  u32x4 v[2][9];
#pragma unroll
  for (int i = 0; i < 9; ++i) v[0][i] = __builtin_amdgcn_raw_buffer_load_b128(rw, vo, (9 * xcd + un) * 128 + i * 8 * 9216, 0);
#pragma unroll
  for (int i = 0; i < 9; ++i) v[1][i] = __builtin_amdgcn_raw_buffer_load_b128(rw, sb < 4 ? vo : OOB, (9 * xcd + un + 1) * 128 + i * 8 * 9216, 0);
  const int drow = 64 * (blk - 40) + 8 * w + (lane >> 3);
  const u32x4 dv = __builtin_amdgcn_raw_buffer_load_b128(mk_rsrc(c.wd, F_ * D_ * 2), mat ? drow * 1152 + 1024 + (lane & 7) * 16 : OOB, 0, 0);
  if (un == 8) {
    const unsigned lo = ((lane & 7) >> 1) * 36864 + (72 * w + (lane >> 3)) * 32 + (lane & 1) * 16;
#pragma unroll
    for (int i = 0; i < 9; ++i) bstore16<16>(v[0][i], rlg, lo, (xcd * 8 + (sb >> 3)) * 18432 + i * 256);
  }
  const int pr = (drow % 576) >= 512 ? ((drow & ~24) | ((drow & 8) << 1) | ((drow & 16) >> 1)) : drow;
  bstore16<16>(dv, rwt, mat ? (((lane & 7) >> 1) * 4608 + pr) * 32 + (lane & 1) * 16 : OOB, 0);
  float a[2][8] = {};
#pragma unroll
  for (int k = 0; k < 2; ++k) {
#pragma unroll
    for (int i = 0; i < 9; ++i)
#pragma unroll
      for (int e = 0; e < 4; ++e) { a[k][2 * e] += bflo(v[k][i][e]); a[k][2 * e + 1] += bfhi(v[k][i][e]); }
#pragma unroll
    for (int e = 0; e < 8; ++e) a[k][e] = sum_x32(sum_x16(a[k][e]));
  }
  if (lane < 16) {
#pragma unroll
    for (int k = 0; k < 2; ++k) {
      ldsw<f32x4>(lds0 + k * 4096 + w * 512 + lane * 32, (f32x4){a[k][0], a[k][1], a[k][2], a[k][3]});
      ldsw<f32x4>(lds0 + k * 4096 + w * 512 + lane * 32 + 16, (f32x4){a[k][4], a[k][5], a[k][6], a[k][7]});
    }
  }
  lds_bar();
  if (w < 2) {
    float t = 0.f;
#pragma unroll
    for (int i = 0; i < 16; ++i) t += ldsr<float>(lds0 + w * 4096 + i * 256 + lane * 4);
    bstore4<16>(__float_as_uint(t), rcs, w == 0 || sb < 4 ? lane * 4 : OOB, ((sb >= 5 ? 4608 : 0) + (9 * xcd + un + w) * 64) * 4);
  }
  STAMP(ws, 3, 5);
  wait_vm<0>();
  bar();
  if (w == 0) set_flag(c, FLS + blk * 64);
  STAMP(ws, 3, 6);
  if (w == 5 && blk == 48) {
    for (int i = 0; i < 4; ++i) poll(c, lane * FS1, FL1 + i * 64 * FS1);
    set_flag(c, 0);
  }
  STAMP_FLUSH(ws, 6);
}

DI void p3_gemm(const Ctx& c) {
  char* ws = c.ws;
  const int w = c.w, lane = fresh_lane();
  const unsigned lds0 = c.lds0;
  const int t16 = lane & 15, g = lane >> 4;
  const int nt = c.r >= 9 ? c.r - 9 : c.r;
  const int T = 16 * c.xcd + 8 * (w >> 2) + 4 * (c.r >= 9) + (w & 3);
  const rsrc_t rof = mk_rsrc(ws + WS_OF, 128 * 18 * 1024);
  STAMP_DECL; STAMP(ws, 3, 0);
  unsigned wa[4];
  {
    const int r2 = 4 * g + ((lane >> 2) & 3);
#pragma unroll
    for (int i = 0; i < 4; ++i) wa[i] = lds0 + P3_WO + r2 * 128 + 32 * (i ^ ((r2 >> 1) & 3)) + 8 * (lane & 3);
  }
  u32x2 xr[4];
  sfor<4>([&](auto I) { bload8(xr[I.value], (16 * T + t16) * 1152 + 128 * nt + 8 * g, mk_rsrc(c.x, S_ * D_ * 2), 32 * I.value); });
  unsigned l16 = lane * 16, so = g == 0 ? t16 * 8 : OOB;
  PIN(l16); PIN(so);
  if (w < 4) { wait_vm<4>(); cnt_add(c, 0); }
  cnt_wait(c, 0, 4);
  STAMP(ws, 3, 1);
  constexpr int AH = 5;
  bf16x8 of[18];
  bf16x4 fr[AH + 1][4][2];
  unsigned wb[4];
#pragma unroll
  for (int i = 0; i < 4; ++i) { wb[i] = wa[i] + 65536; PIN(wb[i]); }
  auto rd1 = [&](auto KS, auto I) {
    constexpr int ks = KS.value, i = I.value;
    if constexpr (ks < 18) fr[ks % (AH + 1)][i >> 1][i & 1] = trh((ks < 16 ? wa : wb)[i >> 1] + (ks & 15) * 4096 + (i & 1) * 2048);
  };
  sfor<AH>([&](auto K) { sfor<8>([&](auto I) { rd1(K, I); }); });
  SBAR();
  const bool all = poll_first(c, (lane & 31) * FS2, FL2 + (T >> 1) * 32 * FS2, 0xffffull);
  asm volatile("" : "+v"(xr[0]), "+v"(xr[1]), "+v"(xr[2]), "+v"(xr[3]));
  sfor<8>([&](auto K) { bload16c(of[K.value], l16, rof, (T * 18 + K.value) * 1024); });
  f32x4 acc[4];
#pragma unroll
  for (int i = 0; i < 4; ++i) { acc[i] = (f32x4){bflo(xr[i][0]), bfhi(xr[i][0]), bflo(xr[i][1]), bfhi(xr[i][1])}; PIN(acc[i]); }
  if (!all) poll(c, (16 + (lane & 15)) * FS2, FL2 + (T >> 1) * 32 * FS2);
  STAMP(ws, 3, 2);
  sfor<10>([&](auto K) { bload16c(of[8 + K.value], l16, rof, (T * 18 + 8 + K.value) * 1024); });
  sfor<18>([&](auto KS) {
    constexpr int ks = KS.value;
    asm volatile("s_waitcnt vmcnt(%1)" : "+v"(of[ks]) : "i"(17 - ks) : "memory");
    if constexpr (ks == 0) STAMP(ws, 3, 3);
    if constexpr (ks == 9) STAMP(ws, 3, 4);
    sfor<4>([&](auto C) {
      mfma16_ip(acc[C.value], cat(fr[ks % (AH + 1)][C.value][0], fr[ks % (AH + 1)][C.value][1]), of[ks]);
      SBAR();
      rd1(ic<ks + AH>{}, ic<2 * C.value>{}); rd1(ic<ks + AH>{}, ic<2 * C.value + 1>{});
      SBAR();
    });
  });
  asm volatile("s_nop 9" : "+v"(acc[0]), "+v"(acc[1]), "+v"(acc[2]), "+v"(acc[3]));
  unsigned d[8];
#pragma unroll
  for (int i = 0; i < 4; ++i) { d[2 * i] = pk2(acc[i][0], acc[i][1]); d[2 * i + 1] = pk2(acc[i][2], acc[i][3]); }
  const rsrc_t rhf = mk_rsrc(ws + WS_HF, 128 * 18 * 1024);
  bstore16<16>((u32x4){d[0], d[1], d[2], d[3]}, rhf, l16, (T * 18 + 2 * nt) * 1024);
  bstore16<16>((u32x4){d[4], d[5], d[6], d[7]}, rhf, l16, (T * 18 + 2 * nt + 1) * 1024);
  float s1[2] = {0.f, 0.f}, s2[2] = {0.f, 0.f};
#pragma unroll
  for (int i = 0; i < 8; ++i) {
    const bf2_t v = __builtin_bit_cast(bf2_t, d[i]);
    s1[i & 1] = __builtin_amdgcn_fdot2_f32_bf16(v, __builtin_bit_cast(bf2_t, ONES2), s1[i & 1], false);
    s2[i & 1] = __builtin_amdgcn_fdot2_f32_bf16(v, v, s2[i & 1], false);
  }
  bstore8<16>((u32x2){__float_as_uint(sum_x32(sum_x16(s1[0] + s1[1]))), __float_as_uint(sum_x32(sum_x16(s2[0] + s2[1])))}, mk_rsrc(ws + WS_ST, 9 * 2048 * 8), so, (T * 9 + nt) * 128);
  STAMP(ws, 3, 5);
  wait_vm<0>();
  set_flag(c, FL3 + (T * 16 + nt) * FS3);
  STAMP(ws, 3, 6);
  STAMP_FLUSH(ws, 3);
}

constexpr int K4_LG = P3_WO;
constexpr int K4_CS = K4_LG + 36864;
constexpr int K4_STP = K4_CS + 4096;
struct K4Op { int kind, idx; };
struct K4Ops { int n; K4Op op[12]; };
constexpr K4Ops k4_ops(int S) {
  K4Ops o = {};
  auto add = [&](int k, int i) { o.op[o.n++] = {k, i}; };
  if (S == 3) add(9, 0);
  if (S == 0) for (int u = 4; u < 8; ++u) { add(1, u); add(2, u); }
  else for (int u = 2 * S + 6; u < 2 * S + 8 && u < 36; ++u) {
    if (S == 2) { add(0, 2 * u - 12); add(0, 2 * u - 11); }
    if (S > 2) add(0, u);
    if (u < 18) { add(1, u); add(2, u); }
  }
  if (S == 4) { add(5, 0); add(5, 1); add(5, 2); }
  if (S == 6) add(6, 0);
  if (S >= 10 && S < 15) add(3, S - 10);
  if (S == 12) add(7, 0);
  if (S == 14) add(8, 0);
  if (S == 10) add(4, 0);
  return o;
}
constexpr int k4_nst(int S) { return (S == 8 || S == 9) ? 2 : S == 7 ? 1 : 0; }
constexpr int k4_issued(int S) { int n = 8; for (int s = 0; s < S; ++s) n += k4_ops(s).n + k4_nst(s); return n; }
constexpr int k4_pos(int kind, int idx) {
  if (kind == 0 && idx < 8) return -1;
  if ((kind == 1 || kind == 2) && idx < 4) return 2 * idx + kind - 1;
  for (int S = 0;; ++S) for (int i = 0; i < k4_ops(S).n; ++i) if (k4_ops(S).op[i].kind == kind && k4_ops(S).op[i].idx == idx) return k4_issued(S) + k4_nst(S) + i;
}
constexpr int k4_need(int S) {
  if (S == 8) return k4_issued(7) - 1;
  if (S >= 16) return k4_issued(15) - 1;
  const int a = S < 9 ? k4_pos(2, 2 * S + 1) : -1, b = k4_pos(0, 2 * S + 2);
  return a > b ? a : b;
}
static_assert(k4_need(5) >= k4_pos(9, 0) && k4_need(7) >= k4_pos(5, 2) && k4_need(12) >= k4_issued(10) - 1, "");
constexpr int K5_SLOT = 17408, K5_BASE = 0, K5_P = 4;
constexpr unsigned k5_so(int v) { return K5_BASE + ((v + 7) & 7) * K5_SLOT; }
DI int k5_u(int v, int nt) {
  if (v < 32) { const int q = ((v >> 2) + nt) & 3; return 18 * (q >> 1) + 8 * (q & 1) + 4 * ((v >> 1) & 1) + (v & 1) + 2 * (v >> 4); }
  const int q = (v + nt) & 3;
  return 18 * (q >> 1) + 16 + (q & 1);
}
DI unsigned k5_mv(int rho, int sp, int nt, int t16) {
  const int rr = sp ? ((rho & 7) | ((rho & 8) << 1) | ((rho & 16) >> 1)) : rho;
  return rr * 1152 + 256 * nt + 32 * ((t16 >> 1) ^ (rho & 7)) + 16 * (t16 & 1);
}
DI unsigned k5_fl(int i, int sg, int mt5) { return (((((mt5 >> 1) * 4 + ((i >> 2) & 3)) * 8 + 2 * sg + ((i >> 4) & 1)) * 8) + 4 * (mt5 & 1) + (i & 3)) * 64; }

DI void p4(const Ctx& c) {
  char* ws = c.ws;
  const int w = c.w, r = c.r, xcd = c.xcd, lane = fresh_lane();
  const int mt = r >> 2, Qd = xcd, j = r & 3;
  const unsigned lds0 = c.lds0;
  const int g = lane >> 4;
  const int T0 = 16 * mt + 2 * w;
  const rsrc_t rwm = mk_rsrc(w < 4 ? c.wg : c.wu, D_ * F_ * 2), rhf = mk_rsrc(ws + WS_HF, 128 * 18 * 1024), rwd = mk_rsrc(c.wd, F_ * D_ * 2);
  const rsrc_t rhh = mk_rsrc(ws + WS_HH, 128 * 144 * 1024);
  const unsigned f4 = FL4 + ((r * 8 + xcd) * 8 + w) * 64;
  const int sg = xcd >> 1, mt5 = 8 * (xcd & 1) + (r >> 2);
  STAMP_DECL; STAMP(ws, 4, 0);
  const unsigned wv = opq(k4_wv(lane, w, Qd, j));
  const unsigned wdst = lds0 + K4_RING + w * 1024, l16 = opq(lane * 16);
  unsigned wa[4];
  {
    const int r2 = 4 * g + ((lane >> 2) & 3);
#pragma unroll
    for (int i = 0; i < 4; ++i) wa[i] = opq(lds0 + K4_RING + r2 * 128 + 32 * (i ^ ((r2 >> 1) & 3)) + 8 * (lane & 3));
  }
  const bool all = poll_first(c, (min(lane & 15, 8) + (lane & 16)) * FS3, FL3 + T0 * 16 * FS3, 0x30003ull);
  bf16x8 act[18][2];
  u32x2 pf;
  auto pf_off = [&] { const int z = opq(l16) >> 4; const unsigned a = opq(FL4 + k5_fl(z, sg, mt5)), b = opq(FLS + (40 + 18 * sg + min(z - 32, 17)) * 64); return z < 32 ? a : b; };
  auto doop = [&](auto KIND, auto IDX) {
    constexpr int kind = KIND.value, u = IDX.value;
    if constexpr (kind == 0) glds16(wdst + k4_so(u), wv, rwm, (u % 18) * 32 * 9216 + (u / 18) * 128);
    else if constexpr (kind <= 2) bload16c(act[u][kind - 1], l16, rhf, (T0 * 18 + 18 * (kind - 1) + u) * 1024);
    else if constexpr (kind == 3) {
      const int pc = min(w + 8 * u, 35);
      glds16c(lds0 + K4_LG + pc * 1024, l16, mk_rsrc(ws + WS_LG, 8 * 4 * 36864), (Qd * 4 + j) * 36864 + pc * 1024);
    }
    else if constexpr (kind == 4) warm(lds0 + LDS_SCR, l16 * 8, rwd, sg * 1327104 + min(r * 8 + w, 161) * 8192);
    else if constexpr (kind == 5) glds16c(lds0 + K4_STP + w * 3072 + u * 1024, l16, mk_rsrc(ws + WS_ST, 9 * 2048 * 8), T0 * 1152 + u * 1024);
    else if constexpr (kind == 6) {
      const unsigned z = opq(l16), f = (576 * Qd + (z < 512 ? 128 * j : 384 + 16 * j)) * 4 + z;
      glds16c(lds0 + K4_CS + (w & 1) * 1024, z < 576 ? f : OOB, mk_rsrc(ws + WS_CS, 2 * 4608 * 4), (w & 1) * 4608 * 4);
    } else if constexpr (kind == 7) set_flag(c, f4);
    else if constexpr (kind == 8) bload8c(pf, pf_off(), fl_rsrc(c), 0);
    else bload8c(pf, min(opq(l16), 13u * 16) * 32, fl_rsrc(c), FLS + xcd * 64);
  };
  sfor<4>([&](auto U) { doop(ic<1>{}, U); doop(ic<2>{}, U); });
  if (w < 4) cnt_add(c, 4);
  if (!all) poll(c, (min(fresh_lane() & 15, 8) + (fresh_lane() & 16)) * FS3, FL3 + T0 * 16 * FS3);
  cnt_wait(c, 4, 4);
  f32x4 ag[4][2], au[4][2], cg, cu;
  bf16x4 fr[4][2];
  unsigned hd[2][2], lga, csa;
  f32x2 nmu = {0.f, 0.f};
  auto rd1 = [&](auto H, auto I) {
    constexpr int n = H.value >> 1, m = H.value & 1, i = I.value;
    if constexpr (n < 144) fr[H.value & 3][i] = trh(wa[n % 4] + k4_so(n / 4) + 4096 * m + 2048 * i);
    else if constexpr (n < 162) fr[H.value & 3][i] = trh(lga + (n - 144) * 1024 + m * 18432 + i * 512);
  };
  auto rd = [&](auto H) { rd1(H, ic<0>{}); rd1(H, ic<1>{}); };
  auto mm = [&](auto N, auto KS, f32x4& g0, f32x4& g1, f32x4& u0, f32x4& u1, auto&& hook) {
    constexpr int n = N.value, ks = KS.value;
    constexpr int h0 = n == 15 ? 999 : 2 * n + 3, h1 = (n == 14 || n == 15) ? 999 : 2 * n + 4;
    const bf16x8 fg = cat(fr[(2 * n) & 3][0], fr[(2 * n) & 3][1]);
    if constexpr (ks == 0) mfma16_z(g0, fg, act[ks][0]); else mfma16_ip(g0, fg, act[ks][0]);
    SBAR(); rd1(ic<h0>{}, ic<0>{}); SBAR(); hook(ic<0>{}); SBAR();
    if constexpr (ks == 0) mfma16_z(g1, fg, act[ks][1]); else mfma16_ip(g1, fg, act[ks][1]);
    SBAR(); rd1(ic<h0>{}, ic<1>{}); SBAR(); hook(ic<1>{}); SBAR();
    const bf16x8 fu = cat(fr[(2 * n + 1) & 3][0], fr[(2 * n + 1) & 3][1]);
    if constexpr (ks == 0) mfma16_z(u1, fu, act[ks][1]); else mfma16_ip(u1, fu, act[ks][1]);
    SBAR(); rd1(ic<h1>{}, ic<0>{}); SBAR(); hook(ic<2>{}); SBAR();
    if constexpr (ks == 0) mfma16_z(u0, fu, act[ks][0]); else mfma16_ip(u0, fu, act[ks][0]);
    SBAR(); rd1(ic<h1>{}, ic<1>{}); SBAR(); hook(ic<3>{}); SBAR();
  };
  auto ldcs = [&](int e) { cg = ldsr<f32x4>(csa + 64 * e); cu = ldsr<f32x4>(csa + 1024 + 64 * e); };
  auto epi = [&](auto E, auto I) {
    constexpr int grp = E.value >> 2, cc = E.value & 3, i = I.value, T = 1 - (i >> 1);
    if constexpr ((i & 1) == 0) {
      ag[cc][T] += cg * nmu[T]; au[cc][T] += cu * nmu[T]; PIN(ag[cc][T]); PIN(au[cc][T]);
      if constexpr (i == 2 && E.value != 3) { SBAR(); ldcs(E.value + 1); }
    }
    else {
      const f32x4 v = ag[cc][T] * au[cc][T];
      const unsigned d0 = pk2(v[0], v[1]), d1 = pk2(v[2], v[3]);
      if constexpr ((cc & 1) == 0) { hd[T][0] = d0; hd[T][1] = d1; PIN(hd[T][0]); PIN(hd[T][1]); }
      else bstore16<16>((u32x4){hd[T][0], hd[T][1], d0, d1}, rhh, l16, ((T0 + T) * 144 + 18 * Qd + 4 * j + 2 * grp + (cc >> 1)) * 1024);
    }
  };
  f32x2 sp[3];
  float rs0;
  auto stats = [&](auto TT, auto P) {
    constexpr int T = TT.value, p = P.value;
    if constexpr (p == 0) {
      const unsigned sa = lds0 + K4_STP + w * 3072 + T * 1152 + (opq(l16) >> 1);
      sp[0] = ldsr<f32x2>(sa); sp[1] = ldsr<f32x2>(sa + 512); sp[2] = ldsr<f32x2>(sa + 1024);
    } else if constexpr (p == 4) { sp[0] = sp[0] + sp[1] + (opq(l16) < 256 ? sp[2] : (f32x2){0.f, 0.f}); PIN(sp[0]); }
    else if constexpr (p == 5) { sp[0][0] = sum_x16(sp[0][0]); PIN(sp[0]); }
    else if constexpr (p == 6) { sp[0][1] = sum_x16(sp[0][1]); PIN(sp[0]); }
    else if constexpr (p == 7) { sp[0][0] = sum_x32(sp[0][0]); PIN(sp[0]); }
    else if constexpr (p == 8) { sp[0][1] = sum_x32(sp[0][1]); PIN(sp[0]); }
    else if constexpr (p == 9) {
      nmu[T] = sp[0][0] * (-1.f / D_);
      const float rs = __builtin_amdgcn_rcpf(fmaxf(sp[0][1] * (1.f / D_) - nmu[T] * nmu[T], 0.f) + 1e-5f);
      PIN(nmu);
      if constexpr (T == 0) { rs0 = rs; PIN(rs0); }
      else {
        const unsigned z = opq(l16);
        bstore4<16>(__float_as_uint((z & 256) ? rs : rs0), mk_rsrc(ws + WS_RS + (sg * 2048 + 16 * T0) * 4, (Qd & 1) == 0 && j == 0 ? 128 : 0), z >> 2, 0);
      }
    }
  };
  unsigned mv5;
  auto pf5 = [&](auto N) {
    constexpr int n = N.value, v = (n - 1) >> 1;
    if constexpr (n == 0) {
      const int vv = min(w, K5_P - 1), uu = k5_u(vv, j);
      glds16c(lds0 + K5_BASE + ((vv + 7) & 7) * K5_SLOT + 16384, l16, mk_rsrc(ws + WS_WT, 4 * 4608 * 32), (j * 144 + 36 * sg + uu) * 1024);
      const unsigned z = opq(l16);
      mv5 = k5_mv(4 * w + (z >> 8), 0, j, (z >> 4) & 15);
    } else if constexpr (n & 1) glds16(lds0 + k5_so(v) + 8192 + w * 1024, mv5, rwd, sg * 1152 * 1152 + k5_u(v, j) * 32 * 1152);
    else glds16c(lds0 + k5_so(v) + w * 1024, l16, rhh, ((8 * mt5 + w) * 144 + 36 * sg + k5_u(v, j)) * 1024);
  };
  sfor<18>([&](auto SS) {
    constexpr int S = SS.value;
    constexpr int nw = k4_issued(S) - k4_need(S) - 1;
    static_assert(nw >= 0 && nw < 64, "");
    if constexpr (S == 5) asm volatile("s_waitcnt vmcnt(%5)" : "+v"(act[2 * S][0]), "+v"(act[2 * S][1]), "+v"(act[2 * S + 1][0]), "+v"(act[2 * S + 1][1]), "+v"(pf) : "i"(nw) : "memory");
    else if constexpr (S < 9) asm volatile("s_waitcnt vmcnt(%4)" : "+v"(act[2 * S][0]), "+v"(act[2 * S][1]), "+v"(act[2 * S + 1][0]), "+v"(act[2 * S + 1][1]) : "i"(nw) : "memory");
    else if constexpr (S == 16) asm volatile("s_waitcnt vmcnt(%1)" : "+v"(pf) : "i"(nw) : "memory");
    else wait_vm<nw>();
    if constexpr (S == 16) { if (__builtin_expect(!tag_ok(c, pf), 0)) poll(c, pf_off(), 0); }
    if constexpr (S == 5) { if (__builtin_expect(!tag_ok(c, pf), 0)) poll(c, min(opq(l16), 13u * 16) * 32, FLS + xcd * 64); }
    if constexpr (S >= 2) bar();
    if constexpr (S == 0) STAMP(ws, 4, 1);
    if constexpr (S == 2) STAMP(ws, 4, 7);
    if constexpr (S == 4) STAMP(ws, 4, 2);
    if constexpr (S == 9) STAMP(ws, 4, 3);
    if constexpr (S == 17) STAMP(ws, 4, 4);
    if constexpr (S == 0 || S == 2) { rd(ic<16 * S>{}); rd(ic<16 * S + 1>{}); rd(ic<16 * S + 2>{}); }
    sfor<8>([&](auto L) {
      constexpr int l = L.value, n = 8 * S + l, t = n / 4, cc = n % 4, ks = t % 18, no = k4_ops(S).n;
      mm(ic<n>{}, ic<ks>{}, ag[cc][0], ag[cc][1], au[cc][0], au[cc][1], [&](auto I) {
        constexpr int i = I.value;
        sfor<no>([&](auto O) {
          constexpr int nd = no ? no : 1, at = O.value * 32 / nd + 16 / nd;
          if constexpr (at == 4 * l + i) doop(ic<k4_ops(S).op[O.value].kind>{}, ic<k4_ops(S).op[O.value].idx>{});
        });
        if constexpr (S == 7) stats(ic<(l >> 2)>{}, ic<4 * (l & 3) + i>{});
        if constexpr ((S == 8 || S == 17) && l == 3 && i == 0) { csa = lds0 + K4_CS + ((opq(l16) >> 4) & 48); PIN(csa); }
        if constexpr ((S == 8 || S == 17) && l == 4 && i == 0) ldcs(S == 8 ? 0 : 4);
        if constexpr (S == 17 && l == 0 && i == 0) { const unsigned z = opq(l16); lga = lds0 + K4_LG + (z >> 6) * 32 + ((z >> 1) & 24); PIN(lga); }
        if constexpr (S == 17 && l < 5 && i == 1) pf5(L);
        if constexpr (ks == 17 && cc >= 1) epi(ic<4 * (t / 18) + cc - 1>{}, I);
        if constexpr (t == 18 && cc == 0) epi(ic<3>{}, I);
      });
    });
  });
  STAMP(ws, 4, 5);
  {
    f32x4 g0, g1, u0, u1;
    sfor<18>([&](auto K) {
      constexpr int ks = K.value, n = 144 + ks;
      mm(ic<n>{}, ic<ks>{}, g0, g1, u0, u1, [&](auto I) {
        if constexpr (ks == 0) epi(ic<7>{}, I);
        if constexpr (ks >= 1 && ks <= 4 && I.value == 1) pf5(ic<4 + ks>{});
        if constexpr (ks == 10 && I.value == 1) { wait_vm<4>(); set_flag(c, f4 + 2048 * 64); }
      });
    });
    asm volatile("s_nop 9" : "+v"(g0), "+v"(g1), "+v"(u0), "+v"(u1));
    const f32x4 v0 = (g0 + cg * nmu[0]) * (u0 + cu * nmu[0]), v1 = (g1 + cg * nmu[1]) * (u1 + cu * nmu[1]);
    const unsigned a0 = pk2(v0[0], v0[1]), a1 = pk2(v0[2], v0[3]), b0 = pk2(v1[0], v1[1]), b1 = pk2(v1[2], v1[3]);
    auto s0 = __builtin_amdgcn_permlane32_swap(a0, b0, false, false);
    auto s1 = __builtin_amdgcn_permlane32_swap(a1, b1, false, false);
    const unsigned z = opq(l16);
    bstore16<16>((u32x4){s0[0], s1[0], s0[1], s1[1]}, rhh, (z >> 9) * 144 * 1024 + (z & 511) + 512 * (j & 1), (T0 * 144 + 18 * Qd + 16 + (j >> 1)) * 1024);
    STAMP(ws, 4, 6);
    STAMP_FLUSH(ws, 4);
  }
}

constexpr int k5_E(int i) { int e = 36; if (K5_P + 2 * (i + 1) < e) e = K5_P + 2 * (i + 1); if (i >= 0 && i + 8 < e) e = i + 8; return e; }
static_assert(k5_E(6) <= 16 && k5_E(20) <= 32, "");

DI void p5(const Ctx& c) {
  char* ws = c.ws;
  const int w = c.w, r = c.r, xcd = c.xcd, lane = fresh_lane();
  const int sg = xcd >> 1, mt = 8 * (xcd & 1) + (r >> 2), nt = r & 3;
  const unsigned lds0 = c.lds0;
  const int t16 = lane & 15, g = lane >> 4;
  const int tile = 4 * mt + nt;
  const rsrc_t rpt = mk_rsrc(ws + WS_PT, 64 * 8 * 4 * PT_REC);
  const unsigned f4 = FL4 + (4096 + (r * 8 + xcd) * 8 + w) * 64;
  STAMP_DECL; STAMP(ws, 5, 0);
  if (w >= 4) {
    const int l = w - 4;
    const rsrc_t rwd = mk_rsrc(c.wd, F_ * D_ * 2), rhh = mk_rsrc(ws + WS_HH, 128 * 144 * 1024);
    const rsrc_t rwt = mk_rsrc(ws + WS_WT, 4 * 4608 * 32);
    unsigned mv[2][2];
#pragma unroll
    for (int sp = 0; sp < 2; ++sp)
#pragma unroll
      for (int i = 0; i < 2; ++i) mv[sp][i] = k5_mv(4 * (2 * l + i) + g, sp, nt, t16);
    const unsigned hs = ((8 * mt + 2 * l) * 144 + 36 * sg) * 1024, wsb = sg * 1152 * 1152, dl = lds0 + l * 2048;
    auto issue = [&](auto V) {
      constexpr int v = V.value, sp = v >= 32;
      const int u = k5_u(v, nt);
      constexpr unsigned so = k5_so(v);
      glds16(dl + so + 8192, mv[sp][0], rwd, wsb + u * 32 * 1152);
      glds16c(dl + so, lane * 16, rhh, hs + u * 1024);
      glds16(dl + so + 8192 + 1024, mv[sp][1], rwd, wsb + u * 32 * 1152);
      glds16c(dl + so + 1024, lane * 16, rhh, hs + (144 + u) * 1024);
      if ((v & 3) == l) glds16c(lds0 + so + 16384, lane * 16, rwt, (nt * 144 + 36 * sg + u) * 1024);
    };
    sfor<36>([&](auto I) {
      constexpr int i = I.value, y = k5_E(i - 1) - (i + 2 < 36 ? i + 2 : 36), yy = k5_E(i - 1) - K5_P;
      if constexpr (i == 0) wait_vm<2>();
      else if constexpr (i == 2) { wait_vm<4 * yy + yy / 4>(); set_flag(c, f4); }
      else if constexpr (i + 2 > K5_P) wait_vm<4 * y + y / 4>();
      bar();
      if constexpr (i == 0) STAMP(ws, 5, 1);
      if constexpr (i == 18) STAMP(ws, 5, 2);
      sfor<k5_E(i) - k5_E(i - 1)>([&](auto U) { issue(ic<k5_E(i - 1) + U.value>{}); });
    });
    bar();
    STAMP(ws, 5, 3);
    bar();
  } else {
    const int r2 = 4 * g + ((lane >> 2) & 3);
    unsigned wa[2][10];
#pragma unroll
    for (int i = 0; i < 8; ++i) wa[0][i] = lds0 + K5_BASE + 8192 + r2 * 256 + 32 * (i ^ (r2 & 7)) + 8 * (lane & 3);
    wa[0][8] = lds0 + K5_BASE + 16384 + r2 * 32 + 8 * (lane & 3);
    wa[0][9] = lds0 + K5_BASE + w * 2048 + lane * 16;
#pragma unroll
    for (int i = 0; i < 10; ++i) { wa[1][i] = wa[0][i] + 4 * K5_SLOT; PIN(wa[0][i]); PIN(wa[1][i]); }
    const unsigned fo = k5_fl(lane & 31, sg, mt);
    f32x4 acc[9][2];
    bf16x4 fr[3][3][2];
    bf16x8 hb[2][2];
    u32x2 pf;
    auto rd1 = [&](auto N, auto I) {
      constexpr int n = N.value, u = n / 3, i = I.value;
      if constexpr (u < 36) {
        constexpr int pos = (u + 7) & 7, hi = pos >> 2, cc = 3 * (n % 3) + i / 2;
        constexpr unsigned so = (pos & 3) * K5_SLOT;
        if constexpr (i >= 6) hb[u & 1][i - 6] = ldsr<bf16x8>(wa[hi][9] + so + (i - 6) * 1024);
        else fr[n % 3][i / 2][i & 1] = trh(wa[hi][cc] + so + (i & 1) * (cc < 8 ? 4096 : 512));
      }
    };
    sfor<36>([&](auto U) {
      constexpr int u = U.value;
      if constexpr (u == 0) wait_vm<2>();
      if constexpr (u == 4) { wait_vm<0>(); set_flag(c, f4); }
      if constexpr (u == 4 || u == 18) bload8c(pf, fo, fl_rsrc(c), FL4 + (u == 4 ? 1 : 2) * 2048 * 64);
      if constexpr (u == 7 || u == 21) {
        asm volatile("s_waitcnt vmcnt(0)" : "+v"(pf)::"memory");
        if (__builtin_expect(!tag_ok(c, pf), 0)) poll(c, fo, FL4 + (u == 7 ? 1 : 2) * 2048 * 64);
      }
      bar();
      if constexpr (u == 0) STAMP(ws, 5, 1);
      if constexpr (u == 18) STAMP(ws, 5, 2);
      if constexpr (u == 0) { rd1(ic<0>{}, ic<6>{}); rd1(ic<0>{}, ic<7>{}); sfor<6>([&](auto I) { rd1(ic<0>{}, I); }); sfor<6>([&](auto I) { rd1(ic<1>{}, I); }); }
      sfor<18>([&](auto M) {
        constexpr int mi = M.value, v = mi / 6, n = 3 * u + v, cc = mi / 2;
        if constexpr (u == 0) mfma16_z(acc[cc][mi & 1], cat(fr[n % 3][cc % 3][0], fr[n % 3][cc % 3][1]), hb[u & 1][mi & 1]);
        else mfma16_ip(acc[cc][mi & 1], cat(fr[n % 3][cc % 3][0], fr[n % 3][cc % 3][1]), hb[u & 1][mi & 1]);
        SBAR();
        if constexpr (mi == 6 || mi == 8) rd1(ic<n + 2>{}, ic<6 + (mi - 6) / 2>{});
        rd1(ic<n + 2>{}, ic<mi % 6>{});
        SBAR();
      });
    });
    asm volatile("s_nop 9" : "+v"(acc[7][0]), "+v"(acc[7][1]), "+v"(acc[8][0]), "+v"(acc[8][1]));
#pragma unroll
    for (int i = 0; i < 7; ++i) asm volatile("" : "+v"(acc[i][0]), "+v"(acc[i][1]));
    bar();
    STAMP(ws, 5, 3);
    if (w == sg) {
      sfor<2>([&](auto T) { sfor<9>([&](auto C) { ldsw<f32x4>(lds0 + (T.value * 9 + C.value) * 1024 + lane * 16, acc[C.value][T.value]); }); });
      lds_bar();
    } else {
      sfor<2>([&](auto T) {
        const unsigned pb = (((tile * 4 + w) * 4 + sg) * 2 + T.value) * PT_REC;
        sfor<4>([&](auto I) {
          constexpr int i = I.value;
          const f32x4 a = acc[2 * i][T.value], b = acc[2 * i + 1][T.value];
          bstore16<16>((u32x4){pk2(a[0], a[1]), pk2(a[2], a[3]), pk2(b[0], b[1]), pk2(b[2], b[3])}, rpt, lane * 16 + i * 1024, pb);
        });
        const f32x4 a = acc[8][T.value];
        bstore8<16>((u32x2){pk2(a[0], a[1]), pk2(a[2], a[3])}, rpt, lane * 8 + 4096, pb);
      });
      bar();
      wait_vm<0>();
      set_flag(c, FL5 + (tile * 4 + w) * 256 + sg * FS5);
    }
  }
  const int T = w >> 2, pi = w & 3, tt = 8 * mt + 2 * sg + T;
  const rsrc_t rhf = mk_rsrc(ws + WS_HF, 128 * 18 * 1024);
  u32x4 hv, pv[3];
  u32x2 hl, pl[3];
  unsigned rs2u;
  STAMP(ws, 5, 4);
  bload16c(hv, lane * 16, rhf, (tt * 18 + 4 * nt + pi) * 1024);
  bload8c(hl, lane * 16 + 8 * (nt & 1), rhf, (tt * 18 + 16 + (nt >> 1)) * 1024);
  bload4c(rs2u, t16 * 4, mk_rsrc(ws + WS_RS, 4 * 2048 * 4), (sg * 2048 + 16 * tt) * 4);
  poll(c, ((sg + 1 + min(lane, 2)) & 3) * FS5, FL5 + (tile * 4 + sg) * 256);
  STAMP(ws, 5, 5);
#pragma unroll
  for (int o = 0; o < 3; ++o) {
    const unsigned so = (((tile * 4 + sg) * 4 + ((sg + 1 + o) & 3)) * 2 + T) * PT_REC;
    bload16c(pv[o], lane * 16 + pi * 1024, rpt, so);
    bload8c(pl[o], pi == 0 ? lane * 8 + 4096 : OOB, rpt, so);
  }
  const unsigned orow = (16 * tt + t16) * 1152, own = lds0 + T * 9216 + lane * 16;
  const f32x4 o0 = ldsr<f32x4>(own + pi * 2048), o1 = ldsr<f32x4>(own + pi * 2048 + 1024), o2 = ldsr<f32x4>(own + 8192);
  asm volatile("s_waitcnt vmcnt(0)" : "+v"(hv), "+v"(hl), "+v"(rs2u), "+v"(pv[0]), "+v"(pv[1]), "+v"(pv[2]), "+v"(pl[0]), "+v"(pl[1]), "+v"(pl[2])::"memory");
  STAMP(ws, 5, 6);
  const float rs2 = __uint_as_float(rs2u);
  const rsrc_t rout = mk_rsrc(c.out, S_ * D_ * 2);
  auto fin = [&](const f32x4& ow, unsigned a0, unsigned a1, unsigned b0, unsigned b1, unsigned c0, unsigned c1, unsigned h0, unsigned h1, unsigned& d0, unsigned& d1) {
    const float v0 = ((ow[0] + bflo(b0)) + (bflo(a0) + bflo(c0))) * rs2 + bflo(h0);
    const float v1 = ((ow[1] + bfhi(b0)) + (bfhi(a0) + bfhi(c0))) * rs2 + bfhi(h0);
    const float v2 = ((ow[2] + bflo(b1)) + (bflo(a1) + bflo(c1))) * rs2 + bflo(h1);
    const float v3 = ((ow[3] + bfhi(b1)) + (bfhi(a1) + bfhi(c1))) * rs2 + bfhi(h1);
    d0 = pk2(v0, v1); d1 = pk2(v2, v3);
  };
  {
    unsigned a0, a1, b0, b1;
    fin(o0, pv[0][0], pv[0][1], pv[1][0], pv[1][1], pv[2][0], pv[2][1], hv[0], hv[1], a0, a1);
    fin(o1, pv[0][2], pv[0][3], pv[1][2], pv[1][3], pv[2][2], pv[2][3], hv[2], hv[3], b0, b1);
    auto s0 = __builtin_amdgcn_permlane16_swap(a0, b0, false, false);
    auto s1 = __builtin_amdgcn_permlane16_swap(a1, b1, false, false);
    bstore16<0>((u32x4){s0[0], s1[0], s0[1], s1[1]}, rout, orow + 256 * nt + 64 * pi + 32 * (g & 1) + 16 * (g >> 1), 0);
  }
  {
    unsigned e0, e1;
    fin(o2, pl[0][0], pl[0][1], pl[1][0], pl[1][1], pl[2][0], pl[2][1], hl[0], hl[1], e0, e1);
    bstore8<0>((u32x2){e0, e1}, rout, pi == 0 ? orow + 1024 + 32 * nt + 8 * g : OOB, 0);
  }
  wait_vm<0>();
  STAMP(ws, 5, 7);
  STAMP_FLUSH(ws, 5);
}

extern "C" __global__ __launch_bounds__(512, 2) void k1_all(const void* x, const void* wqkv, const void* wo, const void* wg, const void* wu, const void* wd, char* ws, void* out) {
  unsigned long long te_; asm volatile("s_memrealtime %0" : "=s"(te_)); SBAR();
  Ctx c = {x, wqkv, wo, wg, wu, wd, ws, out, (int)rfl(threadIdx.x >> 6), (int)(blockIdx.x & 7), (int)(blockIdx.x >> 3), (unsigned)(size_t)smem, 0u, 0u, te_};
  if (c.w == 7) {
    unsigned long long pc; unsigned back, size;
    asm volatile("s_getpc_b64 %0\n.Lk1pc%=:\n\ts_mov_b32 %1, .Lk1pc%=-k1_all\n\ts_mov_b32 %2, k9_end-k1_all" : "=s"(pc), "=s"(back), "=s"(size));
    const int lane = fresh_lane();
    warm(c.lds0 + LDS_SCR, lane < 24 ? (c.r * 24 + lane) * 128 : OOB, mk_rsrc((const void*)(pc - back), size), 0);
  }
  {
    const int mt = 4 * (c.xcd & 3) + (c.r & 3), nt = 8 * (c.xcd >> 2) + (c.r >> 2);
    if (c.w < 4) p1_body<0>(c, mt, nt); else p1_body<1>(c, mt, nt);
  }
  p2(c);
  if (c.r < 18) p3_gemm(c); else { cnt_wait(c, 1, 8); p3_side(c); }
  if (c.w < 4) k4_first<0>(c, fresh_lane());
  else { cnt_wait(c, 1, 8); k4_first<4>(c, fresh_lane()); }
  p4(c);
  p5(c);
  STAMP_OUT();
}

extern "C" __global__ void k9_end() {}

extern "C" __global__ __launch_bounds__(256) void k6_diag(char* ws) {
  __shared__ unsigned ra[256], rb[256];
  const int b = threadIdx.x;
  unsigned long long t0 = __builtin_amdgcn_s_memrealtime();
  const unsigned ref = *(GAS unsigned*)(ws + WS_DBG + ((size_t)1 * 256 * 8 * 16) * 4) - 100000u;
  unsigned a = ~0u, c = MB ? 0u : ~0u;
  const int nbb = KB == 3 ? 144 : 256;
  for (int w = 0; w < 8; ++w) {
    { unsigned v = *(GAS unsigned*)(ws + WS_DBG + ((((size_t)1 * 256 + b) * 8 + w) * 16) * 4) - ref; a = v < a ? v : a; }
    if (b < nbb) { unsigned v = *(GAS unsigned*)(ws + WS_DBG + ((((size_t)KB * 256 + b) * 8 + w) * 16 + IB) * 4) - ref; c = MB ? (v > c ? v : c) : (v < c ? v : c); }
  }
  ra[b] = a; rb[b] = c;
  __syncthreads();
  if (b == 0) {
    for (int i = 1; i < 256; ++i) { a = ra[i] < a ? ra[i] : a; c = MB ? (rb[i] > c ? rb[i] : c) : (rb[i] < c ? rb[i] : c); }
    unsigned long long d = c - a;
    if (d > 10000) d = 10000;
    if (CONSTD) d = 200;
    d *= 10;
    while (__builtin_amdgcn_s_memrealtime() - t0 < d + 300) __builtin_amdgcn_s_sleep(2);
  }
}

extern "C" void kernel_launch(void* const* d_in, const int* in_sizes, int n_in, void* d_out, int out_size, void* d_ws, size_t ws_size,
                              hipStream_t stream) {
  (void)hipFuncSetAttribute((const void*)k1_all, hipFuncAttributeMaxDynamicSharedMemorySize, LDS_ALL);
  k1_all<<<256, 512, LDS_ALL, stream>>>(d_in[0], d_in[1], d_in[2], d_in[4], d_in[5], d_in[6], (char*)d_ws, d_out);
  k6_diag<<<1, 256, 0, stream>>>((char*)d_ws);
}
```

```cpp
#define STAMPS
#define PRE 0
#define BIG0 0
#define MODE 2
#include <hip/hip_runtime.h>
#include <stdint.h>

typedef short bf16x8 __attribute__((ext_vector_type(8)));
typedef short bf16x4 __attribute__((ext_vector_type(4)));
typedef float f32x2 __attribute__((ext_vector_type(2)));
typedef float f32x4 __attribute__((ext_vector_type(4)));
typedef float f32x16 __attribute__((ext_vector_type(16)));
typedef unsigned u32x2 __attribute__((ext_vector_type(2)));
typedef unsigned u32x4 __attribute__((ext_vector_type(4)));
typedef __bf16 bf2_t __attribute__((ext_vector_type(2)));
typedef __amdgpu_buffer_rsrc_t rsrc_t;
#define LAS __attribute__((address_space(3)))
#define GAS __attribute__((address_space(1)))
#define DI __device__ __forceinline__

constexpr int S_ = 2048, D_ = 576, F_ = 4608;
constexpr float QSCALE = 0.23570226039551584f * 1.4426950408889634f;
constexpr unsigned ONES2 = 0x3f803f80u;

constexpr size_t QREC = 1280, KVREC = 2560;
constexpr unsigned OOB = 0x7fffff00u;
#define FS1 64
#define FS2 64
#define FS3 64
#define FS5 64
constexpr unsigned FL1 = 128;
constexpr unsigned FL2 = FL1 + 256 * 64;
constexpr unsigned FL3 = FL2 + 2048 * 64;
constexpr unsigned FLS = FL3 + 2048 * 64;
constexpr unsigned FL4 = FLS + 128 * 64;
constexpr unsigned FL5 = FL4 + 3 * 2048 * 64;
constexpr unsigned FL_END = FL5 + 1024 * 64;
constexpr size_t WS_QF = 1u << 20;
constexpr size_t WS_KV = WS_QF + 32 * 64 * QREC;
constexpr size_t WS_OF = WS_KV + 32 * 64 * KVREC;
constexpr size_t WS_HF = WS_OF + 128 * 18 * 1024;
constexpr size_t WS_ST = WS_HF + 128 * 18 * 1024;
constexpr size_t WS_CS = WS_ST + 9 * 2048 * 8;
constexpr size_t WS_RS = WS_CS + 2 * 4608 * 4;
constexpr size_t WS_LG = WS_RS + 4 * 2048 * 4;
constexpr size_t WS_WT = WS_LG + 8 * 4 * 36864;
constexpr size_t WS_HH = WS_WT + 4 * 4608 * 32;
constexpr size_t PT_REC = 4608;
constexpr size_t WS_PT = WS_HH + (size_t)128 * 144 * 1024;
constexpr size_t WS_END = WS_PT + (size_t)64 * 8 * 4 * PT_REC;

DI unsigned rfl(unsigned v) { return __builtin_amdgcn_readfirstlane(v); }
DI rsrc_t mk_rsrc(const void* p, unsigned bytes) { return __builtin_amdgcn_make_buffer_rsrc((void*)p, (short)0, (int)bytes, 0x00020000); }
DI void glds16(unsigned lds, unsigned voff, rsrc_t r, unsigned soff) {
  asm volatile("s_mov_b32 m0, %0\n\ts_nop 0\n\tbuffer_load_dwordx4 %1, %2, %3 offen lds" ::"s"(lds), "v"(voff), "s"(r), "s"(soff) : "memory", "m0");
}
DI void glds16c(unsigned lds, unsigned voff, rsrc_t r, unsigned soff) {
  asm volatile("s_mov_b32 m0, %0\n\ts_nop 0\n\tbuffer_load_dwordx4 %1, %2, %3 offen sc1 lds" ::"s"(lds), "v"(voff), "s"(r), "s"(soff) : "memory", "m0");
}
template <class T> DI void bload16c(T& d, unsigned voff, rsrc_t r, unsigned soff) {
  asm volatile("buffer_load_dwordx4 %0, %1, %2, %3 offen sc1" : "=v"(d) : "v"(voff), "s"(r), "s"(soff) : "memory");
}
DI void bload8c(u32x2& d, unsigned voff, rsrc_t r, unsigned soff) {
  asm volatile("buffer_load_dwordx2 %0, %1, %2, %3 offen sc1" : "=v"(d) : "v"(voff), "s"(r), "s"(soff) : "memory");
}
DI void bload4c(unsigned& d, unsigned voff, rsrc_t r, unsigned soff) {
  asm volatile("buffer_load_dword %0, %1, %2, %3 offen sc1" : "=v"(d) : "v"(voff), "s"(r), "s"(soff) : "memory");
}
DI void bload8(u32x2& d, unsigned voff, rsrc_t r, unsigned soff) {
  asm volatile("buffer_load_dwordx2 %0, %1, %2, %3 offen" : "=v"(d) : "v"(voff), "s"(r), "s"(soff) : "memory");
}
DI void warm(unsigned lds, unsigned voff, rsrc_t r, unsigned soff) {
  asm volatile("s_mov_b32 m0, %0\n\ts_nop 0\n\tbuffer_load_dword %1, %2, %3 offen lds" ::"s"(lds), "v"(voff), "s"(r), "s"(soff) : "memory", "m0");
}
template <int N> DI void wait_vm() { asm volatile("s_waitcnt vmcnt(%0)" ::"i"(N) : "memory"); }
#define PIN(x) asm volatile("" : "+v"(x))
#define SBAR() __builtin_amdgcn_sched_barrier(0)
DI void bar() { SBAR(); asm volatile("s_barrier" ::: "memory"); SBAR(); }
DI void lds_bar() { SBAR(); asm volatile("s_waitcnt lgkmcnt(0)\n\ts_barrier" ::: "memory"); SBAR(); }
DI bf16x8 trfrag(unsigned a0, unsigned a1) {
  bf16x4 lo = __builtin_amdgcn_ds_read_tr16_b64_v4i16((LAS bf16x4*)(size_t)a0);
  bf16x4 hi = __builtin_amdgcn_ds_read_tr16_b64_v4i16((LAS bf16x4*)(size_t)a1);
  return __builtin_shufflevector(lo, hi, 0, 1, 2, 3, 4, 5, 6, 7);
}
DI bf16x4 trh(unsigned a) { return __builtin_amdgcn_ds_read_tr16_b64_v4i16((LAS bf16x4*)(size_t)a); }
DI bf16x8 cat(bf16x4 lo, bf16x4 hi) { return __builtin_shufflevector(lo, hi, 0, 1, 2, 3, 4, 5, 6, 7); }
template <class T> DI T ldsr(unsigned a) { return *(LAS T*)(size_t)a; }
template <class T> DI void ldsw(unsigned a, T v) { *(LAS T*)(size_t)a = v; }
DI unsigned pk2(float a, float b) { f32x2 v = {a, b}; return __builtin_bit_cast(unsigned, __builtin_convertvector(v, bf2_t)); }
DI float bflo(unsigned u) { return __uint_as_float(u << 16); }
DI float bfhi(unsigned u) { return __uint_as_float(u & 0xffff0000u); }
DI f32x4 mfma16(bf16x8 a, bf16x8 b, f32x4 c) { return __builtin_amdgcn_mfma_f32_16x16x32_bf16(a, b, c, 0, 0, 0); }
DI void mfma16_ip(f32x4& c, bf16x8 a, bf16x8 b) { asm volatile("v_mfma_f32_16x16x32_bf16 %0, %1, %2, %0" : "+v"(c) : "v"(a), "v"(b)); }
DI void mfma16_z(f32x4& c, bf16x8 a, bf16x8 b) { asm volatile("v_mfma_f32_16x16x32_bf16 %0, %1, %2, 0" : "=&v"(c) : "v"(a), "v"(b)); }
DI f32x16 mfma32(bf16x8 a, bf16x8 b, f32x16 c) { return __builtin_amdgcn_mfma_f32_32x32x16_bf16(a, b, c, 0, 0, 0); }
DI float xor32(float v) { auto r = __builtin_amdgcn_permlane32_swap(__float_as_uint(v), __float_as_uint(v), false, false); return __uint_as_float((threadIdx.x & 32) ? r[0] : r[1]); }
DI float sum_x32(float v) { auto r = __builtin_amdgcn_permlane32_swap(__float_as_uint(v), __float_as_uint(v), false, false); return __uint_as_float(r[0]) + __uint_as_float(r[1]); }
DI float max_x32(float v) { auto r = __builtin_amdgcn_permlane32_swap(__float_as_uint(v), __float_as_uint(v), false, false); return fmaxf(__uint_as_float(r[0]), __uint_as_float(r[1])); }
DI float sum_x16(float v) { auto r = __builtin_amdgcn_permlane16_swap(__float_as_uint(v), __float_as_uint(v), false, false); return __uint_as_float(r[0]) + __uint_as_float(r[1]); }
template <int AUX> DI void bstore16(u32x4 v, rsrc_t r, unsigned voff, unsigned soff) {
  if constexpr (AUX == 16) asm volatile("buffer_store_dwordx4 %0, %1, %2, %3 offen sc1\n\ts_nop 1" ::"v"(v), "v"(voff), "s"(r), "s"(soff) : "memory");
  else asm volatile("buffer_store_dwordx4 %0, %1, %2, %3 offen\n\ts_nop 1" ::"v"(v), "v"(voff), "s"(r), "s"(soff) : "memory");
}
template <int AUX> DI void bstore8(u32x2 v, rsrc_t r, unsigned voff, unsigned soff) { __builtin_amdgcn_raw_buffer_store_b64(v, r, voff, soff, AUX); }
template <int AUX> DI void bstore4(unsigned v, rsrc_t r, unsigned voff, unsigned soff) { __builtin_amdgcn_raw_buffer_store_b32(v, r, voff, soff, AUX); }

template <int... I> struct iseq {};
template <int N, int... I> struct mkseq : mkseq<N - 1, N - 1, I...> {};
template <int... I> struct mkseq<0, I...> { typedef iseq<I...> type; };
template <int V> struct ic { static constexpr int value = V; };
template <class Fn, int... I> DI void sfor_(Fn&& f, iseq<I...>) { (f(ic<I>{}), ...); }
template <int N, class Fn> DI void sfor(Fn&& f) { sfor_(f, typename mkseq<N>::type{}); }

extern __shared__ __attribute__((aligned(16))) char smem[];
constexpr size_t WS_DBG = 64u << 20;
constexpr int LDS_DBG = 156 * 1024;
#define STAMP(ws, kid, i) do { SBAR(); unsigned long long t_, c_; asm volatile("s_memrealtime %0\n\ts_memtime %1\n\ts_waitcnt lgkmcnt(0)" : "=s"(t_), "=s"(c_) :: "memory"); \
  asm volatile("v_writelane_b32 %0, %1, %3\n\tv_writelane_b32 %0, %2, %4" : "+v"(stamp_v) : "s"((unsigned)t_), "s"((unsigned)c_), "i"(i), "i"(8 + i)); SBAR(); } while (0)
#define STAMP_DECL unsigned stamp_v = 0
#define STAMP_FLUSH(ws, kid) do { const int l_ = fresh_lane(); if (l_ < 16) ldsw<unsigned>(c.lds0 + LDS_DBG + ((c.w * 7 + kid) * 16 + l_) * 4, stamp_v); } while (0)
#define STAMP_OUT() do { const int l_ = fresh_lane(); if (l_ < 16) for (int k_ = 1; k_ < 7; ++k_) if ((k_ == 3) == (c.r < 18) || (k_ != 3 && k_ != 6)) \
  *(GAS unsigned*)(c.ws + WS_DBG + ((((size_t)k_ * 256 + blockIdx.x) * 8 + c.w) * 16 + l_) * 4) = ldsr<unsigned>(c.lds0 + LDS_DBG + ((c.w * 7 + k_) * 16 + l_) * 4); } while (0)

struct Ctx {
  const void *x, *wqkv, *wo, *wg, *wu, *wd;
  char* ws; void* out;
  int w, xcd, r;
  unsigned lds0, t0, t1;
  unsigned long long te;
};
constexpr int LDS_ALL = 160 * 1024, LDS_SCR = LDS_ALL - 256;
DI int fresh_lane() { int l; asm volatile("v_mbcnt_lo_u32_b32 %0, -1, 0\n\tv_mbcnt_hi_u32_b32 %0, -1, %0" : "=v"(l)); __builtin_assume(l >= 0 && l < 64); return l; }
DI unsigned opq(unsigned v) { PIN(v); return v; }
constexpr int LDS_CNT = 147456;
DI void cnt_add(const Ctx& c, int i) {
  asm volatile("s_mov_b64 exec, 1\n\tds_add_u32 %0, %1\n\ts_mov_b64 exec, -1" ::"v"(c.lds0 + LDS_CNT + 4 * i), "v"(1u) : "memory");
}
DI unsigned cnt_get(const Ctx& c, int i) {
  unsigned v;
  asm volatile("ds_read_b32 %0, %1\n\ts_waitcnt lgkmcnt(0)" : "=v"(v) : "v"(c.lds0 + LDS_CNT + 4 * i) : "memory");
  return rfl(v);
}
DI void cnt_wait(const Ctx& c, int i, unsigned n) {
  if (__builtin_expect(cnt_get(c, i) < n, 0))
    for (int spin = 0; spin < (1 << 22) && cnt_get(c, i) < n; ++spin) __builtin_amdgcn_s_sleep(1);
}
DI rsrc_t fl_rsrc(const Ctx& c) { return mk_rsrc(c.ws, FL_END); }
DI void set_flag(const Ctx& c, unsigned off) {
  u32x2 t;
  asm volatile("v_mov_b32 %0, %1" : "=v"(t[0]) : "s"(c.t0));
  asm volatile("v_mov_b32 %0, %1" : "=v"(t[1]) : "s"(c.t1));
  asm volatile("s_mov_b64 exec, 1\n\tbuffer_store_dwordx2 %0, off, %1, %2 sc1\n\ts_mov_b64 exec, -1" ::"v"(t), "s"(fl_rsrc(c)), "s"(off) : "memory");
}
DI bool tag_ok(const Ctx& c, u32x2 v) { return __builtin_amdgcn_ballot_w64(v[0] != c.t0 || v[1] != c.t1) == 0; }
DI void poll(const Ctx& c, unsigned voff, unsigned soff) {
  for (int spin = 0; spin < (1 << 20); ++spin) {
    u32x2 v;
    asm volatile("buffer_load_dwordx2 %0, %1, %2, %3 offen sc1\n\ts_waitcnt vmcnt(0)" : "=&v"(v) : "v"(voff), "s"(fl_rsrc(c)), "s"(soff) : "memory");
    if (tag_ok(c, v)) break;
  }
}

DI bool poll_first(const Ctx& c, unsigned voff, unsigned soff, unsigned long long first) {
  unsigned long long m = 0;
  for (int spin = 0; spin < (1 << 20); ++spin) {
    u32x2 v;
    asm volatile("buffer_load_dwordx2 %0, %1, %2, %3 offen sc1\n\ts_waitcnt vmcnt(0)" : "=&v"(v) : "v"(voff), "s"(fl_rsrc(c)), "s"(soff) : "memory");
    m = __builtin_amdgcn_ballot_w64(v[0] != c.t0 || v[1] != c.t1);
    if ((m & first) == 0) break;
  }
  return m == 0;
}

constexpr int K1_SLOT = 32768, K1_TBL = K1_SLOT * 4;
extern "C" __global__ void k9_end();

template <int NN> DI void p1_body(Ctx& c, int mt, int nt) {
  char* ws = c.ws;
  const int w = c.w, lane = fresh_lane();
  const unsigned lds0 = c.lds0;
  const rsrc_t rx = mk_rsrc(c.x, S_ * D_ * 2), rw = mk_rsrc(c.wqkv, D_ * 3 * D_ * 2);
  const int t16 = lane & 15, g = lane >> 4, q4 = (lane >> 2) & 3, p = lane & 3, m = w & 3;
  constexpr int NT = NN ? 3 : 4;
  unsigned xv[2], wv[2];
#pragma unroll
  for (int i = 0; i < 2; ++i) {
    const int row = 8 * (2 * w + i) + (lane >> 3);
    xv[i] = (128 * mt + row) * 1152 + 16 * ((lane & 7) ^ ((row >> 1) & 7));
    int li = 64 * (2 * w + i) + lane;
    if (li >= 896) li -= 128;
    const int r64 = (li * 4682) >> 16, cc = li - 14 * r64;
    const int rho = r64 & 31, k = (r64 & 32) + 8 * ((rho >> 2) & 3) + 4 * (rho >> 4) + (rho & 3);
    wv[i] = k * 3456 + ((216 * nt) & ~15) + 16 * cc;
  }
  STAMP_DECL; STAMP(ws, 1, 3);
  asm volatile("v_writelane_b32 %0, %1, 0" : "+v"(stamp_v) : "s"((unsigned)c.te));

  auto issue1 = [&](int slab, int i) {
    const unsigned dst = lds0 + (slab & 3) * K1_SLOT + w * 2048 + (i >> 1) * 1024;
    if (i & 1) glds16(dst + 16384, wv[i >> 1], rw, slab * 64 * 3456);
    else glds16(dst, xv[i >> 1], rx, slab * 128);
  };
#pragma unroll
  for (int sl = 0; sl < 2; ++sl)
#pragma unroll
    for (int i = 0; i < 4; ++i) issue1(sl, i);
  u32x2 ev;
  bload8c(ev, 0u, fl_rsrc(c), 0);
  STAMP(ws, 1, 1);
  const unsigned wa = lds0 + 16384 + (4 * g + q4) * 224 + (nt & 1) * 8;
  unsigned ad[2][5];
  ad[0][0] = wa + 8 * p + 144 * NN;
  ad[0][1] = wa + 72 * (p < 3 ? p : 2) + 32;
  ad[0][2] = NN && m >= 2 ? ad[0][1] : ad[0][0] + 72 * (m >> 1) + 40 * (m & 1);
  ad[0][3] = lds0 + (32 * m + t16) * 128 + 16 * (g ^ ((t16 >> 1) & 7));
  ad[0][4] = ad[0][3] ^ 64;
#pragma unroll
  for (int i = 0; i < 5; ++i) { ad[1][i] = ad[0][i] + 2 * K1_SLOT; PIN(ad[1][i]); }
  const unsigned tb = lds0 + K1_TBL, cb = tb + 1024, dummy = cb + 512;
  unsigned e_cs = t16 == 0 ? cb + (w * 16 + 4 * g) * 4 : dummy, e_st = tb + (32 * m + t16) * 8, e_cc = cb + (64 * NN + 4 * g) * 4;
  unsigned e_s1 = g == 3 ? e_st : dummy, e_s2 = g == q4 ? e_st + 4 : dummy, e_vo[2], e_so[4], e_t2 = 0, e_one = 0;
  unsigned e_sc[2];
  float e_tsc = 0.f;
  {
    constexpr unsigned KV0 = WS_KV - WS_QF;
    const int tile32 = 4 * mt + m, key = 16 * (g >> 1) + t16;
    const unsigned lo_qk = (g & 1) * 512 + key * 16, lo_v = 1280 + key * 32 + (g & 1) * 16;
#pragma unroll
    for (int i = 0; i < 2 - NN; ++i) {
      const int pg = 3 * nt + 2 * NN + i, type = pg >> 4, rl = type ? KVREC : QREC;
      e_so[2 * i] = (type ? KV0 : 0) + (2 * (pg & 15) * 64 + tile32) * rl;
      e_so[2 * i + 1] = e_so[2 * i] + 64 * rl;
      e_vo[i] = type == 2 ? lo_v : lo_qk;
      e_sc[i] = type ? 0x3f800000u : __builtin_bit_cast(unsigned, QSCALE);
      asm volatile("" : "+s"(e_so[2 * i]), "+s"(e_so[2 * i + 1]), "+s"(e_sc[i]), "+v"(e_vo[i]));
    }
    if constexpr (NN) {
      const int pg = 3 * nt + g, type = pg >> 4, rl = type ? KVREC : QREC;
      e_vo[1] = g < 3 ? (type ? KV0 : 0) + (2 * (pg & 15) * 64 + tile32) * rl + (type == 2 ? 2304 : 1024) + t16 * 8 : OOB;
      e_t2 = e_vo[1] + 64 * rl;
      e_one = type == 1 ? 0u : 0x00003f80u;
      e_tsc = type ? 1.f : QSCALE;
      PIN(e_vo[1]); PIN(e_t2); PIN(e_one); PIN(e_tsc); PIN(e_s1); PIN(e_s2);
    }
    PIN(e_cs); PIN(e_st); PIN(e_cc);
  }
  f32x4 acc[4][2] = {}, cs = {};
  bf16x8 ones = __builtin_bit_cast(bf16x8, (u32x4){ONES2, ONES2, ONES2, ONES2});
  PIN(ones);
  bf16x8 fb[2][2];
  bf16x4 fa[2][NT + 1][2];
  constexpr int NR = 2 * NT + 4;
  auto rd1 = [&](auto KS, auto I) {
    constexpr int ks = KS.value, i = I.value, sp = ks & 1, sl = (ks >> 1) & 3, ti = (i - 2) >> 1;
    constexpr unsigned so = (sl & 1) * K1_SLOT;
    if constexpr (ks < 18 && i < NR) {
      if constexpr (i < 2) fb[sp][i] = ldsr<bf16x8>(ad[sl >> 1][3 + sp] + so + i * 2048);
      else {
        constexpr int ai = ti == NT ? 2 : (NN && ti == 2) ? 1 : 0;
        constexpr unsigned co = ai ? 0 : 72 * (ti >> 1) + 40 * (ti & 1);
        fa[sp][ti][i & 1] = trh(ad[sl >> 1][ai] + so + co + sp * 32 * 224 + (i & 1) * 16 * 224);
      }
    }
  };
  sfor<9>([&](auto SL) {
    constexpr int sl = SL.value;
    constexpr int iss = sl == 0 ? 2 : (sl + 3 < 9 ? sl + 3 : 9);
    constexpr int need = sl == 0 ? 0 : (sl + 1 < 9 ? sl + 1 : 8);
    if constexpr (sl == 1) asm volatile("s_waitcnt vmcnt(%1)" : "+v"(ev) : "i"(4 * (iss - need - 1)) : "memory");
    else wait_vm<4 * (iss - need - 1) + (sl == 0)>();
    bar();
    if constexpr (sl == 0) STAMP(ws, 1, 2);
    if constexpr (sl <= 1) sfor<NR>([&](auto I) { rd1(ic<2 * sl>{}, I); });
    if constexpr (sl == 1) {
      const unsigned long long e = (((unsigned long long)rfl(ev[1]) << 32) | rfl(ev[0])) + 1ull;
      c.t0 = (unsigned)e; c.t1 = (unsigned)(e >> 32);
    }
    sfor<2>([&](auto SP) {
      constexpr int sp = SP.value, ks = 2 * sl + sp;
      bf16x8 tl;
      if constexpr (NN) tl = t16 >= 12 ? ones : cat(fa[sp][2][0], fa[sp][2][1]);
      sfor<9>([&](auto M) {
        constexpr int mi = M.value, ti = mi >> 1;
        if constexpr (mi == 8) cs = mfma16(cat(fa[sp][NT][0], fa[sp][NT][1]), ones, cs);
        else if constexpr (NN && ti == 3) acc[3][mi & 1] = mfma16(fb[sp][mi & 1], fb[sp][mi & 1], acc[3][mi & 1]);
        else if constexpr (NN && ti == 2) acc[2][mi & 1] = mfma16(tl, fb[sp][mi & 1], acc[2][mi & 1]);
        else acc[ti][mi & 1] = mfma16(cat(fa[sp][ti][0], fa[sp][ti][1]), fb[sp][mi & 1], acc[ti][mi & 1]);
        SBAR();
        if constexpr (ks != 1) {
          if constexpr (mi < 3) { rd1(ic<ks + 1>{}, ic<2 * mi>{}); rd1(ic<ks + 1>{}, ic<2 * mi + 1>{}); }
          else rd1(ic<ks + 1>{}, ic<3 + mi>{});
        }
        if constexpr (sl == 0) { if constexpr (mi & 1) issue1(2 + sp, mi >> 1); }
        else if constexpr (sl + 3 < 9 && (mi == 2 + 2 * NN || mi == 6 + 2 * NN)) issue1(sl + 3, 2 * sp + (mi >= 6));
        SBAR();
      });
    });
  });
  STAMP(ws, 1, 4);
  ldsw<f32x4>(e_cs, cs);
  if constexpr (NN) sfor<2>([&](auto T) {
    const f32x4 gr = acc[3][T.value];
    const float d01 = (p & 1) ? gr[1] : gr[0], d23 = (p & 1) ? gr[3] : gr[2];
    ldsw<float>(e_s1 + 128 * T.value, acc[2][T.value][0]);
    ldsw<float>(e_s2 + 128 * T.value, (p & 2) ? d23 : d01);
  });
  lds_bar();
  f32x2 sv[2];
  f32x4 cc[4];
  sfor<2>([&](auto T) { sv[T.value] = ldsr<f32x2>(e_st + 128 * T.value); });
  sfor<NT>([&](auto I) { cc[I.value] = ldsr<f32x4>(e_cc + 64 * I.value); });
  SBAR();
  float nmu[2], rs[2];
  sfor<2>([&](auto T) {
    nmu[T.value] = sv[T.value][0] * (-1.f / D_);
    rs[T.value] = __builtin_amdgcn_rsqf(fmaxf(sv[T.value][1] * (1.f / D_) - nmu[T.value] * nmu[T.value], 0.f) + 1e-5f);
  });
  const rsrc_t ro = mk_rsrc(ws + WS_QF, 32 * 64 * (QREC + KVREC));
  sfor<(NN ? 2 : 4)>([&](auto TI) {
    constexpr int ti = TI.value;
    const f32x4 v0 = (acc[ti][0] + cc[ti] * nmu[0]) * (rs[0] * __uint_as_float(e_sc[ti >> 1])), v1 = (acc[ti][1] + cc[ti] * nmu[1]) * (rs[1] * __uint_as_float(e_sc[ti >> 1]));
    auto s0 = __builtin_amdgcn_permlane32_swap(pk2(v0[0], v0[1]), pk2(v1[0], v1[1]), false, false);
    auto s1 = __builtin_amdgcn_permlane32_swap(pk2(v0[2], v0[3]), pk2(v1[2], v1[3]), false, false);
    bstore16<16>((u32x4){s0[0], s1[0], s0[1], s1[1]}, ro, e_vo[ti >> 1], e_so[ti]);
  });
  if constexpr (NN) sfor<2>([&](auto T) {
    const f32x4 v = (acc[2][T.value] + cc[2] * nmu[T.value]) * (rs[T.value] * e_tsc);
    bstore8<16>((u32x2){pk2(v[0], v[1]), e_one}, ro, e_vo[1], T.value * 128);
    bstore8<16>((u32x2){pk2(v[2], v[3]), e_one}, ro, e_t2, T.value * 128);
  });
  STAMP(ws, 1, 5);
  wait_vm<0>();
  STAMP(ws, 1, 6);
  bar();
  if (w == 0) set_flag(c, FL1 + (mt * 16 + nt) * FS1);
  STAMP(ws, 1, 7);
  STAMP_FLUSH(ws, 1);
}

constexpr int P3_WO = 73728;
constexpr int K4_RING = 8192;
constexpr unsigned k4_so(int u) { return ((((u >> 1) + 2) & 3) * 2 + (u & 1)) * 8192; }
DI unsigned k4_wv(int lane, int w, int Qd, int j) {
  const int rho = 8 * (w & 3) + (lane >> 3);
  return rho * 9216 + 1152 * Qd + 256 * j + 32 * (((lane & 7) >> 1) ^ ((rho >> 1) & 3)) + 16 * (lane & 1);
}

template <int U0> DI void k4_first(const Ctx& c, int lane) {
  const rsrc_t rg = mk_rsrc(c.wg, D_ * F_ * 2), ru = mk_rsrc(c.wu, D_ * F_ * 2);
  const unsigned wv4 = k4_wv(lane, c.w, c.xcd, c.r & 3), dst = c.lds0 + K4_RING + (c.w & 3) * 1024;
  sfor<4>([&](auto U) {
    glds16(dst + k4_so(U0 + U.value), wv4, rg, (U0 + U.value) * 32 * 9216);
    glds16(dst + 4096 + k4_so(U0 + U.value), wv4, ru, (U0 + U.value) * 32 * 9216);
  });
}
DI void p2_ahead(const Ctx& c, unsigned flag) {
  const int w = c.w, r = c.r, lane = fresh_lane();
  const rsrc_t rwo = mk_rsrc(c.wo, D_ * D_ * 2);
  const int nt = r >= 9 ? r - 9 : r;
  const int rho = 8 * w + (lane >> 3), rg = (rho >> 2) & 3, rj = 4 * (rho >> 4) + (rho & 3);
  const unsigned cbyte = 128 * nt + 32 * (((lane & 7) >> 1) ^ ((rho >> 1) & 3)) + 16 * (lane & 1);
  const int th = 4 * rg + (rj >> 1);
  const unsigned wv = (20 * (rg >> 1) + 8 * (rg & 1) + rj) * 1152 + cbyte, wvt = (18 * th + ((th & 1) ? 0 : 16) + (rj & 1)) * 1152 + cbyte;
  sfor<16>([&](auto K) {
    glds16(c.lds0 + P3_WO + (w + 4 * K.value) * 1024, wv, rwo, K.value * 36 * 1152);
    if constexpr (K.value == 7) { wait_vm<8>(); set_flag(c, flag); }
  });
  sfor<2>([&](auto K) { glds16(c.lds0 + P3_WO + (w + 64 + 4 * K.value) * 1024, wvt, rwo, K.value * 288 * 1152); });
}

DI void p2(const Ctx& c) {
  const int head = 4 * c.xcd + (c.r & 3), qb = c.r >> 2;
  char* ws = c.ws;
  const int w = c.w, lane = fresh_lane();
  const unsigned lds0 = c.lds0;
  const int q = lane & 31, h = lane >> 5, G = lane >> 4;
  const int qt = 8 * qb + w;
  const rsrc_t rq = mk_rsrc(ws + WS_QF, 32 * 64 * QREC), rkv = mk_rsrc(ws + WS_KV, 32 * 64 * KVREC);
  STAMP_DECL; STAMP(ws, 2, 0);
  unsigned kb = lds0 + w * KVREC + lane * 16, ktb = lds0 + w * KVREC + 1024 + q * 8;
  unsigned vb[4];
#pragma unroll
  for (int i = 0; i < 4; ++i) { const int row = 8 * i + 4 * (G >> 1) + ((lane >> 2) & 3); vb[i] = lds0 + w * KVREC + ((G & 1) ? 2304 + row * 8 : 1280 + row * 32 + 8 * (lane & 3)); PIN(vb[i]); }
  unsigned mf[16], mk[8];
#pragma unroll
  for (int e = 0; e < 16; ++e) { mf[e] = ((e & 3) + 8 * (e >> 2) <= q - 4 * h) ? ~0u : 0u; PIN(mf[e]); }
#pragma unroll
  for (int e = 0; e < 8; ++e) { mk[e] = (mf[2 * e] & 0xffffu) | (mf[2 * e + 1] & 0xffff0000u); PIN(mk[e]); }
  const unsigned tt = 2 * qt + (q >> 4);
  unsigned om_o = (tt * 18 + (head >> 1)) * 1024 + ((q & 15) + 16 * (2 * (head & 1) + h)) * 16;
  unsigned ot_o = h ? OOB : (tt * 18 + 16 + (head >> 4)) * 1024 + ((q & 15) + 16 * ((head >> 2) & 3)) * 16 + (head & 3) * 4;
  unsigned l16 = lane * 16, qto = h ? OOB : 1024 + q * 8, wmo;
  const int wmv = c.r * 8 + w;
  {
    const int l = (wmv >= 162 ? wmv - 162 : wmv >= 81 ? wmv - 81 : wmv) * 64 + lane, row = (l * 7282) >> 16;
    wmo = wmv >= 162 ? l * 128 : row * 9216 + c.xcd * 1152 + (l - 9 * row) * 128;
    PIN(wmo);
  }
  PIN(kb); PIN(ktb); PIN(om_o); PIN(ot_o); PIN(l16); PIN(qto);
  if (qb == 0) {
#pragma unroll
    for (int i = 0; i < 3; ++i) {
      const unsigned o = min(w * 64 + lane + 512 * i, 1279) * 16, m = (o % KVREC - 1024 < 256) ? 0x0000f149u : 0u;
      ldsw<u32x4>(lds0 + o, (u32x4){0u, m, 0u, m});
    }
  }
#define TSL 0
  if (TSL && w) __builtin_amdgcn_s_sleep(TSL);
  if (w >= 1 && w <= 3) warm(lds0 + LDS_SCR, (w * 64 + lane < 224 ? (w * 64 + lane - 64) * 128 : OOB), rkv, (head * 64 + 8 * qb) * KVREC);
  if (w == 4 || w == 5) warm(lds0 + LDS_SCR, (w * 64 + lane < 336 ? (w * 64 + lane - 256) * 128 : OOB), rq, (head * 64 + 8 * qb) * QREC);
  if (w == 0) {
    const int ty = lane < 4 ? 1 : lane < 8 ? 2 : 0, pm = max(2 * qb + (ty ? (lane & 3) - 2 : (lane & 1)), 0);
    ldsw<unsigned>(lds0 + LDS_CNT + 4 * (lane & 7), 0u);
    poll(c, (pm * 16 + (((16 * ty + (head >> 1)) * 43) >> 7)) * FS1, FL1);
  }
  lds_bar();
  STAMP(ws, 2, 7);
  bf16x8 qm; u32x2 qt2;
#pragma unroll
  for (int i = 0; i < 5; ++i) { const int pc = max(w + 8 * i, qb ? 0 : 20 + w); glds16c(lds0 + pc * 1024, l16, rkv, (head * 64 + 8 * qb - 8) * KVREC + pc * 1024); }
  bload16c(qm, l16, rq, (head * 64 + qt) * QREC);
  bload8c(qt2, qto, rq, (head * 64 + qt) * QREC);
  auto toff = [&](int i) { return (unsigned)(i * KVREC); };
  STAMP(ws, 2, 1);
  asm volatile("s_waitcnt vmcnt(0)" : "+v"(qm), "+v"(qt2)::"memory");
  bar();
  STAMP(ws, 2, 2);
  const bf16x4 qt4 = __builtin_bit_cast(bf16x4, qt2);
  bf16x8 km[9]; bf16x4 kt[9];
  constexpr int ord[9] = {0, 8, 1, 2, 3, 4, 5, 6, 7};
  sfor<9>([&](auto I) { constexpr int i = ord[I.value]; km[i] = ldsr<bf16x8>(kb + toff(i)); kt[i] = ldsr<bf16x4>(ktb + toff(i)); });
  SBAR();
  f32x16 s[9];
  float m4[4];
  auto qk = [&](auto I, auto J) {
    constexpr int i = I.value, j = J.value;
    f32x16 a = mfma32(km[i], qm, (f32x16){}), b;
    if constexpr (j < 9) b = mfma32(km[j], qm, (f32x16){});
    s[i] = __builtin_amdgcn_mfma_f32_32x32x8bf16_1k(kt[i], qt4, a, 0, 0, 0);
    if constexpr (j < 9) s[j] = __builtin_amdgcn_mfma_f32_32x32x8bf16_1k(kt[j], qt4, b, 0, 0, 0);
    SBAR();
  };
  auto mx1 = [&](auto I) {
    constexpr int i = I.value;
#pragma unroll
    for (int e = 0; e < 8; ++e) m4[e & 3] = fmaxf(fmaxf(m4[e & 3], s[i][2 * e]), s[i][2 * e + 1]);
  };
  bf16x4 vf[2][4];
  auto rdv = [&](int set, unsigned off) {
#pragma unroll
    for (int i = 0; i < 4; ++i) vf[set][i] = trh(vb[i] + off);
  };
  qk(ic<0>{}, ic<8>{});
  qk(ic<1>{}, ic<2>{});
  {
    size_t p0 = (size_t)c.wg, p1 = (size_t)c.wu, p2 = (size_t)c.wo;
    asm("" : "+s"(p0), "+s"(p1), "+s"(p2));
    p0 = wmv >= 81 ? p1 : p0;
    warm(lds0 + LDS_SCR, wmo, mk_rsrc((const void*)(wmv >= 162 ? p2 : p0), wmv >= 162 ? D_ * D_ * 2 : D_ * F_ * 2), 0);
  }
  SBAR();
#pragma unroll
  for (int e = 0; e < 16; ++e) s[8][e] = __uint_as_float((__float_as_uint(s[8][e]) & mf[e]) | (__float_as_uint(s[0][e]) & ~mf[e]));
#pragma unroll
  for (int e = 0; e < 4; ++e) asm("v_max_f32 %0, %1, %2\n\tv_max3_f32 %0, %0, %3, %4" : "=&v"(m4[e]) : "v"(s[8][4 * e]), "v"(s[8][4 * e + 1]), "v"(s[8][4 * e + 2]), "v"(s[8][4 * e + 3]));
  SBAR();
  qk(ic<3>{}, ic<4>{});
  mx1(ic<1>{}); mx1(ic<2>{}); SBAR();
  qk(ic<5>{}, ic<6>{});
  mx1(ic<3>{}); mx1(ic<4>{}); SBAR();
  qk(ic<7>{}, ic<9>{});
  rdv(1, toff(1));
  mx1(ic<5>{}); mx1(ic<6>{}); SBAR();
  mx1(ic<7>{});
  float mx = max_x32(fmaxf(fmaxf(m4[0], m4[1]), fmaxf(m4[2], m4[3])));
  STAMP(ws, 2, 3);
  f32x16 o = {};
  u32x4 pp;
  sfor<16>([&](auto H) {
    constexpr int hh = H.value, i = 1 + hh / 2, ks = hh & 1;
    f32x2 dd[4];
#pragma unroll
    for (int e = 0; e < 4; ++e) dd[e] = (f32x2){s[i][8 * ks + 2 * e], s[i][8 * ks + 2 * e + 1]} - (f32x2){mx, mx};
    SBAR();
    if constexpr (hh > 0 && hh < 15) o = mfma32(cat(vf[(1 + (hh - 1) / 2) & 1][2 * (ks ^ 1)], vf[(1 + (hh - 1) / 2) & 1][2 * (ks ^ 1) + 1]), __builtin_bit_cast(bf16x8, pp), o);
    if constexpr (hh == 15) {
      const u32x4 pd = {pp[0] & mk[0], pp[1] & mk[1], pp[2] & mk[2], pp[3] & mk[3]};
      o = mfma32(cat(vf[0][0], vf[0][1]), __builtin_bit_cast(bf16x8, pd), o);
      o = mfma32(cat(vf[1][0], vf[1][1]), __builtin_bit_cast(bf16x8, pp ^ pd), o);
    }
    SBAR();
    if constexpr (ks == 0) rdv((i + 1) & 1, toff(i < 8 ? i + 1 : 0));
    SBAR();
    float pv[8];
#pragma unroll
    for (int e = 0; e < 4; ++e) { pv[2 * e] = __builtin_amdgcn_exp2f(dd[e][0]); pv[2 * e + 1] = __builtin_amdgcn_exp2f(dd[e][1]); }
    SBAR();
    pp = (u32x4){pk2(pv[0], pv[1]), pk2(pv[2], pv[3]), pk2(pv[4], pv[5]), pk2(pv[6], pv[7])};
    SBAR();
  });
  {
    const u32x4 pd = {pp[0] & mk[4], pp[1] & mk[5], pp[2] & mk[6], pp[3] & mk[7]};
    o = mfma32(cat(vf[0][2], vf[0][3]), __builtin_bit_cast(bf16x8, pd), o);
    o = mfma32(cat(vf[1][2], vf[1][3]), __builtin_bit_cast(bf16x8, pp ^ pd), o);
  }
  PIN(o);
  STAMP(ws, 2, 4);
  auto lr = __builtin_amdgcn_permlane32_swap(__float_as_uint(o[10]), __float_as_uint(o[10]), false, false);
  const float inv = __builtin_amdgcn_rcpf(__uint_as_float(lr[0]));
  const rsrc_t ro = mk_rsrc(ws + WS_OF, 128 * 18 * 1024);
  const u32x4 om = {pk2(o[0] * inv, o[1] * inv), pk2(o[2] * inv, o[3] * inv), pk2(o[4] * inv, o[5] * inv), pk2(o[6] * inv, o[7] * inv)};
  bstore16<16>(om, ro, om_o, 0);
  bstore4<16>(pk2(o[8] * inv, o[9] * inv), ro, ot_o, 0);
  STAMP(ws, 2, 5);
  cnt_add(c, 1);
  if (w < 4 && c.r < 18) p2_ahead(c, FL2 + (qt * 32 + head) * FS2);
  else { wait_vm<0>(); set_flag(c, FL2 + (qt * 32 + head) * FS2); }
  STAMP(ws, 2, 6);
  STAMP_FLUSH(ws, 2);
}

DI void p3_side(const Ctx& c) {
  char* ws = c.ws;
  const int w = c.w, xcd = c.xcd, lane = fresh_lane();
  const unsigned lds0 = c.lds0;
  const int sb = c.r - 18, blk = sb * 8 + xcd;
  const rsrc_t rwt = mk_rsrc(ws + WS_WT, 4 * 4608 * 32), rlg = mk_rsrc(ws + WS_LG, 8 * 4 * 36864), rcs = mk_rsrc(ws + WS_CS, 2 * 4608 * 4);
  STAMP_DECL; STAMP(ws, 3, 0);
  const int mat = sb >= 5, un = sb < 5 ? 2 * sb : sb - 5;
  const rsrc_t rw = mk_rsrc(mat ? c.wu : c.wg, D_ * F_ * 2);
  const unsigned vo = (lane & 7) * 16 + (72 * w + (lane >> 3)) * 9216;
  u32x4 v[2][9];
#pragma unroll
  for (int i = 0; i < 9; ++i) v[0][i] = __builtin_amdgcn_raw_buffer_load_b128(rw, vo, (9 * xcd + un) * 128 + i * 8 * 9216, 0);
#pragma unroll
  for (int i = 0; i < 9; ++i) v[1][i] = __builtin_amdgcn_raw_buffer_load_b128(rw, sb < 4 ? vo : OOB, (9 * xcd + un + 1) * 128 + i * 8 * 9216, 0);
  const int drow = 64 * (blk - 40) + 8 * w + (lane >> 3);
  const u32x4 dv = __builtin_amdgcn_raw_buffer_load_b128(mk_rsrc(c.wd, F_ * D_ * 2), mat ? drow * 1152 + 1024 + (lane & 7) * 16 : OOB, 0, 0);
  if (un == 8) {
    const unsigned lo = ((lane & 7) >> 1) * 36864 + (72 * w + (lane >> 3)) * 32 + (lane & 1) * 16;
#pragma unroll
    for (int i = 0; i < 9; ++i) bstore16<16>(v[0][i], rlg, lo, (xcd * 8 + (sb >> 3)) * 18432 + i * 256);
  }
  const int pr = (drow % 576) >= 512 ? ((drow & ~24) | ((drow & 8) << 1) | ((drow & 16) >> 1)) : drow;
  bstore16<16>(dv, rwt, mat ? (((lane & 7) >> 1) * 4608 + pr) * 32 + (lane & 1) * 16 : OOB, 0);
  float a[2][8] = {};
#pragma unroll
  for (int k = 0; k < 2; ++k) {
#pragma unroll
    for (int i = 0; i < 9; ++i)
#pragma unroll
      for (int e = 0; e < 4; ++e) { a[k][2 * e] += bflo(v[k][i][e]); a[k][2 * e + 1] += bfhi(v[k][i][e]); }
#pragma unroll
    for (int e = 0; e < 8; ++e) a[k][e] = sum_x32(sum_x16(a[k][e]));
  }
  if (lane < 16) {
#pragma unroll
    for (int k = 0; k < 2; ++k) {
      ldsw<f32x4>(lds0 + k * 4096 + w * 512 + lane * 32, (f32x4){a[k][0], a[k][1], a[k][2], a[k][3]});
      ldsw<f32x4>(lds0 + k * 4096 + w * 512 + lane * 32 + 16, (f32x4){a[k][4], a[k][5], a[k][6], a[k][7]});
    }
  }
  lds_bar();
  if (w < 2) {
    float t = 0.f;
#pragma unroll
    for (int i = 0; i < 16; ++i) t += ldsr<float>(lds0 + w * 4096 + i * 256 + lane * 4);
    bstore4<16>(__float_as_uint(t), rcs, w == 0 || sb < 4 ? lane * 4 : OOB, ((sb >= 5 ? 4608 : 0) + (9 * xcd + un + w) * 64) * 4);
  }
  STAMP(ws, 3, 5);
  wait_vm<0>();
  bar();
  if (w == 0) set_flag(c, FLS + blk * 64);
  STAMP(ws, 3, 6);
  if (w == 5 && blk == 48) {
    for (int i = 0; i < 4; ++i) poll(c, lane * FS1, FL1 + i * 64 * FS1);
    set_flag(c, 0);
  }
  STAMP_FLUSH(ws, 6);
}

DI void p3_gemm(const Ctx& c) {
  char* ws = c.ws;
  const int w = c.w, lane = fresh_lane();
  const unsigned lds0 = c.lds0;
  const int t16 = lane & 15, g = lane >> 4;
  const int nt = c.r >= 9 ? c.r - 9 : c.r;
  const int T = 16 * c.xcd + 8 * (w >> 2) + 4 * (c.r >= 9) + (w & 3);
  const rsrc_t rof = mk_rsrc(ws + WS_OF, 128 * 18 * 1024);
  STAMP_DECL; STAMP(ws, 3, 0);
  unsigned wa[4];
  {
    const int r2 = 4 * g + ((lane >> 2) & 3);
#pragma unroll
    for (int i = 0; i < 4; ++i) wa[i] = lds0 + P3_WO + r2 * 128 + 32 * (i ^ ((r2 >> 1) & 3)) + 8 * (lane & 3);
  }
  u32x2 xr[4];
  sfor<4>([&](auto I) { bload8(xr[I.value], (16 * T + t16) * 1152 + 128 * nt + 8 * g, mk_rsrc(c.x, S_ * D_ * 2), 32 * I.value); });
  unsigned l16 = lane * 16, so = g == 0 ? t16 * 8 : OOB;
  PIN(l16); PIN(so);
  if (w < 4) { wait_vm<4>(); cnt_add(c, 0); }
  cnt_wait(c, 0, 4);
  STAMP(ws, 3, 1);
  constexpr int AH = 5;
  bf16x8 of[18];
  bf16x4 fr[AH + 1][4][2];
  unsigned wb[4];
#pragma unroll
  for (int i = 0; i < 4; ++i) { wb[i] = wa[i] + 65536; PIN(wb[i]); }
  auto rd1 = [&](auto KS, auto I) {
    constexpr int ks = KS.value, i = I.value;
    if constexpr (ks < 18) fr[ks % (AH + 1)][i >> 1][i & 1] = trh((ks < 16 ? wa : wb)[i >> 1] + (ks & 15) * 4096 + (i & 1) * 2048);
  };
  sfor<AH>([&](auto K) { sfor<8>([&](auto I) { rd1(K, I); }); });
  SBAR();
  const bool all = poll_first(c, (lane & 31) * FS2, FL2 + (T >> 1) * 32 * FS2, 0xffffull);
  asm volatile("" : "+v"(xr[0]), "+v"(xr[1]), "+v"(xr[2]), "+v"(xr[3]));
  sfor<8>([&](auto K) { bload16c(of[K.value], l16, rof, (T * 18 + K.value) * 1024); });
  f32x4 acc[4];
#pragma unroll
  for (int i = 0; i < 4; ++i) { acc[i] = (f32x4){bflo(xr[i][0]), bfhi(xr[i][0]), bflo(xr[i][1]), bfhi(xr[i][1])}; PIN(acc[i]); }
  if (!all) poll(c, (16 + (lane & 15)) * FS2, FL2 + (T >> 1) * 32 * FS2);
  STAMP(ws, 3, 2);
  sfor<10>([&](auto K) { bload16c(of[8 + K.value], l16, rof, (T * 18 + 8 + K.value) * 1024); });
  sfor<18>([&](auto KS) {
    constexpr int ks = KS.value;
    asm volatile("s_waitcnt vmcnt(%1)" : "+v"(of[ks]) : "i"(17 - ks) : "memory");
    if constexpr (ks == 0) STAMP(ws, 3, 3);
    if constexpr (ks == 9) STAMP(ws, 3, 4);
    sfor<4>([&](auto C) {
      mfma16_ip(acc[C.value], cat(fr[ks % (AH + 1)][C.value][0], fr[ks % (AH + 1)][C.value][1]), of[ks]);
      SBAR();
      rd1(ic<ks + AH>{}, ic<2 * C.value>{}); rd1(ic<ks + AH>{}, ic<2 * C.value + 1>{});
      SBAR();
    });
  });
  asm volatile("s_nop 9" : "+v"(acc[0]), "+v"(acc[1]), "+v"(acc[2]), "+v"(acc[3]));
  unsigned d[8];
#pragma unroll
  for (int i = 0; i < 4; ++i) { d[2 * i] = pk2(acc[i][0], acc[i][1]); d[2 * i + 1] = pk2(acc[i][2], acc[i][3]); }
  const rsrc_t rhf = mk_rsrc(ws + WS_HF, 128 * 18 * 1024);
  bstore16<16>((u32x4){d[0], d[1], d[2], d[3]}, rhf, l16, (T * 18 + 2 * nt) * 1024);
  bstore16<16>((u32x4){d[4], d[5], d[6], d[7]}, rhf, l16, (T * 18 + 2 * nt + 1) * 1024);
  float s1[2] = {0.f, 0.f}, s2[2] = {0.f, 0.f};
#pragma unroll
  for (int i = 0; i < 8; ++i) {
    const bf2_t v = __builtin_bit_cast(bf2_t, d[i]);
    s1[i & 1] = __builtin_amdgcn_fdot2_f32_bf16(v, __builtin_bit_cast(bf2_t, ONES2), s1[i & 1], false);
    s2[i & 1] = __builtin_amdgcn_fdot2_f32_bf16(v, v, s2[i & 1], false);
  }
  bstore8<16>((u32x2){__float_as_uint(sum_x32(sum_x16(s1[0] + s1[1]))), __float_as_uint(sum_x32(sum_x16(s2[0] + s2[1])))}, mk_rsrc(ws + WS_ST, 9 * 2048 * 8), so, (T * 9 + nt) * 128);
  STAMP(ws, 3, 5);
  wait_vm<0>();
  set_flag(c, FL3 + (T * 16 + nt) * FS3);
  STAMP(ws, 3, 6);
  STAMP_FLUSH(ws, 3);
}

constexpr int K4_LG = P3_WO;
constexpr int K4_CS = K4_LG + 36864;
constexpr int K4_STP = K4_CS + 4096;
struct K4Op { int kind, idx; };
struct K4Ops { int n; K4Op op[12]; };
constexpr K4Ops k4_ops(int S) {
  K4Ops o = {};
  auto add = [&](int k, int i) { o.op[o.n++] = {k, i}; };
  if (S == 3) add(9, 0);
  if (S == 0) for (int u = 4; u < 8; ++u) { add(1, u); add(2, u); }
  else for (int u = 2 * S + 6; u < 2 * S + 8 && u < 36; ++u) {
    if (S == 2) { add(0, 2 * u - 12); add(0, 2 * u - 11); }
    if (S > 2) add(0, u);
    if (u < 18) { add(1, u); add(2, u); }
  }
  if (S == 4) { add(5, 0); add(5, 1); add(5, 2); }
  if (S == 6) add(6, 0);
  if (S >= 10 && S < 15) add(3, S - 10);
  if (S == 12) add(7, 0);
  if (S == 14) add(8, 0);
  if (S == 10) add(4, 0);
  return o;
}
constexpr int k4_nst(int S) { return (S == 8 || S == 9) ? 2 : S == 7 ? 1 : 0; }
constexpr int k4_issued(int S) { int n = 8; for (int s = 0; s < S; ++s) n += k4_ops(s).n + k4_nst(s); return n; }
constexpr int k4_pos(int kind, int idx) {
  if (kind == 0 && idx < 8) return -1;
  if ((kind == 1 || kind == 2) && idx < 4) return 2 * idx + kind - 1;
  for (int S = 0;; ++S) for (int i = 0; i < k4_ops(S).n; ++i) if (k4_ops(S).op[i].kind == kind && k4_ops(S).op[i].idx == idx) return k4_issued(S) + k4_nst(S) + i;
}
constexpr int k4_need(int S) {
  if (S == 8) return k4_issued(7) - 1;
  if (S >= 16) return k4_issued(15) - 1;
  const int a = S < 9 ? k4_pos(2, 2 * S + 1) : -1, b = k4_pos(0, 2 * S + 2);
  return a > b ? a : b;
}
static_assert(k4_need(5) >= k4_pos(9, 0) && k4_need(7) >= k4_pos(5, 2) && k4_need(12) >= k4_issued(10) - 1, "");
constexpr int K5_SLOT = 17408, K5_BASE = 0, K5_P = 4;
constexpr unsigned k5_so(int v) { return K5_BASE + ((v + 7) & 7) * K5_SLOT; }
DI int k5_u(int v, int nt) {
  if (v < 32) { const int q = ((v >> 2) + nt) & 3; return 18 * (q >> 1) + 8 * (q & 1) + 4 * ((v >> 1) & 1) + (v & 1) + 2 * (v >> 4); }
  const int q = (v + nt) & 3;
  return 18 * (q >> 1) + 16 + (q & 1);
}
DI unsigned k5_mv(int rho, int sp, int nt, int t16) {
  const int rr = sp ? ((rho & 7) | ((rho & 8) << 1) | ((rho & 16) >> 1)) : rho;
  return rr * 1152 + 256 * nt + 32 * ((t16 >> 1) ^ (rho & 7)) + 16 * (t16 & 1);
}
DI unsigned k5_fl(int i, int sg, int mt5) { return (((((mt5 >> 1) * 4 + ((i >> 2) & 3)) * 8 + 2 * sg + ((i >> 4) & 1)) * 8) + 4 * (mt5 & 1) + (i & 3)) * 64; }

DI void p4(const Ctx& c) {
  char* ws = c.ws;
  const int w = c.w, r = c.r, xcd = c.xcd, lane = fresh_lane();
  const int mt = r >> 2, Qd = xcd, j = r & 3;
  const unsigned lds0 = c.lds0;
  const int g = lane >> 4;
  const int T0 = 16 * mt + 2 * w;
  const rsrc_t rwm = mk_rsrc(w < 4 ? c.wg : c.wu, D_ * F_ * 2), rhf = mk_rsrc(ws + WS_HF, 128 * 18 * 1024), rwd = mk_rsrc(c.wd, F_ * D_ * 2);
  const rsrc_t rhh = mk_rsrc(ws + WS_HH, 128 * 144 * 1024);
  const unsigned f4 = FL4 + ((r * 8 + xcd) * 8 + w) * 64;
  const int sg = xcd >> 1, mt5 = 8 * (xcd & 1) + (r >> 2);
  STAMP_DECL; STAMP(ws, 4, 0);
  const unsigned wv = opq(k4_wv(lane, w, Qd, j));
  const unsigned wdst = lds0 + K4_RING + w * 1024, l16 = opq(lane * 16);
  unsigned wa[4];
  {
    const int r2 = 4 * g + ((lane >> 2) & 3);
#pragma unroll
    for (int i = 0; i < 4; ++i) wa[i] = opq(lds0 + K4_RING + r2 * 128 + 32 * (i ^ ((r2 >> 1) & 3)) + 8 * (lane & 3));
  }
  const bool all = poll_first(c, (min(lane & 15, 8) + (lane & 16)) * FS3, FL3 + T0 * 16 * FS3, 0x30003ull);
  bf16x8 act[18][2];
  u32x2 pf;
  auto pf_off = [&] { const int z = opq(l16) >> 4; const unsigned a = opq(FL4 + k5_fl(z, sg, mt5)), b = opq(FLS + (40 + 18 * sg + min(z - 32, 17)) * 64); return z < 32 ? a : b; };
  auto doop = [&](auto KIND, auto IDX) {
    constexpr int kind = KIND.value, u = IDX.value;
    if constexpr (kind == 0) glds16(wdst + k4_so(u), wv, rwm, (u % 18) * 32 * 9216 + (u / 18) * 128);
    else if constexpr (kind <= 2) bload16c(act[u][kind - 1], l16, rhf, (T0 * 18 + 18 * (kind - 1) + u) * 1024);
    else if constexpr (kind == 3) {
      const int pc = min(w + 8 * u, 35);
      glds16c(lds0 + K4_LG + pc * 1024, l16, mk_rsrc(ws + WS_LG, 8 * 4 * 36864), (Qd * 4 + j) * 36864 + pc * 1024);
    }
    else if constexpr (kind == 4) warm(lds0 + LDS_SCR, l16 * 8, rwd, sg * 1327104 + min(r * 8 + w, 161) * 8192);
    else if constexpr (kind == 5) glds16c(lds0 + K4_STP + w * 3072 + u * 1024, l16, mk_rsrc(ws + WS_ST, 9 * 2048 * 8), T0 * 1152 + u * 1024);
    else if constexpr (kind == 6) {
      const unsigned z = opq(l16), f = (576 * Qd + (z < 512 ? 128 * j : 384 + 16 * j)) * 4 + z;
      glds16c(lds0 + K4_CS + (w & 1) * 1024, z < 576 ? f : OOB, mk_rsrc(ws + WS_CS, 2 * 4608 * 4), (w & 1) * 4608 * 4);
    } else if constexpr (kind == 7) set_flag(c, f4);
    else if constexpr (kind == 8) bload8c(pf, pf_off(), fl_rsrc(c), 0);
    else bload8c(pf, min(opq(l16), 13u * 16) * 32, fl_rsrc(c), FLS + xcd * 64);
  };
  sfor<4>([&](auto U) { doop(ic<1>{}, U); doop(ic<2>{}, U); });
  if (w < 4) cnt_add(c, 4);
  if (!all) poll(c, (min(fresh_lane() & 15, 8) + (fresh_lane() & 16)) * FS3, FL3 + T0 * 16 * FS3);
  cnt_wait(c, 4, 4);
  f32x4 ag[4][2], au[4][2], cg, cu;
  bf16x4 fr[4][2];
  unsigned hd[2][2], lga, csa;
  f32x2 nmu = {0.f, 0.f};
  auto rd1 = [&](auto H, auto I) {
    constexpr int n = H.value >> 1, m = H.value & 1, i = I.value;
    if constexpr (n < 144) fr[H.value & 3][i] = trh(wa[n % 4] + k4_so(n / 4) + 4096 * m + 2048 * i);
    else if constexpr (n < 162) fr[H.value & 3][i] = trh(lga + (n - 144) * 1024 + m * 18432 + i * 512);
  };
  auto rd = [&](auto H) { rd1(H, ic<0>{}); rd1(H, ic<1>{}); };
  auto mm = [&](auto N, auto KS, f32x4& g0, f32x4& g1, f32x4& u0, f32x4& u1, auto&& hook) {
    constexpr int n = N.value, ks = KS.value;
    constexpr int h0 = n == 15 ? 999 : 2 * n + 3, h1 = (n == 14 || n == 15) ? 999 : 2 * n + 4;
    const bf16x8 fg = cat(fr[(2 * n) & 3][0], fr[(2 * n) & 3][1]);
    if constexpr (ks == 0) mfma16_z(g0, fg, act[ks][0]); else mfma16_ip(g0, fg, act[ks][0]);
    SBAR(); rd1(ic<h0>{}, ic<0>{}); SBAR(); hook(ic<0>{}); SBAR();
    if constexpr (ks == 0) mfma16_z(g1, fg, act[ks][1]); else mfma16_ip(g1, fg, act[ks][1]);
    SBAR(); rd1(ic<h0>{}, ic<1>{}); SBAR(); hook(ic<1>{}); SBAR();
    const bf16x8 fu = cat(fr[(2 * n + 1) & 3][0], fr[(2 * n + 1) & 3][1]);
    if constexpr (ks == 0) mfma16_z(u1, fu, act[ks][1]); else mfma16_ip(u1, fu, act[ks][1]);
    SBAR(); rd1(ic<h1>{}, ic<0>{}); SBAR(); hook(ic<2>{}); SBAR();
    if constexpr (ks == 0) mfma16_z(u0, fu, act[ks][0]); else mfma16_ip(u0, fu, act[ks][0]);
    SBAR(); rd1(ic<h1>{}, ic<1>{}); SBAR(); hook(ic<3>{}); SBAR();
  };
  auto ldcs = [&](int e) { cg = ldsr<f32x4>(csa + 64 * e); cu = ldsr<f32x4>(csa + 1024 + 64 * e); };
  auto epi = [&](auto E, auto I) {
    constexpr int grp = E.value >> 2, cc = E.value & 3, i = I.value, T = 1 - (i >> 1);
    if constexpr ((i & 1) == 0) {
      ag[cc][T] += cg * nmu[T]; au[cc][T] += cu * nmu[T]; PIN(ag[cc][T]); PIN(au[cc][T]);
      if constexpr (i == 2 && E.value != 3) { SBAR(); ldcs(E.value + 1); }
    }
    else {
      const f32x4 v = ag[cc][T] * au[cc][T];
      const unsigned d0 = pk2(v[0], v[1]), d1 = pk2(v[2], v[3]);
      if constexpr ((cc & 1) == 0) { hd[T][0] = d0; hd[T][1] = d1; PIN(hd[T][0]); PIN(hd[T][1]); }
      else bstore16<16>((u32x4){hd[T][0], hd[T][1], d0, d1}, rhh, l16, ((T0 + T) * 144 + 18 * Qd + 4 * j + 2 * grp + (cc >> 1)) * 1024);
    }
  };
  f32x2 sp[3];
  float rs0;
  auto stats = [&](auto TT, auto P) {
    constexpr int T = TT.value, p = P.value;
    if constexpr (p == 0) {
      const unsigned sa = lds0 + K4_STP + w * 3072 + T * 1152 + (opq(l16) >> 1);
      sp[0] = ldsr<f32x2>(sa); sp[1] = ldsr<f32x2>(sa + 512); sp[2] = ldsr<f32x2>(sa + 1024);
    } else if constexpr (p == 4) { sp[0] = sp[0] + sp[1] + (opq(l16) < 256 ? sp[2] : (f32x2){0.f, 0.f}); PIN(sp[0]); }
    else if constexpr (p == 5) { sp[0][0] = sum_x16(sp[0][0]); PIN(sp[0]); }
    else if constexpr (p == 6) { sp[0][1] = sum_x16(sp[0][1]); PIN(sp[0]); }
    else if constexpr (p == 7) { sp[0][0] = sum_x32(sp[0][0]); PIN(sp[0]); }
    else if constexpr (p == 8) { sp[0][1] = sum_x32(sp[0][1]); PIN(sp[0]); }
    else if constexpr (p == 9) {
      nmu[T] = sp[0][0] * (-1.f / D_);
      const float rs = __builtin_amdgcn_rcpf(fmaxf(sp[0][1] * (1.f / D_) - nmu[T] * nmu[T], 0.f) + 1e-5f);
      PIN(nmu);
      if constexpr (T == 0) { rs0 = rs; PIN(rs0); }
      else {
        const unsigned z = opq(l16);
        bstore4<16>(__float_as_uint((z & 256) ? rs : rs0), mk_rsrc(ws + WS_RS + (sg * 2048 + 16 * T0) * 4, (Qd & 1) == 0 && j == 0 ? 128 : 0), z >> 2, 0);
      }
    }
  };
  unsigned mv5;
  auto pf5 = [&](auto N) {
    constexpr int n = N.value, v = (n - 1) >> 1;
    if constexpr (n == 0) {
      const int vv = min(w, K5_P - 1), uu = k5_u(vv, j);
      glds16c(lds0 + K5_BASE + ((vv + 7) & 7) * K5_SLOT + 16384, l16, mk_rsrc(ws + WS_WT, 4 * 4608 * 32), (j * 144 + 36 * sg + uu) * 1024);
      const unsigned z = opq(l16);
      mv5 = k5_mv(4 * w + (z >> 8), 0, j, (z >> 4) & 15);
    } else if constexpr (n & 1) glds16(lds0 + k5_so(v) + 8192 + w * 1024, mv5, rwd, sg * 1152 * 1152 + k5_u(v, j) * 32 * 1152);
    else glds16c(lds0 + k5_so(v) + w * 1024, l16, rhh, ((8 * mt5 + w) * 144 + 36 * sg + k5_u(v, j)) * 1024);
  };
  sfor<18>([&](auto SS) {
    constexpr int S = SS.value;
    constexpr int nw = k4_issued(S) - k4_need(S) - 1;
    static_assert(nw >= 0 && nw < 64, "");
    if constexpr (S == 5) asm volatile("s_waitcnt vmcnt(%5)" : "+v"(act[2 * S][0]), "+v"(act[2 * S][1]), "+v"(act[2 * S + 1][0]), "+v"(act[2 * S + 1][1]), "+v"(pf) : "i"(nw) : "memory");
    else if constexpr (S < 9) asm volatile("s_waitcnt vmcnt(%4)" : "+v"(act[2 * S][0]), "+v"(act[2 * S][1]), "+v"(act[2 * S + 1][0]), "+v"(act[2 * S + 1][1]) : "i"(nw) : "memory");
    else if constexpr (S == 16) asm volatile("s_waitcnt vmcnt(%1)" : "+v"(pf) : "i"(nw) : "memory");
    else wait_vm<nw>();
    if constexpr (S == 16) { if (__builtin_expect(!tag_ok(c, pf), 0)) poll(c, pf_off(), 0); }
    if constexpr (S == 5) { if (__builtin_expect(!tag_ok(c, pf), 0)) poll(c, min(opq(l16), 13u * 16) * 32, FLS + xcd * 64); }
    if constexpr (S >= 2) bar();
    if constexpr (S == 0) STAMP(ws, 4, 1);
    if constexpr (S == 2) STAMP(ws, 4, 7);
    if constexpr (S == 4) STAMP(ws, 4, 2);
    if constexpr (S == 9) STAMP(ws, 4, 3);
    if constexpr (S == 17) STAMP(ws, 4, 4);
    if constexpr (S == 0 || S == 2) { rd(ic<16 * S>{}); rd(ic<16 * S + 1>{}); rd(ic<16 * S + 2>{}); }
    sfor<8>([&](auto L) {
      constexpr int l = L.value, n = 8 * S + l, t = n / 4, cc = n % 4, ks = t % 18, no = k4_ops(S).n;
      mm(ic<n>{}, ic<ks>{}, ag[cc][0], ag[cc][1], au[cc][0], au[cc][1], [&](auto I) {
        constexpr int i = I.value;
        sfor<no>([&](auto O) {
          constexpr int nd = no ? no : 1, at = O.value * 32 / nd + 16 / nd;
          if constexpr (at == 4 * l + i) doop(ic<k4_ops(S).op[O.value].kind>{}, ic<k4_ops(S).op[O.value].idx>{});
        });
        if constexpr (S == 7) stats(ic<(l >> 2)>{}, ic<4 * (l & 3) + i>{});
        if constexpr ((S == 8 || S == 17) && l == 3 && i == 0) { csa = lds0 + K4_CS + ((opq(l16) >> 4) & 48); PIN(csa); }
        if constexpr ((S == 8 || S == 17) && l == 4 && i == 0) ldcs(S == 8 ? 0 : 4);
        if constexpr (S == 17 && l == 0 && i == 0) { const unsigned z = opq(l16); lga = lds0 + K4_LG + (z >> 6) * 32 + ((z >> 1) & 24); PIN(lga); }
        if constexpr (S == 17 && l < 5 && i == 1) pf5(L);
        if constexpr (ks == 17 && cc >= 1) epi(ic<4 * (t / 18) + cc - 1>{}, I);
        if constexpr (t == 18 && cc == 0) epi(ic<3>{}, I);
      });
    });
  });
  STAMP(ws, 4, 5);
  {
    f32x4 g0, g1, u0, u1;
    sfor<18>([&](auto K) {
      constexpr int ks = K.value, n = 144 + ks;
      mm(ic<n>{}, ic<ks>{}, g0, g1, u0, u1, [&](auto I) {
        if constexpr (ks == 0) epi(ic<7>{}, I);
        if constexpr (ks >= 1 && ks <= 4 && I.value == 1) pf5(ic<4 + ks>{});
        if constexpr (ks == 10 && I.value == 1) { wait_vm<4>(); set_flag(c, f4 + 2048 * 64); }
      });
    });
    asm volatile("s_nop 9" : "+v"(g0), "+v"(g1), "+v"(u0), "+v"(u1));
    const f32x4 v0 = (g0 + cg * nmu[0]) * (u0 + cu * nmu[0]), v1 = (g1 + cg * nmu[1]) * (u1 + cu * nmu[1]);
    const unsigned a0 = pk2(v0[0], v0[1]), a1 = pk2(v0[2], v0[3]), b0 = pk2(v1[0], v1[1]), b1 = pk2(v1[2], v1[3]);
    auto s0 = __builtin_amdgcn_permlane32_swap(a0, b0, false, false);
    auto s1 = __builtin_amdgcn_permlane32_swap(a1, b1, false, false);
    const unsigned z = opq(l16);
    bstore16<16>((u32x4){s0[0], s1[0], s0[1], s1[1]}, rhh, (z >> 9) * 144 * 1024 + (z & 511) + 512 * (j & 1), (T0 * 144 + 18 * Qd + 16 + (j >> 1)) * 1024);
    STAMP(ws, 4, 6);
    STAMP_FLUSH(ws, 4);
  }
}

constexpr int k5_E(int i) { int e = 36; if (K5_P + 2 * (i + 1) < e) e = K5_P + 2 * (i + 1); if (i >= 0 && i + 8 < e) e = i + 8; return e; }
static_assert(k5_E(6) <= 16 && k5_E(20) <= 32, "");

DI void p5(const Ctx& c) {
  char* ws = c.ws;
  const int w = c.w, r = c.r, xcd = c.xcd, lane = fresh_lane();
  const int sg = xcd >> 1, mt = 8 * (xcd & 1) + (r >> 2), nt = r & 3;
  const unsigned lds0 = c.lds0;
  const int t16 = lane & 15, g = lane >> 4;
  const int tile = 4 * mt + nt;
  const rsrc_t rpt = mk_rsrc(ws + WS_PT, 64 * 8 * 4 * PT_REC);
  const unsigned f4 = FL4 + (4096 + (r * 8 + xcd) * 8 + w) * 64;
  STAMP_DECL; STAMP(ws, 5, 0);
  if (w >= 4) {
    const int l = w - 4;
    const rsrc_t rwd = mk_rsrc(c.wd, F_ * D_ * 2), rhh = mk_rsrc(ws + WS_HH, 128 * 144 * 1024);
    const rsrc_t rwt = mk_rsrc(ws + WS_WT, 4 * 4608 * 32);
    unsigned mv[2][2];
#pragma unroll
    for (int sp = 0; sp < 2; ++sp)
#pragma unroll
      for (int i = 0; i < 2; ++i) mv[sp][i] = k5_mv(4 * (2 * l + i) + g, sp, nt, t16);
    const unsigned hs = ((8 * mt + 2 * l) * 144 + 36 * sg) * 1024, wsb = sg * 1152 * 1152, dl = lds0 + l * 2048;
    auto issue = [&](auto V) {
      constexpr int v = V.value, sp = v >= 32;
      const int u = k5_u(v, nt);
      constexpr unsigned so = k5_so(v);
      glds16(dl + so + 8192, mv[sp][0], rwd, wsb + u * 32 * 1152);
      glds16c(dl + so, lane * 16, rhh, hs + u * 1024);
      glds16(dl + so + 8192 + 1024, mv[sp][1], rwd, wsb + u * 32 * 1152);
      glds16c(dl + so + 1024, lane * 16, rhh, hs + (144 + u) * 1024);
      if ((v & 3) == l) glds16c(lds0 + so + 16384, lane * 16, rwt, (nt * 144 + 36 * sg + u) * 1024);
    };
    sfor<36>([&](auto I) {
      constexpr int i = I.value, y = k5_E(i - 1) - (i + 2 < 36 ? i + 2 : 36), yy = k5_E(i - 1) - K5_P;
      if constexpr (i == 0) wait_vm<2>();
      else if constexpr (i == 2) { wait_vm<4 * yy + yy / 4>(); set_flag(c, f4); }
      else if constexpr (i + 2 > K5_P) wait_vm<4 * y + y / 4>();
      bar();
      if constexpr (i == 0) STAMP(ws, 5, 1);
      if constexpr (i == 18) STAMP(ws, 5, 2);
      sfor<k5_E(i) - k5_E(i - 1)>([&](auto U) { issue(ic<k5_E(i - 1) + U.value>{}); });
    });
    bar();
    STAMP(ws, 5, 3);
    bar();
  } else {
    const int r2 = 4 * g + ((lane >> 2) & 3);
    unsigned wa[2][10];
#pragma unroll
    for (int i = 0; i < 8; ++i) wa[0][i] = lds0 + K5_BASE + 8192 + r2 * 256 + 32 * (i ^ (r2 & 7)) + 8 * (lane & 3);
    wa[0][8] = lds0 + K5_BASE + 16384 + r2 * 32 + 8 * (lane & 3);
    wa[0][9] = lds0 + K5_BASE + w * 2048 + lane * 16;
#pragma unroll
    for (int i = 0; i < 10; ++i) { wa[1][i] = wa[0][i] + 4 * K5_SLOT; PIN(wa[0][i]); PIN(wa[1][i]); }
    const unsigned fo = k5_fl(lane & 31, sg, mt);
    f32x4 acc[9][2];
    bf16x4 fr[3][3][2];
    bf16x8 hb[2][2];
    u32x2 pf;
    auto rd1 = [&](auto N, auto I) {
      constexpr int n = N.value, u = n / 3, i = I.value;
      if constexpr (u < 36) {
        constexpr int pos = (u + 7) & 7, hi = pos >> 2, cc = 3 * (n % 3) + i / 2;
        constexpr unsigned so = (pos & 3) * K5_SLOT;
        if constexpr (i >= 6) hb[u & 1][i - 6] = ldsr<bf16x8>(wa[hi][9] + so + (i - 6) * 1024);
        else fr[n % 3][i / 2][i & 1] = trh(wa[hi][cc] + so + (i & 1) * (cc < 8 ? 4096 : 512));
      }
    };
    sfor<36>([&](auto U) {
      constexpr int u = U.value;
      if constexpr (u == 0) wait_vm<2>();
      if constexpr (u == 4) { wait_vm<0>(); set_flag(c, f4); }
      if constexpr (u == 4 || u == 18) bload8c(pf, fo, fl_rsrc(c), FL4 + (u == 4 ? 1 : 2) * 2048 * 64);
      if constexpr (u == 7 || u == 21) {
        asm volatile("s_waitcnt vmcnt(0)" : "+v"(pf)::"memory");
        if (__builtin_expect(!tag_ok(c, pf), 0)) poll(c, fo, FL4 + (u == 7 ? 1 : 2) * 2048 * 64);
      }
      bar();
      if constexpr (u == 0) STAMP(ws, 5, 1);
      if constexpr (u == 18) STAMP(ws, 5, 2);
      if constexpr (u == 0) { rd1(ic<0>{}, ic<6>{}); rd1(ic<0>{}, ic<7>{}); sfor<6>([&](auto I) { rd1(ic<0>{}, I); }); sfor<6>([&](auto I) { rd1(ic<1>{}, I); }); }
      sfor<18>([&](auto M) {
        constexpr int mi = M.value, v = mi / 6, n = 3 * u + v, cc = mi / 2;
        if constexpr (u == 0) mfma16_z(acc[cc][mi & 1], cat(fr[n % 3][cc % 3][0], fr[n % 3][cc % 3][1]), hb[u & 1][mi & 1]);
        else mfma16_ip(acc[cc][mi & 1], cat(fr[n % 3][cc % 3][0], fr[n % 3][cc % 3][1]), hb[u & 1][mi & 1]);
        SBAR();
        if constexpr (mi == 6 || mi == 8) rd1(ic<n + 2>{}, ic<6 + (mi - 6) / 2>{});
        rd1(ic<n + 2>{}, ic<mi % 6>{});
        SBAR();
      });
    });
    asm volatile("s_nop 9" : "+v"(acc[7][0]), "+v"(acc[7][1]), "+v"(acc[8][0]), "+v"(acc[8][1]));
#pragma unroll
    for (int i = 0; i < 7; ++i) asm volatile("" : "+v"(acc[i][0]), "+v"(acc[i][1]));
    bar();
    STAMP(ws, 5, 3);
    if (w == sg) {
      sfor<2>([&](auto T) { sfor<9>([&](auto C) { ldsw<f32x4>(lds0 + (T.value * 9 + C.value) * 1024 + lane * 16, acc[C.value][T.value]); }); });
      lds_bar();
    } else {
      sfor<2>([&](auto T) {
        const unsigned pb = (((tile * 4 + w) * 4 + sg) * 2 + T.value) * PT_REC;
        sfor<4>([&](auto I) {
          constexpr int i = I.value;
          const f32x4 a = acc[2 * i][T.value], b = acc[2 * i + 1][T.value];
          bstore16<16>((u32x4){pk2(a[0], a[1]), pk2(a[2], a[3]), pk2(b[0], b[1]), pk2(b[2], b[3])}, rpt, lane * 16 + i * 1024, pb);
        });
        const f32x4 a = acc[8][T.value];
        bstore8<16>((u32x2){pk2(a[0], a[1]), pk2(a[2], a[3])}, rpt, lane * 8 + 4096, pb);
      });
      bar();
      wait_vm<0>();
      set_flag(c, FL5 + (tile * 4 + w) * 256 + sg * FS5);
    }
  }
  const int T = w >> 2, pi = w & 3, tt = 8 * mt + 2 * sg + T;
  const rsrc_t rhf = mk_rsrc(ws + WS_HF, 128 * 18 * 1024);
  u32x4 hv, pv[3];
  u32x2 hl, pl[3];
  unsigned rs2u;
  STAMP(ws, 5, 4);
  bload16c(hv, lane * 16, rhf, (tt * 18 + 4 * nt + pi) * 1024);
  bload8c(hl, lane * 16 + 8 * (nt & 1), rhf, (tt * 18 + 16 + (nt >> 1)) * 1024);
  bload4c(rs2u, t16 * 4, mk_rsrc(ws + WS_RS, 4 * 2048 * 4), (sg * 2048 + 16 * tt) * 4);
  poll(c, ((sg + 1 + min(lane, 2)) & 3) * FS5, FL5 + (tile * 4 + sg) * 256);
  STAMP(ws, 5, 5);
#pragma unroll
  for (int o = 0; o < 3; ++o) {
    const unsigned so = (((tile * 4 + sg) * 4 + ((sg + 1 + o) & 3)) * 2 + T) * PT_REC;
    bload16c(pv[o], lane * 16 + pi * 1024, rpt, so);
    bload8c(pl[o], pi == 0 ? lane * 8 + 4096 : OOB, rpt, so);
  }
  const unsigned orow = (16 * tt + t16) * 1152, own = lds0 + T * 9216 + lane * 16;
  const f32x4 o0 = ldsr<f32x4>(own + pi * 2048), o1 = ldsr<f32x4>(own + pi * 2048 + 1024), o2 = ldsr<f32x4>(own + 8192);
  asm volatile("s_waitcnt vmcnt(0)" : "+v"(hv), "+v"(hl), "+v"(rs2u), "+v"(pv[0]), "+v"(pv[1]), "+v"(pv[2]), "+v"(pl[0]), "+v"(pl[1]), "+v"(pl[2])::"memory");
  STAMP(ws, 5, 6);
  const float rs2 = __uint_as_float(rs2u);
  const rsrc_t rout = mk_rsrc(c.out, S_ * D_ * 2);
  auto fin = [&](const f32x4& ow, unsigned a0, unsigned a1, unsigned b0, unsigned b1, unsigned c0, unsigned c1, unsigned h0, unsigned h1, unsigned& d0, unsigned& d1) {
    const float v0 = ((ow[0] + bflo(b0)) + (bflo(a0) + bflo(c0))) * rs2 + bflo(h0);
    const float v1 = ((ow[1] + bfhi(b0)) + (bfhi(a0) + bfhi(c0))) * rs2 + bfhi(h0);
    const float v2 = ((ow[2] + bflo(b1)) + (bflo(a1) + bflo(c1))) * rs2 + bflo(h1);
    const float v3 = ((ow[3] + bfhi(b1)) + (bfhi(a1) + bfhi(c1))) * rs2 + bfhi(h1);
    d0 = pk2(v0, v1); d1 = pk2(v2, v3);
  };
  {
    unsigned a0, a1, b0, b1;
    fin(o0, pv[0][0], pv[0][1], pv[1][0], pv[1][1], pv[2][0], pv[2][1], hv[0], hv[1], a0, a1);
    fin(o1, pv[0][2], pv[0][3], pv[1][2], pv[1][3], pv[2][2], pv[2][3], hv[2], hv[3], b0, b1);
    auto s0 = __builtin_amdgcn_permlane16_swap(a0, b0, false, false);
    auto s1 = __builtin_amdgcn_permlane16_swap(a1, b1, false, false);
    bstore16<0>((u32x4){s0[0], s1[0], s0[1], s1[1]}, rout, orow + 256 * nt + 64 * pi + 32 * (g & 1) + 16 * (g >> 1), 0);
  }
  {
    unsigned e0, e1;
    fin(o2, pl[0][0], pl[0][1], pl[1][0], pl[1][1], pl[2][0], pl[2][1], hl[0], hl[1], e0, e1);
    bstore8<0>((u32x2){e0, e1}, rout, pi == 0 ? orow + 1024 + 32 * nt + 8 * g : OOB, 0);
  }
  wait_vm<0>();
  STAMP(ws, 5, 7);
  STAMP_FLUSH(ws, 5);
}

extern "C" __global__ __launch_bounds__(512, 2) void k1_all(const void* x, const void* wqkv, const void* wo, const void* wg, const void* wu, const void* wd, char* ws, void* out) {
  unsigned long long te_; asm volatile("s_memrealtime %0" : "=s"(te_)); SBAR();
  Ctx c = {x, wqkv, wo, wg, wu, wd, ws, out, (int)rfl(threadIdx.x >> 6), (int)(blockIdx.x & 7), (int)(blockIdx.x >> 3), (unsigned)(size_t)smem, 0u, 0u, te_};
  if (c.w == 7) {
    unsigned long long pc; unsigned back, size;
    asm volatile("s_getpc_b64 %0\n.Lk1pc%=:\n\ts_mov_b32 %1, .Lk1pc%=-k1_all\n\ts_mov_b32 %2, k9_end-k1_all" : "=s"(pc), "=s"(back), "=s"(size));
    const int lane = fresh_lane();
    warm(c.lds0 + LDS_SCR, lane < 24 ? (c.r * 24 + lane) * 128 : OOB, mk_rsrc((const void*)(pc - back), size), 0);
  }
  {
    const int mt = 4 * (c.xcd & 3) + (c.r & 3), nt = 8 * (c.xcd >> 2) + (c.r >> 2);
    if (c.w < 4) p1_body<0>(c, mt, nt); else p1_body<1>(c, mt, nt);
  }
  p2(c);
  if (c.r < 18) p3_gemm(c); else { cnt_wait(c, 1, 8); p3_side(c); }
  if (c.w < 4) k4_first<0>(c, fresh_lane());
  else { cnt_wait(c, 1, 8); k4_first<4>(c, fresh_lane()); }
  p4(c);
  p5(c);
  STAMP_OUT();
}

extern "C" __global__ void k9_end() {}

extern "C" __global__ __launch_bounds__(512) void k0_pre(char* ws) {
  unsigned long long t0 = __builtin_amdgcn_s_memrealtime();
  if (blockIdx.x == 0 && threadIdx.x == 0) *(GAS unsigned*)(ws + WS_DBG - 256) = (unsigned)t0;
}
extern "C" __global__ __launch_bounds__(256) void k6_diag(char* ws) {
  __shared__ unsigned ra[256], rb[256];
  const int b = threadIdx.x;
  unsigned long long t0 = __builtin_amdgcn_s_memrealtime();
  const unsigned ref = *(GAS unsigned*)(ws + WS_DBG + ((size_t)1 * 256 * 8 * 16) * 4) - 100000u;
  unsigned a = ~0u, c = 0u;
  for (int w = 0; w < 8; ++w) {
    { unsigned v = *(GAS unsigned*)(ws + WS_DBG + ((((size_t)1 * 256 + b) * 8 + w) * 16) * 4) - ref; a = v < a ? v : a; }
    { unsigned v = *(GAS unsigned*)(ws + WS_DBG + ((((size_t)5 * 256 + b) * 8 + w) * 16 + 7) * 4) - ref; c = v > c ? v : c; }
  }
  ra[b] = a; rb[b] = c;
  __syncthreads();
  if (b == 0) {
    for (int i = 1; i < 256; ++i) { a = ra[i] < a ? ra[i] : a; c = rb[i] > c ? rb[i] : c; }
    unsigned long long d = 200;
    if (MODE == 1) d = a - (*(GAS unsigned*)(ws + WS_DBG - 256) - ref);
    if (MODE == 2) d = ((unsigned)t0 - ref) - c;
    if (d > 10000) d = 10000;
    d *= 10;
    while (__builtin_amdgcn_s_memrealtime() - t0 < d + 300) __builtin_amdgcn_s_sleep(2);
  }
}

extern "C" void kernel_launch(void* const* d_in, const int* in_sizes, int n_in, void* d_out, int out_size, void* d_ws, size_t ws_size,
                              hipStream_t stream) {
  (void)hipFuncSetAttribute((const void*)k1_all, hipFuncAttributeMaxDynamicSharedMemorySize, LDS_ALL);
  k1_all<<<256, 512, LDS_ALL, stream>>>(d_in[0], d_in[1], d_in[2], d_in[4], d_in[5], d_in[6], (char*)d_ws, d_out);
  k6_diag<<<1, 256, 0, stream>>>((char*)d_ws);
}
```

```cpp
#define STAMPS
#define KB 1
#define IB 0
#define MB 0
#define CONSTD 1
#include <hip/hip_runtime.h>
#include <stdint.h>

typedef short bf16x8 __attribute__((ext_vector_type(8)));
typedef short bf16x4 __attribute__((ext_vector_type(4)));
typedef float f32x2 __attribute__((ext_vector_type(2)));
typedef float f32x4 __attribute__((ext_vector_type(4)));
typedef float f32x16 __attribute__((ext_vector_type(16)));
typedef unsigned u32x2 __attribute__((ext_vector_type(2)));
typedef unsigned u32x4 __attribute__((ext_vector_type(4)));
typedef __bf16 bf2_t __attribute__((ext_vector_type(2)));
typedef __amdgpu_buffer_rsrc_t rsrc_t;
#define LAS __attribute__((address_space(3)))
#define GAS __attribute__((address_space(1)))
#define DI __device__ __forceinline__

constexpr int S_ = 2048, D_ = 576, F_ = 4608;
constexpr float QSCALE = 0.23570226039551584f * 1.4426950408889634f;
constexpr unsigned ONES2 = 0x3f803f80u;

constexpr size_t QREC = 1280, KVREC = 2560;
constexpr unsigned OOB = 0x7fffff00u;
#define FS1 64
#define FS2 64
#define FS3 64
#define FS5 64
constexpr unsigned FL1 = 128;
constexpr unsigned FL2 = FL1 + 256 * 64;
constexpr unsigned FL3 = FL2 + 2048 * 64;
constexpr unsigned FLS = FL3 + 2048 * 64;
constexpr unsigned FL4 = FLS + 128 * 64;
constexpr unsigned FL5 = FL4 + 3 * 2048 * 64;
constexpr unsigned FL_END = FL5 + 1024 * 64;
constexpr size_t WS_QF = 1u << 20;
constexpr size_t WS_KV = WS_QF + 32 * 64 * QREC;
constexpr size_t WS_OF = WS_KV + 32 * 64 * KVREC;
constexpr size_t WS_HF = WS_OF + 128 * 18 * 1024;
constexpr size_t WS_ST = WS_HF + 128 * 18 * 1024;
constexpr size_t WS_CS = WS_ST + 9 * 2048 * 8;
constexpr size_t WS_RS = WS_CS + 2 * 4608 * 4;
constexpr size_t WS_LG = WS_RS + 4 * 2048 * 4;
constexpr size_t WS_WT = WS_LG + 8 * 4 * 36864;
constexpr size_t WS_HH = WS_WT + 4 * 4608 * 32;
constexpr size_t PT_REC = 4608;
constexpr size_t WS_PT = WS_HH + (size_t)128 * 144 * 1024;
constexpr size_t WS_END = WS_PT + (size_t)64 * 8 * 4 * PT_REC;

DI unsigned rfl(unsigned v) { return __builtin_amdgcn_readfirstlane(v); }
DI rsrc_t mk_rsrc(const void* p, unsigned bytes) { return __builtin_amdgcn_make_buffer_rsrc((void*)p, (short)0, (int)bytes, 0x00020000); }
DI void glds16(unsigned lds, unsigned voff, rsrc_t r, unsigned soff) {
  asm volatile("s_mov_b32 m0, %0\n\ts_nop 0\n\tbuffer_load_dwordx4 %1, %2, %3 offen lds" ::"s"(lds), "v"(voff), "s"(r), "s"(soff) : "memory", "m0");
}
DI void glds16c(unsigned lds, unsigned voff, rsrc_t r, unsigned soff) {
  asm volatile("s_mov_b32 m0, %0\n\ts_nop 0\n\tbuffer_load_dwordx4 %1, %2, %3 offen sc1 lds" ::"s"(lds), "v"(voff), "s"(r), "s"(soff) : "memory", "m0");
}
template <class T> DI void bload16c(T& d, unsigned voff, rsrc_t r, unsigned soff) {
  asm volatile("buffer_load_dwordx4 %0, %1, %2, %3 offen sc1" : "=v"(d) : "v"(voff), "s"(r), "s"(soff) : "memory");
}
DI void bload8c(u32x2& d, unsigned voff, rsrc_t r, unsigned soff) {
  asm volatile("buffer_load_dwordx2 %0, %1, %2, %3 offen sc1" : "=v"(d) : "v"(voff), "s"(r), "s"(soff) : "memory");
}
DI void bload4c(unsigned& d, unsigned voff, rsrc_t r, unsigned soff) {
  asm volatile("buffer_load_dword %0, %1, %2, %3 offen sc1" : "=v"(d) : "v"(voff), "s"(r), "s"(soff) : "memory");
}
DI void bload8(u32x2& d, unsigned voff, rsrc_t r, unsigned soff) {
  asm volatile("buffer_load_dwordx2 %0, %1, %2, %3 offen" : "=v"(d) : "v"(voff), "s"(r), "s"(soff) : "memory");
}
DI void warm(unsigned lds, unsigned voff, rsrc_t r, unsigned soff) {
  asm volatile("s_mov_b32 m0, %0\n\ts_nop 0\n\tbuffer_load_dword %1, %2, %3 offen lds" ::"s"(lds), "v"(voff), "s"(r), "s"(soff) : "memory", "m0");
}
template <int N> DI void wait_vm() { asm volatile("s_waitcnt vmcnt(%0)" ::"i"(N) : "memory"); }
#define PIN(x) asm volatile("" : "+v"(x))
#define SBAR() __builtin_amdgcn_sched_barrier(0)
DI void bar() { SBAR(); asm volatile("s_barrier" ::: "memory"); SBAR(); }
DI void lds_bar() { SBAR(); asm volatile("s_waitcnt lgkmcnt(0)\n\ts_barrier" ::: "memory"); SBAR(); }
DI bf16x8 trfrag(unsigned a0, unsigned a1) {
  bf16x4 lo = __builtin_amdgcn_ds_read_tr16_b64_v4i16((LAS bf16x4*)(size_t)a0);
  bf16x4 hi = __builtin_amdgcn_ds_read_tr16_b64_v4i16((LAS bf16x4*)(size_t)a1);
  return __builtin_shufflevector(lo, hi, 0, 1, 2, 3, 4, 5, 6, 7);
}
DI bf16x4 trh(unsigned a) { return __builtin_amdgcn_ds_read_tr16_b64_v4i16((LAS bf16x4*)(size_t)a); }
DI bf16x8 cat(bf16x4 lo, bf16x4 hi) { return __builtin_shufflevector(lo, hi, 0, 1, 2, 3, 4, 5, 6, 7); }
template <class T> DI T ldsr(unsigned a) { return *(LAS T*)(size_t)a; }
template <class T> DI void ldsw(unsigned a, T v) { *(LAS T*)(size_t)a = v; }
DI unsigned pk2(float a, float b) { f32x2 v = {a, b}; return __builtin_bit_cast(unsigned, __builtin_convertvector(v, bf2_t)); }
DI float bflo(unsigned u) { return __uint_as_float(u << 16); }
DI float bfhi(unsigned u) { return __uint_as_float(u & 0xffff0000u); }
DI f32x4 mfma16(bf16x8 a, bf16x8 b, f32x4 c) { return __builtin_amdgcn_mfma_f32_16x16x32_bf16(a, b, c, 0, 0, 0); }
DI void mfma16_ip(f32x4& c, bf16x8 a, bf16x8 b) { asm volatile("v_mfma_f32_16x16x32_bf16 %0, %1, %2, %0" : "+v"(c) : "v"(a), "v"(b)); }
DI void mfma16_z(f32x4& c, bf16x8 a, bf16x8 b) { asm volatile("v_mfma_f32_16x16x32_bf16 %0, %1, %2, 0" : "=&v"(c) : "v"(a), "v"(b)); }
DI f32x16 mfma32(bf16x8 a, bf16x8 b, f32x16 c) { return __builtin_amdgcn_mfma_f32_32x32x16_bf16(a, b, c, 0, 0, 0); }
DI float xor32(float v) { auto r = __builtin_amdgcn_permlane32_swap(__float_as_uint(v), __float_as_uint(v), false, false); return __uint_as_float((threadIdx.x & 32) ? r[0] : r[1]); }
DI float sum_x32(float v) { auto r = __builtin_amdgcn_permlane32_swap(__float_as_uint(v), __float_as_uint(v), false, false); return __uint_as_float(r[0]) + __uint_as_float(r[1]); }
DI float max_x32(float v) { auto r = __builtin_amdgcn_permlane32_swap(__float_as_uint(v), __float_as_uint(v), false, false); return fmaxf(__uint_as_float(r[0]), __uint_as_float(r[1])); }
DI float sum_x16(float v) { auto r = __builtin_amdgcn_permlane16_swap(__float_as_uint(v), __float_as_uint(v), false, false); return __uint_as_float(r[0]) + __uint_as_float(r[1]); }
template <int AUX> DI void bstore16(u32x4 v, rsrc_t r, unsigned voff, unsigned soff) {
  if constexpr (AUX == 16) asm volatile("buffer_store_dwordx4 %0, %1, %2, %3 offen sc1\n\ts_nop 1" ::"v"(v), "v"(voff), "s"(r), "s"(soff) : "memory");
  else asm volatile("buffer_store_dwordx4 %0, %1, %2, %3 offen\n\ts_nop 1" ::"v"(v), "v"(voff), "s"(r), "s"(soff) : "memory");
}
template <int AUX> DI void bstore8(u32x2 v, rsrc_t r, unsigned voff, unsigned soff) { __builtin_amdgcn_raw_buffer_store_b64(v, r, voff, soff, AUX); }
template <int AUX> DI void bstore4(unsigned v, rsrc_t r, unsigned voff, unsigned soff) { __builtin_amdgcn_raw_buffer_store_b32(v, r, voff, soff, AUX); }

template <int... I> struct iseq {};
template <int N, int... I> struct mkseq : mkseq<N - 1, N - 1, I...> {};
template <int... I> struct mkseq<0, I...> { typedef iseq<I...> type; };
template <int V> struct ic { static constexpr int value = V; };
template <class Fn, int... I> DI void sfor_(Fn&& f, iseq<I...>) { (f(ic<I>{}), ...); }
template <int N, class Fn> DI void sfor(Fn&& f) { sfor_(f, typename mkseq<N>::type{}); }

extern __shared__ __attribute__((aligned(16))) char smem[];
constexpr size_t WS_DBG = 64u << 20;
constexpr int LDS_DBG = 156 * 1024;
#define STAMP(ws, kid, i) do { SBAR(); unsigned long long t_, c_; asm volatile("s_memrealtime %0\n\ts_memtime %1\n\ts_waitcnt lgkmcnt(0)" : "=s"(t_), "=s"(c_) :: "memory"); \
  asm volatile("v_writelane_b32 %0, %1, %3\n\tv_writelane_b32 %0, %2, %4" : "+v"(stamp_v) : "s"((unsigned)t_), "s"((unsigned)c_), "i"(i), "i"(8 + i)); SBAR(); } while (0)
#define STAMP_DECL unsigned stamp_v = 0
#define STAMP_FLUSH(ws, kid) do { const int l_ = fresh_lane(); if (l_ < 16) ldsw<unsigned>(c.lds0 + LDS_DBG + ((c.w * 7 + kid) * 16 + l_) * 4, stamp_v); } while (0)
#define STAMP_OUT() do { const int l_ = fresh_lane(); if (l_ < 16) for (int k_ = 1; k_ < 7; ++k_) if ((k_ == 3) == (c.r < 18) || (k_ != 3 && k_ != 6)) \
  *(GAS unsigned*)(c.ws + WS_DBG + ((((size_t)k_ * 256 + blockIdx.x) * 8 + c.w) * 16 + l_) * 4) = ldsr<unsigned>(c.lds0 + LDS_DBG + ((c.w * 7 + k_) * 16 + l_) * 4); } while (0)

struct Ctx {
  const void *x, *wqkv, *wo, *wg, *wu, *wd;
  char* ws; void* out;
  int w, xcd, r;
  unsigned lds0, t0, t1;
  unsigned long long te;
};
constexpr int LDS_ALL = 160 * 1024, LDS_SCR = LDS_ALL - 256;
DI int fresh_lane() { int l; asm volatile("v_mbcnt_lo_u32_b32 %0, -1, 0\n\tv_mbcnt_hi_u32_b32 %0, -1, %0" : "=v"(l)); __builtin_assume(l >= 0 && l < 64); return l; }
DI unsigned opq(unsigned v) { PIN(v); return v; }
constexpr int LDS_CNT = 147456;
DI void cnt_add(const Ctx& c, int i) {
  asm volatile("s_mov_b64 exec, 1\n\tds_add_u32 %0, %1\n\ts_mov_b64 exec, -1" ::"v"(c.lds0 + LDS_CNT + 4 * i), "v"(1u) : "memory");
}
DI unsigned cnt_get(const Ctx& c, int i) {
  unsigned v;
  asm volatile("ds_read_b32 %0, %1\n\ts_waitcnt lgkmcnt(0)" : "=v"(v) : "v"(c.lds0 + LDS_CNT + 4 * i) : "memory");
  return rfl(v);
}
DI void cnt_wait(const Ctx& c, int i, unsigned n) {
  if (__builtin_expect(cnt_get(c, i) < n, 0))
    for (int spin = 0; spin < (1 << 22) && cnt_get(c, i) < n; ++spin) __builtin_amdgcn_s_sleep(1);
}
DI rsrc_t fl_rsrc(const Ctx& c) { return mk_rsrc(c.ws, FL_END); }
DI void set_flag(const Ctx& c, unsigned off) {
  u32x2 t;
  asm volatile("v_mov_b32 %0, %1" : "=v"(t[0]) : "s"(c.t0));
  asm volatile("v_mov_b32 %0, %1" : "=v"(t[1]) : "s"(c.t1));
  asm volatile("s_mov_b64 exec, 1\n\tbuffer_store_dwordx2 %0, off, %1, %2 sc1\n\ts_mov_b64 exec, -1" ::"v"(t), "s"(fl_rsrc(c)), "s"(off) : "memory");
}
DI bool tag_ok(const Ctx& c, u32x2 v) { return __builtin_amdgcn_ballot_w64(v[0] != c.t0 || v[1] != c.t1) == 0; }
DI void poll(const Ctx& c, unsigned voff, unsigned soff) {
  for (int spin = 0; spin < (1 << 20); ++spin) {
    u32x2 v;
    asm volatile("buffer_load_dwordx2 %0, %1, %2, %3 offen sc1\n\ts_waitcnt vmcnt(0)" : "=&v"(v) : "v"(voff), "s"(fl_rsrc(c)), "s"(soff) : "memory");
    if (tag_ok(c, v)) break;
  }
}

DI bool poll_first(const Ctx& c, unsigned voff, unsigned soff, unsigned long long first) {
  unsigned long long m = 0;
  for (int spin = 0; spin < (1 << 20); ++spin) {
    u32x2 v;
    asm volatile("buffer_load_dwordx2 %0, %1, %2, %3 offen sc1\n\ts_waitcnt vmcnt(0)" : "=&v"(v) : "v"(voff), "s"(fl_rsrc(c)), "s"(soff) : "memory");
    m = __builtin_amdgcn_ballot_w64(v[0] != c.t0 || v[1] != c.t1);
    if ((m & first) == 0) break;
  }
  return m == 0;
}

constexpr int K1_SLOT = 32768, K1_TBL = K1_SLOT * 4;
extern "C" __global__ void k9_end();

template <int NN> DI void p1_body(Ctx& c, int mt, int nt) {
  char* ws = c.ws;
  const int w = c.w, lane = fresh_lane();
  const unsigned lds0 = c.lds0;
  const rsrc_t rx = mk_rsrc(c.x, S_ * D_ * 2), rw = mk_rsrc(c.wqkv, D_ * 3 * D_ * 2);
  const int t16 = lane & 15, g = lane >> 4, q4 = (lane >> 2) & 3, p = lane & 3, m = w & 3;
  constexpr int NT = NN ? 3 : 4;
  unsigned xv[2], wv[2];
#pragma unroll
  for (int i = 0; i < 2; ++i) {
    const int row = 8 * (2 * w + i) + (lane >> 3);
    xv[i] = (128 * mt + row) * 1152 + 16 * ((lane & 7) ^ ((row >> 1) & 7));
    int li = 64 * (2 * w + i) + lane;
    if (li >= 896) li -= 128;
    const int r64 = (li * 4682) >> 16, cc = li - 14 * r64;
    const int rho = r64 & 31, k = (r64 & 32) + 8 * ((rho >> 2) & 3) + 4 * (rho >> 4) + (rho & 3);
    wv[i] = k * 3456 + ((216 * nt) & ~15) + 16 * cc;
  }
  STAMP_DECL; STAMP(ws, 1, 3);
  asm volatile("v_writelane_b32 %0, %1, 0" : "+v"(stamp_v) : "s"((unsigned)c.te));

  auto issue1 = [&](int slab, int i) {
    const unsigned dst = lds0 + (slab & 3) * K1_SLOT + w * 2048 + (i >> 1) * 1024;
    if (i & 1) glds16(dst + 16384, wv[i >> 1], rw, slab * 64 * 3456);
    else glds16(dst, xv[i >> 1], rx, slab * 128);
  };
#pragma unroll
  for (int sl = 0; sl < 2; ++sl)
#pragma unroll
    for (int i = 0; i < 4; ++i) issue1(sl, i);
  u32x2 ev;
  bload8c(ev, 0u, fl_rsrc(c), 0);
  STAMP(ws, 1, 1);
  const unsigned wa = lds0 + 16384 + (4 * g + q4) * 224 + (nt & 1) * 8;
  unsigned ad[2][5];
  ad[0][0] = wa + 8 * p + 144 * NN;
  ad[0][1] = wa + 72 * (p < 3 ? p : 2) + 32;
  ad[0][2] = NN && m >= 2 ? ad[0][1] : ad[0][0] + 72 * (m >> 1) + 40 * (m & 1);
  ad[0][3] = lds0 + (32 * m + t16) * 128 + 16 * (g ^ ((t16 >> 1) & 7));
  ad[0][4] = ad[0][3] ^ 64;
#pragma unroll
  for (int i = 0; i < 5; ++i) { ad[1][i] = ad[0][i] + 2 * K1_SLOT; PIN(ad[1][i]); }
  const unsigned tb = lds0 + K1_TBL, cb = tb + 1024, dummy = cb + 512;
  unsigned e_cs = t16 == 0 ? cb + (w * 16 + 4 * g) * 4 : dummy, e_st = tb + (32 * m + t16) * 8, e_cc = cb + (64 * NN + 4 * g) * 4;
  unsigned e_s1 = g == 3 ? e_st : dummy, e_s2 = g == q4 ? e_st + 4 : dummy, e_vo[2], e_so[4], e_t2 = 0, e_one = 0;
  unsigned e_sc[2];
  float e_tsc = 0.f;
  {
    constexpr unsigned KV0 = WS_KV - WS_QF;
    const int tile32 = 4 * mt + m, key = 16 * (g >> 1) + t16;
    const unsigned lo_qk = (g & 1) * 512 + key * 16, lo_v = 1280 + key * 32 + (g & 1) * 16;
#pragma unroll
    for (int i = 0; i < 2 - NN; ++i) {
      const int pg = 3 * nt + 2 * NN + i, type = pg >> 4, rl = type ? KVREC : QREC;
      e_so[2 * i] = (type ? KV0 : 0) + (2 * (pg & 15) * 64 + tile32) * rl;
      e_so[2 * i + 1] = e_so[2 * i] + 64 * rl;
      e_vo[i] = type == 2 ? lo_v : lo_qk;
      e_sc[i] = type ? 0x3f800000u : __builtin_bit_cast(unsigned, QSCALE);
      asm volatile("" : "+s"(e_so[2 * i]), "+s"(e_so[2 * i + 1]), "+s"(e_sc[i]), "+v"(e_vo[i]));
    }
    if constexpr (NN) {
      const int pg = 3 * nt + g, type = pg >> 4, rl = type ? KVREC : QREC;
      e_vo[1] = g < 3 ? (type ? KV0 : 0) + (2 * (pg & 15) * 64 + tile32) * rl + (type == 2 ? 2304 : 1024) + t16 * 8 : OOB;
      e_t2 = e_vo[1] + 64 * rl;
      e_one = type == 1 ? 0u : 0x00003f80u;
      e_tsc = type ? 1.f : QSCALE;
      PIN(e_vo[1]); PIN(e_t2); PIN(e_one); PIN(e_tsc); PIN(e_s1); PIN(e_s2);
    }
    PIN(e_cs); PIN(e_st); PIN(e_cc);
  }
  f32x4 acc[4][2] = {}, cs = {};
  bf16x8 ones = __builtin_bit_cast(bf16x8, (u32x4){ONES2, ONES2, ONES2, ONES2});
  PIN(ones);
  bf16x8 fb[2][2];
  bf16x4 fa[2][NT + 1][2];
  constexpr int NR = 2 * NT + 4;
  auto rd1 = [&](auto KS, auto I) {
    constexpr int ks = KS.value, i = I.value, sp = ks & 1, sl = (ks >> 1) & 3, ti = (i - 2) >> 1;
    constexpr unsigned so = (sl & 1) * K1_SLOT;
    if constexpr (ks < 18 && i < NR) {
      if constexpr (i < 2) fb[sp][i] = ldsr<bf16x8>(ad[sl >> 1][3 + sp] + so + i * 2048);
      else {
        constexpr int ai = ti == NT ? 2 : (NN && ti == 2) ? 1 : 0;
        constexpr unsigned co = ai ? 0 : 72 * (ti >> 1) + 40 * (ti & 1);
        fa[sp][ti][i & 1] = trh(ad[sl >> 1][ai] + so + co + sp * 32 * 224 + (i & 1) * 16 * 224);
      }
    }
  };
  sfor<9>([&](auto SL) {
    constexpr int sl = SL.value;
    constexpr int iss = sl == 0 ? 2 : (sl + 3 < 9 ? sl + 3 : 9);
    constexpr int need = sl == 0 ? 0 : (sl + 1 < 9 ? sl + 1 : 8);
    if constexpr (sl == 1) asm volatile("s_waitcnt vmcnt(%1)" : "+v"(ev) : "i"(4 * (iss - need - 1)) : "memory");
    else wait_vm<4 * (iss - need - 1) + (sl == 0)>();
    bar();
    if constexpr (sl == 0) STAMP(ws, 1, 2);
    if constexpr (sl <= 1) sfor<NR>([&](auto I) { rd1(ic<2 * sl>{}, I); });
    if constexpr (sl == 1) {
      const unsigned long long e = (((unsigned long long)rfl(ev[1]) << 32) | rfl(ev[0])) + 1ull;
      c.t0 = (unsigned)e; c.t1 = (unsigned)(e >> 32);
    }
    sfor<2>([&](auto SP) {
      constexpr int sp = SP.value, ks = 2 * sl + sp;
      bf16x8 tl;
      if constexpr (NN) tl = t16 >= 12 ? ones : cat(fa[sp][2][0], fa[sp][2][1]);
      sfor<9>([&](auto M) {
        constexpr int mi = M.value, ti = mi >> 1;
        if constexpr (mi == 8) cs = mfma16(cat(fa[sp][NT][0], fa[sp][NT][1]), ones, cs);
        else if constexpr (NN && ti == 3) acc[3][mi & 1] = mfma16(fb[sp][mi & 1], fb[sp][mi & 1], acc[3][mi & 1]);
        else if constexpr (NN && ti == 2) acc[2][mi & 1] = mfma16(tl, fb[sp][mi & 1], acc[2][mi & 1]);
        else acc[ti][mi & 1] = mfma16(cat(fa[sp][ti][0], fa[sp][ti][1]), fb[sp][mi & 1], acc[ti][mi & 1]);
        SBAR();
        if constexpr (ks != 1) {
          if constexpr (mi < 3) { rd1(ic<ks + 1>{}, ic<2 * mi>{}); rd1(ic<ks + 1>{}, ic<2 * mi + 1>{}); }
          else rd1(ic<ks + 1>{}, ic<3 + mi>{});
        }
        if constexpr (sl == 0) { if constexpr (mi & 1) issue1(2 + sp, mi >> 1); }
        else if constexpr (sl + 3 < 9 && (mi == 2 + 2 * NN || mi == 6 + 2 * NN)) issue1(sl + 3, 2 * sp + (mi >= 6));
        SBAR();
      });
    });
  });
  STAMP(ws, 1, 4);
  ldsw<f32x4>(e_cs, cs);
  if constexpr (NN) sfor<2>([&](auto T) {
    const f32x4 gr = acc[3][T.value];
    const float d01 = (p & 1) ? gr[1] : gr[0], d23 = (p & 1) ? gr[3] : gr[2];
    ldsw<float>(e_s1 + 128 * T.value, acc[2][T.value][0]);
    ldsw<float>(e_s2 + 128 * T.value, (p & 2) ? d23 : d01);
  });
  lds_bar();
  f32x2 sv[2];
  f32x4 cc[4];
  sfor<2>([&](auto T) { sv[T.value] = ldsr<f32x2>(e_st + 128 * T.value); });
  sfor<NT>([&](auto I) { cc[I.value] = ldsr<f32x4>(e_cc + 64 * I.value); });
  SBAR();
  float nmu[2], rs[2];
  sfor<2>([&](auto T) {
    nmu[T.value] = sv[T.value][0] * (-1.f / D_);
    rs[T.value] = __builtin_amdgcn_rsqf(fmaxf(sv[T.value][1] * (1.f / D_) - nmu[T.value] * nmu[T.value], 0.f) + 1e-5f);
  });
  const rsrc_t ro = mk_rsrc(ws + WS_QF, 32 * 64 * (QREC + KVREC));
  sfor<(NN ? 2 : 4)>([&](auto TI) {
    constexpr int ti = TI.value;
    const f32x4 v0 = (acc[ti][0] + cc[ti] * nmu[0]) * (rs[0] * __uint_as_float(e_sc[ti >> 1])), v1 = (acc[ti][1] + cc[ti] * nmu[1]) * (rs[1] * __uint_as_float(e_sc[ti >> 1]));
    auto s0 = __builtin_amdgcn_permlane32_swap(pk2(v0[0], v0[1]), pk2(v1[0], v1[1]), false, false);
    auto s1 = __builtin_amdgcn_permlane32_swap(pk2(v0[2], v0[3]), pk2(v1[2], v1[3]), false, false);
    bstore16<16>((u32x4){s0[0], s1[0], s0[1], s1[1]}, ro, e_vo[ti >> 1], e_so[ti]);
  });
  if constexpr (NN) sfor<2>([&](auto T) {
    const f32x4 v = (acc[2][T.value] + cc[2] * nmu[T.value]) * (rs[T.value] * e_tsc);
    bstore8<16>((u32x2){pk2(v[0], v[1]), e_one}, ro, e_vo[1], T.value * 128);
    bstore8<16>((u32x2){pk2(v[2], v[3]), e_one}, ro, e_t2, T.value * 128);
  });
  STAMP(ws, 1, 5);
  wait_vm<0>();
  STAMP(ws, 1, 6);
  bar();
  if (w == 0) set_flag(c, FL1 + (mt * 16 + nt) * FS1);
  STAMP(ws, 1, 7);
  STAMP_FLUSH(ws, 1);
}

constexpr int P3_WO = 73728;
constexpr int K4_RING = 8192;
constexpr unsigned k4_so(int u) { return ((((u >> 1) + 2) & 3) * 2 + (u & 1)) * 8192; }
DI unsigned k4_wv(int lane, int w, int Qd, int j) {
  const int rho = 8 * (w & 3) + (lane >> 3);
  return rho * 9216 + 1152 * Qd + 256 * j + 32 * (((lane & 7) >> 1) ^ ((rho >> 1) & 3)) + 16 * (lane & 1);
}

template <int U0> DI void k4_first(const Ctx& c, int lane) {
  const rsrc_t rg = mk_rsrc(c.wg, D_ * F_ * 2), ru = mk_rsrc(c.wu, D_ * F_ * 2);
  const unsigned wv4 = k4_wv(lane, c.w, c.xcd, c.r & 3), dst = c.lds0 + K4_RING + (c.w & 3) * 1024;
  sfor<4>([&](auto U) {
    glds16(dst + k4_so(U0 + U.value), wv4, rg, (U0 + U.value) * 32 * 9216);
    glds16(dst + 4096 + k4_so(U0 + U.value), wv4, ru, (U0 + U.value) * 32 * 9216);
  });
}
DI void p2_ahead(const Ctx& c, unsigned flag) {
  const int w = c.w, r = c.r, lane = fresh_lane();
  const rsrc_t rwo = mk_rsrc(c.wo, D_ * D_ * 2);
  const int nt = r >= 9 ? r - 9 : r;
  const int rho = 8 * w + (lane >> 3), rg = (rho >> 2) & 3, rj = 4 * (rho >> 4) + (rho & 3);
  const unsigned cbyte = 128 * nt + 32 * (((lane & 7) >> 1) ^ ((rho >> 1) & 3)) + 16 * (lane & 1);
  const int th = 4 * rg + (rj >> 1);
  const unsigned wv = (20 * (rg >> 1) + 8 * (rg & 1) + rj) * 1152 + cbyte, wvt = (18 * th + ((th & 1) ? 0 : 16) + (rj & 1)) * 1152 + cbyte;
  sfor<16>([&](auto K) {
    glds16(c.lds0 + P3_WO + (w + 4 * K.value) * 1024, wv, rwo, K.value * 36 * 1152);
    if constexpr (K.value == 7) { wait_vm<8>(); set_flag(c, flag); }
  });
  sfor<2>([&](auto K) { glds16(c.lds0 + P3_WO + (w + 64 + 4 * K.value) * 1024, wvt, rwo, K.value * 288 * 1152); });
}

DI void p2(const Ctx& c) {
  const int head = 4 * c.xcd + (c.r & 3), qb = c.r >> 2;
  char* ws = c.ws;
  const int w = c.w, lane = fresh_lane();
  const unsigned lds0 = c.lds0;
  const int q = lane & 31, h = lane >> 5, G = lane >> 4;
  const int qt = 8 * qb + w;
  const rsrc_t rq = mk_rsrc(ws + WS_QF, 32 * 64 * QREC), rkv = mk_rsrc(ws + WS_KV, 32 * 64 * KVREC);
  STAMP_DECL; STAMP(ws, 2, 0);
  unsigned kb = lds0 + w * KVREC + lane * 16, ktb = lds0 + w * KVREC + 1024 + q * 8;
  unsigned vb[4];
#pragma unroll
  for (int i = 0; i < 4; ++i) { const int row = 8 * i + 4 * (G >> 1) + ((lane >> 2) & 3); vb[i] = lds0 + w * KVREC + ((G & 1) ? 2304 + row * 8 : 1280 + row * 32 + 8 * (lane & 3)); PIN(vb[i]); }
  unsigned mf[16], mk[8];
#pragma unroll
  for (int e = 0; e < 16; ++e) { mf[e] = ((e & 3) + 8 * (e >> 2) <= q - 4 * h) ? ~0u : 0u; PIN(mf[e]); }
#pragma unroll
  for (int e = 0; e < 8; ++e) { mk[e] = (mf[2 * e] & 0xffffu) | (mf[2 * e + 1] & 0xffff0000u); PIN(mk[e]); }
  const unsigned tt = 2 * qt + (q >> 4);
  unsigned om_o = (tt * 18 + (head >> 1)) * 1024 + ((q & 15) + 16 * (2 * (head & 1) + h)) * 16;
  unsigned ot_o = h ? OOB : (tt * 18 + 16 + (head >> 4)) * 1024 + ((q & 15) + 16 * ((head >> 2) & 3)) * 16 + (head & 3) * 4;
  unsigned l16 = lane * 16, qto = h ? OOB : 1024 + q * 8, wmo;
  const int wmv = c.r * 8 + w;
  {
    const int l = (wmv >= 162 ? wmv - 162 : wmv >= 81 ? wmv - 81 : wmv) * 64 + lane, row = (l * 7282) >> 16;
    wmo = wmv >= 162 ? l * 128 : row * 9216 + c.xcd * 1152 + (l - 9 * row) * 128;
    PIN(wmo);
  }
  PIN(kb); PIN(ktb); PIN(om_o); PIN(ot_o); PIN(l16); PIN(qto);
  if (qb == 0) {
#pragma unroll
    for (int i = 0; i < 3; ++i) {
      const unsigned o = min(w * 64 + lane + 512 * i, 1279) * 16, m = (o % KVREC - 1024 < 256) ? 0x0000f149u : 0u;
      ldsw<u32x4>(lds0 + o, (u32x4){0u, m, 0u, m});
    }
  }
#define TSL 0
  if (TSL && w) __builtin_amdgcn_s_sleep(TSL);
  if (w >= 1 && w <= 3) warm(lds0 + LDS_SCR, (w * 64 + lane < 224 ? (w * 64 + lane - 64) * 128 : OOB), rkv, (head * 64 + 8 * qb) * KVREC);
  if (w == 4 || w == 5) warm(lds0 + LDS_SCR, (w * 64 + lane < 336 ? (w * 64 + lane - 256) * 128 : OOB), rq, (head * 64 + 8 * qb) * QREC);
  if (w == 0) {
    const int ty = lane < 4 ? 1 : lane < 8 ? 2 : 0, pm = max(2 * qb + (ty ? (lane & 3) - 2 : (lane & 1)), 0);
    ldsw<unsigned>(lds0 + LDS_CNT + 4 * (lane & 7), 0u);
    poll(c, (pm * 16 + (((16 * ty + (head >> 1)) * 43) >> 7)) * FS1, FL1);
  }
  lds_bar();
  STAMP(ws, 2, 7);
  bf16x8 qm; u32x2 qt2;
#pragma unroll
  for (int i = 0; i < 5; ++i) { const int pc = max(w + 8 * i, qb ? 0 : 20 + w); glds16c(lds0 + pc * 1024, l16, rkv, (head * 64 + 8 * qb - 8) * KVREC + pc * 1024); }
  bload16c(qm, l16, rq, (head * 64 + qt) * QREC);
  bload8c(qt2, qto, rq, (head * 64 + qt) * QREC);
  auto toff = [&](int i) { return (unsigned)(i * KVREC); };
  STAMP(ws, 2, 1);
  asm volatile("s_waitcnt vmcnt(0)" : "+v"(qm), "+v"(qt2)::"memory");
  bar();
  STAMP(ws, 2, 2);
  const bf16x4 qt4 = __builtin_bit_cast(bf16x4, qt2);
  bf16x8 km[9]; bf16x4 kt[9];
  constexpr int ord[9] = {0, 8, 1, 2, 3, 4, 5, 6, 7};
  sfor<9>([&](auto I) { constexpr int i = ord[I.value]; km[i] = ldsr<bf16x8>(kb + toff(i)); kt[i] = ldsr<bf16x4>(ktb + toff(i)); });
  SBAR();
  f32x16 s[9];
  float m4[4];
  auto qk = [&](auto I, auto J) {
    constexpr int i = I.value, j = J.value;
    f32x16 a = mfma32(km[i], qm, (f32x16){}), b;
    if constexpr (j < 9) b = mfma32(km[j], qm, (f32x16){});
    s[i] = __builtin_amdgcn_mfma_f32_32x32x8bf16_1k(kt[i], qt4, a, 0, 0, 0);
    if constexpr (j < 9) s[j] = __builtin_amdgcn_mfma_f32_32x32x8bf16_1k(kt[j], qt4, b, 0, 0, 0);
    SBAR();
  };
  auto mx1 = [&](auto I) {
    constexpr int i = I.value;
#pragma unroll
    for (int e = 0; e < 8; ++e) m4[e & 3] = fmaxf(fmaxf(m4[e & 3], s[i][2 * e]), s[i][2 * e + 1]);
  };
  bf16x4 vf[2][4];
  auto rdv = [&](int set, unsigned off) {
#pragma unroll
    for (int i = 0; i < 4; ++i) vf[set][i] = trh(vb[i] + off);
  };
  qk(ic<0>{}, ic<8>{});
  qk(ic<1>{}, ic<2>{});
  {
    size_t p0 = (size_t)c.wg, p1 = (size_t)c.wu, p2 = (size_t)c.wo;
    asm("" : "+s"(p0), "+s"(p1), "+s"(p2));
    p0 = wmv >= 81 ? p1 : p0;
    warm(lds0 + LDS_SCR, wmo, mk_rsrc((const void*)(wmv >= 162 ? p2 : p0), wmv >= 162 ? D_ * D_ * 2 : D_ * F_ * 2), 0);
  }
  SBAR();
#pragma unroll
  for (int e = 0; e < 16; ++e) s[8][e] = __uint_as_float((__float_as_uint(s[8][e]) & mf[e]) | (__float_as_uint(s[0][e]) & ~mf[e]));
#pragma unroll
  for (int e = 0; e < 4; ++e) asm("v_max_f32 %0, %1, %2\n\tv_max3_f32 %0, %0, %3, %4" : "=&v"(m4[e]) : "v"(s[8][4 * e]), "v"(s[8][4 * e + 1]), "v"(s[8][4 * e + 2]), "v"(s[8][4 * e + 3]));
  SBAR();
  qk(ic<3>{}, ic<4>{});
  mx1(ic<1>{}); mx1(ic<2>{}); SBAR();
  qk(ic<5>{}, ic<6>{});
  mx1(ic<3>{}); mx1(ic<4>{}); SBAR();
  qk(ic<7>{}, ic<9>{});
  rdv(1, toff(1));
  mx1(ic<5>{}); mx1(ic<6>{}); SBAR();
  mx1(ic<7>{});
  float mx = max_x32(fmaxf(fmaxf(m4[0], m4[1]), fmaxf(m4[2], m4[3])));
  STAMP(ws, 2, 3);
  f32x16 o = {};
  u32x4 pp;
  sfor<16>([&](auto H) {
    constexpr int hh = H.value, i = 1 + hh / 2, ks = hh & 1;
    f32x2 dd[4];
#pragma unroll
    for (int e = 0; e < 4; ++e) dd[e] = (f32x2){s[i][8 * ks + 2 * e], s[i][8 * ks + 2 * e + 1]} - (f32x2){mx, mx};
    SBAR();
    if constexpr (hh > 0 && hh < 15) o = mfma32(cat(vf[(1 + (hh - 1) / 2) & 1][2 * (ks ^ 1)], vf[(1 + (hh - 1) / 2) & 1][2 * (ks ^ 1) + 1]), __builtin_bit_cast(bf16x8, pp), o);
    if constexpr (hh == 15) {
      const u32x4 pd = {pp[0] & mk[0], pp[1] & mk[1], pp[2] & mk[2], pp[3] & mk[3]};
      o = mfma32(cat(vf[0][0], vf[0][1]), __builtin_bit_cast(bf16x8, pd), o);
      o = mfma32(cat(vf[1][0], vf[1][1]), __builtin_bit_cast(bf16x8, pp ^ pd), o);
    }
    SBAR();
    if constexpr (ks == 0) rdv((i + 1) & 1, toff(i < 8 ? i + 1 : 0));
    SBAR();
    float pv[8];
#pragma unroll
    for (int e = 0; e < 4; ++e) { pv[2 * e] = __builtin_amdgcn_exp2f(dd[e][0]); pv[2 * e + 1] = __builtin_amdgcn_exp2f(dd[e][1]); }
    SBAR();
    pp = (u32x4){pk2(pv[0], pv[1]), pk2(pv[2], pv[3]), pk2(pv[4], pv[5]), pk2(pv[6], pv[7])};
    SBAR();
  });
  {
    const u32x4 pd = {pp[0] & mk[4], pp[1] & mk[5], pp[2] & mk[6], pp[3] & mk[7]};
    o = mfma32(cat(vf[0][2], vf[0][3]), __builtin_bit_cast(bf16x8, pd), o);
    o = mfma32(cat(vf[1][2], vf[1][3]), __builtin_bit_cast(bf16x8, pp ^ pd), o);
  }
  PIN(o);
  STAMP(ws, 2, 4);
  auto lr = __builtin_amdgcn_permlane32_swap(__float_as_uint(o[10]), __float_as_uint(o[10]), false, false);
  const float inv = __builtin_amdgcn_rcpf(__uint_as_float(lr[0]));
  const rsrc_t ro = mk_rsrc(ws + WS_OF, 128 * 18 * 1024);
  const u32x4 om = {pk2(o[0] * inv, o[1] * inv), pk2(o[2] * inv, o[3] * inv), pk2(o[4] * inv, o[5] * inv), pk2(o[6] * inv, o[7] * inv)};
  bstore16<16>(om, ro, om_o, 0);
  bstore4<16>(pk2(o[8] * inv, o[9] * inv), ro, ot_o, 0);
  STAMP(ws, 2, 5);
  cnt_add(c, 1);
  if (w < 4 && c.r < 18) p2_ahead(c, FL2 + (qt * 32 + head) * FS2);
  else { wait_vm<0>(); set_flag(c, FL2 + (qt * 32 + head) * FS2); }
  STAMP(ws, 2, 6);
  STAMP_FLUSH(ws, 2);
}

DI void p3_side(const Ctx& c) {
  char* ws = c.ws;
  const int w = c.w, xcd = c.xcd, lane = fresh_lane();
  const unsigned lds0 = c.lds0;
  const int sb = c.r - 18, blk = sb * 8 + xcd;
  const rsrc_t rwt = mk_rsrc(ws + WS_WT, 4 * 4608 * 32), rlg = mk_rsrc(ws + WS_LG, 8 * 4 * 36864), rcs = mk_rsrc(ws + WS_CS, 2 * 4608 * 4);
  STAMP_DECL; STAMP(ws, 3, 0);
  const int mat = sb >= 5, un = sb < 5 ? 2 * sb : sb - 5;
  const rsrc_t rw = mk_rsrc(mat ? c.wu : c.wg, D_ * F_ * 2);
  const unsigned vo = (lane & 7) * 16 + (72 * w + (lane >> 3)) * 9216;
  u32x4 v[2][9];
#pragma unroll
  for (int i = 0; i < 9; ++i) v[0][i] = __builtin_amdgcn_raw_buffer_load_b128(rw, vo, (9 * xcd + un) * 128 + i * 8 * 9216, 0);
#pragma unroll
  for (int i = 0; i < 9; ++i) v[1][i] = __builtin_amdgcn_raw_buffer_load_b128(rw, sb < 4 ? vo : OOB, (9 * xcd + un + 1) * 128 + i * 8 * 9216, 0);
  const int drow = 64 * (blk - 40) + 8 * w + (lane >> 3);
  const u32x4 dv = __builtin_amdgcn_raw_buffer_load_b128(mk_rsrc(c.wd, F_ * D_ * 2), mat ? drow * 1152 + 1024 + (lane & 7) * 16 : OOB, 0, 0);
  if (un == 8) {
    const unsigned lo = ((lane & 7) >> 1) * 36864 + (72 * w + (lane >> 3)) * 32 + (lane & 1) * 16;
#pragma unroll
    for (int i = 0; i < 9; ++i) bstore16<16>(v[0][i], rlg, lo, (xcd * 8 + (sb >> 3)) * 18432 + i * 256);
  }
  const int pr = (drow % 576) >= 512 ? ((drow & ~24) | ((drow & 8) << 1) | ((drow & 16) >> 1)) : drow;
  bstore16<16>(dv, rwt, mat ? (((lane & 7) >> 1) * 4608 + pr) * 32 + (lane & 1) * 16 : OOB, 0);
  float a[2][8] = {};
#pragma unroll
  for (int k = 0; k < 2; ++k) {
#pragma unroll
    for (int i = 0; i < 9; ++i)
#pragma unroll
      for (int e = 0; e < 4; ++e) { a[k][2 * e] += bflo(v[k][i][e]); a[k][2 * e + 1] += bfhi(v[k][i][e]); }
#pragma unroll
    for (int e = 0; e < 8; ++e) a[k][e] = sum_x32(sum_x16(a[k][e]));
  }
  if (lane < 16) {
#pragma unroll
    for (int k = 0; k < 2; ++k) {
      ldsw<f32x4>(lds0 + k * 4096 + w * 512 + lane * 32, (f32x4){a[k][0], a[k][1], a[k][2], a[k][3]});
      ldsw<f32x4>(lds0 + k * 4096 + w * 512 + lane * 32 + 16, (f32x4){a[k][4], a[k][5], a[k][6], a[k][7]});
    }
  }
  lds_bar();
  if (w < 2) {
    float t = 0.f;
#pragma unroll
    for (int i = 0; i < 16; ++i) t += ldsr<float>(lds0 + w * 4096 + i * 256 + lane * 4);
    bstore4<16>(__float_as_uint(t), rcs, w == 0 || sb < 4 ? lane * 4 : OOB, ((sb >= 5 ? 4608 : 0) + (9 * xcd + un + w) * 64) * 4);
  }
  STAMP(ws, 3, 5);
  wait_vm<0>();
  bar();
  if (w == 0) set_flag(c, FLS + blk * 64);
  STAMP(ws, 3, 6);
  if (w == 5 && blk == 48) {
    for (int i = 0; i < 4; ++i) poll(c, lane * FS1, FL1 + i * 64 * FS1);
    set_flag(c, 0);
  }
  STAMP_FLUSH(ws, 6);
}

DI void p3_gemm(const Ctx& c) {
  char* ws = c.ws;
  const int w = c.w, lane = fresh_lane();
  const unsigned lds0 = c.lds0;
  const int t16 = lane & 15, g = lane >> 4;
  const int nt = c.r >= 9 ? c.r - 9 : c.r;
  const int T = 16 * c.xcd + 8 * (w >> 2) + 4 * (c.r >= 9) + (w & 3);
  const rsrc_t rof = mk_rsrc(ws + WS_OF, 128 * 18 * 1024);
  STAMP_DECL; STAMP(ws, 3, 0);
  unsigned wa[4];
  {
    const int r2 = 4 * g + ((lane >> 2) & 3);
#pragma unroll
    for (int i = 0; i < 4; ++i) wa[i] = lds0 + P3_WO + r2 * 128 + 32 * (i ^ ((r2 >> 1) & 3)) + 8 * (lane & 3);
  }
  u32x2 xr[4];
  sfor<4>([&](auto I) { bload8(xr[I.value], (16 * T + t16) * 1152 + 128 * nt + 8 * g, mk_rsrc(c.x, S_ * D_ * 2), 32 * I.value); });
  unsigned l16 = lane * 16, so = g == 0 ? t16 * 8 : OOB;
  PIN(l16); PIN(so);
  if (w < 4) { wait_vm<4>(); cnt_add(c, 0); }
  cnt_wait(c, 0, 4);
  STAMP(ws, 3, 1);
  constexpr int AH = 5;
  bf16x8 of[18];
  bf16x4 fr[AH + 1][4][2];
  unsigned wb[4];
#pragma unroll
  for (int i = 0; i < 4; ++i) { wb[i] = wa[i] + 65536; PIN(wb[i]); }
  auto rd1 = [&](auto KS, auto I) {
    constexpr int ks = KS.value, i = I.value;
    if constexpr (ks < 18) fr[ks % (AH + 1)][i >> 1][i & 1] = trh((ks < 16 ? wa : wb)[i >> 1] + (ks & 15) * 4096 + (i & 1) * 2048);
  };
  sfor<AH>([&](auto K) { sfor<8>([&](auto I) { rd1(K, I); }); });
  SBAR();
  const bool all = poll_first(c, (lane & 31) * FS2, FL2 + (T >> 1) * 32 * FS2, 0xffffull);
  asm volatile("" : "+v"(xr[0]), "+v"(xr[1]), "+v"(xr[2]), "+v"(xr[3]));
  sfor<8>([&](auto K) { bload16c(of[K.value], l16, rof, (T * 18 + K.value) * 1024); });
  f32x4 acc[4];
#pragma unroll
  for (int i = 0; i < 4; ++i) { acc[i] = (f32x4){bflo(xr[i][0]), bfhi(xr[i][0]), bflo(xr[i][1]), bfhi(xr[i][1])}; PIN(acc[i]); }
  if (!all) poll(c, (16 + (lane & 15)) * FS2, FL2 + (T >> 1) * 32 * FS2);
  STAMP(ws, 3, 2);
  sfor<10>([&](auto K) { bload16c(of[8 + K.value], l16, rof, (T * 18 + 8 + K.value) * 1024); });
  sfor<18>([&](auto KS) {
    constexpr int ks = KS.value;
    asm volatile("s_waitcnt vmcnt(%1)" : "+v"(of[ks]) : "i"(17 - ks) : "memory");
    if constexpr (ks == 0) STAMP(ws, 3, 3);
    if constexpr (ks == 9) STAMP(ws, 3, 4);
    sfor<4>([&](auto C) {
      mfma16_ip(acc[C.value], cat(fr[ks % (AH + 1)][C.value][0], fr[ks % (AH + 1)][C.value][1]), of[ks]);
      SBAR();
      rd1(ic<ks + AH>{}, ic<2 * C.value>{}); rd1(ic<ks + AH>{}, ic<2 * C.value + 1>{});
      SBAR();
    });
  });
  asm volatile("s_nop 9" : "+v"(acc[0]), "+v"(acc[1]), "+v"(acc[2]), "+v"(acc[3]));
  unsigned d[8];
#pragma unroll
  for (int i = 0; i < 4; ++i) { d[2 * i] = pk2(acc[i][0], acc[i][1]); d[2 * i + 1] = pk2(acc[i][2], acc[i][3]); }
  const rsrc_t rhf = mk_rsrc(ws + WS_HF, 128 * 18 * 1024);
  bstore16<16>((u32x4){d[0], d[1], d[2], d[3]}, rhf, l16, (T * 18 + 2 * nt) * 1024);
  bstore16<16>((u32x4){d[4], d[5], d[6], d[7]}, rhf, l16, (T * 18 + 2 * nt + 1) * 1024);
  float s1[2] = {0.f, 0.f}, s2[2] = {0.f, 0.f};
#pragma unroll
  for (int i = 0; i < 8; ++i) {
    const bf2_t v = __builtin_bit_cast(bf2_t, d[i]);
    s1[i & 1] = __builtin_amdgcn_fdot2_f32_bf16(v, __builtin_bit_cast(bf2_t, ONES2), s1[i & 1], false);
    s2[i & 1] = __builtin_amdgcn_fdot2_f32_bf16(v, v, s2[i & 1], false);
  }
  bstore8<16>((u32x2){__float_as_uint(sum_x32(sum_x16(s1[0] + s1[1]))), __float_as_uint(sum_x32(sum_x16(s2[0] + s2[1])))}, mk_rsrc(ws + WS_ST, 9 * 2048 * 8), so, (T * 9 + nt) * 128);
  STAMP(ws, 3, 5);
  wait_vm<0>();
  set_flag(c, FL3 + (T * 16 + nt) * FS3);
  STAMP(ws, 3, 6);
  STAMP_FLUSH(ws, 3);
}

constexpr int K4_LG = P3_WO;
constexpr int K4_CS = K4_LG + 36864;
constexpr int K4_STP = K4_CS + 4096;
struct K4Op { int kind, idx; };
struct K4Ops { int n; K4Op op[12]; };
constexpr K4Ops k4_ops(int S) {
  K4Ops o = {};
  auto add = [&](int k, int i) { o.op[o.n++] = {k, i}; };
  if (S == 3) add(9, 0);
  if (S == 0) for (int u = 4; u < 8; ++u) { add(1, u); add(2, u); }
  else for (int u = 2 * S + 6; u < 2 * S + 8 && u < 36; ++u) {
    if (S == 2) { add(0, 2 * u - 12); add(0, 2 * u - 11); }
    if (S > 2) add(0, u);
    if (u < 18) { add(1, u); add(2, u); }
  }
  if (S == 4) { add(5, 0); add(5, 1); add(5, 2); }
  if (S == 6) add(6, 0);
  if (S >= 10 && S < 15) add(3, S - 10);
  if (S == 12) add(7, 0);
  if (S == 14) add(8, 0);
  if (S == 10) add(4, 0);
  return o;
}
constexpr int k4_nst(int S) { return (S == 8 || S == 9) ? 2 : S == 7 ? 1 : 0; }
constexpr int k4_issued(int S) { int n = 8; for (int s = 0; s < S; ++s) n += k4_ops(s).n + k4_nst(s); return n; }
constexpr int k4_pos(int kind, int idx) {
  if (kind == 0 && idx < 8) return -1;
  if ((kind == 1 || kind == 2) && idx < 4) return 2 * idx + kind - 1;
  for (int S = 0;; ++S) for (int i = 0; i < k4_ops(S).n; ++i) if (k4_ops(S).op[i].kind == kind && k4_ops(S).op[i].idx == idx) return k4_issued(S) + k4_nst(S) + i;
}
constexpr int k4_need(int S) {
  if (S == 8) return k4_issued(7) - 1;
  if (S >= 16) return k4_issued(15) - 1;
  const int a = S < 9 ? k4_pos(2, 2 * S + 1) : -1, b = k4_pos(0, 2 * S + 2);
  return a > b ? a : b;
}
static_assert(k4_need(5) >= k4_pos(9, 0) && k4_need(7) >= k4_pos(5, 2) && k4_need(12) >= k4_issued(10) - 1, "");
constexpr int K5_SLOT = 17408, K5_BASE = 0, K5_P = 4;
constexpr unsigned k5_so(int v) { return K5_BASE + ((v + 7) & 7) * K5_SLOT; }
DI int k5_u(int v, int nt) {
  if (v < 32) { const int q = ((v >> 2) + nt) & 3; return 18 * (q >> 1) + 8 * (q & 1) + 4 * ((v >> 1) & 1) + (v & 1) + 2 * (v >> 4); }
  const int q = (v + nt) & 3;
  return 18 * (q >> 1) + 16 + (q & 1);
}
DI unsigned k5_mv(int rho, int sp, int nt, int t16) {
  const int rr = sp ? ((rho & 7) | ((rho & 8) << 1) | ((rho & 16) >> 1)) : rho;
  return rr * 1152 + 256 * nt + 32 * ((t16 >> 1) ^ (rho & 7)) + 16 * (t16 & 1);
}
DI unsigned k5_fl(int i, int sg, int mt5) { return (((((mt5 >> 1) * 4 + ((i >> 2) & 3)) * 8 + 2 * sg + ((i >> 4) & 1)) * 8) + 4 * (mt5 & 1) + (i & 3)) * 64; }

DI void p4(const Ctx& c) {
  char* ws = c.ws;
  const int w = c.w, r = c.r, xcd = c.xcd, lane = fresh_lane();
  const int mt = r >> 2, Qd = xcd, j = r & 3;
  const unsigned lds0 = c.lds0;
  const int g = lane >> 4;
  const int T0 = 16 * mt + 2 * w;
  const rsrc_t rwm = mk_rsrc(w < 4 ? c.wg : c.wu, D_ * F_ * 2), rhf = mk_rsrc(ws + WS_HF, 128 * 18 * 1024), rwd = mk_rsrc(c.wd, F_ * D_ * 2);
  const rsrc_t rhh = mk_rsrc(ws + WS_HH, 128 * 144 * 1024);
  const unsigned f4 = FL4 + ((r * 8 + xcd) * 8 + w) * 64;
  const int sg = xcd >> 1, mt5 = 8 * (xcd & 1) + (r >> 2);
  STAMP_DECL; STAMP(ws, 4, 0);
  const unsigned wv = opq(k4_wv(lane, w, Qd, j));
  const unsigned wdst = lds0 + K4_RING + w * 1024, l16 = opq(lane * 16);
  unsigned wa[4];
  {
    const int r2 = 4 * g + ((lane >> 2) & 3);
#pragma unroll
    for (int i = 0; i < 4; ++i) wa[i] = opq(lds0 + K4_RING + r2 * 128 + 32 * (i ^ ((r2 >> 1) & 3)) + 8 * (lane & 3));
  }
  const bool all = poll_first(c, (min(lane & 15, 8) + (lane & 16)) * FS3, FL3 + T0 * 16 * FS3, 0x30003ull);
  bf16x8 act[18][2];
  u32x2 pf;
  auto pf_off = [&] { const int z = opq(l16) >> 4; const unsigned a = opq(FL4 + k5_fl(z, sg, mt5)), b = opq(FLS + (40 + 18 * sg + min(z - 32, 17)) * 64); return z < 32 ? a : b; };
  auto doop = [&](auto KIND, auto IDX) {
    constexpr int kind = KIND.value, u = IDX.value;
    if constexpr (kind == 0) glds16(wdst + k4_so(u), wv, rwm, (u % 18) * 32 * 9216 + (u / 18) * 128);
    else if constexpr (kind <= 2) bload16c(act[u][kind - 1], l16, rhf, (T0 * 18 + 18 * (kind - 1) + u) * 1024);
    else if constexpr (kind == 3) {
      const int pc = min(w + 8 * u, 35);
      glds16c(lds0 + K4_LG + pc * 1024, l16, mk_rsrc(ws + WS_LG, 8 * 4 * 36864), (Qd * 4 + j) * 36864 + pc * 1024);
    }
    else if constexpr (kind == 4) warm(lds0 + LDS_SCR, l16 * 8, rwd, sg * 1327104 + min(r * 8 + w, 161) * 8192);
    else if constexpr (kind == 5) glds16c(lds0 + K4_STP + w * 3072 + u * 1024, l16, mk_rsrc(ws + WS_ST, 9 * 2048 * 8), T0 * 1152 + u * 1024);
    else if constexpr (kind == 6) {
      const unsigned z = opq(l16), f = (576 * Qd + (z < 512 ? 128 * j : 384 + 16 * j)) * 4 + z;
      glds16c(lds0 + K4_CS + (w & 1) * 1024, z < 576 ? f : OOB, mk_rsrc(ws + WS_CS, 2 * 4608 * 4), (w & 1) * 4608 * 4);
    } else if constexpr (kind == 7) set_flag(c, f4);
    else if constexpr (kind == 8) bload8c(pf, pf_off(), fl_rsrc(c), 0);
    else bload8c(pf, min(opq(l16), 13u * 16) * 32, fl_rsrc(c), FLS + xcd * 64);
  };
  sfor<4>([&](auto U) { doop(ic<1>{}, U); doop(ic<2>{}, U); });
  if (w < 4) cnt_add(c, 4);
  if (!all) poll(c, (min(fresh_lane() & 15, 8) + (fresh_lane() & 16)) * FS3, FL3 + T0 * 16 * FS3);
  cnt_wait(c, 4, 4);
  f32x4 ag[4][2], au[4][2], cg, cu;
  bf16x4 fr[4][2];
  unsigned hd[2][2], lga, csa;
  f32x2 nmu = {0.f, 0.f};
  auto rd1 = [&](auto H, auto I) {
    constexpr int n = H.value >> 1, m = H.value & 1, i = I.value;
    if constexpr (n < 144) fr[H.value & 3][i] = trh(wa[n % 4] + k4_so(n / 4) + 4096 * m + 2048 * i);
    else if constexpr (n < 162) fr[H.value & 3][i] = trh(lga + (n - 144) * 1024 + m * 18432 + i * 512);
  };
  auto rd = [&](auto H) { rd1(H, ic<0>{}); rd1(H, ic<1>{}); };
  auto mm = [&](auto N, auto KS, f32x4& g0, f32x4& g1, f32x4& u0, f32x4& u1, auto&& hook) {
    constexpr int n = N.value, ks = KS.value;
    constexpr int h0 = n == 15 ? 999 : 2 * n + 3, h1 = (n == 14 || n == 15) ? 999 : 2 * n + 4;
    const bf16x8 fg = cat(fr[(2 * n) & 3][0], fr[(2 * n) & 3][1]);
    if constexpr (ks == 0) mfma16_z(g0, fg, act[ks][0]); else mfma16_ip(g0, fg, act[ks][0]);
    SBAR(); rd1(ic<h0>{}, ic<0>{}); SBAR(); hook(ic<0>{}); SBAR();
    if constexpr (ks == 0) mfma16_z(g1, fg, act[ks][1]); else mfma16_ip(g1, fg, act[ks][1]);
    SBAR(); rd1(ic<h0>{}, ic<1>{}); SBAR(); hook(ic<1>{}); SBAR();
    const bf16x8 fu = cat(fr[(2 * n + 1) & 3][0], fr[(2 * n + 1) & 3][1]);
    if constexpr (ks == 0) mfma16_z(u1, fu, act[ks][1]); else mfma16_ip(u1, fu, act[ks][1]);
    SBAR(); rd1(ic<h1>{}, ic<0>{}); SBAR(); hook(ic<2>{}); SBAR();
    if constexpr (ks == 0) mfma16_z(u0, fu, act[ks][0]); else mfma16_ip(u0, fu, act[ks][0]);
    SBAR(); rd1(ic<h1>{}, ic<1>{}); SBAR(); hook(ic<3>{}); SBAR();
  };
  auto ldcs = [&](int e) { cg = ldsr<f32x4>(csa + 64 * e); cu = ldsr<f32x4>(csa + 1024 + 64 * e); };
  auto epi = [&](auto E, auto I) {
    constexpr int grp = E.value >> 2, cc = E.value & 3, i = I.value, T = 1 - (i >> 1);
    if constexpr ((i & 1) == 0) {
      ag[cc][T] += cg * nmu[T]; au[cc][T] += cu * nmu[T]; PIN(ag[cc][T]); PIN(au[cc][T]);
      if constexpr (i == 2 && E.value != 3) { SBAR(); ldcs(E.value + 1); }
    }
    else {
      const f32x4 v = ag[cc][T] * au[cc][T];
      const unsigned d0 = pk2(v[0], v[1]), d1 = pk2(v[2], v[3]);
      if constexpr ((cc & 1) == 0) { hd[T][0] = d0; hd[T][1] = d1; PIN(hd[T][0]); PIN(hd[T][1]); }
      else bstore16<16>((u32x4){hd[T][0], hd[T][1], d0, d1}, rhh, l16, ((T0 + T) * 144 + 18 * Qd + 4 * j + 2 * grp + (cc >> 1)) * 1024);
    }
  };
  f32x2 sp[3];
  float rs0;
  auto stats = [&](auto TT, auto P) {
    constexpr int T = TT.value, p = P.value;
    if constexpr (p == 0) {
      const unsigned sa = lds0 + K4_STP + w * 3072 + T * 1152 + (opq(l16) >> 1);
      sp[0] = ldsr<f32x2>(sa); sp[1] = ldsr<f32x2>(sa + 512); sp[2] = ldsr<f32x2>(sa + 1024);
    } else if constexpr (p == 4) { sp[0] = sp[0] + sp[1] + (opq(l16) < 256 ? sp[2] : (f32x2){0.f, 0.f}); PIN(sp[0]); }
    else if constexpr (p == 5) { sp[0][0] = sum_x16(sp[0][0]); PIN(sp[0]); }
    else if constexpr (p == 6) { sp[0][1] = sum_x16(sp[0][1]); PIN(sp[0]); }
    else if constexpr (p == 7) { sp[0][0] = sum_x32(sp[0][0]); PIN(sp[0]); }
    else if constexpr (p == 8) { sp[0][1] = sum_x32(sp[0][1]); PIN(sp[0]); }
    else if constexpr (p == 9) {
      nmu[T] = sp[0][0] * (-1.f / D_);
      const float rs = __builtin_amdgcn_rcpf(fmaxf(sp[0][1] * (1.f / D_) - nmu[T] * nmu[T], 0.f) + 1e-5f);
      PIN(nmu);
      if constexpr (T == 0) { rs0 = rs; PIN(rs0); }
      else {
        const unsigned z = opq(l16);
        bstore4<16>(__float_as_uint((z & 256) ? rs : rs0), mk_rsrc(ws + WS_RS + (sg * 2048 + 16 * T0) * 4, (Qd & 1) == 0 && j == 0 ? 128 : 0), z >> 2, 0);
      }
    }
  };
  unsigned mv5;
  auto pf5 = [&](auto N) {
    constexpr int n = N.value, v = (n - 1) >> 1;
    if constexpr (n == 0) {
      const int vv = min(w, K5_P - 1), uu = k5_u(vv, j);
      glds16c(lds0 + K5_BASE + ((vv + 7) & 7) * K5_SLOT + 16384, l16, mk_rsrc(ws + WS_WT, 4 * 4608 * 32), (j * 144 + 36 * sg + uu) * 1024);
      const unsigned z = opq(l16);
      mv5 = k5_mv(4 * w + (z >> 8), 0, j, (z >> 4) & 15);
    } else if constexpr (n & 1) glds16(lds0 + k5_so(v) + 8192 + w * 1024, mv5, rwd, sg * 1152 * 1152 + k5_u(v, j) * 32 * 1152);
    else glds16c(lds0 + k5_so(v) + w * 1024, l16, rhh, ((8 * mt5 + w) * 144 + 36 * sg + k5_u(v, j)) * 1024);
  };
  sfor<18>([&](auto SS) {
    constexpr int S = SS.value;
    constexpr int nw = k4_issued(S) - k4_need(S) - 1;
    static_assert(nw >= 0 && nw < 64, "");
    if constexpr (S == 5) asm volatile("s_waitcnt vmcnt(%5)" : "+v"(act[2 * S][0]), "+v"(act[2 * S][1]), "+v"(act[2 * S + 1][0]), "+v"(act[2 * S + 1][1]), "+v"(pf) : "i"(nw) : "memory");
    else if constexpr (S < 9) asm volatile("s_waitcnt vmcnt(%4)" : "+v"(act[2 * S][0]), "+v"(act[2 * S][1]), "+v"(act[2 * S + 1][0]), "+v"(act[2 * S + 1][1]) : "i"(nw) : "memory");
    else if constexpr (S == 16) asm volatile("s_waitcnt vmcnt(%1)" : "+v"(pf) : "i"(nw) : "memory");
    else wait_vm<nw>();
    if constexpr (S == 16) { if (__builtin_expect(!tag_ok(c, pf), 0)) poll(c, pf_off(), 0); }
    if constexpr (S == 5) { if (__builtin_expect(!tag_ok(c, pf), 0)) poll(c, min(opq(l16), 13u * 16) * 32, FLS + xcd * 64); }
    if constexpr (S >= 2) bar();
    if constexpr (S == 0) STAMP(ws, 4, 1);
    if constexpr (S == 2) STAMP(ws, 4, 7);
    if constexpr (S == 4) STAMP(ws, 4, 2);
    if constexpr (S == 9) STAMP(ws, 4, 3);
    if constexpr (S == 17) STAMP(ws, 4, 4);
    if constexpr (S == 0 || S == 2) { rd(ic<16 * S>{}); rd(ic<16 * S + 1>{}); rd(ic<16 * S + 2>{}); }
    sfor<8>([&](auto L) {
      constexpr int l = L.value, n = 8 * S + l, t = n / 4, cc = n % 4, ks = t % 18, no = k4_ops(S).n;
      mm(ic<n>{}, ic<ks>{}, ag[cc][0], ag[cc][1], au[cc][0], au[cc][1], [&](auto I) {
        constexpr int i = I.value;
        sfor<no>([&](auto O) {
          constexpr int nd = no ? no : 1, at = O.value * 32 / nd + 16 / nd;
          if constexpr (at == 4 * l + i) doop(ic<k4_ops(S).op[O.value].kind>{}, ic<k4_ops(S).op[O.value].idx>{});
        });
        if constexpr (S == 7) stats(ic<(l >> 2)>{}, ic<4 * (l & 3) + i>{});
        if constexpr ((S == 8 || S == 17) && l == 3 && i == 0) { csa = lds0 + K4_CS + ((opq(l16) >> 4) & 48); PIN(csa); }
        if constexpr ((S == 8 || S == 17) && l == 4 && i == 0) ldcs(S == 8 ? 0 : 4);
        if constexpr (S == 17 && l == 0 && i == 0) { const unsigned z = opq(l16); lga = lds0 + K4_LG + (z >> 6) * 32 + ((z >> 1) & 24); PIN(lga); }
        if constexpr (S == 17 && l < 5 && i == 1) pf5(L);
        if constexpr (ks == 17 && cc >= 1) epi(ic<4 * (t / 18) + cc - 1>{}, I);
        if constexpr (t == 18 && cc == 0) epi(ic<3>{}, I);
      });
    });
  });
  STAMP(ws, 4, 5);
  {
    f32x4 g0, g1, u0, u1;
    sfor<18>([&](auto K) {
      constexpr int ks = K.value, n = 144 + ks;
      mm(ic<n>{}, ic<ks>{}, g0, g1, u0, u1, [&](auto I) {
        if constexpr (ks == 0) epi(ic<7>{}, I);
        if constexpr (ks >= 1 && ks <= 4 && I.value == 1) pf5(ic<4 + ks>{});
        if constexpr (ks == 10 && I.value == 1) { wait_vm<4>(); set_flag(c, f4 + 2048 * 64); }
      });
    });
    asm volatile("s_nop 9" : "+v"(g0), "+v"(g1), "+v"(u0), "+v"(u1));
    const f32x4 v0 = (g0 + cg * nmu[0]) * (u0 + cu * nmu[0]), v1 = (g1 + cg * nmu[1]) * (u1 + cu * nmu[1]);
    const unsigned a0 = pk2(v0[0], v0[1]), a1 = pk2(v0[2], v0[3]), b0 = pk2(v1[0], v1[1]), b1 = pk2(v1[2], v1[3]);
    auto s0 = __builtin_amdgcn_permlane32_swap(a0, b0, false, false);
    auto s1 = __builtin_amdgcn_permlane32_swap(a1, b1, false, false);
    const unsigned z = opq(l16);
    bstore16<16>((u32x4){s0[0], s1[0], s0[1], s1[1]}, rhh, (z >> 9) * 144 * 1024 + (z & 511) + 512 * (j & 1), (T0 * 144 + 18 * Qd + 16 + (j >> 1)) * 1024);
    STAMP(ws, 4, 6);
    STAMP_FLUSH(ws, 4);
  }
}

constexpr int k5_E(int i) { int e = 36; if (K5_P + 2 * (i + 1) < e) e = K5_P + 2 * (i + 1); if (i >= 0 && i + 8 < e) e = i + 8; return e; }
static_assert(k5_E(6) <= 16 && k5_E(20) <= 32, "");

DI void p5(const Ctx& c) {
  char* ws = c.ws;
  const int w = c.w, r = c.r, xcd = c.xcd, lane = fresh_lane();
  const int sg = xcd >> 1, mt = 8 * (xcd & 1) + (r >> 2), nt = r & 3;
  const unsigned lds0 = c.lds0;
  const int t16 = lane & 15, g = lane >> 4;
  const int tile = 4 * mt + nt;
  const rsrc_t rpt = mk_rsrc(ws + WS_PT, 64 * 8 * 4 * PT_REC);
  const unsigned f4 = FL4 + (4096 + (r * 8 + xcd) * 8 + w) * 64;
  STAMP_DECL; STAMP(ws, 5, 0);
  if (w >= 4) {
    const int l = w - 4;
    const rsrc_t rwd = mk_rsrc(c.wd, F_ * D_ * 2), rhh = mk_rsrc(ws + WS_HH, 128 * 144 * 1024);
    const rsrc_t rwt = mk_rsrc(ws + WS_WT, 4 * 4608 * 32);
    unsigned mv[2][2];
#pragma unroll
    for (int sp = 0; sp < 2; ++sp)
#pragma unroll
      for (int i = 0; i < 2; ++i) mv[sp][i] = k5_mv(4 * (2 * l + i) + g, sp, nt, t16);
    const unsigned hs = ((8 * mt + 2 * l) * 144 + 36 * sg) * 1024, wsb = sg * 1152 * 1152, dl = lds0 + l * 2048;
    auto issue = [&](auto V) {
      constexpr int v = V.value, sp = v >= 32;
      const int u = k5_u(v, nt);
      constexpr unsigned so = k5_so(v);
      glds16(dl + so + 8192, mv[sp][0], rwd, wsb + u * 32 * 1152);
      glds16c(dl + so, lane * 16, rhh, hs + u * 1024);
      glds16(dl + so + 8192 + 1024, mv[sp][1], rwd, wsb + u * 32 * 1152);
      glds16c(dl + so + 1024, lane * 16, rhh, hs + (144 + u) * 1024);
      if ((v & 3) == l) glds16c(lds0 + so + 16384, lane * 16, rwt, (nt * 144 + 36 * sg + u) * 1024);
    };
    sfor<36>([&](auto I) {
      constexpr int i = I.value, y = k5_E(i - 1) - (i + 2 < 36 ? i + 2 : 36), yy = k5_E(i - 1) - K5_P;
      if constexpr (i == 0) wait_vm<2>();
      else if constexpr (i == 2) { wait_vm<4 * yy + yy / 4>(); set_flag(c, f4); }
      else if constexpr (i + 2 > K5_P) wait_vm<4 * y + y / 4>();
      bar();
      if constexpr (i == 0) STAMP(ws, 5, 1);
      if constexpr (i == 18) STAMP(ws, 5, 2);
      sfor<k5_E(i) - k5_E(i - 1)>([&](auto U) { issue(ic<k5_E(i - 1) + U.value>{}); });
    });
    bar();
    STAMP(ws, 5, 3);
    bar();
  } else {
    const int r2 = 4 * g + ((lane >> 2) & 3);
    unsigned wa[2][10];
#pragma unroll
    for (int i = 0; i < 8; ++i) wa[0][i] = lds0 + K5_BASE + 8192 + r2 * 256 + 32 * (i ^ (r2 & 7)) + 8 * (lane & 3);
    wa[0][8] = lds0 + K5_BASE + 16384 + r2 * 32 + 8 * (lane & 3);
    wa[0][9] = lds0 + K5_BASE + w * 2048 + lane * 16;
#pragma unroll
    for (int i = 0; i < 10; ++i) { wa[1][i] = wa[0][i] + 4 * K5_SLOT; PIN(wa[0][i]); PIN(wa[1][i]); }
    const unsigned fo = k5_fl(lane & 31, sg, mt);
    f32x4 acc[9][2];
    bf16x4 fr[3][3][2];
    bf16x8 hb[2][2];
    u32x2 pf;
    auto rd1 = [&](auto N, auto I) {
      constexpr int n = N.value, u = n / 3, i = I.value;
      if constexpr (u < 36) {
        constexpr int pos = (u + 7) & 7, hi = pos >> 2, cc = 3 * (n % 3) + i / 2;
        constexpr unsigned so = (pos & 3) * K5_SLOT;
        if constexpr (i >= 6) hb[u & 1][i - 6] = ldsr<bf16x8>(wa[hi][9] + so + (i - 6) * 1024);
        else fr[n % 3][i / 2][i & 1] = trh(wa[hi][cc] + so + (i & 1) * (cc < 8 ? 4096 : 512));
      }
    };
    sfor<36>([&](auto U) {
      constexpr int u = U.value;
      if constexpr (u == 0) wait_vm<2>();
      if constexpr (u == 4) { wait_vm<0>(); set_flag(c, f4); }
      if constexpr (u == 4 || u == 18) bload8c(pf, fo, fl_rsrc(c), FL4 + (u == 4 ? 1 : 2) * 2048 * 64);
      if constexpr (u == 7 || u == 21) {
        asm volatile("s_waitcnt vmcnt(0)" : "+v"(pf)::"memory");
        if (__builtin_expect(!tag_ok(c, pf), 0)) poll(c, fo, FL4 + (u == 7 ? 1 : 2) * 2048 * 64);
      }
      bar();
      if constexpr (u == 0) STAMP(ws, 5, 1);
      if constexpr (u == 18) STAMP(ws, 5, 2);
      if constexpr (u == 0) { rd1(ic<0>{}, ic<6>{}); rd1(ic<0>{}, ic<7>{}); sfor<6>([&](auto I) { rd1(ic<0>{}, I); }); sfor<6>([&](auto I) { rd1(ic<1>{}, I); }); }
      sfor<18>([&](auto M) {
        constexpr int mi = M.value, v = mi / 6, n = 3 * u + v, cc = mi / 2;
        if constexpr (u == 0) mfma16_z(acc[cc][mi & 1], cat(fr[n % 3][cc % 3][0], fr[n % 3][cc % 3][1]), hb[u & 1][mi & 1]);
        else mfma16_ip(acc[cc][mi & 1], cat(fr[n % 3][cc % 3][0], fr[n % 3][cc % 3][1]), hb[u & 1][mi & 1]);
        SBAR();
        if constexpr (mi == 6 || mi == 8) rd1(ic<n + 2>{}, ic<6 + (mi - 6) / 2>{});
        rd1(ic<n + 2>{}, ic<mi % 6>{});
        SBAR();
      });
    });
    asm volatile("s_nop 9" : "+v"(acc[7][0]), "+v"(acc[7][1]), "+v"(acc[8][0]), "+v"(acc[8][1]));
#pragma unroll
    for (int i = 0; i < 7; ++i) asm volatile("" : "+v"(acc[i][0]), "+v"(acc[i][1]));
    bar();
    STAMP(ws, 5, 3);
    if (w == sg) {
      sfor<2>([&](auto T) { sfor<9>([&](auto C) { ldsw<f32x4>(lds0 + (T.value * 9 + C.value) * 1024 + lane * 16, acc[C.value][T.value]); }); });
      lds_bar();
    } else {
      sfor<2>([&](auto T) {
        const unsigned pb = (((tile * 4 + w) * 4 + sg) * 2 + T.value) * PT_REC;
        sfor<4>([&](auto I) {
          constexpr int i = I.value;
          const f32x4 a = acc[2 * i][T.value], b = acc[2 * i + 1][T.value];
          bstore16<16>((u32x4){pk2(a[0], a[1]), pk2(a[2], a[3]), pk2(b[0], b[1]), pk2(b[2], b[3])}, rpt, lane * 16 + i * 1024, pb);
        });
        const f32x4 a = acc[8][T.value];
        bstore8<16>((u32x2){pk2(a[0], a[1]), pk2(a[2], a[3])}, rpt, lane * 8 + 4096, pb);
      });
      bar();
      wait_vm<0>();
      set_flag(c, FL5 + (tile * 4 + w) * 256 + sg * FS5);
    }
  }
  const int T = w >> 2, pi = w & 3, tt = 8 * mt + 2 * sg + T;
  const rsrc_t rhf = mk_rsrc(ws + WS_HF, 128 * 18 * 1024);
  u32x4 hv, pv[3];
  u32x2 hl, pl[3];
  unsigned rs2u;
  STAMP(ws, 5, 4);
  bload16c(hv, lane * 16, rhf, (tt * 18 + 4 * nt + pi) * 1024);
  bload8c(hl, lane * 16 + 8 * (nt & 1), rhf, (tt * 18 + 16 + (nt >> 1)) * 1024);
  bload4c(rs2u, t16 * 4, mk_rsrc(ws + WS_RS, 4 * 2048 * 4), (sg * 2048 + 16 * tt) * 4);
  poll(c, ((sg + 1 + min(lane, 2)) & 3) * FS5, FL5 + (tile * 4 + sg) * 256);
  STAMP(ws, 5, 5);
#pragma unroll
  for (int o = 0; o < 3; ++o) {
    const unsigned so = (((tile * 4 + sg) * 4 + ((sg + 1 + o) & 3)) * 2 + T) * PT_REC;
    bload16c(pv[o], lane * 16 + pi * 1024, rpt, so);
    bload8c(pl[o], pi == 0 ? lane * 8 + 4096 : OOB, rpt, so);
  }
  const unsigned orow = (16 * tt + t16) * 1152, own = lds0 + T * 9216 + lane * 16;
  const f32x4 o0 = ldsr<f32x4>(own + pi * 2048), o1 = ldsr<f32x4>(own + pi * 2048 + 1024), o2 = ldsr<f32x4>(own + 8192);
  asm volatile("s_waitcnt vmcnt(0)" : "+v"(hv), "+v"(hl), "+v"(rs2u), "+v"(pv[0]), "+v"(pv[1]), "+v"(pv[2]), "+v"(pl[0]), "+v"(pl[1]), "+v"(pl[2])::"memory");
  STAMP(ws, 5, 6);
  const float rs2 = __uint_as_float(rs2u);
  const rsrc_t rout = mk_rsrc(c.out, S_ * D_ * 2);
  auto fin = [&](const f32x4& ow, unsigned a0, unsigned a1, unsigned b0, unsigned b1, unsigned c0, unsigned c1, unsigned h0, unsigned h1, unsigned& d0, unsigned& d1) {
    const float v0 = ((ow[0] + bflo(b0)) + (bflo(a0) + bflo(c0))) * rs2 + bflo(h0);
    const float v1 = ((ow[1] + bfhi(b0)) + (bfhi(a0) + bfhi(c0))) * rs2 + bfhi(h0);
    const float v2 = ((ow[2] + bflo(b1)) + (bflo(a1) + bflo(c1))) * rs2 + bflo(h1);
    const float v3 = ((ow[3] + bfhi(b1)) + (bfhi(a1) + bfhi(c1))) * rs2 + bfhi(h1);
    d0 = pk2(v0, v1); d1 = pk2(v2, v3);
  };
  {
    unsigned a0, a1, b0, b1;
    fin(o0, pv[0][0], pv[0][1], pv[1][0], pv[1][1], pv[2][0], pv[2][1], hv[0], hv[1], a0, a1);
    fin(o1, pv[0][2], pv[0][3], pv[1][2], pv[1][3], pv[2][2], pv[2][3], hv[2], hv[3], b0, b1);
    auto s0 = __builtin_amdgcn_permlane16_swap(a0, b0, false, false);
    auto s1 = __builtin_amdgcn_permlane16_swap(a1, b1, false, false);
    bstore16<0>((u32x4){s0[0], s1[0], s0[1], s1[1]}, rout, orow + 256 * nt + 64 * pi + 32 * (g & 1) + 16 * (g >> 1), 0);
  }
  {
    unsigned e0, e1;
    fin(o2, pl[0][0], pl[0][1], pl[1][0], pl[1][1], pl[2][0], pl[2][1], hl[0], hl[1], e0, e1);
    bstore8<0>((u32x2){e0, e1}, rout, pi == 0 ? orow + 1024 + 32 * nt + 8 * g : OOB, 0);
  }
  wait_vm<0>();
  STAMP(ws, 5, 7);
  STAMP_FLUSH(ws, 5);
}

extern "C" __global__ __launch_bounds__(512, 2) void k1_all(const void* x, const void* wqkv, const void* wo, const void* wg, const void* wu, const void* wd, char* ws, void* out) {
  unsigned long long te_; asm volatile("s_memrealtime %0" : "=s"(te_)); SBAR();
  Ctx c = {x, wqkv, wo, wg, wu, wd, ws, out, (int)rfl(threadIdx.x >> 6), (int)(blockIdx.x & 7), (int)(blockIdx.x >> 3), (unsigned)(size_t)smem, 0u, 0u, te_};
  if (c.w == 7) {
    unsigned long long pc; unsigned back, size;
    asm volatile("s_getpc_b64 %0\n.Lk1pc%=:\n\ts_mov_b32 %1, .Lk1pc%=-k1_all\n\ts_mov_b32 %2, k9_end-k1_all" : "=s"(pc), "=s"(back), "=s"(size));
    const int lane = fresh_lane();
    warm(c.lds0 + LDS_SCR, lane < 24 ? (c.r * 24 + lane) * 128 : OOB, mk_rsrc((const void*)(pc - back), size), 0);
  }
  {
    const int mt = 4 * (c.xcd & 3) + (c.r & 3), nt = 8 * (c.xcd >> 2) + (c.r >> 2);
    if (c.w < 4) p1_body<0>(c, mt, nt); else p1_body<1>(c, mt, nt);
  }
  p2(c);
  if (c.r < 18) p3_gemm(c); else { cnt_wait(c, 1, 8); p3_side(c); }
  if (c.w < 4) k4_first<0>(c, fresh_lane());
  else { cnt_wait(c, 1, 8); k4_first<4>(c, fresh_lane()); }
  p4(c);
  p5(c);
  STAMP_OUT();
}

extern "C" __global__ void k9_end() {}

extern "C" __global__ __launch_bounds__(256) void k6_diag(char* ws) {
  __shared__ unsigned ra[256], rb[256];
  const int b = threadIdx.x;
  unsigned long long t0 = __builtin_amdgcn_s_memrealtime();
  const unsigned ref = *(GAS unsigned*)(ws + WS_DBG + ((size_t)1 * 256 * 8 * 16) * 4) - 100000u;
  unsigned a = ~0u, c = MB ? 0u : ~0u;
  const int nbb = KB == 3 ? 144 : 256;
  for (int w = 0; w < 8; ++w) {
    { unsigned v = *(GAS unsigned*)(ws + WS_DBG + ((((size_t)1 * 256 + b) * 8 + w) * 16) * 4) - ref; a = v < a ? v : a; }
    if (b < nbb) { unsigned v = *(GAS unsigned*)(ws + WS_DBG + ((((size_t)KB * 256 + b) * 8 + w) * 16 + IB) * 4) - ref; c = MB ? (v > c ? v : c) : (v < c ? v : c); }
  }
  ra[b] = a; rb[b] = c;
  __syncthreads();
  if (b == 0) {
    for (int i = 1; i < 256; ++i) { a = ra[i] < a ? ra[i] : a; c = MB ? (rb[i] > c ? rb[i] : c) : (rb[i] < c ? rb[i] : c); }
    unsigned long long d = c - a;
    if (d > 10000) d = 10000;
    if (CONSTD) d = 200;
    d *= 10;
    while (__builtin_amdgcn_s_memrealtime() - t0 < d + 300) __builtin_amdgcn_s_sleep(2);
  }
}

extern "C" void kernel_launch(void* const* d_in, const int* in_sizes, int n_in, void* d_out, int out_size, void* d_ws, size_t ws_size,
                              hipStream_t stream) {
  (void)hipFuncSetAttribute((const void*)k1_all, hipFuncAttributeMaxDynamicSharedMemorySize, LDS_ALL);
  k1_all<<<256, 512, LDS_ALL, stream>>>(d_in[0], d_in[1], d_in[2], d_in[4], d_in[5], d_in[6], (char*)d_ws, d_out);
  k6_diag<<<1, 256, 0, stream>>>((char*)d_ws);
}
```

```cpp
#define STAMPS
#define KB 4
#define IB 7
#define JB 3
#define CYC 0
#define W0 0
#define W1 8
#include <hip/hip_runtime.h>
#include <stdint.h>

typedef short bf16x8 __attribute__((ext_vector_type(8)));
typedef short bf16x4 __attribute__((ext_vector_type(4)));
typedef float f32x2 __attribute__((ext_vector_type(2)));
typedef float f32x4 __attribute__((ext_vector_type(4)));
typedef float f32x16 __attribute__((ext_vector_type(16)));
typedef unsigned u32x2 __attribute__((ext_vector_type(2)));
typedef unsigned u32x4 __attribute__((ext_vector_type(4)));
typedef __bf16 bf2_t __attribute__((ext_vector_type(2)));
typedef __amdgpu_buffer_rsrc_t rsrc_t;
#define LAS __attribute__((address_space(3)))
#define GAS __attribute__((address_space(1)))
#define DI __device__ __forceinline__

constexpr int S_ = 2048, D_ = 576, F_ = 4608;
constexpr float QSCALE = 0.23570226039551584f * 1.4426950408889634f;
constexpr unsigned ONES2 = 0x3f803f80u;

constexpr size_t QREC = 1280, KVREC = 2560;
constexpr unsigned OOB = 0x7fffff00u;
#define FS1 64
#define FS2 64
#define FS3 64
#define FS5 64
constexpr unsigned FL1 = 128;
constexpr unsigned FL2 = FL1 + 256 * 64;
constexpr unsigned FL3 = FL2 + 2048 * 64;
constexpr unsigned FLS = FL3 + 2048 * 64;
constexpr unsigned FL4 = FLS + 128 * 64;
constexpr unsigned FL5 = FL4 + 3 * 2048 * 64;
constexpr unsigned FL_END = FL5 + 1024 * 64;
constexpr size_t WS_QF = 1u << 20;
constexpr size_t WS_KV = WS_QF + 32 * 64 * QREC;
constexpr size_t WS_OF = WS_KV + 32 * 64 * KVREC;
constexpr size_t WS_HF = WS_OF + 128 * 18 * 1024;
constexpr size_t WS_ST = WS_HF + 128 * 18 * 1024;
constexpr size_t WS_CS = WS_ST + 9 * 2048 * 8;
constexpr size_t WS_RS = WS_CS + 2 * 4608 * 4;
constexpr size_t WS_LG = WS_RS + 4 * 2048 * 4;
constexpr size_t WS_WT = WS_LG + 8 * 4 * 36864;
constexpr size_t WS_HH = WS_WT + 4 * 4608 * 32;
constexpr size_t PT_REC = 4608;
constexpr size_t WS_PT = WS_HH + (size_t)128 * 144 * 1024;
constexpr size_t WS_END = WS_PT + (size_t)64 * 8 * 4 * PT_REC;

DI unsigned rfl(unsigned v) { return __builtin_amdgcn_readfirstlane(v); }
DI rsrc_t mk_rsrc(const void* p, unsigned bytes) { return __builtin_amdgcn_make_buffer_rsrc((void*)p, (short)0, (int)bytes, 0x00020000); }
DI void glds16(unsigned lds, unsigned voff, rsrc_t r, unsigned soff) {
  asm volatile("s_mov_b32 m0, %0\n\ts_nop 0\n\tbuffer_load_dwordx4 %1, %2, %3 offen lds" ::"s"(lds), "v"(voff), "s"(r), "s"(soff) : "memory", "m0");
}
DI void glds16c(unsigned lds, unsigned voff, rsrc_t r, unsigned soff) {
  asm volatile("s_mov_b32 m0, %0\n\ts_nop 0\n\tbuffer_load_dwordx4 %1, %2, %3 offen sc1 lds" ::"s"(lds), "v"(voff), "s"(r), "s"(soff) : "memory", "m0");
}
template <class T> DI void bload16c(T& d, unsigned voff, rsrc_t r, unsigned soff) {
  asm volatile("buffer_load_dwordx4 %0, %1, %2, %3 offen sc1" : "=v"(d) : "v"(voff), "s"(r), "s"(soff) : "memory");
}
DI void bload8c(u32x2& d, unsigned voff, rsrc_t r, unsigned soff) {
  asm volatile("buffer_load_dwordx2 %0, %1, %2, %3 offen sc1" : "=v"(d) : "v"(voff), "s"(r), "s"(soff) : "memory");
}
DI void bload4c(unsigned& d, unsigned voff, rsrc_t r, unsigned soff) {
  asm volatile("buffer_load_dword %0, %1, %2, %3 offen sc1" : "=v"(d) : "v"(voff), "s"(r), "s"(soff) : "memory");
}
DI void bload8(u32x2& d, unsigned voff, rsrc_t r, unsigned soff) {
  asm volatile("buffer_load_dwordx2 %0, %1, %2, %3 offen" : "=v"(d) : "v"(voff), "s"(r), "s"(soff) : "memory");
}
DI void warm(unsigned lds, unsigned voff, rsrc_t r, unsigned soff) {
  asm volatile("s_mov_b32 m0, %0\n\ts_nop 0\n\tbuffer_load_dword %1, %2, %3 offen lds" ::"s"(lds), "v"(voff), "s"(r), "s"(soff) : "memory", "m0");
}
template <int N> DI void wait_vm() { asm volatile("s_waitcnt vmcnt(%0)" ::"i"(N) : "memory"); }
#define PIN(x) asm volatile("" : "+v"(x))
#define SBAR() __builtin_amdgcn_sched_barrier(0)
DI void bar() { SBAR(); asm volatile("s_barrier" ::: "memory"); SBAR(); }
DI void lds_bar() { SBAR(); asm volatile("s_waitcnt lgkmcnt(0)\n\ts_barrier" ::: "memory"); SBAR(); }
DI bf16x8 trfrag(unsigned a0, unsigned a1) {
  bf16x4 lo = __builtin_amdgcn_ds_read_tr16_b64_v4i16((LAS bf16x4*)(size_t)a0);
  bf16x4 hi = __builtin_amdgcn_ds_read_tr16_b64_v4i16((LAS bf16x4*)(size_t)a1);
  return __builtin_shufflevector(lo, hi, 0, 1, 2, 3, 4, 5, 6, 7);
}
DI bf16x4 trh(unsigned a) { return __builtin_amdgcn_ds_read_tr16_b64_v4i16((LAS bf16x4*)(size_t)a); }
DI bf16x8 cat(bf16x4 lo, bf16x4 hi) { return __builtin_shufflevector(lo, hi, 0, 1, 2, 3, 4, 5, 6, 7); }
template <class T> DI T ldsr(unsigned a) { return *(LAS T*)(size_t)a; }
template <class T> DI void ldsw(unsigned a, T v) { *(LAS T*)(size_t)a = v; }
DI unsigned pk2(float a, float b) { f32x2 v = {a, b}; return __builtin_bit_cast(unsigned, __builtin_convertvector(v, bf2_t)); }
DI float bflo(unsigned u) { return __uint_as_float(u << 16); }
DI float bfhi(unsigned u) { return __uint_as_float(u & 0xffff0000u); }
DI f32x4 mfma16(bf16x8 a, bf16x8 b, f32x4 c) { return __builtin_amdgcn_mfma_f32_16x16x32_bf16(a, b, c, 0, 0, 0); }
DI void mfma16_ip(f32x4& c, bf16x8 a, bf16x8 b) { asm volatile("v_mfma_f32_16x16x32_bf16 %0, %1, %2, %0" : "+v"(c) : "v"(a), "v"(b)); }
DI void mfma16_z(f32x4& c, bf16x8 a, bf16x8 b) { asm volatile("v_mfma_f32_16x16x32_bf16 %0, %1, %2, 0" : "=&v"(c) : "v"(a), "v"(b)); }
DI f32x16 mfma32(bf16x8 a, bf16x8 b, f32x16 c) { return __builtin_amdgcn_mfma_f32_32x32x16_bf16(a, b, c, 0, 0, 0); }
DI float xor32(float v) { auto r = __builtin_amdgcn_permlane32_swap(__float_as_uint(v), __float_as_uint(v), false, false); return __uint_as_float((threadIdx.x & 32) ? r[0] : r[1]); }
DI float sum_x32(float v) { auto r = __builtin_amdgcn_permlane32_swap(__float_as_uint(v), __float_as_uint(v), false, false); return __uint_as_float(r[0]) + __uint_as_float(r[1]); }
DI float max_x32(float v) { auto r = __builtin_amdgcn_permlane32_swap(__float_as_uint(v), __float_as_uint(v), false, false); return fmaxf(__uint_as_float(r[0]), __uint_as_float(r[1])); }
DI float sum_x16(float v) { auto r = __builtin_amdgcn_permlane16_swap(__float_as_uint(v), __float_as_uint(v), false, false); return __uint_as_float(r[0]) + __uint_as_float(r[1]); }
template <int AUX> DI void bstore16(u32x4 v, rsrc_t r, unsigned voff, unsigned soff) {
  if constexpr (AUX == 16) asm volatile("buffer_store_dwordx4 %0, %1, %2, %3 offen sc1\n\ts_nop 1" ::"v"(v), "v"(voff), "s"(r), "s"(soff) : "memory");
  else asm volatile("buffer_store_dwordx4 %0, %1, %2, %3 offen\n\ts_nop 1" ::"v"(v), "v"(voff), "s"(r), "s"(soff) : "memory");
}
template <int AUX> DI void bstore8(u32x2 v, rsrc_t r, unsigned voff, unsigned soff) { __builtin_amdgcn_raw_buffer_store_b64(v, r, voff, soff, AUX); }
template <int AUX> DI void bstore4(unsigned v, rsrc_t r, unsigned voff, unsigned soff) { __builtin_amdgcn_raw_buffer_store_b32(v, r, voff, soff, AUX); }

template <int... I> struct iseq {};
template <int N, int... I> struct mkseq : mkseq<N - 1, N - 1, I...> {};
template <int... I> struct mkseq<0, I...> { typedef iseq<I...> type; };
template <int V> struct ic { static constexpr int value = V; };
template <class Fn, int... I> DI void sfor_(Fn&& f, iseq<I...>) { (f(ic<I>{}), ...); }
template <int N, class Fn> DI void sfor(Fn&& f) { sfor_(f, typename mkseq<N>::type{}); }

extern __shared__ __attribute__((aligned(16))) char smem[];
constexpr size_t WS_DBG = 64u << 20;
constexpr int LDS_DBG = 156 * 1024;
#define STAMP(ws, kid, i) do { SBAR(); unsigned long long t_, c_; asm volatile("s_memrealtime %0\n\ts_memtime %1\n\ts_waitcnt lgkmcnt(0)" : "=s"(t_), "=s"(c_) :: "memory"); \
  asm volatile("v_writelane_b32 %0, %1, %3\n\tv_writelane_b32 %0, %2, %4" : "+v"(stamp_v) : "s"((unsigned)t_), "s"((unsigned)c_), "i"(i), "i"(8 + i)); SBAR(); } while (0)
#define STAMP_DECL unsigned stamp_v = 0
#define STAMP_FLUSH(ws, kid) do { const int l_ = fresh_lane(); if (l_ < 16) ldsw<unsigned>(c.lds0 + LDS_DBG + ((c.w * 7 + kid) * 16 + l_) * 4, stamp_v); } while (0)
#define STAMP_OUT() do { const int l_ = fresh_lane(); if (l_ < 16) for (int k_ = 1; k_ < 7; ++k_) if ((k_ == 3) == (c.r < 18) || (k_ != 3 && k_ != 6)) \
  *(GAS unsigned*)(c.ws + WS_DBG + ((((size_t)k_ * 256 + blockIdx.x) * 8 + c.w) * 16 + l_) * 4) = ldsr<unsigned>(c.lds0 + LDS_DBG + ((c.w * 7 + k_) * 16 + l_) * 4); } while (0)

struct Ctx {
  const void *x, *wqkv, *wo, *wg, *wu, *wd;
  char* ws; void* out;
  int w, xcd, r;
  unsigned lds0, t0, t1;
  unsigned long long te;
};
constexpr int LDS_ALL = 160 * 1024, LDS_SCR = LDS_ALL - 256;
DI int fresh_lane() { int l; asm volatile("v_mbcnt_lo_u32_b32 %0, -1, 0\n\tv_mbcnt_hi_u32_b32 %0, -1, %0" : "=v"(l)); __builtin_assume(l >= 0 && l < 64); return l; }
DI unsigned opq(unsigned v) { PIN(v); return v; }
constexpr int LDS_CNT = 147456;
DI void cnt_add(const Ctx& c, int i) {
  asm volatile("s_mov_b64 exec, 1\n\tds_add_u32 %0, %1\n\ts_mov_b64 exec, -1" ::"v"(c.lds0 + LDS_CNT + 4 * i), "v"(1u) : "memory");
}
DI unsigned cnt_get(const Ctx& c, int i) {
  unsigned v;
  asm volatile("ds_read_b32 %0, %1\n\ts_waitcnt lgkmcnt(0)" : "=v"(v) : "v"(c.lds0 + LDS_CNT + 4 * i) : "memory");
  return rfl(v);
}
DI void cnt_wait(const Ctx& c, int i, unsigned n) {
  if (__builtin_expect(cnt_get(c, i) < n, 0))
    for (int spin = 0; spin < (1 << 22) && cnt_get(c, i) < n; ++spin) __builtin_amdgcn_s_sleep(1);
}
DI rsrc_t fl_rsrc(const Ctx& c) { return mk_rsrc(c.ws, FL_END); }
DI void set_flag(const Ctx& c, unsigned off) {
  u32x2 t;
  asm volatile("v_mov_b32 %0, %1" : "=v"(t[0]) : "s"(c.t0));
  asm volatile("v_mov_b32 %0, %1" : "=v"(t[1]) : "s"(c.t1));
  asm volatile("s_mov_b64 exec, 1\n\tbuffer_store_dwordx2 %0, off, %1, %2 sc1\n\ts_mov_b64 exec, -1" ::"v"(t), "s"(fl_rsrc(c)), "s"(off) : "memory");
}
DI bool tag_ok(const Ctx& c, u32x2 v) { return __builtin_amdgcn_ballot_w64(v[0] != c.t0 || v[1] != c.t1) == 0; }
DI void poll(const Ctx& c, unsigned voff, unsigned soff) {
  for (int spin = 0; spin < (1 << 20); ++spin) {
    u32x2 v;
    asm volatile("buffer_load_dwordx2 %0, %1, %2, %3 offen sc1\n\ts_waitcnt vmcnt(0)" : "=&v"(v) : "v"(voff), "s"(fl_rsrc(c)), "s"(soff) : "memory");
    if (tag_ok(c, v)) break;
  }
}

DI bool poll_first(const Ctx& c, unsigned voff, unsigned soff, unsigned long long first) {
  unsigned long long m = 0;
  for (int spin = 0; spin < (1 << 20); ++spin) {
    u32x2 v;
    asm volatile("buffer_load_dwordx2 %0, %1, %2, %3 offen sc1\n\ts_waitcnt vmcnt(0)" : "=&v"(v) : "v"(voff), "s"(fl_rsrc(c)), "s"(soff) : "memory");
    m = __builtin_amdgcn_ballot_w64(v[0] != c.t0 || v[1] != c.t1);
    if ((m & first) == 0) break;
  }
  return m == 0;
}

constexpr int K1_SLOT = 32768, K1_TBL = K1_SLOT * 4;
extern "C" __global__ void k9_end();

template <int NN> DI void p1_body(Ctx& c, int mt, int nt) {
  char* ws = c.ws;
  const int w = c.w, lane = fresh_lane();
  const unsigned lds0 = c.lds0;
  const rsrc_t rx = mk_rsrc(c.x, S_ * D_ * 2), rw = mk_rsrc(c.wqkv, D_ * 3 * D_ * 2);
  const int t16 = lane & 15, g = lane >> 4, q4 = (lane >> 2) & 3, p = lane & 3, m = w & 3;
  constexpr int NT = NN ? 3 : 4;
  unsigned xv[2], wv[2];
#pragma unroll
  for (int i = 0; i < 2; ++i) {
    const int row = 8 * (2 * w + i) + (lane >> 3);
    xv[i] = (128 * mt + row) * 1152 + 16 * ((lane & 7) ^ ((row >> 1) & 7));
    int li = 64 * (2 * w + i) + lane;
    if (li >= 896) li -= 128;
    const int r64 = (li * 4682) >> 16, cc = li - 14 * r64;
    const int rho = r64 & 31, k = (r64 & 32) + 8 * ((rho >> 2) & 3) + 4 * (rho >> 4) + (rho & 3);
    wv[i] = k * 3456 + ((216 * nt) & ~15) + 16 * cc;
  }
  STAMP_DECL; STAMP(ws, 1, 3);
  asm volatile("v_writelane_b32 %0, %1, 0" : "+v"(stamp_v) : "s"((unsigned)c.te));

  auto issue1 = [&](int slab, int i) {
    const unsigned dst = lds0 + (slab & 3) * K1_SLOT + w * 2048 + (i >> 1) * 1024;
    if (i & 1) glds16(dst + 16384, wv[i >> 1], rw, slab * 64 * 3456);
    else glds16(dst, xv[i >> 1], rx, slab * 128);
  };
#pragma unroll
  for (int sl = 0; sl < 2; ++sl)
#pragma unroll
    for (int i = 0; i < 4; ++i) issue1(sl, i);
  u32x2 ev;
  bload8c(ev, 0u, fl_rsrc(c), 0);
  STAMP(ws, 1, 1);
  const unsigned wa = lds0 + 16384 + (4 * g + q4) * 224 + (nt & 1) * 8;
  unsigned ad[2][5];
  ad[0][0] = wa + 8 * p + 144 * NN;
  ad[0][1] = wa + 72 * (p < 3 ? p : 2) + 32;
  ad[0][2] = NN && m >= 2 ? ad[0][1] : ad[0][0] + 72 * (m >> 1) + 40 * (m & 1);
  ad[0][3] = lds0 + (32 * m + t16) * 128 + 16 * (g ^ ((t16 >> 1) & 7));
  ad[0][4] = ad[0][3] ^ 64;
#pragma unroll
  for (int i = 0; i < 5; ++i) { ad[1][i] = ad[0][i] + 2 * K1_SLOT; PIN(ad[1][i]); }
  const unsigned tb = lds0 + K1_TBL, cb = tb + 1024, dummy = cb + 512;
  unsigned e_cs = t16 == 0 ? cb + (w * 16 + 4 * g) * 4 : dummy, e_st = tb + (32 * m + t16) * 8, e_cc = cb + (64 * NN + 4 * g) * 4;
  unsigned e_s1 = g == 3 ? e_st : dummy, e_s2 = g == q4 ? e_st + 4 : dummy, e_vo[2], e_so[4], e_t2 = 0, e_one = 0;
  unsigned e_sc[2];
  float e_tsc = 0.f;
  {
    constexpr unsigned KV0 = WS_KV - WS_QF;
    const int tile32 = 4 * mt + m, key = 16 * (g >> 1) + t16;
    const unsigned lo_qk = (g & 1) * 512 + key * 16, lo_v = 1280 + key * 32 + (g & 1) * 16;
#pragma unroll
    for (int i = 0; i < 2 - NN; ++i) {
      const int pg = 3 * nt + 2 * NN + i, type = pg >> 4, rl = type ? KVREC : QREC;
      e_so[2 * i] = (type ? KV0 : 0) + (2 * (pg & 15) * 64 + tile32) * rl;
      e_so[2 * i + 1] = e_so[2 * i] + 64 * rl;
      e_vo[i] = type == 2 ? lo_v : lo_qk;
      e_sc[i] = type ? 0x3f800000u : __builtin_bit_cast(unsigned, QSCALE);
      asm volatile("" : "+s"(e_so[2 * i]), "+s"(e_so[2 * i + 1]), "+s"(e_sc[i]), "+v"(e_vo[i]));
    }
    if constexpr (NN) {
      const int pg = 3 * nt + g, type = pg >> 4, rl = type ? KVREC : QREC;
      e_vo[1] = g < 3 ? (type ? KV0 : 0) + (2 * (pg & 15) * 64 + tile32) * rl + (type == 2 ? 2304 : 1024) + t16 * 8 : OOB;
      e_t2 = e_vo[1] + 64 * rl;
      e_one = type == 1 ? 0u : 0x00003f80u;
      e_tsc = type ? 1.f : QSCALE;
      PIN(e_vo[1]); PIN(e_t2); PIN(e_one); PIN(e_tsc); PIN(e_s1); PIN(e_s2);
    }
    PIN(e_cs); PIN(e_st); PIN(e_cc);
  }
  f32x4 acc[4][2] = {}, cs = {};
  bf16x8 ones = __builtin_bit_cast(bf16x8, (u32x4){ONES2, ONES2, ONES2, ONES2});
  PIN(ones);
  bf16x8 fb[2][2];
  bf16x4 fa[2][NT + 1][2];
  constexpr int NR = 2 * NT + 4;
  auto rd1 = [&](auto KS, auto I) {
    constexpr int ks = KS.value, i = I.value, sp = ks & 1, sl = (ks >> 1) & 3, ti = (i - 2) >> 1;
    constexpr unsigned so = (sl & 1) * K1_SLOT;
    if constexpr (ks < 18 && i < NR) {
      if constexpr (i < 2) fb[sp][i] = ldsr<bf16x8>(ad[sl >> 1][3 + sp] + so + i * 2048);
      else {
        constexpr int ai = ti == NT ? 2 : (NN && ti == 2) ? 1 : 0;
        constexpr unsigned co = ai ? 0 : 72 * (ti >> 1) + 40 * (ti & 1);
        fa[sp][ti][i & 1] = trh(ad[sl >> 1][ai] + so + co + sp * 32 * 224 + (i & 1) * 16 * 224);
      }
    }
  };
  sfor<9>([&](auto SL) {
    constexpr int sl = SL.value;
    constexpr int iss = sl == 0 ? 2 : (sl + 3 < 9 ? sl + 3 : 9);
    constexpr int need = sl == 0 ? 0 : (sl + 1 < 9 ? sl + 1 : 8);
    if constexpr (sl == 1) asm volatile("s_waitcnt vmcnt(%1)" : "+v"(ev) : "i"(4 * (iss - need - 1)) : "memory");
    else wait_vm<4 * (iss - need - 1) + (sl == 0)>();
    bar();
    if constexpr (sl == 0) STAMP(ws, 1, 2);
    if constexpr (sl <= 1) sfor<NR>([&](auto I) { rd1(ic<2 * sl>{}, I); });
    if constexpr (sl == 1) {
      const unsigned long long e = (((unsigned long long)rfl(ev[1]) << 32) | rfl(ev[0])) + 1ull;
      c.t0 = (unsigned)e; c.t1 = (unsigned)(e >> 32);
    }
    sfor<2>([&](auto SP) {
      constexpr int sp = SP.value, ks = 2 * sl + sp;
      bf16x8 tl;
      if constexpr (NN) tl = t16 >= 12 ? ones : cat(fa[sp][2][0], fa[sp][2][1]);
      sfor<9>([&](auto M) {
        constexpr int mi = M.value, ti = mi >> 1;
        if constexpr (mi == 8) cs = mfma16(cat(fa[sp][NT][0], fa[sp][NT][1]), ones, cs);
        else if constexpr (NN && ti == 3) acc[3][mi & 1] = mfma16(fb[sp][mi & 1], fb[sp][mi & 1], acc[3][mi & 1]);
        else if constexpr (NN && ti == 2) acc[2][mi & 1] = mfma16(tl, fb[sp][mi & 1], acc[2][mi & 1]);
        else acc[ti][mi & 1] = mfma16(cat(fa[sp][ti][0], fa[sp][ti][1]), fb[sp][mi & 1], acc[ti][mi & 1]);
        SBAR();
        if constexpr (ks != 1) {
          if constexpr (mi < 3) { rd1(ic<ks + 1>{}, ic<2 * mi>{}); rd1(ic<ks + 1>{}, ic<2 * mi + 1>{}); }
          else rd1(ic<ks + 1>{}, ic<3 + mi>{});
        }
        if constexpr (sl == 0) { if constexpr (mi & 1) issue1(2 + sp, mi >> 1); }
        else if constexpr (sl + 3 < 9 && (mi == 2 + 2 * NN || mi == 6 + 2 * NN)) issue1(sl + 3, 2 * sp + (mi >= 6));
        SBAR();
      });
    });
  });
  STAMP(ws, 1, 4);
  ldsw<f32x4>(e_cs, cs);
  if constexpr (NN) sfor<2>([&](auto T) {
    const f32x4 gr = acc[3][T.value];
    const float d01 = (p & 1) ? gr[1] : gr[0], d23 = (p & 1) ? gr[3] : gr[2];
    ldsw<float>(e_s1 + 128 * T.value, acc[2][T.value][0]);
    ldsw<float>(e_s2 + 128 * T.value, (p & 2) ? d23 : d01);
  });
  lds_bar();
  f32x2 sv[2];
  f32x4 cc[4];
  sfor<2>([&](auto T) { sv[T.value] = ldsr<f32x2>(e_st + 128 * T.value); });
  sfor<NT>([&](auto I) { cc[I.value] = ldsr<f32x4>(e_cc + 64 * I.value); });
  SBAR();
  float nmu[2], rs[2];
  sfor<2>([&](auto T) {
    nmu[T.value] = sv[T.value][0] * (-1.f / D_);
    rs[T.value] = __builtin_amdgcn_rsqf(fmaxf(sv[T.value][1] * (1.f / D_) - nmu[T.value] * nmu[T.value], 0.f) + 1e-5f);
  });
  const rsrc_t ro = mk_rsrc(ws + WS_QF, 32 * 64 * (QREC + KVREC));
  sfor<(NN ? 2 : 4)>([&](auto TI) {
    constexpr int ti = TI.value;
    const f32x4 v0 = (acc[ti][0] + cc[ti] * nmu[0]) * (rs[0] * __uint_as_float(e_sc[ti >> 1])), v1 = (acc[ti][1] + cc[ti] * nmu[1]) * (rs[1] * __uint_as_float(e_sc[ti >> 1]));
    auto s0 = __builtin_amdgcn_permlane32_swap(pk2(v0[0], v0[1]), pk2(v1[0], v1[1]), false, false);
    auto s1 = __builtin_amdgcn_permlane32_swap(pk2(v0[2], v0[3]), pk2(v1[2], v1[3]), false, false);
    bstore16<16>((u32x4){s0[0], s1[0], s0[1], s1[1]}, ro, e_vo[ti >> 1], e_so[ti]);
  });
  if constexpr (NN) sfor<2>([&](auto T) {
    const f32x4 v = (acc[2][T.value] + cc[2] * nmu[T.value]) * (rs[T.value] * e_tsc);
    bstore8<16>((u32x2){pk2(v[0], v[1]), e_one}, ro, e_vo[1], T.value * 128);
    bstore8<16>((u32x2){pk2(v[2], v[3]), e_one}, ro, e_t2, T.value * 128);
  });
  STAMP(ws, 1, 5);
  wait_vm<0>();
  STAMP(ws, 1, 6);
  bar();
  if (w == 0) set_flag(c, FL1 + (mt * 16 + nt) * FS1);
  STAMP(ws, 1, 7);
  STAMP_FLUSH(ws, 1);
}

constexpr int P3_WO = 73728;
constexpr int K4_RING = 8192;
constexpr unsigned k4_so(int u) { return ((((u >> 1) + 2) & 3) * 2 + (u & 1)) * 8192; }
DI unsigned k4_wv(int lane, int w, int Qd, int j) {
  const int rho = 8 * (w & 3) + (lane >> 3);
  return rho * 9216 + 1152 * Qd + 256 * j + 32 * (((lane & 7) >> 1) ^ ((rho >> 1) & 3)) + 16 * (lane & 1);
}

template <int U0> DI void k4_first(const Ctx& c, int lane) {
  const rsrc_t rg = mk_rsrc(c.wg, D_ * F_ * 2), ru = mk_rsrc(c.wu, D_ * F_ * 2);
  const unsigned wv4 = k4_wv(lane, c.w, c.xcd, c.r & 3), dst = c.lds0 + K4_RING + (c.w & 3) * 1024;
  sfor<4>([&](auto U) {
    glds16(dst + k4_so(U0 + U.value), wv4, rg, (U0 + U.value) * 32 * 9216);
    glds16(dst + 4096 + k4_so(U0 + U.value), wv4, ru, (U0 + U.value) * 32 * 9216);
  });
}
DI void p2_ahead(const Ctx& c, unsigned flag) {
  const int w = c.w, r = c.r, lane = fresh_lane();
  const rsrc_t rwo = mk_rsrc(c.wo, D_ * D_ * 2);
  const int nt = r >= 9 ? r - 9 : r;
  const int rho = 8 * w + (lane >> 3), rg = (rho >> 2) & 3, rj = 4 * (rho >> 4) + (rho & 3);
  const unsigned cbyte = 128 * nt + 32 * (((lane & 7) >> 1) ^ ((rho >> 1) & 3)) + 16 * (lane & 1);
  const int th = 4 * rg + (rj >> 1);
  const unsigned wv = (20 * (rg >> 1) + 8 * (rg & 1) + rj) * 1152 + cbyte, wvt = (18 * th + ((th & 1) ? 0 : 16) + (rj & 1)) * 1152 + cbyte;
  sfor<16>([&](auto K) {
    glds16(c.lds0 + P3_WO + (w + 4 * K.value) * 1024, wv, rwo, K.value * 36 * 1152);
    if constexpr (K.value == 7) { wait_vm<8>(); set_flag(c, flag); }
  });
  sfor<2>([&](auto K) { glds16(c.lds0 + P3_WO + (w + 64 + 4 * K.value) * 1024, wvt, rwo, K.value * 288 * 1152); });
}

DI void p2(const Ctx& c) {
  const int head = 4 * c.xcd + (c.r & 3), qb = c.r >> 2;
  char* ws = c.ws;
  const int w = c.w, lane = fresh_lane();
  const unsigned lds0 = c.lds0;
  const int q = lane & 31, h = lane >> 5, G = lane >> 4;
  const int qt = 8 * qb + w;
  const rsrc_t rq = mk_rsrc(ws + WS_QF, 32 * 64 * QREC), rkv = mk_rsrc(ws + WS_KV, 32 * 64 * KVREC);
  STAMP_DECL; STAMP(ws, 2, 0);
  unsigned kb = lds0 + w * KVREC + lane * 16, ktb = lds0 + w * KVREC + 1024 + q * 8;
  unsigned vb[4];
#pragma unroll
  for (int i = 0; i < 4; ++i) { const int row = 8 * i + 4 * (G >> 1) + ((lane >> 2) & 3); vb[i] = lds0 + w * KVREC + ((G & 1) ? 2304 + row * 8 : 1280 + row * 32 + 8 * (lane & 3)); PIN(vb[i]); }
  unsigned mf[16], mk[8];
#pragma unroll
  for (int e = 0; e < 16; ++e) { mf[e] = ((e & 3) + 8 * (e >> 2) <= q - 4 * h) ? ~0u : 0u; PIN(mf[e]); }
#pragma unroll
  for (int e = 0; e < 8; ++e) { mk[e] = (mf[2 * e] & 0xffffu) | (mf[2 * e + 1] & 0xffff0000u); PIN(mk[e]); }
  const unsigned tt = 2 * qt + (q >> 4);
  unsigned om_o = (tt * 18 + (head >> 1)) * 1024 + ((q & 15) + 16 * (2 * (head & 1) + h)) * 16;
  unsigned ot_o = h ? OOB : (tt * 18 + 16 + (head >> 4)) * 1024 + ((q & 15) + 16 * ((head >> 2) & 3)) * 16 + (head & 3) * 4;
  unsigned l16 = lane * 16, qto = h ? OOB : 1024 + q * 8, wmo;
  const int wmv = c.r * 8 + w;
  {
    const int l = (wmv >= 162 ? wmv - 162 : wmv >= 81 ? wmv - 81 : wmv) * 64 + lane, row = (l * 7282) >> 16;
    wmo = wmv >= 162 ? l * 128 : row * 9216 + c.xcd * 1152 + (l - 9 * row) * 128;
    PIN(wmo);
  }
  PIN(kb); PIN(ktb); PIN(om_o); PIN(ot_o); PIN(l16); PIN(qto);
  if (qb == 0) {
#pragma unroll
    for (int i = 0; i < 3; ++i) {
      const unsigned o = min(w * 64 + lane + 512 * i, 1279) * 16, m = (o % KVREC - 1024 < 256) ? 0x0000f149u : 0u;
      ldsw<u32x4>(lds0 + o, (u32x4){0u, m, 0u, m});
    }
  }
#define TSL 0
  if (TSL && w) __builtin_amdgcn_s_sleep(TSL);
  if (w >= 1 && w <= 3) warm(lds0 + LDS_SCR, (w * 64 + lane < 224 ? (w * 64 + lane - 64) * 128 : OOB), rkv, (head * 64 + 8 * qb) * KVREC);
  if (w == 4 || w == 5) warm(lds0 + LDS_SCR, (w * 64 + lane < 336 ? (w * 64 + lane - 256) * 128 : OOB), rq, (head * 64 + 8 * qb) * QREC);
  if (w == 0) {
    const int ty = lane < 4 ? 1 : lane < 8 ? 2 : 0, pm = max(2 * qb + (ty ? (lane & 3) - 2 : (lane & 1)), 0);
    ldsw<unsigned>(lds0 + LDS_CNT + 4 * (lane & 7), 0u);
    poll(c, (pm * 16 + (((16 * ty + (head >> 1)) * 43) >> 7)) * FS1, FL1);
  }
  lds_bar();
  STAMP(ws, 2, 7);
  bf16x8 qm; u32x2 qt2;
#pragma unroll
  for (int i = 0; i < 5; ++i) { const int pc = max(w + 8 * i, qb ? 0 : 20 + w); glds16c(lds0 + pc * 1024, l16, rkv, (head * 64 + 8 * qb - 8) * KVREC + pc * 1024); }
  bload16c(qm, l16, rq, (head * 64 + qt) * QREC);
  bload8c(qt2, qto, rq, (head * 64 + qt) * QREC);
  auto toff = [&](int i) { return (unsigned)(i * KVREC); };
  STAMP(ws, 2, 1);
  asm volatile("s_waitcnt vmcnt(0)" : "+v"(qm), "+v"(qt2)::"memory");
  bar();
  STAMP(ws, 2, 2);
  const bf16x4 qt4 = __builtin_bit_cast(bf16x4, qt2);
  bf16x8 km[9]; bf16x4 kt[9];
  constexpr int ord[9] = {0, 8, 1, 2, 3, 4, 5, 6, 7};
  sfor<9>([&](auto I) { constexpr int i = ord[I.value]; km[i] = ldsr<bf16x8>(kb + toff(i)); kt[i] = ldsr<bf16x4>(ktb + toff(i)); });
  SBAR();
  f32x16 s[9];
  float m4[4];
  auto qk = [&](auto I, auto J) {
    constexpr int i = I.value, j = J.value;
    f32x16 a = mfma32(km[i], qm, (f32x16){}), b;
    if constexpr (j < 9) b = mfma32(km[j], qm, (f32x16){});
    s[i] = __builtin_amdgcn_mfma_f32_32x32x8bf16_1k(kt[i], qt4, a, 0, 0, 0);
    if constexpr (j < 9) s[j] = __builtin_amdgcn_mfma_f32_32x32x8bf16_1k(kt[j], qt4, b, 0, 0, 0);
    SBAR();
  };
  auto mx1 = [&](auto I) {
    constexpr int i = I.value;
#pragma unroll
    for (int e = 0; e < 8; ++e) m4[e & 3] = fmaxf(fmaxf(m4[e & 3], s[i][2 * e]), s[i][2 * e + 1]);
  };
  bf16x4 vf[2][4];
  auto rdv = [&](int set, unsigned off) {
#pragma unroll
    for (int i = 0; i < 4; ++i) vf[set][i] = trh(vb[i] + off);
  };
  qk(ic<0>{}, ic<8>{});
  qk(ic<1>{}, ic<2>{});
  {
    size_t p0 = (size_t)c.wg, p1 = (size_t)c.wu, p2 = (size_t)c.wo;
    asm("" : "+s"(p0), "+s"(p1), "+s"(p2));
    p0 = wmv >= 81 ? p1 : p0;
    warm(lds0 + LDS_SCR, wmo, mk_rsrc((const void*)(wmv >= 162 ? p2 : p0), wmv >= 162 ? D_ * D_ * 2 : D_ * F_ * 2), 0);
  }
  SBAR();
#pragma unroll
  for (int e = 0; e < 16; ++e) s[8][e] = __uint_as_float((__float_as_uint(s[8][e]) & mf[e]) | (__float_as_uint(s[0][e]) & ~mf[e]));
#pragma unroll
  for (int e = 0; e < 4; ++e) asm("v_max_f32 %0, %1, %2\n\tv_max3_f32 %0, %0, %3, %4" : "=&v"(m4[e]) : "v"(s[8][4 * e]), "v"(s[8][4 * e + 1]), "v"(s[8][4 * e + 2]), "v"(s[8][4 * e + 3]));
  SBAR();
  qk(ic<3>{}, ic<4>{});
  mx1(ic<1>{}); mx1(ic<2>{}); SBAR();
  qk(ic<5>{}, ic<6>{});
  mx1(ic<3>{}); mx1(ic<4>{}); SBAR();
  qk(ic<7>{}, ic<9>{});
  rdv(1, toff(1));
  mx1(ic<5>{}); mx1(ic<6>{}); SBAR();
  mx1(ic<7>{});
  float mx = max_x32(fmaxf(fmaxf(m4[0], m4[1]), fmaxf(m4[2], m4[3])));
  STAMP(ws, 2, 3);
  f32x16 o = {};
  u32x4 pp;
  sfor<16>([&](auto H) {
    constexpr int hh = H.value, i = 1 + hh / 2, ks = hh & 1;
    f32x2 dd[4];
#pragma unroll
    for (int e = 0; e < 4; ++e) dd[e] = (f32x2){s[i][8 * ks + 2 * e], s[i][8 * ks + 2 * e + 1]} - (f32x2){mx, mx};
    SBAR();
    if constexpr (hh > 0 && hh < 15) o = mfma32(cat(vf[(1 + (hh - 1) / 2) & 1][2 * (ks ^ 1)], vf[(1 + (hh - 1) / 2) & 1][2 * (ks ^ 1) + 1]), __builtin_bit_cast(bf16x8, pp), o);
    if constexpr (hh == 15) {
      const u32x4 pd = {pp[0] & mk[0], pp[1] & mk[1], pp[2] & mk[2], pp[3] & mk[3]};
      o = mfma32(cat(vf[0][0], vf[0][1]), __builtin_bit_cast(bf16x8, pd), o);
      o = mfma32(cat(vf[1][0], vf[1][1]), __builtin_bit_cast(bf16x8, pp ^ pd), o);
    }
    SBAR();
    if constexpr (ks == 0) rdv((i + 1) & 1, toff(i < 8 ? i + 1 : 0));
    SBAR();
    float pv[8];
#pragma unroll
    for (int e = 0; e < 4; ++e) { pv[2 * e] = __builtin_amdgcn_exp2f(dd[e][0]); pv[2 * e + 1] = __builtin_amdgcn_exp2f(dd[e][1]); }
    SBAR();
    pp = (u32x4){pk2(pv[0], pv[1]), pk2(pv[2], pv[3]), pk2(pv[4], pv[5]), pk2(pv[6], pv[7])};
    SBAR();
  });
  {
    const u32x4 pd = {pp[0] & mk[4], pp[1] & mk[5], pp[2] & mk[6], pp[3] & mk[7]};
    o = mfma32(cat(vf[0][2], vf[0][3]), __builtin_bit_cast(bf16x8, pd), o);
    o = mfma32(cat(vf[1][2], vf[1][3]), __builtin_bit_cast(bf16x8, pp ^ pd), o);
  }
  PIN(o);
  STAMP(ws, 2, 4);
  auto lr = __builtin_amdgcn_permlane32_swap(__float_as_uint(o[10]), __float_as_uint(o[10]), false, false);
  const float inv = __builtin_amdgcn_rcpf(__uint_as_float(lr[0]));
  const rsrc_t ro = mk_rsrc(ws + WS_OF, 128 * 18 * 1024);
  const u32x4 om = {pk2(o[0] * inv, o[1] * inv), pk2(o[2] * inv, o[3] * inv), pk2(o[4] * inv, o[5] * inv), pk2(o[6] * inv, o[7] * inv)};
  bstore16<16>(om, ro, om_o, 0);
  bstore4<16>(pk2(o[8] * inv, o[9] * inv), ro, ot_o, 0);
  STAMP(ws, 2, 5);
  cnt_add(c, 1);
  if (w < 4 && c.r < 18) p2_ahead(c, FL2 + (qt * 32 + head) * FS2);
  else { wait_vm<0>(); set_flag(c, FL2 + (qt * 32 + head) * FS2); }
  STAMP(ws, 2, 6);
  STAMP_FLUSH(ws, 2);
}

DI void p3_side(const Ctx& c) {
  char* ws = c.ws;
  const int w = c.w, xcd = c.xcd, lane = fresh_lane();
  const unsigned lds0 = c.lds0;
  const int sb = c.r - 18, blk = sb * 8 + xcd;
  const rsrc_t rwt = mk_rsrc(ws + WS_WT, 4 * 4608 * 32), rlg = mk_rsrc(ws + WS_LG, 8 * 4 * 36864), rcs = mk_rsrc(ws + WS_CS, 2 * 4608 * 4);
  STAMP_DECL; STAMP(ws, 3, 0);
  const int mat = sb >= 5, un = sb < 5 ? 2 * sb : sb - 5;
  const rsrc_t rw = mk_rsrc(mat ? c.wu : c.wg, D_ * F_ * 2);
  const unsigned vo = (lane & 7) * 16 + (72 * w + (lane >> 3)) * 9216;
  u32x4 v[2][9];
#pragma unroll
  for (int i = 0; i < 9; ++i) v[0][i] = __builtin_amdgcn_raw_buffer_load_b128(rw, vo, (9 * xcd + un) * 128 + i * 8 * 9216, 0);
#pragma unroll
  for (int i = 0; i < 9; ++i) v[1][i] = __builtin_amdgcn_raw_buffer_load_b128(rw, sb < 4 ? vo : OOB, (9 * xcd + un + 1) * 128 + i * 8 * 9216, 0);
  const int drow = 64 * (blk - 40) + 8 * w + (lane >> 3);
  const u32x4 dv = __builtin_amdgcn_raw_buffer_load_b128(mk_rsrc(c.wd, F_ * D_ * 2), mat ? drow * 1152 + 1024 + (lane & 7) * 16 : OOB, 0, 0);
  if (un == 8) {
    const unsigned lo = ((lane & 7) >> 1) * 36864 + (72 * w + (lane >> 3)) * 32 + (lane & 1) * 16;
#pragma unroll
    for (int i = 0; i < 9; ++i) bstore16<16>(v[0][i], rlg, lo, (xcd * 8 + (sb >> 3)) * 18432 + i * 256);
  }
  const int pr = (drow % 576) >= 512 ? ((drow & ~24) | ((drow & 8) << 1) | ((drow & 16) >> 1)) : drow;
  bstore16<16>(dv, rwt, mat ? (((lane & 7) >> 1) * 4608 + pr) * 32 + (lane & 1) * 16 : OOB, 0);
  float a[2][8] = {};
#pragma unroll
  for (int k = 0; k < 2; ++k) {
#pragma unroll
    for (int i = 0; i < 9; ++i)
#pragma unroll
      for (int e = 0; e < 4; ++e) { a[k][2 * e] += bflo(v[k][i][e]); a[k][2 * e + 1] += bfhi(v[k][i][e]); }
#pragma unroll
    for (int e = 0; e < 8; ++e) a[k][e] = sum_x32(sum_x16(a[k][e]));
  }
  if (lane < 16) {
#pragma unroll
    for (int k = 0; k < 2; ++k) {
      ldsw<f32x4>(lds0 + k * 4096 + w * 512 + lane * 32, (f32x4){a[k][0], a[k][1], a[k][2], a[k][3]});
      ldsw<f32x4>(lds0 + k * 4096 + w * 512 + lane * 32 + 16, (f32x4){a[k][4], a[k][5], a[k][6], a[k][7]});
    }
  }
  lds_bar();
  if (w < 2) {
    float t = 0.f;
#pragma unroll
    for (int i = 0; i < 16; ++i) t += ldsr<float>(lds0 + w * 4096 + i * 256 + lane * 4);
    bstore4<16>(__float_as_uint(t), rcs, w == 0 || sb < 4 ? lane * 4 : OOB, ((sb >= 5 ? 4608 : 0) + (9 * xcd + un + w) * 64) * 4);
  }
  STAMP(ws, 3, 5);
  wait_vm<0>();
  bar();
  if (w == 0) set_flag(c, FLS + blk * 64);
  STAMP(ws, 3, 6);
  if (w == 5 && blk == 48) {
    for (int i = 0; i < 4; ++i) poll(c, lane * FS1, FL1 + i * 64 * FS1);
    set_flag(c, 0);
  }
  STAMP_FLUSH(ws, 6);
}

DI void p3_gemm(const Ctx& c) {
  char* ws = c.ws;
  const int w = c.w, lane = fresh_lane();
  const unsigned lds0 = c.lds0;
  const int t16 = lane & 15, g = lane >> 4;
  const int nt = c.r >= 9 ? c.r - 9 : c.r;
  const int T = 16 * c.xcd + 8 * (w >> 2) + 4 * (c.r >= 9) + (w & 3);
  const rsrc_t rof = mk_rsrc(ws + WS_OF, 128 * 18 * 1024);
  STAMP_DECL; STAMP(ws, 3, 0);
  unsigned wa[4];
  {
    const int r2 = 4 * g + ((lane >> 2) & 3);
#pragma unroll
    for (int i = 0; i < 4; ++i) wa[i] = lds0 + P3_WO + r2 * 128 + 32 * (i ^ ((r2 >> 1) & 3)) + 8 * (lane & 3);
  }
  u32x2 xr[4];
  sfor<4>([&](auto I) { bload8(xr[I.value], (16 * T + t16) * 1152 + 128 * nt + 8 * g, mk_rsrc(c.x, S_ * D_ * 2), 32 * I.value); });
  unsigned l16 = lane * 16, so = g == 0 ? t16 * 8 : OOB;
  PIN(l16); PIN(so);
  if (w < 4) { wait_vm<4>(); cnt_add(c, 0); }
  cnt_wait(c, 0, 4);
  STAMP(ws, 3, 1);
  constexpr int AH = 5;
  bf16x8 of[18];
  bf16x4 fr[AH + 1][4][2];
  unsigned wb[4];
#pragma unroll
  for (int i = 0; i < 4; ++i) { wb[i] = wa[i] + 65536; PIN(wb[i]); }
  auto rd1 = [&](auto KS, auto I) {
    constexpr int ks = KS.value, i = I.value;
    if constexpr (ks < 18) fr[ks % (AH + 1)][i >> 1][i & 1] = trh((ks < 16 ? wa : wb)[i >> 1] + (ks & 15) * 4096 + (i & 1) * 2048);
  };
  sfor<AH>([&](auto K) { sfor<8>([&](auto I) { rd1(K, I); }); });
  SBAR();
  const bool all = poll_first(c, (lane & 31) * FS2, FL2 + (T >> 1) * 32 * FS2, 0xffffull);
  asm volatile("" : "+v"(xr[0]), "+v"(xr[1]), "+v"(xr[2]), "+v"(xr[3]));
  sfor<8>([&](auto K) { bload16c(of[K.value], l16, rof, (T * 18 + K.value) * 1024); });
  f32x4 acc[4];
#pragma unroll
  for (int i = 0; i < 4; ++i) { acc[i] = (f32x4){bflo(xr[i][0]), bfhi(xr[i][0]), bflo(xr[i][1]), bfhi(xr[i][1])}; PIN(acc[i]); }
  if (!all) poll(c, (16 + (lane & 15)) * FS2, FL2 + (T >> 1) * 32 * FS2);
  STAMP(ws, 3, 2);
  sfor<10>([&](auto K) { bload16c(of[8 + K.value], l16, rof, (T * 18 + 8 + K.value) * 1024); });
  sfor<18>([&](auto KS) {
    constexpr int ks = KS.value;
    asm volatile("s_waitcnt vmcnt(%1)" : "+v"(of[ks]) : "i"(17 - ks) : "memory");
    if constexpr (ks == 0) STAMP(ws, 3, 3);
    if constexpr (ks == 9) STAMP(ws, 3, 4);
    sfor<4>([&](auto C) {
      mfma16_ip(acc[C.value], cat(fr[ks % (AH + 1)][C.value][0], fr[ks % (AH + 1)][C.value][1]), of[ks]);
      SBAR();
      rd1(ic<ks + AH>{}, ic<2 * C.value>{}); rd1(ic<ks + AH>{}, ic<2 * C.value + 1>{});
      SBAR();
    });
  });
  asm volatile("s_nop 9" : "+v"(acc[0]), "+v"(acc[1]), "+v"(acc[2]), "+v"(acc[3]));
  unsigned d[8];
#pragma unroll
  for (int i = 0; i < 4; ++i) { d[2 * i] = pk2(acc[i][0], acc[i][1]); d[2 * i + 1] = pk2(acc[i][2], acc[i][3]); }
  const rsrc_t rhf = mk_rsrc(ws + WS_HF, 128 * 18 * 1024);
  bstore16<16>((u32x4){d[0], d[1], d[2], d[3]}, rhf, l16, (T * 18 + 2 * nt) * 1024);
  bstore16<16>((u32x4){d[4], d[5], d[6], d[7]}, rhf, l16, (T * 18 + 2 * nt + 1) * 1024);
  float s1[2] = {0.f, 0.f}, s2[2] = {0.f, 0.f};
#pragma unroll
  for (int i = 0; i < 8; ++i) {
    const bf2_t v = __builtin_bit_cast(bf2_t, d[i]);
    s1[i & 1] = __builtin_amdgcn_fdot2_f32_bf16(v, __builtin_bit_cast(bf2_t, ONES2), s1[i & 1], false);
    s2[i & 1] = __builtin_amdgcn_fdot2_f32_bf16(v, v, s2[i & 1], false);
  }
  bstore8<16>((u32x2){__float_as_uint(sum_x32(sum_x16(s1[0] + s1[1]))), __float_as_uint(sum_x32(sum_x16(s2[0] + s2[1])))}, mk_rsrc(ws + WS_ST, 9 * 2048 * 8), so, (T * 9 + nt) * 128);
  STAMP(ws, 3, 5);
  wait_vm<0>();
  set_flag(c, FL3 + (T * 16 + nt) * FS3);
  STAMP(ws, 3, 6);
  STAMP_FLUSH(ws, 3);
}

constexpr int K4_LG = P3_WO;
constexpr int K4_CS = K4_LG + 36864;
constexpr int K4_STP = K4_CS + 4096;
struct K4Op { int kind, idx; };
struct K4Ops { int n; K4Op op[12]; };
constexpr K4Ops k4_ops(int S) {
  K4Ops o = {};
  auto add = [&](int k, int i) { o.op[o.n++] = {k, i}; };
  if (S == 3) add(9, 0);
  if (S == 0) for (int u = 4; u < 8; ++u) { add(1, u); add(2, u); }
  else for (int u = 2 * S + 6; u < 2 * S + 8 && u < 36; ++u) {
    if (S == 2) { add(0, 2 * u - 12); add(0, 2 * u - 11); }
    if (S > 2) add(0, u);
    if (u < 18) { add(1, u); add(2, u); }
  }
  if (S == 4) { add(5, 0); add(5, 1); add(5, 2); }
  if (S == 6) add(6, 0);
  if (S >= 10 && S < 15) add(3, S - 10);
  if (S == 12) add(7, 0);
  if (S == 14) add(8, 0);
  if (S == 10) add(4, 0);
  return o;
}
constexpr int k4_nst(int S) { return (S == 8 || S == 9) ? 2 : S == 7 ? 1 : 0; }
constexpr int k4_issued(int S) { int n = 8; for (int s = 0; s < S; ++s) n += k4_ops(s).n + k4_nst(s); return n; }
constexpr int k4_pos(int kind, int idx) {
  if (kind == 0 && idx < 8) return -1;
  if ((kind == 1 || kind == 2) && idx < 4) return 2 * idx + kind - 1;
  for (int S = 0;; ++S) for (int i = 0; i < k4_ops(S).n; ++i) if (k4_ops(S).op[i].kind == kind && k4_ops(S).op[i].idx == idx) return k4_issued(S) + k4_nst(S) + i;
}
constexpr int k4_need(int S) {
  if (S == 8) return k4_issued(7) - 1;
  if (S >= 16) return k4_issued(15) - 1;
  const int a = S < 9 ? k4_pos(2, 2 * S + 1) : -1, b = k4_pos(0, 2 * S + 2);
  return a > b ? a : b;
}
static_assert(k4_need(5) >= k4_pos(9, 0) && k4_need(7) >= k4_pos(5, 2) && k4_need(12) >= k4_issued(10) - 1, "");
constexpr int K5_SLOT = 17408, K5_BASE = 0, K5_P = 4;
constexpr unsigned k5_so(int v) { return K5_BASE + ((v + 7) & 7) * K5_SLOT; }
DI int k5_u(int v, int nt) {
  if (v < 32) { const int q = ((v >> 2) + nt) & 3; return 18 * (q >> 1) + 8 * (q & 1) + 4 * ((v >> 1) & 1) + (v & 1) + 2 * (v >> 4); }
  const int q = (v + nt) & 3;
  return 18 * (q >> 1) + 16 + (q & 1);
}
DI unsigned k5_mv(int rho, int sp, int nt, int t16) {
  const int rr = sp ? ((rho & 7) | ((rho & 8) << 1) | ((rho & 16) >> 1)) : rho;
  return rr * 1152 + 256 * nt + 32 * ((t16 >> 1) ^ (rho & 7)) + 16 * (t16 & 1);
}
DI unsigned k5_fl(int i, int sg, int mt5) { return (((((mt5 >> 1) * 4 + ((i >> 2) & 3)) * 8 + 2 * sg + ((i >> 4) & 1)) * 8) + 4 * (mt5 & 1) + (i & 3)) * 64; }

DI void p4(const Ctx& c) {
  char* ws = c.ws;
  const int w = c.w, r = c.r, xcd = c.xcd, lane = fresh_lane();
  const int mt = r >> 2, Qd = xcd, j = r & 3;
  const unsigned lds0 = c.lds0;
  const int g = lane >> 4;
  const int T0 = 16 * mt + 2 * w;
  const rsrc_t rwm = mk_rsrc(w < 4 ? c.wg : c.wu, D_ * F_ * 2), rhf = mk_rsrc(ws + WS_HF, 128 * 18 * 1024), rwd = mk_rsrc(c.wd, F_ * D_ * 2);
  const rsrc_t rhh = mk_rsrc(ws + WS_HH, 128 * 144 * 1024);
  const unsigned f4 = FL4 + ((r * 8 + xcd) * 8 + w) * 64;
  const int sg = xcd >> 1, mt5 = 8 * (xcd & 1) + (r >> 2);
  STAMP_DECL; STAMP(ws, 4, 0);
  const unsigned wv = opq(k4_wv(lane, w, Qd, j));
  const unsigned wdst = lds0 + K4_RING + w * 1024, l16 = opq(lane * 16);
  unsigned wa[4];
  {
    const int r2 = 4 * g + ((lane >> 2) & 3);
#pragma unroll
    for (int i = 0; i < 4; ++i) wa[i] = opq(lds0 + K4_RING + r2 * 128 + 32 * (i ^ ((r2 >> 1) & 3)) + 8 * (lane & 3));
  }
  const bool all = poll_first(c, (min(lane & 15, 8) + (lane & 16)) * FS3, FL3 + T0 * 16 * FS3, 0x30003ull);
  bf16x8 act[18][2];
  u32x2 pf;
  auto pf_off = [&] { const int z = opq(l16) >> 4; const unsigned a = opq(FL4 + k5_fl(z, sg, mt5)), b = opq(FLS + (40 + 18 * sg + min(z - 32, 17)) * 64); return z < 32 ? a : b; };
  auto doop = [&](auto KIND, auto IDX) {
    constexpr int kind = KIND.value, u = IDX.value;
    if constexpr (kind == 0) glds16(wdst + k4_so(u), wv, rwm, (u % 18) * 32 * 9216 + (u / 18) * 128);
    else if constexpr (kind <= 2) bload16c(act[u][kind - 1], l16, rhf, (T0 * 18 + 18 * (kind - 1) + u) * 1024);
    else if constexpr (kind == 3) {
      const int pc = min(w + 8 * u, 35);
      glds16c(lds0 + K4_LG + pc * 1024, l16, mk_rsrc(ws + WS_LG, 8 * 4 * 36864), (Qd * 4 + j) * 36864 + pc * 1024);
    }
    else if constexpr (kind == 4) warm(lds0 + LDS_SCR, l16 * 8, rwd, sg * 1327104 + min(r * 8 + w, 161) * 8192);
    else if constexpr (kind == 5) glds16c(lds0 + K4_STP + w * 3072 + u * 1024, l16, mk_rsrc(ws + WS_ST, 9 * 2048 * 8), T0 * 1152 + u * 1024);
    else if constexpr (kind == 6) {
      const unsigned z = opq(l16), f = (576 * Qd + (z < 512 ? 128 * j : 384 + 16 * j)) * 4 + z;
      glds16c(lds0 + K4_CS + (w & 1) * 1024, z < 576 ? f : OOB, mk_rsrc(ws + WS_CS, 2 * 4608 * 4), (w & 1) * 4608 * 4);
    } else if constexpr (kind == 7) set_flag(c, f4);
    else if constexpr (kind == 8) bload8c(pf, pf_off(), fl_rsrc(c), 0);
    else bload8c(pf, min(opq(l16), 13u * 16) * 32, fl_rsrc(c), FLS + xcd * 64);
  };
  sfor<4>([&](auto U) { doop(ic<1>{}, U); doop(ic<2>{}, U); });
  if (w < 4) cnt_add(c, 4);
  if (!all) poll(c, (min(fresh_lane() & 15, 8) + (fresh_lane() & 16)) * FS3, FL3 + T0 * 16 * FS3);
  cnt_wait(c, 4, 4);
  f32x4 ag[4][2], au[4][2], cg, cu;
  bf16x4 fr[4][2];
  unsigned hd[2][2], lga, csa;
  f32x2 nmu = {0.f, 0.f};
  auto rd1 = [&](auto H, auto I) {
    constexpr int n = H.value >> 1, m = H.value & 1, i = I.value;
    if constexpr (n < 144) fr[H.value & 3][i] = trh(wa[n % 4] + k4_so(n / 4) + 4096 * m + 2048 * i);
    else if constexpr (n < 162) fr[H.value & 3][i] = trh(lga + (n - 144) * 1024 + m * 18432 + i * 512);
  };
  auto rd = [&](auto H) { rd1(H, ic<0>{}); rd1(H, ic<1>{}); };
  auto mm = [&](auto N, auto KS, f32x4& g0, f32x4& g1, f32x4& u0, f32x4& u1, auto&& hook) {
    constexpr int n = N.value, ks = KS.value;
    constexpr int h0 = n == 15 ? 999 : 2 * n + 3, h1 = (n == 14 || n == 15) ? 999 : 2 * n + 4;
    const bf16x8 fg = cat(fr[(2 * n) & 3][0], fr[(2 * n) & 3][1]);
    if constexpr (ks == 0) mfma16_z(g0, fg, act[ks][0]); else mfma16_ip(g0, fg, act[ks][0]);
    SBAR(); rd1(ic<h0>{}, ic<0>{}); SBAR(); hook(ic<0>{}); SBAR();
    if constexpr (ks == 0) mfma16_z(g1, fg, act[ks][1]); else mfma16_ip(g1, fg, act[ks][1]);
    SBAR(); rd1(ic<h0>{}, ic<1>{}); SBAR(); hook(ic<1>{}); SBAR();
    const bf16x8 fu = cat(fr[(2 * n + 1) & 3][0], fr[(2 * n + 1) & 3][1]);
    if constexpr (ks == 0) mfma16_z(u1, fu, act[ks][1]); else mfma16_ip(u1, fu, act[ks][1]);
    SBAR(); rd1(ic<h1>{}, ic<0>{}); SBAR(); hook(ic<2>{}); SBAR();
    if constexpr (ks == 0) mfma16_z(u0, fu, act[ks][0]); else mfma16_ip(u0, fu, act[ks][0]);
    SBAR(); rd1(ic<h1>{}, ic<1>{}); SBAR(); hook(ic<3>{}); SBAR();
  };
  auto ldcs = [&](int e) { cg = ldsr<f32x4>(csa + 64 * e); cu = ldsr<f32x4>(csa + 1024 + 64 * e); };
  auto epi = [&](auto E, auto I) {
    constexpr int grp = E.value >> 2, cc = E.value & 3, i = I.value, T = 1 - (i >> 1);
    if constexpr ((i & 1) == 0) {
      ag[cc][T] += cg * nmu[T]; au[cc][T] += cu * nmu[T]; PIN(ag[cc][T]); PIN(au[cc][T]);
      if constexpr (i == 2 && E.value != 3) { SBAR(); ldcs(E.value + 1); }
    }
    else {
      const f32x4 v = ag[cc][T] * au[cc][T];
      const unsigned d0 = pk2(v[0], v[1]), d1 = pk2(v[2], v[3]);
      if constexpr ((cc & 1) == 0) { hd[T][0] = d0; hd[T][1] = d1; PIN(hd[T][0]); PIN(hd[T][1]); }
      else bstore16<16>((u32x4){hd[T][0], hd[T][1], d0, d1}, rhh, l16, ((T0 + T) * 144 + 18 * Qd + 4 * j + 2 * grp + (cc >> 1)) * 1024);
    }
  };
  f32x2 sp[3];
  float rs0;
  auto stats = [&](auto TT, auto P) {
    constexpr int T = TT.value, p = P.value;
    if constexpr (p == 0) {
      const unsigned sa = lds0 + K4_STP + w * 3072 + T * 1152 + (opq(l16) >> 1);
      sp[0] = ldsr<f32x2>(sa); sp[1] = ldsr<f32x2>(sa + 512); sp[2] = ldsr<f32x2>(sa + 1024);
    } else if constexpr (p == 4) { sp[0] = sp[0] + sp[1] + (opq(l16) < 256 ? sp[2] : (f32x2){0.f, 0.f}); PIN(sp[0]); }
    else if constexpr (p == 5) { sp[0][0] = sum_x16(sp[0][0]); PIN(sp[0]); }
    else if constexpr (p == 6) { sp[0][1] = sum_x16(sp[0][1]); PIN(sp[0]); }
    else if constexpr (p == 7) { sp[0][0] = sum_x32(sp[0][0]); PIN(sp[0]); }
    else if constexpr (p == 8) { sp[0][1] = sum_x32(sp[0][1]); PIN(sp[0]); }
    else if constexpr (p == 9) {
      nmu[T] = sp[0][0] * (-1.f / D_);
      const float rs = __builtin_amdgcn_rcpf(fmaxf(sp[0][1] * (1.f / D_) - nmu[T] * nmu[T], 0.f) + 1e-5f);
      PIN(nmu);
      if constexpr (T == 0) { rs0 = rs; PIN(rs0); }
      else {
        const unsigned z = opq(l16);
        bstore4<16>(__float_as_uint((z & 256) ? rs : rs0), mk_rsrc(ws + WS_RS + (sg * 2048 + 16 * T0) * 4, (Qd & 1) == 0 && j == 0 ? 128 : 0), z >> 2, 0);
      }
    }
  };
  unsigned mv5;
  auto pf5 = [&](auto N) {
    constexpr int n = N.value, v = (n - 1) >> 1;
    if constexpr (n == 0) {
      const int vv = min(w, K5_P - 1), uu = k5_u(vv, j);
      glds16c(lds0 + K5_BASE + ((vv + 7) & 7) * K5_SLOT + 16384, l16, mk_rsrc(ws + WS_WT, 4 * 4608 * 32), (j * 144 + 36 * sg + uu) * 1024);
      const unsigned z = opq(l16);
      mv5 = k5_mv(4 * w + (z >> 8), 0, j, (z >> 4) & 15);
    } else if constexpr (n & 1) glds16(lds0 + k5_so(v) + 8192 + w * 1024, mv5, rwd, sg * 1152 * 1152 + k5_u(v, j) * 32 * 1152);
    else glds16c(lds0 + k5_so(v) + w * 1024, l16, rhh, ((8 * mt5 + w) * 144 + 36 * sg + k5_u(v, j)) * 1024);
  };
  sfor<18>([&](auto SS) {
    constexpr int S = SS.value;
    constexpr int nw = k4_issued(S) - k4_need(S) - 1;
    static_assert(nw >= 0 && nw < 64, "");
    if constexpr (S == 5) asm volatile("s_waitcnt vmcnt(%5)" : "+v"(act[2 * S][0]), "+v"(act[2 * S][1]), "+v"(act[2 * S + 1][0]), "+v"(act[2 * S + 1][1]), "+v"(pf) : "i"(nw) : "memory");
    else if constexpr (S < 9) asm volatile("s_waitcnt vmcnt(%4)" : "+v"(act[2 * S][0]), "+v"(act[2 * S][1]), "+v"(act[2 * S + 1][0]), "+v"(act[2 * S + 1][1]) : "i"(nw) : "memory");
    else if constexpr (S == 16) asm volatile("s_waitcnt vmcnt(%1)" : "+v"(pf) : "i"(nw) : "memory");
    else wait_vm<nw>();
    if constexpr (S == 16) { if (__builtin_expect(!tag_ok(c, pf), 0)) poll(c, pf_off(), 0); }
    if constexpr (S == 5) { if (__builtin_expect(!tag_ok(c, pf), 0)) poll(c, min(opq(l16), 13u * 16) * 32, FLS + xcd * 64); }
    if constexpr (S >= 2) bar();
    if constexpr (S == 0) STAMP(ws, 4, 1);
    if constexpr (S == 2) STAMP(ws, 4, 7);
    if constexpr (S == 4) STAMP(ws, 4, 2);
    if constexpr (S == 9) STAMP(ws, 4, 3);
    if constexpr (S == 17) STAMP(ws, 4, 4);
    if constexpr (S == 0 || S == 2) { rd(ic<16 * S>{}); rd(ic<16 * S + 1>{}); rd(ic<16 * S + 2>{}); }
    sfor<8>([&](auto L) {
      constexpr int l = L.value, n = 8 * S + l, t = n / 4, cc = n % 4, ks = t % 18, no = k4_ops(S).n;
      mm(ic<n>{}, ic<ks>{}, ag[cc][0], ag[cc][1], au[cc][0], au[cc][1], [&](auto I) {
        constexpr int i = I.value;
        sfor<no>([&](auto O) {
          constexpr int nd = no ? no : 1, at = O.value * 32 / nd + 16 / nd;
          if constexpr (at == 4 * l + i) doop(ic<k4_ops(S).op[O.value].kind>{}, ic<k4_ops(S).op[O.value].idx>{});
        });
        if constexpr (S == 7) stats(ic<(l >> 2)>{}, ic<4 * (l & 3) + i>{});
        if constexpr ((S == 8 || S == 17) && l == 3 && i == 0) { csa = lds0 + K4_CS + ((opq(l16) >> 4) & 48); PIN(csa); }
        if constexpr ((S == 8 || S == 17) && l == 4 && i == 0) ldcs(S == 8 ? 0 : 4);
        if constexpr (S == 17 && l == 0 && i == 0) { const unsigned z = opq(l16); lga = lds0 + K4_LG + (z >> 6) * 32 + ((z >> 1) & 24); PIN(lga); }
        if constexpr (S == 17 && l < 5 && i == 1) pf5(L);
        if constexpr (ks == 17 && cc >= 1) epi(ic<4 * (t / 18) + cc - 1>{}, I);
        if constexpr (t == 18 && cc == 0) epi(ic<3>{}, I);
      });
    });
  });
  STAMP(ws, 4, 5);
  {
    f32x4 g0, g1, u0, u1;
    sfor<18>([&](auto K) {
      constexpr int ks = K.value, n = 144 + ks;
      mm(ic<n>{}, ic<ks>{}, g0, g1, u0, u1, [&](auto I) {
        if constexpr (ks == 0) epi(ic<7>{}, I);
        if constexpr (ks >= 1 && ks <= 4 && I.value == 1) pf5(ic<4 + ks>{});
        if constexpr (ks == 10 && I.value == 1) { wait_vm<4>(); set_flag(c, f4 + 2048 * 64); }
      });
    });
    asm volatile("s_nop 9" : "+v"(g0), "+v"(g1), "+v"(u0), "+v"(u1));
    const f32x4 v0 = (g0 + cg * nmu[0]) * (u0 + cu * nmu[0]), v1 = (g1 + cg * nmu[1]) * (u1 + cu * nmu[1]);
    const unsigned a0 = pk2(v0[0], v0[1]), a1 = pk2(v0[2], v0[3]), b0 = pk2(v1[0], v1[1]), b1 = pk2(v1[2], v1[3]);
    auto s0 = __builtin_amdgcn_permlane32_swap(a0, b0, false, false);
    auto s1 = __builtin_amdgcn_permlane32_swap(a1, b1, false, false);
    const unsigned z = opq(l16);
    bstore16<16>((u32x4){s0[0], s1[0], s0[1], s1[1]}, rhh, (z >> 9) * 144 * 1024 + (z & 511) + 512 * (j & 1), (T0 * 144 + 18 * Qd + 16 + (j >> 1)) * 1024);
    STAMP(ws, 4, 6);
    STAMP_FLUSH(ws, 4);
  }
}

constexpr int k5_E(int i) { int e = 36; if (K5_P + 2 * (i + 1) < e) e = K5_P + 2 * (i + 1); if (i >= 0 && i + 8 < e) e = i + 8; return e; }
static_assert(k5_E(6) <= 16 && k5_E(20) <= 32, "");

DI void p5(const Ctx& c) {
  char* ws = c.ws;
  const int w = c.w, r = c.r, xcd = c.xcd, lane = fresh_lane();
  const int sg = xcd >> 1, mt = 8 * (xcd & 1) + (r >> 2), nt = r & 3;
  const unsigned lds0 = c.lds0;
  const int t16 = lane & 15, g = lane >> 4;
  const int tile = 4 * mt + nt;
  const rsrc_t rpt = mk_rsrc(ws + WS_PT, 64 * 8 * 4 * PT_REC);
  const unsigned f4 = FL4 + (4096 + (r * 8 + xcd) * 8 + w) * 64;
  STAMP_DECL; STAMP(ws, 5, 0);
  if (w >= 4) {
    const int l = w - 4;
    const rsrc_t rwd = mk_rsrc(c.wd, F_ * D_ * 2), rhh = mk_rsrc(ws + WS_HH, 128 * 144 * 1024);
    const rsrc_t rwt = mk_rsrc(ws + WS_WT, 4 * 4608 * 32);
    unsigned mv[2][2];
#pragma unroll
    for (int sp = 0; sp < 2; ++sp)
#pragma unroll
      for (int i = 0; i < 2; ++i) mv[sp][i] = k5_mv(4 * (2 * l + i) + g, sp, nt, t16);
    const unsigned hs = ((8 * mt + 2 * l) * 144 + 36 * sg) * 1024, wsb = sg * 1152 * 1152, dl = lds0 + l * 2048;
    auto issue = [&](auto V) {
      constexpr int v = V.value, sp = v >= 32;
      const int u = k5_u(v, nt);
      constexpr unsigned so = k5_so(v);
      glds16(dl + so + 8192, mv[sp][0], rwd, wsb + u * 32 * 1152);
      glds16c(dl + so, lane * 16, rhh, hs + u * 1024);
      glds16(dl + so + 8192 + 1024, mv[sp][1], rwd, wsb + u * 32 * 1152);
      glds16c(dl + so + 1024, lane * 16, rhh, hs + (144 + u) * 1024);
      if ((v & 3) == l) glds16c(lds0 + so + 16384, lane * 16, rwt, (nt * 144 + 36 * sg + u) * 1024);
    };
    sfor<36>([&](auto I) {
      constexpr int i = I.value, y = k5_E(i - 1) - (i + 2 < 36 ? i + 2 : 36), yy = k5_E(i - 1) - K5_P;
      if constexpr (i == 0) wait_vm<2>();
      else if constexpr (i == 2) { wait_vm<4 * yy + yy / 4>(); set_flag(c, f4); }
      else if constexpr (i + 2 > K5_P) wait_vm<4 * y + y / 4>();
      bar();
      if constexpr (i == 0) STAMP(ws, 5, 1);
      if constexpr (i == 18) STAMP(ws, 5, 2);
      sfor<k5_E(i) - k5_E(i - 1)>([&](auto U) { issue(ic<k5_E(i - 1) + U.value>{}); });
    });
    bar();
    STAMP(ws, 5, 3);
    bar();
  } else {
    const int r2 = 4 * g + ((lane >> 2) & 3);
    unsigned wa[2][10];
#pragma unroll
    for (int i = 0; i < 8; ++i) wa[0][i] = lds0 + K5_BASE + 8192 + r2 * 256 + 32 * (i ^ (r2 & 7)) + 8 * (lane & 3);
    wa[0][8] = lds0 + K5_BASE + 16384 + r2 * 32 + 8 * (lane & 3);
    wa[0][9] = lds0 + K5_BASE + w * 2048 + lane * 16;
#pragma unroll
    for (int i = 0; i < 10; ++i) { wa[1][i] = wa[0][i] + 4 * K5_SLOT; PIN(wa[0][i]); PIN(wa[1][i]); }
    const unsigned fo = k5_fl(lane & 31, sg, mt);
    f32x4 acc[9][2];
    bf16x4 fr[3][3][2];
    bf16x8 hb[2][2];
    u32x2 pf;
    auto rd1 = [&](auto N, auto I) {
      constexpr int n = N.value, u = n / 3, i = I.value;
      if constexpr (u < 36) {
        constexpr int pos = (u + 7) & 7, hi = pos >> 2, cc = 3 * (n % 3) + i / 2;
        constexpr unsigned so = (pos & 3) * K5_SLOT;
        if constexpr (i >= 6) hb[u & 1][i - 6] = ldsr<bf16x8>(wa[hi][9] + so + (i - 6) * 1024);
        else fr[n % 3][i / 2][i & 1] = trh(wa[hi][cc] + so + (i & 1) * (cc < 8 ? 4096 : 512));
      }
    };
    sfor<36>([&](auto U) {
      constexpr int u = U.value;
      if constexpr (u == 0) wait_vm<2>();
      if constexpr (u == 4) { wait_vm<0>(); set_flag(c, f4); }
      if constexpr (u == 4 || u == 18) bload8c(pf, fo, fl_rsrc(c), FL4 + (u == 4 ? 1 : 2) * 2048 * 64);
      if constexpr (u == 7 || u == 21) {
        asm volatile("s_waitcnt vmcnt(0)" : "+v"(pf)::"memory");
        if (__builtin_expect(!tag_ok(c, pf), 0)) poll(c, fo, FL4 + (u == 7 ? 1 : 2) * 2048 * 64);
      }
      bar();
      if constexpr (u == 0) STAMP(ws, 5, 1);
      if constexpr (u == 18) STAMP(ws, 5, 2);
      if constexpr (u == 0) { rd1(ic<0>{}, ic<6>{}); rd1(ic<0>{}, ic<7>{}); sfor<6>([&](auto I) { rd1(ic<0>{}, I); }); sfor<6>([&](auto I) { rd1(ic<1>{}, I); }); }
      sfor<18>([&](auto M) {
        constexpr int mi = M.value, v = mi / 6, n = 3 * u + v, cc = mi / 2;
        if constexpr (u == 0) mfma16_z(acc[cc][mi & 1], cat(fr[n % 3][cc % 3][0], fr[n % 3][cc % 3][1]), hb[u & 1][mi & 1]);
        else mfma16_ip(acc[cc][mi & 1], cat(fr[n % 3][cc % 3][0], fr[n % 3][cc % 3][1]), hb[u & 1][mi & 1]);
        SBAR();
        if constexpr (mi == 6 || mi == 8) rd1(ic<n + 2>{}, ic<6 + (mi - 6) / 2>{});
        rd1(ic<n + 2>{}, ic<mi % 6>{});
        SBAR();
      });
    });
    asm volatile("s_nop 9" : "+v"(acc[7][0]), "+v"(acc[7][1]), "+v"(acc[8][0]), "+v"(acc[8][1]));
#pragma unroll
    for (int i = 0; i < 7; ++i) asm volatile("" : "+v"(acc[i][0]), "+v"(acc[i][1]));
    bar();
    STAMP(ws, 5, 3);
    if (w == sg) {
      sfor<2>([&](auto T) { sfor<9>([&](auto C) { ldsw<f32x4>(lds0 + (T.value * 9 + C.value) * 1024 + lane * 16, acc[C.value][T.value]); }); });
      lds_bar();
    } else {
      sfor<2>([&](auto T) {
        const unsigned pb = (((tile * 4 + w) * 4 + sg) * 2 + T.value) * PT_REC;
        sfor<4>([&](auto I) {
          constexpr int i = I.value;
          const f32x4 a = acc[2 * i][T.value], b = acc[2 * i + 1][T.value];
          bstore16<16>((u32x4){pk2(a[0], a[1]), pk2(a[2], a[3]), pk2(b[0], b[1]), pk2(b[2], b[3])}, rpt, lane * 16 + i * 1024, pb);
        });
        const f32x4 a = acc[8][T.value];
        bstore8<16>((u32x2){pk2(a[0], a[1]), pk2(a[2], a[3])}, rpt, lane * 8 + 4096, pb);
      });
      bar();
      wait_vm<0>();
      set_flag(c, FL5 + (tile * 4 + w) * 256 + sg * FS5);
    }
  }
  const int T = w >> 2, pi = w & 3, tt = 8 * mt + 2 * sg + T;
  const rsrc_t rhf = mk_rsrc(ws + WS_HF, 128 * 18 * 1024);
  u32x4 hv, pv[3];
  u32x2 hl, pl[3];
  unsigned rs2u;
  STAMP(ws, 5, 4);
  bload16c(hv, lane * 16, rhf, (tt * 18 + 4 * nt + pi) * 1024);
  bload8c(hl, lane * 16 + 8 * (nt & 1), rhf, (tt * 18 + 16 + (nt >> 1)) * 1024);
  bload4c(rs2u, t16 * 4, mk_rsrc(ws + WS_RS, 4 * 2048 * 4), (sg * 2048 + 16 * tt) * 4);
  poll(c, ((sg + 1 + min(lane, 2)) & 3) * FS5, FL5 + (tile * 4 + sg) * 256);
  STAMP(ws, 5, 5);
#pragma unroll
  for (int o = 0; o < 3; ++o) {
    const unsigned so = (((tile * 4 + sg) * 4 + ((sg + 1 + o) & 3)) * 2 + T) * PT_REC;
    bload16c(pv[o], lane * 16 + pi * 1024, rpt, so);
    bload8c(pl[o], pi == 0 ? lane * 8 + 4096 : OOB, rpt, so);
  }
  const unsigned orow = (16 * tt + t16) * 1152, own = lds0 + T * 9216 + lane * 16;
  const f32x4 o0 = ldsr<f32x4>(own + pi * 2048), o1 = ldsr<f32x4>(own + pi * 2048 + 1024), o2 = ldsr<f32x4>(own + 8192);
  asm volatile("s_waitcnt vmcnt(0)" : "+v"(hv), "+v"(hl), "+v"(rs2u), "+v"(pv[0]), "+v"(pv[1]), "+v"(pv[2]), "+v"(pl[0]), "+v"(pl[1]), "+v"(pl[2])::"memory");
  STAMP(ws, 5, 6);
  const float rs2 = __uint_as_float(rs2u);
  const rsrc_t rout = mk_rsrc(c.out, S_ * D_ * 2);
  auto fin = [&](const f32x4& ow, unsigned a0, unsigned a1, unsigned b0, unsigned b1, unsigned c0, unsigned c1, unsigned h0, unsigned h1, unsigned& d0, unsigned& d1) {
    const float v0 = ((ow[0] + bflo(b0)) + (bflo(a0) + bflo(c0))) * rs2 + bflo(h0);
    const float v1 = ((ow[1] + bfhi(b0)) + (bfhi(a0) + bfhi(c0))) * rs2 + bfhi(h0);
    const float v2 = ((ow[2] + bflo(b1)) + (bflo(a1) + bflo(c1))) * rs2 + bflo(h1);
    const float v3 = ((ow[3] + bfhi(b1)) + (bfhi(a1) + bfhi(c1))) * rs2 + bfhi(h1);
    d0 = pk2(v0, v1); d1 = pk2(v2, v3);
  };
  {
    unsigned a0, a1, b0, b1;
    fin(o0, pv[0][0], pv[0][1], pv[1][0], pv[1][1], pv[2][0], pv[2][1], hv[0], hv[1], a0, a1);
    fin(o1, pv[0][2], pv[0][3], pv[1][2], pv[1][3], pv[2][2], pv[2][3], hv[2], hv[3], b0, b1);
    auto s0 = __builtin_amdgcn_permlane16_swap(a0, b0, false, false);
    auto s1 = __builtin_amdgcn_permlane16_swap(a1, b1, false, false);
    bstore16<0>((u32x4){s0[0], s1[0], s0[1], s1[1]}, rout, orow + 256 * nt + 64 * pi + 32 * (g & 1) + 16 * (g >> 1), 0);
  }
  {
    unsigned e0, e1;
    fin(o2, pl[0][0], pl[0][1], pl[1][0], pl[1][1], pl[2][0], pl[2][1], hl[0], hl[1], e0, e1);
    bstore8<0>((u32x2){e0, e1}, rout, pi == 0 ? orow + 1024 + 32 * nt + 8 * g : OOB, 0);
  }
  wait_vm<0>();
  STAMP(ws, 5, 7);
  STAMP_FLUSH(ws, 5);
}

extern "C" __global__ __launch_bounds__(512, 2) void k1_all(const void* x, const void* wqkv, const void* wo, const void* wg, const void* wu, const void* wd, char* ws, void* out) {
  unsigned long long te_; asm volatile("s_memrealtime %0" : "=s"(te_)); SBAR();
  Ctx c = {x, wqkv, wo, wg, wu, wd, ws, out, (int)rfl(threadIdx.x >> 6), (int)(blockIdx.x & 7), (int)(blockIdx.x >> 3), (unsigned)(size_t)smem, 0u, 0u, te_};
  if (c.w == 7) {
    unsigned long long pc; unsigned back, size;
    asm volatile("s_getpc_b64 %0\n.Lk1pc%=:\n\ts_mov_b32 %1, .Lk1pc%=-k1_all\n\ts_mov_b32 %2, k9_end-k1_all" : "=s"(pc), "=s"(back), "=s"(size));
    const int lane = fresh_lane();
    warm(c.lds0 + LDS_SCR, lane < 24 ? (c.r * 24 + lane) * 128 : OOB, mk_rsrc((const void*)(pc - back), size), 0);
  }
  {
    const int mt = 4 * (c.xcd & 3) + (c.r & 3), nt = 8 * (c.xcd >> 2) + (c.r >> 2);
    if (c.w < 4) p1_body<0>(c, mt, nt); else p1_body<1>(c, mt, nt);
  }
  p2(c);
  if (c.r < 18) p3_gemm(c); else { cnt_wait(c, 1, 8); p3_side(c); }
  if (c.w < 4) k4_first<0>(c, fresh_lane());
  else { cnt_wait(c, 1, 8); k4_first<4>(c, fresh_lane()); }
  p4(c);
  p5(c);
  STAMP_OUT();
}

extern "C" __global__ void k9_end() {}

extern "C" __global__ __launch_bounds__(256) void k6_diag(char* ws) {
  __shared__ unsigned ra[256];
  const int b = threadIdx.x;
  unsigned long long t0 = __builtin_amdgcn_s_memrealtime();
  unsigned a = 0;
  const int nbb = KB == 3 ? 144 : 256;
  for (int w = W0; w < W1; ++w) if (b < nbb) {
    const GAS unsigned* p = (const GAS unsigned*)(ws + WS_DBG + ((((size_t)KB * 256 + b) * 8 + w) * 16 + 8 * CYC) * 4);
    a += p[JB] - p[IB];
  }
  ra[b] = a;
  __syncthreads();
  if (b == 0) {
    unsigned long long d = 0;
    for (int i = 0; i < 256; ++i) d += ra[i];
    if (!CYC) d *= 10;
    d /= (unsigned long long)nbb * (W1 - W0);
    if (d > 100000) d = 100000;
    while (__builtin_amdgcn_s_memrealtime() - t0 < d + 300) __builtin_amdgcn_s_sleep(2);
  }
}

extern "C" void kernel_launch(void* const* d_in, const int* in_sizes, int n_in, void* d_out, int out_size, void* d_ws, size_t ws_size,
                              hipStream_t stream) {
  (void)hipFuncSetAttribute((const void*)k1_all, hipFuncAttributeMaxDynamicSharedMemorySize, LDS_ALL);
  k1_all<<<256, 512, LDS_ALL, stream>>>(d_in[0], d_in[1], d_in[2], d_in[4], d_in[5], d_in[6], (char*)d_ws, d_out);
  k6_diag<<<1, 256, 0, stream>>>((char*)d_ws);
}
```

```cpp
#include <hip/hip_runtime.h>
#include <stdint.h>

typedef short bf16x8 __attribute__((ext_vector_type(8)));
typedef short bf16x4 __attribute__((ext_vector_type(4)));
typedef float f32x2 __attribute__((ext_vector_type(2)));
typedef float f32x4 __attribute__((ext_vector_type(4)));
typedef float f32x16 __attribute__((ext_vector_type(16)));
typedef unsigned u32x2 __attribute__((ext_vector_type(2)));
typedef unsigned u32x4 __attribute__((ext_vector_type(4)));
typedef __bf16 bf2_t __attribute__((ext_vector_type(2)));
typedef __amdgpu_buffer_rsrc_t rsrc_t;
#define LAS __attribute__((address_space(3)))
#define GAS __attribute__((address_space(1)))
#define DI __device__ __forceinline__

constexpr int S_ = 2048, D_ = 576, F_ = 4608;
constexpr float QSCALE = 0.23570226039551584f * 1.4426950408889634f;
constexpr unsigned ONES2 = 0x3f803f80u;

constexpr size_t QREC = 1280, KVREC = 2560;
constexpr unsigned OOB = 0x7fffff00u;
#define FS1 64
#define FS2 64
#define FS3 64
#define FS5 64
constexpr unsigned FL1 = 128;
constexpr unsigned FL2 = FL1 + 256 * 64;
constexpr unsigned FL3 = FL2 + 2048 * 64;
constexpr unsigned FLS = FL3 + 2048 * 64;
constexpr unsigned FL4 = FLS + 128 * 64;
constexpr unsigned FL5 = FL4 + 3 * 2048 * 64;
constexpr unsigned FL_END = FL5 + 1024 * 64;
constexpr size_t WS_QF = 1u << 20;
constexpr size_t WS_KV = WS_QF + 32 * 64 * QREC;
constexpr size_t WS_OF = WS_KV + 32 * 64 * KVREC;
constexpr size_t WS_HF = WS_OF + 128 * 18 * 1024;
constexpr size_t WS_ST = WS_HF + 128 * 18 * 1024;
constexpr size_t WS_CS = WS_ST + 9 * 2048 * 8;
constexpr size_t WS_RS = WS_CS + 2 * 4608 * 4;
constexpr size_t WS_LG = WS_RS + 4 * 2048 * 4;
constexpr size_t WS_WT = WS_LG + 8 * 4 * 36864;
constexpr size_t WS_HH = WS_WT + 4 * 4608 * 32;
constexpr size_t PT_REC = 4608;
constexpr size_t WS_PT = WS_HH + (size_t)128 * 144 * 1024;
constexpr size_t WS_END = WS_PT + (size_t)64 * 8 * 4 * PT_REC;

DI unsigned rfl(unsigned v) { return __builtin_amdgcn_readfirstlane(v); }
DI rsrc_t mk_rsrc(const void* p, unsigned bytes) { return __builtin_amdgcn_make_buffer_rsrc((void*)p, (short)0, (int)bytes, 0x00020000); }
DI void glds16(unsigned lds, unsigned voff, rsrc_t r, unsigned soff) {
  asm volatile("s_mov_b32 m0, %0\n\ts_nop 0\n\tbuffer_load_dwordx4 %1, %2, %3 offen lds" ::"s"(lds), "v"(voff), "s"(r), "s"(soff) : "memory", "m0");
}
DI void glds16c(unsigned lds, unsigned voff, rsrc_t r, unsigned soff) {
  asm volatile("s_mov_b32 m0, %0\n\ts_nop 0\n\tbuffer_load_dwordx4 %1, %2, %3 offen sc1 lds" ::"s"(lds), "v"(voff), "s"(r), "s"(soff) : "memory", "m0");
}
template <class T> DI void bload16c(T& d, unsigned voff, rsrc_t r, unsigned soff) {
  asm volatile("buffer_load_dwordx4 %0, %1, %2, %3 offen sc1" : "=v"(d) : "v"(voff), "s"(r), "s"(soff) : "memory");
}
DI void bload8c(u32x2& d, unsigned voff, rsrc_t r, unsigned soff) {
  asm volatile("buffer_load_dwordx2 %0, %1, %2, %3 offen sc1" : "=v"(d) : "v"(voff), "s"(r), "s"(soff) : "memory");
}
DI void bload4c(unsigned& d, unsigned voff, rsrc_t r, unsigned soff) {
  asm volatile("buffer_load_dword %0, %1, %2, %3 offen sc1" : "=v"(d) : "v"(voff), "s"(r), "s"(soff) : "memory");
}
DI void bload8(u32x2& d, unsigned voff, rsrc_t r, unsigned soff) {
  asm volatile("buffer_load_dwordx2 %0, %1, %2, %3 offen" : "=v"(d) : "v"(voff), "s"(r), "s"(soff) : "memory");
}
DI void warm(unsigned lds, unsigned voff, rsrc_t r, unsigned soff) {
  asm volatile("s_mov_b32 m0, %0\n\ts_nop 0\n\tbuffer_load_dword %1, %2, %3 offen lds" ::"s"(lds), "v"(voff), "s"(r), "s"(soff) : "memory", "m0");
}
template <int N> DI void wait_vm() { asm volatile("s_waitcnt vmcnt(%0)" ::"i"(N) : "memory"); }
#define PIN(x) asm volatile("" : "+v"(x))
#define SBAR() __builtin_amdgcn_sched_barrier(0)
DI void bar() { SBAR(); asm volatile("s_barrier" ::: "memory"); SBAR(); }
DI void lds_bar() { SBAR(); asm volatile("s_waitcnt lgkmcnt(0)\n\ts_barrier" ::: "memory"); SBAR(); }
DI bf16x8 trfrag(unsigned a0, unsigned a1) {
  bf16x4 lo = __builtin_amdgcn_ds_read_tr16_b64_v4i16((LAS bf16x4*)(size_t)a0);
  bf16x4 hi = __builtin_amdgcn_ds_read_tr16_b64_v4i16((LAS bf16x4*)(size_t)a1);
  return __builtin_shufflevector(lo, hi, 0, 1, 2, 3, 4, 5, 6, 7);
}
DI bf16x4 trh(unsigned a) { return __builtin_amdgcn_ds_read_tr16_b64_v4i16((LAS bf16x4*)(size_t)a); }
DI bf16x8 cat(bf16x4 lo, bf16x4 hi) { return __builtin_shufflevector(lo, hi, 0, 1, 2, 3, 4, 5, 6, 7); }
template <class T> DI T ldsr(unsigned a) { return *(LAS T*)(size_t)a; }
template <class T> DI void ldsw(unsigned a, T v) { *(LAS T*)(size_t)a = v; }
DI unsigned pk2(float a, float b) { f32x2 v = {a, b}; return __builtin_bit_cast(unsigned, __builtin_convertvector(v, bf2_t)); }
DI float bflo(unsigned u) { return __uint_as_float(u << 16); }
DI float bfhi(unsigned u) { return __uint_as_float(u & 0xffff0000u); }
DI f32x4 mfma16(bf16x8 a, bf16x8 b, f32x4 c) { return __builtin_amdgcn_mfma_f32_16x16x32_bf16(a, b, c, 0, 0, 0); }
DI void mfma16_ip(f32x4& c, bf16x8 a, bf16x8 b) { asm volatile("v_mfma_f32_16x16x32_bf16 %0, %1, %2, %0" : "+v"(c) : "v"(a), "v"(b)); }
DI void mfma16_z(f32x4& c, bf16x8 a, bf16x8 b) { asm volatile("v_mfma_f32_16x16x32_bf16 %0, %1, %2, 0" : "=&v"(c) : "v"(a), "v"(b)); }
DI f32x16 mfma32(bf16x8 a, bf16x8 b, f32x16 c) { return __builtin_amdgcn_mfma_f32_32x32x16_bf16(a, b, c, 0, 0, 0); }
DI float xor32(float v) { auto r = __builtin_amdgcn_permlane32_swap(__float_as_uint(v), __float_as_uint(v), false, false); return __uint_as_float((threadIdx.x & 32) ? r[0] : r[1]); }
DI float sum_x32(float v) { auto r = __builtin_amdgcn_permlane32_swap(__float_as_uint(v), __float_as_uint(v), false, false); return __uint_as_float(r[0]) + __uint_as_float(r[1]); }
DI float max_x32(float v) { auto r = __builtin_amdgcn_permlane32_swap(__float_as_uint(v), __float_as_uint(v), false, false); return fmaxf(__uint_as_float(r[0]), __uint_as_float(r[1])); }
DI float sum_x16(float v) { auto r = __builtin_amdgcn_permlane16_swap(__float_as_uint(v), __float_as_uint(v), false, false); return __uint_as_float(r[0]) + __uint_as_float(r[1]); }
template <int AUX> DI void bstore16(u32x4 v, rsrc_t r, unsigned voff, unsigned soff) {
  if constexpr (AUX == 16) asm volatile("buffer_store_dwordx4 %0, %1, %2, %3 offen sc1\n\ts_nop 1" ::"v"(v), "v"(voff), "s"(r), "s"(soff) : "memory");
  else asm volatile("buffer_store_dwordx4 %0, %1, %2, %3 offen\n\ts_nop 1" ::"v"(v), "v"(voff), "s"(r), "s"(soff) : "memory");
}
template <int AUX> DI void bstore8(u32x2 v, rsrc_t r, unsigned voff, unsigned soff) { __builtin_amdgcn_raw_buffer_store_b64(v, r, voff, soff, AUX); }
template <int AUX> DI void bstore4(unsigned v, rsrc_t r, unsigned voff, unsigned soff) { __builtin_amdgcn_raw_buffer_store_b32(v, r, voff, soff, AUX); }

template <int... I> struct iseq {};
template <int N, int... I> struct mkseq : mkseq<N - 1, N - 1, I...> {};
template <int... I> struct mkseq<0, I...> { typedef iseq<I...> type; };
template <int V> struct ic { static constexpr int value = V; };
template <class Fn, int... I> DI void sfor_(Fn&& f, iseq<I...>) { (f(ic<I>{}), ...); }
template <int N, class Fn> DI void sfor(Fn&& f) { sfor_(f, typename mkseq<N>::type{}); }

extern __shared__ __attribute__((aligned(16))) char smem[];
#define STAMP(ws, kid, i)
#define STAMP_DECL
#define STAMP_FLUSH(ws, kid)
#define STAMP_OUT()

struct Ctx {
  const void *x, *wqkv, *wo, *wg, *wu, *wd;
  char* ws; void* out;
  int w, xcd, r;
  unsigned lds0, t0, t1;
};
constexpr int LDS_ALL = 160 * 1024, LDS_SCR = LDS_ALL - 256;
DI int fresh_lane() { int l; asm volatile("v_mbcnt_lo_u32_b32 %0, -1, 0\n\tv_mbcnt_hi_u32_b32 %0, -1, %0" : "=v"(l)); __builtin_assume(l >= 0 && l < 64); return l; }
DI unsigned opq(unsigned v) { PIN(v); return v; }
constexpr int LDS_CNT = 147456;
DI void cnt_add(const Ctx& c, int i) {
  asm volatile("s_mov_b64 exec, 1\n\tds_add_u32 %0, %1\n\ts_mov_b64 exec, -1" ::"v"(c.lds0 + LDS_CNT + 4 * i), "v"(1u) : "memory");
}
DI unsigned cnt_get(const Ctx& c, int i) {
  unsigned v;
  asm volatile("ds_read_b32 %0, %1\n\ts_waitcnt lgkmcnt(0)" : "=v"(v) : "v"(c.lds0 + LDS_CNT + 4 * i) : "memory");
  return rfl(v);
}
DI void cnt_wait(const Ctx& c, int i, unsigned n) {
  if (__builtin_expect(cnt_get(c, i) < n, 0))
    for (int spin = 0; spin < (1 << 22) && cnt_get(c, i) < n; ++spin) __builtin_amdgcn_s_sleep(1);
}
DI rsrc_t fl_rsrc(const Ctx& c) { return mk_rsrc(c.ws, FL_END); }
DI void set_flag(const Ctx& c, unsigned off) {
  u32x2 t;
  asm volatile("v_mov_b32 %0, %1" : "=v"(t[0]) : "s"(c.t0));
  asm volatile("v_mov_b32 %0, %1" : "=v"(t[1]) : "s"(c.t1));
  asm volatile("s_mov_b64 exec, 1\n\tbuffer_store_dwordx2 %0, off, %1, %2 sc1\n\ts_mov_b64 exec, -1" ::"v"(t), "s"(fl_rsrc(c)), "s"(off) : "memory");
}
DI bool tag_ok(const Ctx& c, u32x2 v) { return __builtin_amdgcn_ballot_w64(v[0] != c.t0 || v[1] != c.t1) == 0; }
DI void poll(const Ctx& c, unsigned voff, unsigned soff) {
  for (int spin = 0; spin < (1 << 20); ++spin) {
    u32x2 v;
    asm volatile("buffer_load_dwordx2 %0, %1, %2, %3 offen sc1\n\ts_waitcnt vmcnt(0)" : "=&v"(v) : "v"(voff), "s"(fl_rsrc(c)), "s"(soff) : "memory");
    if (tag_ok(c, v)) break;
  }
}

DI bool poll_first(const Ctx& c, unsigned voff, unsigned soff, unsigned long long first) {
  unsigned long long m = 0;
  for (int spin = 0; spin < (1 << 20); ++spin) {
    u32x2 v;
    asm volatile("buffer_load_dwordx2 %0, %1, %2, %3 offen sc1\n\ts_waitcnt vmcnt(0)" : "=&v"(v) : "v"(voff), "s"(fl_rsrc(c)), "s"(soff) : "memory");
    m = __builtin_amdgcn_ballot_w64(v[0] != c.t0 || v[1] != c.t1);
    if ((m & first) == 0) break;
  }
  return m == 0;
}

constexpr int K1_SLOT = 32768, K1_TBL = K1_SLOT * 4;
extern "C" __global__ void k9_end();

template <int NN> DI void p1_body(Ctx& c, int mt, int nt) {
  char* ws = c.ws;
  const int w = c.w, lane = fresh_lane();
  const unsigned lds0 = c.lds0;
  const rsrc_t rx = mk_rsrc(c.x, S_ * D_ * 2), rw = mk_rsrc(c.wqkv, D_ * 3 * D_ * 2);
  const int t16 = lane & 15, g = lane >> 4, q4 = (lane >> 2) & 3, p = lane & 3, m = w & 3;
  constexpr int NT = NN ? 3 : 4;
  unsigned xv[2], wv[2];
#pragma unroll
  for (int i = 0; i < 2; ++i) {
    const int row = 8 * (2 * w + i) + (lane >> 3);
    xv[i] = (128 * mt + row) * 1152 + 16 * ((lane & 7) ^ ((row >> 1) & 7));
    int li = 64 * (2 * w + i) + lane;
    if (li >= 896) li -= 128;
    const int r64 = (li * 4682) >> 16, cc = li - 14 * r64;
    const int rho = r64 & 31, k = (r64 & 32) + 8 * ((rho >> 2) & 3) + 4 * (rho >> 4) + (rho & 3);
    wv[i] = k * 3456 + ((216 * nt) & ~15) + 16 * cc;
  }
  STAMP_DECL; STAMP(ws, 1, 0);
  auto issue1 = [&](int slab, int i) {
    const unsigned dst = lds0 + (slab & 3) * K1_SLOT + w * 2048 + (i >> 1) * 1024;
    if (i & 1) glds16(dst + 16384, wv[i >> 1], rw, slab * 64 * 3456);
    else glds16(dst, xv[i >> 1], rx, slab * 128);
  };
#pragma unroll
  for (int sl = 0; sl < 2; ++sl)
#pragma unroll
    for (int i = 0; i < 4; ++i) issue1(sl, i);
  u32x2 ev;
  bload8c(ev, 0u, fl_rsrc(c), 0);
  STAMP(ws, 1, 1);
  const unsigned wa = lds0 + 16384 + (4 * g + q4) * 224 + (nt & 1) * 8;
  unsigned ad[2][5];
  ad[0][0] = wa + 8 * p + 144 * NN;
  ad[0][1] = wa + 72 * (p < 3 ? p : 2) + 32;
  ad[0][2] = NN && m >= 2 ? ad[0][1] : ad[0][0] + 72 * (m >> 1) + 40 * (m & 1);
  ad[0][3] = lds0 + (32 * m + t16) * 128 + 16 * (g ^ ((t16 >> 1) & 7));
  ad[0][4] = ad[0][3] ^ 64;
#pragma unroll
  for (int i = 0; i < 5; ++i) { ad[1][i] = ad[0][i] + 2 * K1_SLOT; PIN(ad[1][i]); }
  const unsigned tb = lds0 + K1_TBL, cb = tb + 1024, dummy = cb + 512;
  unsigned e_cs = t16 == 0 ? cb + (w * 16 + 4 * g) * 4 : dummy, e_st = tb + (32 * m + t16) * 8, e_cc = cb + (64 * NN + 4 * g) * 4;
  unsigned e_s1 = g == 3 ? e_st : dummy, e_s2 = g == q4 ? e_st + 4 : dummy, e_vo[2], e_so[4], e_t2 = 0, e_one = 0;
  unsigned e_sc[2];
  float e_tsc = 0.f;
  {
    constexpr unsigned KV0 = WS_KV - WS_QF;
    const int tile32 = 4 * mt + m, key = 16 * (g >> 1) + t16;
    const unsigned lo_qk = (g & 1) * 512 + key * 16, lo_v = 1280 + key * 32 + (g & 1) * 16;
#pragma unroll
    for (int i = 0; i < 2 - NN; ++i) {
      const int pg = 3 * nt + 2 * NN + i, type = pg >> 4, rl = type ? KVREC : QREC;
      e_so[2 * i] = (type ? KV0 : 0) + (2 * (pg & 15) * 64 + tile32) * rl;
      e_so[2 * i + 1] = e_so[2 * i] + 64 * rl;
      e_vo[i] = type == 2 ? lo_v : lo_qk;
      e_sc[i] = type ? 0x3f800000u : __builtin_bit_cast(unsigned, QSCALE);
      asm volatile("" : "+s"(e_so[2 * i]), "+s"(e_so[2 * i + 1]), "+s"(e_sc[i]), "+v"(e_vo[i]));
    }
    if constexpr (NN) {
      const int pg = 3 * nt + g, type = pg >> 4, rl = type ? KVREC : QREC;
      e_vo[1] = g < 3 ? (type ? KV0 : 0) + (2 * (pg & 15) * 64 + tile32) * rl + (type == 2 ? 2304 : 1024) + t16 * 8 : OOB;
      e_t2 = e_vo[1] + 64 * rl;
      e_one = type == 1 ? 0u : 0x00003f80u;
      e_tsc = type ? 1.f : QSCALE;
      PIN(e_vo[1]); PIN(e_t2); PIN(e_one); PIN(e_tsc); PIN(e_s1); PIN(e_s2);
    }
    PIN(e_cs); PIN(e_st); PIN(e_cc);
  }
  f32x4 acc[4][2] = {}, cs = {};
  bf16x8 ones = __builtin_bit_cast(bf16x8, (u32x4){ONES2, ONES2, ONES2, ONES2});
  PIN(ones);
  bf16x8 fb[2][2];
  bf16x4 fa[2][NT + 1][2];
  constexpr int NR = 2 * NT + 4;
  auto rd1 = [&](auto KS, auto I) {
    constexpr int ks = KS.value, i = I.value, sp = ks & 1, sl = (ks >> 1) & 3, ti = (i - 2) >> 1;
    constexpr unsigned so = (sl & 1) * K1_SLOT;
    if constexpr (ks < 18 && i < NR) {
      if constexpr (i < 2) fb[sp][i] = ldsr<bf16x8>(ad[sl >> 1][3 + sp] + so + i * 2048);
      else {
        constexpr int ai = ti == NT ? 2 : (NN && ti == 2) ? 1 : 0;
        constexpr unsigned co = ai ? 0 : 72 * (ti >> 1) + 40 * (ti & 1);
        fa[sp][ti][i & 1] = trh(ad[sl >> 1][ai] + so + co + sp * 32 * 224 + (i & 1) * 16 * 224);
      }
    }
  };
  sfor<9>([&](auto SL) {
    constexpr int sl = SL.value;
    constexpr int iss = sl == 0 ? 2 : (sl + 3 < 9 ? sl + 3 : 9);
    constexpr int need = sl == 0 ? 0 : (sl + 1 < 9 ? sl + 1 : 8);
    if constexpr (sl == 1) asm volatile("s_waitcnt vmcnt(%1)" : "+v"(ev) : "i"(4 * (iss - need - 1)) : "memory");
    else wait_vm<4 * (iss - need - 1) + (sl == 0)>();
    bar();
    if constexpr (sl == 0) STAMP(ws, 1, 2);
    if constexpr (sl == 4) STAMP(ws, 1, 3);
    if constexpr (sl <= 1) sfor<NR>([&](auto I) { rd1(ic<2 * sl>{}, I); });
    if constexpr (sl == 1) {
      const unsigned long long e = (((unsigned long long)rfl(ev[1]) << 32) | rfl(ev[0])) + 1ull;
      c.t0 = (unsigned)e; c.t1 = (unsigned)(e >> 32);
    }
    sfor<2>([&](auto SP) {
      constexpr int sp = SP.value, ks = 2 * sl + sp;
      bf16x8 tl;
      if constexpr (NN) tl = t16 >= 12 ? ones : cat(fa[sp][2][0], fa[sp][2][1]);
      sfor<9>([&](auto M) {
        constexpr int mi = M.value, ti = mi >> 1;
        if constexpr (mi == 8) cs = mfma16(cat(fa[sp][NT][0], fa[sp][NT][1]), ones, cs);
        else if constexpr (NN && ti == 3) acc[3][mi & 1] = mfma16(fb[sp][mi & 1], fb[sp][mi & 1], acc[3][mi & 1]);
        else if constexpr (NN && ti == 2) acc[2][mi & 1] = mfma16(tl, fb[sp][mi & 1], acc[2][mi & 1]);
        else acc[ti][mi & 1] = mfma16(cat(fa[sp][ti][0], fa[sp][ti][1]), fb[sp][mi & 1], acc[ti][mi & 1]);
        SBAR();
        if constexpr (ks != 1) {
          if constexpr (mi < 3) { rd1(ic<ks + 1>{}, ic<2 * mi>{}); rd1(ic<ks + 1>{}, ic<2 * mi + 1>{}); }
          else rd1(ic<ks + 1>{}, ic<3 + mi>{});
        }
        if constexpr (sl == 0) { if constexpr (mi & 1) issue1(2 + sp, mi >> 1); }
        else if constexpr (sl + 3 < 9 && (mi == 2 + 2 * NN || mi == 6 + 2 * NN)) issue1(sl + 3, 2 * sp + (mi >= 6));
        SBAR();
      });
    });
  });
  STAMP(ws, 1, 4);
  ldsw<f32x4>(e_cs, cs);
  if constexpr (NN) sfor<2>([&](auto T) {
    const f32x4 gr = acc[3][T.value];
    const float d01 = (p & 1) ? gr[1] : gr[0], d23 = (p & 1) ? gr[3] : gr[2];
    ldsw<float>(e_s1 + 128 * T.value, acc[2][T.value][0]);
    ldsw<float>(e_s2 + 128 * T.value, (p & 2) ? d23 : d01);
  });
  lds_bar();
  f32x2 sv[2];
  f32x4 cc[4];
  sfor<2>([&](auto T) { sv[T.value] = ldsr<f32x2>(e_st + 128 * T.value); });
  sfor<NT>([&](auto I) { cc[I.value] = ldsr<f32x4>(e_cc + 64 * I.value); });
  SBAR();
  float nmu[2], rs[2];
  sfor<2>([&](auto T) {
    nmu[T.value] = sv[T.value][0] * (-1.f / D_);
    rs[T.value] = __builtin_amdgcn_rsqf(fmaxf(sv[T.value][1] * (1.f / D_) - nmu[T.value] * nmu[T.value], 0.f) + 1e-5f);
  });
  const rsrc_t ro = mk_rsrc(ws + WS_QF, 32 * 64 * (QREC + KVREC));
  sfor<(NN ? 2 : 4)>([&](auto TI) {
    constexpr int ti = TI.value;
    const f32x4 v0 = (acc[ti][0] + cc[ti] * nmu[0]) * (rs[0] * __uint_as_float(e_sc[ti >> 1])), v1 = (acc[ti][1] + cc[ti] * nmu[1]) * (rs[1] * __uint_as_float(e_sc[ti >> 1]));
    auto s0 = __builtin_amdgcn_permlane32_swap(pk2(v0[0], v0[1]), pk2(v1[0], v1[1]), false, false);
    auto s1 = __builtin_amdgcn_permlane32_swap(pk2(v0[2], v0[3]), pk2(v1[2], v1[3]), false, false);
    bstore16<16>((u32x4){s0[0], s1[0], s0[1], s1[1]}, ro, e_vo[ti >> 1], e_so[ti]);
  });
  if constexpr (NN) sfor<2>([&](auto T) {
    const f32x4 v = (acc[2][T.value] + cc[2] * nmu[T.value]) * (rs[T.value] * e_tsc);
    bstore8<16>((u32x2){pk2(v[0], v[1]), e_one}, ro, e_vo[1], T.value * 128);
    bstore8<16>((u32x2){pk2(v[2], v[3]), e_one}, ro, e_t2, T.value * 128);
  });
  STAMP(ws, 1, 5);
  wait_vm<0>();
  STAMP(ws, 1, 6);
  bar();
  if (w == 0) set_flag(c, FL1 + (mt * 16 + nt) * FS1);
  STAMP(ws, 1, 7);
  STAMP_FLUSH(ws, 1);
}

constexpr int P3_WO = 73728;
constexpr int K4_RING = 8192;
constexpr unsigned k4_so(int u) { return ((((u >> 1) + 2) & 3) * 2 + (u & 1)) * 8192; }
DI unsigned k4_wv(int lane, int w, int Qd, int j) {
  const int rho = 8 * (w & 3) + (lane >> 3);
  return rho * 9216 + 1152 * Qd + 256 * j + 32 * (((lane & 7) >> 1) ^ ((rho >> 1) & 3)) + 16 * (lane & 1);
}

template <int U0> DI void k4_first(const Ctx& c, int lane) {
  const rsrc_t rg = mk_rsrc(c.wg, D_ * F_ * 2), ru = mk_rsrc(c.wu, D_ * F_ * 2);
  const unsigned wv4 = k4_wv(lane, c.w, c.xcd, c.r & 3), dst = c.lds0 + K4_RING + (c.w & 3) * 1024;
  sfor<4>([&](auto U) {
    glds16(dst + k4_so(U0 + U.value), wv4, rg, (U0 + U.value) * 32 * 9216);
    glds16(dst + 4096 + k4_so(U0 + U.value), wv4, ru, (U0 + U.value) * 32 * 9216);
  });
}
DI void p2_ahead(const Ctx& c, unsigned flag) {
  const int w = c.w, r = c.r, lane = fresh_lane();
  const rsrc_t rwo = mk_rsrc(c.wo, D_ * D_ * 2);
  const int nt = r >= 9 ? r - 9 : r;
  const int rho = 8 * w + (lane >> 3), rg = (rho >> 2) & 3, rj = 4 * (rho >> 4) + (rho & 3);
  const unsigned cbyte = 128 * nt + 32 * (((lane & 7) >> 1) ^ ((rho >> 1) & 3)) + 16 * (lane & 1);
  const int th = 4 * rg + (rj >> 1);
  const unsigned wv = (20 * (rg >> 1) + 8 * (rg & 1) + rj) * 1152 + cbyte, wvt = (18 * th + ((th & 1) ? 0 : 16) + (rj & 1)) * 1152 + cbyte;
  sfor<16>([&](auto K) {
    glds16(c.lds0 + P3_WO + (w + 4 * K.value) * 1024, wv, rwo, K.value * 36 * 1152);
    if constexpr (K.value == 7) { wait_vm<8>(); set_flag(c, flag); }
  });
  sfor<2>([&](auto K) { glds16(c.lds0 + P3_WO + (w + 64 + 4 * K.value) * 1024, wvt, rwo, K.value * 288 * 1152); });
}

DI void p2(const Ctx& c) {
  const int head = 4 * c.xcd + (c.r & 3), qb = c.r >> 2;
  char* ws = c.ws;
  const int w = c.w, lane = fresh_lane();
  const unsigned lds0 = c.lds0;
  const int q = lane & 31, h = lane >> 5, G = lane >> 4;
  const int qt = 8 * qb + w;
  const rsrc_t rq = mk_rsrc(ws + WS_QF, 32 * 64 * QREC), rkv = mk_rsrc(ws + WS_KV, 32 * 64 * KVREC);
  STAMP_DECL; STAMP(ws, 2, 0);
  unsigned kb = lds0 + w * KVREC + lane * 16, ktb = lds0 + w * KVREC + 1024 + q * 8;
  unsigned vb[4];
#pragma unroll
  for (int i = 0; i < 4; ++i) { const int row = 8 * i + 4 * (G >> 1) + ((lane >> 2) & 3); vb[i] = lds0 + w * KVREC + ((G & 1) ? 2304 + row * 8 : 1280 + row * 32 + 8 * (lane & 3)); PIN(vb[i]); }
  unsigned mf[16], mk[8];
#pragma unroll
  for (int e = 0; e < 16; ++e) { mf[e] = ((e & 3) + 8 * (e >> 2) <= q - 4 * h) ? ~0u : 0u; PIN(mf[e]); }
#pragma unroll
  for (int e = 0; e < 8; ++e) { mk[e] = (mf[2 * e] & 0xffffu) | (mf[2 * e + 1] & 0xffff0000u); PIN(mk[e]); }
  const unsigned tt = 2 * qt + (q >> 4);
  unsigned om_o = (tt * 18 + (head >> 1)) * 1024 + ((q & 15) + 16 * (2 * (head & 1) + h)) * 16;
  unsigned ot_o = h ? OOB : (tt * 18 + 16 + (head >> 4)) * 1024 + ((q & 15) + 16 * ((head >> 2) & 3)) * 16 + (head & 3) * 4;
  unsigned l16 = lane * 16, qto = h ? OOB : 1024 + q * 8, wmo;
  const int wmv = c.r * 8 + w;
  {
    const int l = (wmv >= 162 ? wmv - 162 : wmv >= 81 ? wmv - 81 : wmv) * 64 + lane, row = (l * 7282) >> 16;
    wmo = wmv >= 162 ? l * 128 : row * 9216 + c.xcd * 1152 + (l - 9 * row) * 128;
    PIN(wmo);
  }
  PIN(kb); PIN(ktb); PIN(om_o); PIN(ot_o); PIN(l16); PIN(qto);
  if (qb == 0) {
#pragma unroll
    for (int i = 0; i < 3; ++i) {
      const unsigned o = min(w * 64 + lane + 512 * i, 1279) * 16, m = (o % KVREC - 1024 < 256) ? 0x0000f149u : 0u;
      ldsw<u32x4>(lds0 + o, (u32x4){0u, m, 0u, m});
    }
  }
#define TSL 0
  if (TSL && w) __builtin_amdgcn_s_sleep(TSL);
  if (w >= 1 && w <= 3) warm(lds0 + LDS_SCR, (w * 64 + lane < 224 ? (w * 64 + lane - 64) * 128 : OOB), rkv, (head * 64 + 8 * qb) * KVREC);
  if (w == 4 || w == 5) warm(lds0 + LDS_SCR, (w * 64 + lane < 336 ? (w * 64 + lane - 256) * 128 : OOB), rq, (head * 64 + 8 * qb) * QREC);
  if (w == 0) {
    const int ty = lane < 4 ? 1 : lane < 8 ? 2 : 0, pm = max(2 * qb + (ty ? (lane & 3) - 2 : (lane & 1)), 0);
    ldsw<unsigned>(lds0 + LDS_CNT + 4 * (lane & 7), 0u);
    poll(c, (pm * 16 + (((16 * ty + (head >> 1)) * 43) >> 7)) * FS1, FL1);
  }
  lds_bar();
  STAMP(ws, 2, 7);
  bf16x8 qm; u32x2 qt2;
#pragma unroll
  for (int i = 0; i < 5; ++i) { const int pc = max(w + 8 * i, qb ? 0 : 20 + w); glds16c(lds0 + pc * 1024, l16, rkv, (head * 64 + 8 * qb - 8) * KVREC + pc * 1024); }
  bload16c(qm, l16, rq, (head * 64 + qt) * QREC);
  bload8c(qt2, qto, rq, (head * 64 + qt) * QREC);
  auto toff = [&](int i) { return (unsigned)(i * KVREC); };
  STAMP(ws, 2, 1);
  asm volatile("s_waitcnt vmcnt(0)" : "+v"(qm), "+v"(qt2)::"memory");
  bar();
  STAMP(ws, 2, 2);
  const bf16x4 qt4 = __builtin_bit_cast(bf16x4, qt2);
  bf16x8 km[9]; bf16x4 kt[9];
  constexpr int ord[9] = {0, 8, 1, 2, 3, 4, 5, 6, 7};
  sfor<9>([&](auto I) { constexpr int i = ord[I.value]; km[i] = ldsr<bf16x8>(kb + toff(i)); kt[i] = ldsr<bf16x4>(ktb + toff(i)); });
  SBAR();
  f32x16 s[9];
  float m4[4];
  auto qk = [&](auto I, auto J) {
    constexpr int i = I.value, j = J.value;
    f32x16 a = mfma32(km[i], qm, (f32x16){}), b;
    if constexpr (j < 9) b = mfma32(km[j], qm, (f32x16){});
    s[i] = __builtin_amdgcn_mfma_f32_32x32x8bf16_1k(kt[i], qt4, a, 0, 0, 0);
    if constexpr (j < 9) s[j] = __builtin_amdgcn_mfma_f32_32x32x8bf16_1k(kt[j], qt4, b, 0, 0, 0);
    SBAR();
  };
  auto mx1 = [&](auto I) {
    constexpr int i = I.value;
#pragma unroll
    for (int e = 0; e < 8; ++e) m4[e & 3] = fmaxf(fmaxf(m4[e & 3], s[i][2 * e]), s[i][2 * e + 1]);
  };
  bf16x4 vf[2][4];
  auto rdv = [&](int set, unsigned off) {
#pragma unroll
    for (int i = 0; i < 4; ++i) vf[set][i] = trh(vb[i] + off);
  };
  qk(ic<0>{}, ic<8>{});
  qk(ic<1>{}, ic<2>{});
  {
    size_t p0 = (size_t)c.wg, p1 = (size_t)c.wu, p2 = (size_t)c.wo;
    asm("" : "+s"(p0), "+s"(p1), "+s"(p2));
    p0 = wmv >= 81 ? p1 : p0;
    warm(lds0 + LDS_SCR, wmo, mk_rsrc((const void*)(wmv >= 162 ? p2 : p0), wmv >= 162 ? D_ * D_ * 2 : D_ * F_ * 2), 0);
  }
  SBAR();
#pragma unroll
  for (int e = 0; e < 16; ++e) s[8][e] = __uint_as_float((__float_as_uint(s[8][e]) & mf[e]) | (__float_as_uint(s[0][e]) & ~mf[e]));
#pragma unroll
  for (int e = 0; e < 4; ++e) asm("v_max_f32 %0, %1, %2\n\tv_max3_f32 %0, %0, %3, %4" : "=&v"(m4[e]) : "v"(s[8][4 * e]), "v"(s[8][4 * e + 1]), "v"(s[8][4 * e + 2]), "v"(s[8][4 * e + 3]));
  SBAR();
  qk(ic<3>{}, ic<4>{});
  mx1(ic<1>{}); mx1(ic<2>{}); SBAR();
  qk(ic<5>{}, ic<6>{});
  mx1(ic<3>{}); mx1(ic<4>{}); SBAR();
  qk(ic<7>{}, ic<9>{});
  rdv(1, toff(1));
  mx1(ic<5>{}); mx1(ic<6>{}); SBAR();
  mx1(ic<7>{});
  float mx = max_x32(fmaxf(fmaxf(m4[0], m4[1]), fmaxf(m4[2], m4[3])));
  STAMP(ws, 2, 3);
  f32x16 o = {};
  u32x4 pp;
  sfor<16>([&](auto H) {
    constexpr int hh = H.value, i = 1 + hh / 2, ks = hh & 1;
    f32x2 dd[4];
#pragma unroll
    for (int e = 0; e < 4; ++e) dd[e] = (f32x2){s[i][8 * ks + 2 * e], s[i][8 * ks + 2 * e + 1]} - (f32x2){mx, mx};
    SBAR();
    if constexpr (hh > 0 && hh < 15) o = mfma32(cat(vf[(1 + (hh - 1) / 2) & 1][2 * (ks ^ 1)], vf[(1 + (hh - 1) / 2) & 1][2 * (ks ^ 1) + 1]), __builtin_bit_cast(bf16x8, pp), o);
    if constexpr (hh == 15) {
      const u32x4 pd = {pp[0] & mk[0], pp[1] & mk[1], pp[2] & mk[2], pp[3] & mk[3]};
      o = mfma32(cat(vf[0][0], vf[0][1]), __builtin_bit_cast(bf16x8, pd), o);
      o = mfma32(cat(vf[1][0], vf[1][1]), __builtin_bit_cast(bf16x8, pp ^ pd), o);
    }
    SBAR();
    if constexpr (ks == 0) rdv((i + 1) & 1, toff(i < 8 ? i + 1 : 0));
    SBAR();
    float pv[8];
#pragma unroll
    for (int e = 0; e < 4; ++e) { pv[2 * e] = __builtin_amdgcn_exp2f(dd[e][0]); pv[2 * e + 1] = __builtin_amdgcn_exp2f(dd[e][1]); }
    SBAR();
    pp = (u32x4){pk2(pv[0], pv[1]), pk2(pv[2], pv[3]), pk2(pv[4], pv[5]), pk2(pv[6], pv[7])};
    SBAR();
  });
  {
    const u32x4 pd = {pp[0] & mk[4], pp[1] & mk[5], pp[2] & mk[6], pp[3] & mk[7]};
    o = mfma32(cat(vf[0][2], vf[0][3]), __builtin_bit_cast(bf16x8, pd), o);
    o = mfma32(cat(vf[1][2], vf[1][3]), __builtin_bit_cast(bf16x8, pp ^ pd), o);
  }
  STAMP(ws, 2, 4);
  auto lr = __builtin_amdgcn_permlane32_swap(__float_as_uint(o[10]), __float_as_uint(o[10]), false, false);
  const float inv = __builtin_amdgcn_rcpf(__uint_as_float(lr[0]));
  const rsrc_t ro = mk_rsrc(ws + WS_OF, 128 * 18 * 1024);
  const u32x4 om = {pk2(o[0] * inv, o[1] * inv), pk2(o[2] * inv, o[3] * inv), pk2(o[4] * inv, o[5] * inv), pk2(o[6] * inv, o[7] * inv)};
  bstore16<16>(om, ro, om_o, 0);
  bstore4<16>(pk2(o[8] * inv, o[9] * inv), ro, ot_o, 0);
  STAMP(ws, 2, 5);
  cnt_add(c, 1);
  if (w < 4 && c.r < 18) p2_ahead(c, FL2 + (qt * 32 + head) * FS2);
  else { wait_vm<0>(); set_flag(c, FL2 + (qt * 32 + head) * FS2); }
  STAMP(ws, 2, 6);
  STAMP_FLUSH(ws, 2);
}

DI void p3_side(const Ctx& c) {
  char* ws = c.ws;
  const int w = c.w, xcd = c.xcd, lane = fresh_lane();
  const unsigned lds0 = c.lds0;
  const int sb = c.r - 18, blk = sb * 8 + xcd;
  const rsrc_t rwt = mk_rsrc(ws + WS_WT, 4 * 4608 * 32), rlg = mk_rsrc(ws + WS_LG, 8 * 4 * 36864), rcs = mk_rsrc(ws + WS_CS, 2 * 4608 * 4);
  STAMP_DECL; STAMP(ws, 3, 0);
  const int mat = sb >= 5, un = sb < 5 ? 2 * sb : sb - 5;
  const rsrc_t rw = mk_rsrc(mat ? c.wu : c.wg, D_ * F_ * 2);
  const unsigned vo = (lane & 7) * 16 + (72 * w + (lane >> 3)) * 9216;
  u32x4 v[2][9];
#pragma unroll
  for (int i = 0; i < 9; ++i) v[0][i] = __builtin_amdgcn_raw_buffer_load_b128(rw, vo, (9 * xcd + un) * 128 + i * 8 * 9216, 0);
#pragma unroll
  for (int i = 0; i < 9; ++i) v[1][i] = __builtin_amdgcn_raw_buffer_load_b128(rw, sb < 4 ? vo : OOB, (9 * xcd + un + 1) * 128 + i * 8 * 9216, 0);
  const int drow = 64 * (blk - 40) + 8 * w + (lane >> 3);
  const u32x4 dv = __builtin_amdgcn_raw_buffer_load_b128(mk_rsrc(c.wd, F_ * D_ * 2), mat ? drow * 1152 + 1024 + (lane & 7) * 16 : OOB, 0, 0);
  if (un == 8) {
    const unsigned lo = ((lane & 7) >> 1) * 36864 + (72 * w + (lane >> 3)) * 32 + (lane & 1) * 16;
#pragma unroll
    for (int i = 0; i < 9; ++i) bstore16<16>(v[0][i], rlg, lo, (xcd * 8 + (sb >> 3)) * 18432 + i * 256);
  }
  const int pr = (drow % 576) >= 512 ? ((drow & ~24) | ((drow & 8) << 1) | ((drow & 16) >> 1)) : drow;
  bstore16<16>(dv, rwt, mat ? (((lane & 7) >> 1) * 4608 + pr) * 32 + (lane & 1) * 16 : OOB, 0);
  float a[2][8] = {};
#pragma unroll
  for (int k = 0; k < 2; ++k) {
#pragma unroll
    for (int i = 0; i < 9; ++i)
#pragma unroll
      for (int e = 0; e < 4; ++e) { a[k][2 * e] += bflo(v[k][i][e]); a[k][2 * e + 1] += bfhi(v[k][i][e]); }
#pragma unroll
    for (int e = 0; e < 8; ++e) a[k][e] = sum_x32(sum_x16(a[k][e]));
  }
  if (lane < 16) {
#pragma unroll
    for (int k = 0; k < 2; ++k) {
      ldsw<f32x4>(lds0 + k * 4096 + w * 512 + lane * 32, (f32x4){a[k][0], a[k][1], a[k][2], a[k][3]});
      ldsw<f32x4>(lds0 + k * 4096 + w * 512 + lane * 32 + 16, (f32x4){a[k][4], a[k][5], a[k][6], a[k][7]});
    }
  }
  lds_bar();
  if (w < 2) {
    float t = 0.f;
#pragma unroll
    for (int i = 0; i < 16; ++i) t += ldsr<float>(lds0 + w * 4096 + i * 256 + lane * 4);
    bstore4<16>(__float_as_uint(t), rcs, w == 0 || sb < 4 ? lane * 4 : OOB, ((sb >= 5 ? 4608 : 0) + (9 * xcd + un + w) * 64) * 4);
  }
  STAMP(ws, 3, 5);
  wait_vm<0>();
  bar();
  if (w == 0) set_flag(c, FLS + blk * 64);
  STAMP(ws, 3, 6);
  if (w == 5 && blk == 48) {
    for (int i = 0; i < 4; ++i) poll(c, lane * FS1, FL1 + i * 64 * FS1);
    set_flag(c, 0);
  }
  STAMP_FLUSH(ws, 6);
}

DI void p3_gemm(const Ctx& c) {
  char* ws = c.ws;
  const int w = c.w, lane = fresh_lane();
  const unsigned lds0 = c.lds0;
  const int t16 = lane & 15, g = lane >> 4;
  const int nt = c.r >= 9 ? c.r - 9 : c.r;
  const int T = 16 * c.xcd + 8 * (w >> 2) + 4 * (c.r >= 9) + (w & 3);
  const rsrc_t rof = mk_rsrc(ws + WS_OF, 128 * 18 * 1024);
  STAMP_DECL; STAMP(ws, 3, 0);
  unsigned wa[4];
  {
    const int r2 = 4 * g + ((lane >> 2) & 3);
#pragma unroll
    for (int i = 0; i < 4; ++i) wa[i] = lds0 + P3_WO + r2 * 128 + 32 * (i ^ ((r2 >> 1) & 3)) + 8 * (lane & 3);
  }
  u32x2 xr[4];
  sfor<4>([&](auto I) { bload8(xr[I.value], (16 * T + t16) * 1152 + 128 * nt + 8 * g, mk_rsrc(c.x, S_ * D_ * 2), 32 * I.value); });
  unsigned l16 = lane * 16, so = g == 0 ? t16 * 8 : OOB;
  PIN(l16); PIN(so);
  if (w < 4) { wait_vm<4>(); cnt_add(c, 0); }
  cnt_wait(c, 0, 4);
  STAMP(ws, 3, 1);
  constexpr int AH = 5;
  bf16x8 of[18];
  bf16x4 fr[AH + 1][4][2];
  unsigned wb[4];
#pragma unroll
  for (int i = 0; i < 4; ++i) { wb[i] = wa[i] + 65536; PIN(wb[i]); }
  auto rd1 = [&](auto KS, auto I) {
    constexpr int ks = KS.value, i = I.value;
    if constexpr (ks < 18) fr[ks % (AH + 1)][i >> 1][i & 1] = trh((ks < 16 ? wa : wb)[i >> 1] + (ks & 15) * 4096 + (i & 1) * 2048);
  };
  sfor<AH>([&](auto K) { sfor<8>([&](auto I) { rd1(K, I); }); });
  SBAR();
  const bool all = poll_first(c, (lane & 31) * FS2, FL2 + (T >> 1) * 32 * FS2, 0xffffull);
  asm volatile("" : "+v"(xr[0]), "+v"(xr[1]), "+v"(xr[2]), "+v"(xr[3]));
  sfor<8>([&](auto K) { bload16c(of[K.value], l16, rof, (T * 18 + K.value) * 1024); });
  f32x4 acc[4];
#pragma unroll
  for (int i = 0; i < 4; ++i) { acc[i] = (f32x4){bflo(xr[i][0]), bfhi(xr[i][0]), bflo(xr[i][1]), bfhi(xr[i][1])}; PIN(acc[i]); }
  if (!all) poll(c, (16 + (lane & 15)) * FS2, FL2 + (T >> 1) * 32 * FS2);
  STAMP(ws, 3, 2);
  sfor<10>([&](auto K) { bload16c(of[8 + K.value], l16, rof, (T * 18 + 8 + K.value) * 1024); });
  sfor<18>([&](auto KS) {
    constexpr int ks = KS.value;
    asm volatile("s_waitcnt vmcnt(%1)" : "+v"(of[ks]) : "i"(17 - ks) : "memory");
    if constexpr (ks == 0) STAMP(ws, 3, 3);
    if constexpr (ks == 9) STAMP(ws, 3, 4);
    sfor<4>([&](auto C) {
      mfma16_ip(acc[C.value], cat(fr[ks % (AH + 1)][C.value][0], fr[ks % (AH + 1)][C.value][1]), of[ks]);
      SBAR();
      rd1(ic<ks + AH>{}, ic<2 * C.value>{}); rd1(ic<ks + AH>{}, ic<2 * C.value + 1>{});
      SBAR();
    });
  });
  asm volatile("s_nop 9" : "+v"(acc[0]), "+v"(acc[1]), "+v"(acc[2]), "+v"(acc[3]));
  unsigned d[8];
#pragma unroll
  for (int i = 0; i < 4; ++i) { d[2 * i] = pk2(acc[i][0], acc[i][1]); d[2 * i + 1] = pk2(acc[i][2], acc[i][3]); }
  const rsrc_t rhf = mk_rsrc(ws + WS_HF, 128 * 18 * 1024);
  bstore16<16>((u32x4){d[0], d[1], d[2], d[3]}, rhf, l16, (T * 18 + 2 * nt) * 1024);
  bstore16<16>((u32x4){d[4], d[5], d[6], d[7]}, rhf, l16, (T * 18 + 2 * nt + 1) * 1024);
  float s1[2] = {0.f, 0.f}, s2[2] = {0.f, 0.f};
#pragma unroll
  for (int i = 0; i < 8; ++i) {
    const bf2_t v = __builtin_bit_cast(bf2_t, d[i]);
    s1[i & 1] = __builtin_amdgcn_fdot2_f32_bf16(v, __builtin_bit_cast(bf2_t, ONES2), s1[i & 1], false);
    s2[i & 1] = __builtin_amdgcn_fdot2_f32_bf16(v, v, s2[i & 1], false);
  }
  bstore8<16>((u32x2){__float_as_uint(sum_x32(sum_x16(s1[0] + s1[1]))), __float_as_uint(sum_x32(sum_x16(s2[0] + s2[1])))}, mk_rsrc(ws + WS_ST, 9 * 2048 * 8), so, (T * 9 + nt) * 128);
  STAMP(ws, 3, 5);
  wait_vm<0>();
  set_flag(c, FL3 + (T * 16 + nt) * FS3);
  STAMP(ws, 3, 6);
  STAMP_FLUSH(ws, 3);
}

constexpr int K4_LG = P3_WO;
constexpr int K4_CS = K4_LG + 36864;
constexpr int K4_STP = K4_CS + 4096;
struct K4Op { int kind, idx; };
struct K4Ops { int n; K4Op op[12]; };
constexpr K4Ops k4_ops(int S) {
  K4Ops o = {};
  auto add = [&](int k, int i) { o.op[o.n++] = {k, i}; };
  if (S == 3) add(9, 0);
  if (S == 0) for (int u = 4; u < 8; ++u) { add(1, u); add(2, u); }
  else for (int u = 2 * S + 6; u < 2 * S + 8 && u < 36; ++u) {
    if (S == 2) { add(0, 2 * u - 12); add(0, 2 * u - 11); }
    if (S > 2) add(0, u);
    if (u < 18) { add(1, u); add(2, u); }
  }
  if (S == 4) { add(5, 0); add(5, 1); add(5, 2); }
  if (S == 6) add(6, 0);
  if (S >= 10 && S < 15) add(3, S - 10);
  if (S == 12) add(7, 0);
  if (S == 14) add(8, 0);
  if (S == 10) add(4, 0);
  return o;
}
constexpr int k4_nst(int S) { return (S == 8 || S == 9) ? 2 : S == 7 ? 1 : 0; }
constexpr int k4_issued(int S) { int n = 8; for (int s = 0; s < S; ++s) n += k4_ops(s).n + k4_nst(s); return n; }
constexpr int k4_pos(int kind, int idx) {
  if (kind == 0 && idx < 8) return -1;
  if ((kind == 1 || kind == 2) && idx < 4) return 2 * idx + kind - 1;
  for (int S = 0;; ++S) for (int i = 0; i < k4_ops(S).n; ++i) if (k4_ops(S).op[i].kind == kind && k4_ops(S).op[i].idx == idx) return k4_issued(S) + k4_nst(S) + i;
}
constexpr int k4_need(int S) {
  if (S == 8) return k4_issued(7) - 1;
  if (S >= 16) return k4_issued(15) - 1;
  const int a = S < 9 ? k4_pos(2, 2 * S + 1) : -1, b = k4_pos(0, 2 * S + 2);
  return a > b ? a : b;
}
static_assert(k4_need(5) >= k4_pos(9, 0) && k4_need(7) >= k4_pos(5, 2) && k4_need(12) >= k4_issued(10) - 1, "");
constexpr int K5_SLOT = 17408, K5_BASE = 0, K5_P = 4;
constexpr unsigned k5_so(int v) { return K5_BASE + ((v + 7) & 7) * K5_SLOT; }
DI int k5_u(int v, int nt) {
  if (v < 32) { const int q = ((v >> 2) + nt) & 3; return 18 * (q >> 1) + 8 * (q & 1) + 4 * ((v >> 1) & 1) + (v & 1) + 2 * (v >> 4); }
  const int q = (v + nt) & 3;
  return 18 * (q >> 1) + 16 + (q & 1);
}
DI unsigned k5_mv(int rho, int sp, int nt, int t16) {
  const int rr = sp ? ((rho & 7) | ((rho & 8) << 1) | ((rho & 16) >> 1)) : rho;
  return rr * 1152 + 256 * nt + 32 * ((t16 >> 1) ^ (rho & 7)) + 16 * (t16 & 1);
}
DI unsigned k5_fl(int i, int sg, int mt5) { return (((((mt5 >> 1) * 4 + ((i >> 2) & 3)) * 8 + 2 * sg + ((i >> 4) & 1)) * 8) + 4 * (mt5 & 1) + (i & 3)) * 64; }

DI void p4(const Ctx& c) {
  char* ws = c.ws;
  const int w = c.w, r = c.r, xcd = c.xcd, lane = fresh_lane();
  const int mt = r >> 2, Qd = xcd, j = r & 3;
  const unsigned lds0 = c.lds0;
  const int g = lane >> 4;
  const int T0 = 16 * mt + 2 * w;
  const rsrc_t rwm = mk_rsrc(w < 4 ? c.wg : c.wu, D_ * F_ * 2), rhf = mk_rsrc(ws + WS_HF, 128 * 18 * 1024), rwd = mk_rsrc(c.wd, F_ * D_ * 2);
  const rsrc_t rhh = mk_rsrc(ws + WS_HH, 128 * 144 * 1024);
  const unsigned f4 = FL4 + ((r * 8 + xcd) * 8 + w) * 64;
  const int sg = xcd >> 1, mt5 = 8 * (xcd & 1) + (r >> 2);
  STAMP_DECL; STAMP(ws, 4, 0);
  const unsigned wv = opq(k4_wv(lane, w, Qd, j));
  const unsigned wdst = lds0 + K4_RING + w * 1024, l16 = opq(lane * 16);
  unsigned wa[4];
  {
    const int r2 = 4 * g + ((lane >> 2) & 3);
#pragma unroll
    for (int i = 0; i < 4; ++i) wa[i] = opq(lds0 + K4_RING + r2 * 128 + 32 * (i ^ ((r2 >> 1) & 3)) + 8 * (lane & 3));
  }
  const bool all = poll_first(c, (min(lane & 15, 8) + (lane & 16)) * FS3, FL3 + T0 * 16 * FS3, 0x30003ull);
  bf16x8 act[18][2];
  u32x2 pf;
  auto pf_off = [&] { const int z = opq(l16) >> 4; const unsigned a = opq(FL4 + k5_fl(z, sg, mt5)), b = opq(FLS + (40 + 18 * sg + min(z - 32, 17)) * 64); return z < 32 ? a : b; };
  auto doop = [&](auto KIND, auto IDX) {
    constexpr int kind = KIND.value, u = IDX.value;
    if constexpr (kind == 0) glds16(wdst + k4_so(u), wv, rwm, (u % 18) * 32 * 9216 + (u / 18) * 128);
    else if constexpr (kind <= 2) bload16c(act[u][kind - 1], l16, rhf, (T0 * 18 + 18 * (kind - 1) + u) * 1024);
    else if constexpr (kind == 3) {
      const int pc = min(w + 8 * u, 35);
      glds16c(lds0 + K4_LG + pc * 1024, l16, mk_rsrc(ws + WS_LG, 8 * 4 * 36864), (Qd * 4 + j) * 36864 + pc * 1024);
    }
    else if constexpr (kind == 4) warm(lds0 + LDS_SCR, l16 * 8, rwd, sg * 1327104 + min(r * 8 + w, 161) * 8192);
    else if constexpr (kind == 5) glds16c(lds0 + K4_STP + w * 3072 + u * 1024, l16, mk_rsrc(ws + WS_ST, 9 * 2048 * 8), T0 * 1152 + u * 1024);
    else if constexpr (kind == 6) {
      const unsigned z = opq(l16), f = (576 * Qd + (z < 512 ? 128 * j : 384 + 16 * j)) * 4 + z;
      glds16c(lds0 + K4_CS + (w & 1) * 1024, z < 576 ? f : OOB, mk_rsrc(ws + WS_CS, 2 * 4608 * 4), (w & 1) * 4608 * 4);
    } else if constexpr (kind == 7) set_flag(c, f4);
    else if constexpr (kind == 8) bload8c(pf, pf_off(), fl_rsrc(c), 0);
    else bload8c(pf, min(opq(l16), 13u * 16) * 32, fl_rsrc(c), FLS + xcd * 64);
  };
  sfor<4>([&](auto U) { doop(ic<1>{}, U); doop(ic<2>{}, U); });
  if (w < 4) cnt_add(c, 4);
  if (!all) poll(c, (min(fresh_lane() & 15, 8) + (fresh_lane() & 16)) * FS3, FL3 + T0 * 16 * FS3);
  cnt_wait(c, 4, 4);
  f32x4 ag[4][2], au[4][2], cg, cu;
  bf16x4 fr[4][2];
  unsigned hd[2][2], lga, csa;
  f32x2 nmu = {0.f, 0.f};
  auto rd1 = [&](auto H, auto I) {
    constexpr int n = H.value >> 1, m = H.value & 1, i = I.value;
    if constexpr (n < 144) fr[H.value & 3][i] = trh(wa[n % 4] + k4_so(n / 4) + 4096 * m + 2048 * i);
    else if constexpr (n < 162) fr[H.value & 3][i] = trh(lga + (n - 144) * 1024 + m * 18432 + i * 512);
  };
  auto rd = [&](auto H) { rd1(H, ic<0>{}); rd1(H, ic<1>{}); };
  auto mm = [&](auto N, auto KS, f32x4& g0, f32x4& g1, f32x4& u0, f32x4& u1, auto&& hook) {
    constexpr int n = N.value, ks = KS.value;
    constexpr int h0 = n == 15 ? 999 : 2 * n + 3, h1 = (n == 14 || n == 15) ? 999 : 2 * n + 4;
    const bf16x8 fg = cat(fr[(2 * n) & 3][0], fr[(2 * n) & 3][1]);
    if constexpr (ks == 0) mfma16_z(g0, fg, act[ks][0]); else mfma16_ip(g0, fg, act[ks][0]);
    SBAR(); rd1(ic<h0>{}, ic<0>{}); SBAR(); hook(ic<0>{}); SBAR();
    if constexpr (ks == 0) mfma16_z(g1, fg, act[ks][1]); else mfma16_ip(g1, fg, act[ks][1]);
    SBAR(); rd1(ic<h0>{}, ic<1>{}); SBAR(); hook(ic<1>{}); SBAR();
    const bf16x8 fu = cat(fr[(2 * n + 1) & 3][0], fr[(2 * n + 1) & 3][1]);
    if constexpr (ks == 0) mfma16_z(u1, fu, act[ks][1]); else mfma16_ip(u1, fu, act[ks][1]);
    SBAR(); rd1(ic<h1>{}, ic<0>{}); SBAR(); hook(ic<2>{}); SBAR();
    if constexpr (ks == 0) mfma16_z(u0, fu, act[ks][0]); else mfma16_ip(u0, fu, act[ks][0]);
    SBAR(); rd1(ic<h1>{}, ic<1>{}); SBAR(); hook(ic<3>{}); SBAR();
  };
  auto ldcs = [&](int e) { cg = ldsr<f32x4>(csa + 64 * e); cu = ldsr<f32x4>(csa + 1024 + 64 * e); };
  auto epi = [&](auto E, auto I) {
    constexpr int grp = E.value >> 2, cc = E.value & 3, i = I.value, T = 1 - (i >> 1);
    if constexpr ((i & 1) == 0) {
      ag[cc][T] += cg * nmu[T]; au[cc][T] += cu * nmu[T]; PIN(ag[cc][T]); PIN(au[cc][T]);
      if constexpr (i == 2 && E.value != 3) { SBAR(); ldcs(E.value + 1); }
    }
    else {
      const f32x4 v = ag[cc][T] * au[cc][T];
      const unsigned d0 = pk2(v[0], v[1]), d1 = pk2(v[2], v[3]);
      if constexpr ((cc & 1) == 0) { hd[T][0] = d0; hd[T][1] = d1; PIN(hd[T][0]); PIN(hd[T][1]); }
      else bstore16<16>((u32x4){hd[T][0], hd[T][1], d0, d1}, rhh, l16, ((T0 + T) * 144 + 18 * Qd + 4 * j + 2 * grp + (cc >> 1)) * 1024);
    }
  };
  f32x2 sp[3];
  float rs0;
  auto stats = [&](auto TT, auto P) {
    constexpr int T = TT.value, p = P.value;
    if constexpr (p == 0) {
      const unsigned sa = lds0 + K4_STP + w * 3072 + T * 1152 + (opq(l16) >> 1);
      sp[0] = ldsr<f32x2>(sa); sp[1] = ldsr<f32x2>(sa + 512); sp[2] = ldsr<f32x2>(sa + 1024);
    } else if constexpr (p == 4) { sp[0] = sp[0] + sp[1] + (opq(l16) < 256 ? sp[2] : (f32x2){0.f, 0.f}); PIN(sp[0]); }
    else if constexpr (p == 5) { sp[0][0] = sum_x16(sp[0][0]); PIN(sp[0]); }
    else if constexpr (p == 6) { sp[0][1] = sum_x16(sp[0][1]); PIN(sp[0]); }
    else if constexpr (p == 7) { sp[0][0] = sum_x32(sp[0][0]); PIN(sp[0]); }
    else if constexpr (p == 8) { sp[0][1] = sum_x32(sp[0][1]); PIN(sp[0]); }
    else if constexpr (p == 9) {
      nmu[T] = sp[0][0] * (-1.f / D_);
      const float rs = __builtin_amdgcn_rcpf(fmaxf(sp[0][1] * (1.f / D_) - nmu[T] * nmu[T], 0.f) + 1e-5f);
      PIN(nmu);
      if constexpr (T == 0) { rs0 = rs; PIN(rs0); }
      else {
        const unsigned z = opq(l16);
        bstore4<16>(__float_as_uint((z & 256) ? rs : rs0), mk_rsrc(ws + WS_RS + (sg * 2048 + 16 * T0) * 4, (Qd & 1) == 0 && j == 0 ? 128 : 0), z >> 2, 0);
      }
    }
  };
  unsigned mv5;
  auto pf5 = [&](auto N) {
    constexpr int n = N.value, v = (n - 1) >> 1;
    if constexpr (n == 0) {
      const int vv = min(w, K5_P - 1), uu = k5_u(vv, j);
      glds16c(lds0 + K5_BASE + ((vv + 7) & 7) * K5_SLOT + 16384, l16, mk_rsrc(ws + WS_WT, 4 * 4608 * 32), (j * 144 + 36 * sg + uu) * 1024);
      const unsigned z = opq(l16);
      mv5 = k5_mv(4 * w + (z >> 8), 0, j, (z >> 4) & 15);
    } else if constexpr (n & 1) glds16(lds0 + k5_so(v) + 8192 + w * 1024, mv5, rwd, sg * 1152 * 1152 + k5_u(v, j) * 32 * 1152);
    else glds16c(lds0 + k5_so(v) + w * 1024, l16, rhh, ((8 * mt5 + w) * 144 + 36 * sg + k5_u(v, j)) * 1024);
  };
  sfor<18>([&](auto SS) {
    constexpr int S = SS.value;
    constexpr int nw = k4_issued(S) - k4_need(S) - 1;
    static_assert(nw >= 0 && nw < 64, "");
    if constexpr (S == 5) asm volatile("s_waitcnt vmcnt(%5)" : "+v"(act[2 * S][0]), "+v"(act[2 * S][1]), "+v"(act[2 * S + 1][0]), "+v"(act[2 * S + 1][1]), "+v"(pf) : "i"(nw) : "memory");
    else if constexpr (S < 9) asm volatile("s_waitcnt vmcnt(%4)" : "+v"(act[2 * S][0]), "+v"(act[2 * S][1]), "+v"(act[2 * S + 1][0]), "+v"(act[2 * S + 1][1]) : "i"(nw) : "memory");
    else if constexpr (S == 16) asm volatile("s_waitcnt vmcnt(%1)" : "+v"(pf) : "i"(nw) : "memory");
    else wait_vm<nw>();
    if constexpr (S == 16) { if (__builtin_expect(!tag_ok(c, pf), 0)) poll(c, pf_off(), 0); }
    if constexpr (S == 5) { if (__builtin_expect(!tag_ok(c, pf), 0)) poll(c, min(opq(l16), 13u * 16) * 32, FLS + xcd * 64); }
    if constexpr (S >= 2) bar();
    if constexpr (S == 0) STAMP(ws, 4, 1);
    if constexpr (S == 2) STAMP(ws, 4, 7);
    if constexpr (S == 4) STAMP(ws, 4, 2);
    if constexpr (S == 9) STAMP(ws, 4, 3);
    if constexpr (S == 17) STAMP(ws, 4, 4);
    if constexpr (S == 0 || S == 2) { rd(ic<16 * S>{}); rd(ic<16 * S + 1>{}); rd(ic<16 * S + 2>{}); }
    sfor<8>([&](auto L) {
      constexpr int l = L.value, n = 8 * S + l, t = n / 4, cc = n % 4, ks = t % 18, no = k4_ops(S).n;
      mm(ic<n>{}, ic<ks>{}, ag[cc][0], ag[cc][1], au[cc][0], au[cc][1], [&](auto I) {
        constexpr int i = I.value;
        sfor<no>([&](auto O) {
          constexpr int nd = no ? no : 1, at = O.value * 32 / nd + 16 / nd;
          if constexpr (at == 4 * l + i) doop(ic<k4_ops(S).op[O.value].kind>{}, ic<k4_ops(S).op[O.value].idx>{});
        });
        if constexpr (S == 7) stats(ic<(l >> 2)>{}, ic<4 * (l & 3) + i>{});
        if constexpr ((S == 8 || S == 17) && l == 3 && i == 0) { csa = lds0 + K4_CS + ((opq(l16) >> 4) & 48); PIN(csa); }
        if constexpr ((S == 8 || S == 17) && l == 4 && i == 0) ldcs(S == 8 ? 0 : 4);
        if constexpr (S == 17 && l == 0 && i == 0) { const unsigned z = opq(l16); lga = lds0 + K4_LG + (z >> 6) * 32 + ((z >> 1) & 24); PIN(lga); }
        if constexpr (S == 17 && l < 5 && i == 1) pf5(L);
        if constexpr (ks == 17 && cc >= 1) epi(ic<4 * (t / 18) + cc - 1>{}, I);
        if constexpr (t == 18 && cc == 0) epi(ic<3>{}, I);
      });
    });
  });
  STAMP(ws, 4, 5);
  {
    f32x4 g0, g1, u0, u1;
    sfor<18>([&](auto K) {
      constexpr int ks = K.value, n = 144 + ks;
      mm(ic<n>{}, ic<ks>{}, g0, g1, u0, u1, [&](auto I) {
        if constexpr (ks == 0) epi(ic<7>{}, I);
        if constexpr (ks >= 1 && ks <= 4 && I.value == 1) pf5(ic<4 + ks>{});
        if constexpr (ks == 10 && I.value == 1) { wait_vm<4>(); set_flag(c, f4 + 2048 * 64); }
      });
    });
    asm volatile("s_nop 9" : "+v"(g0), "+v"(g1), "+v"(u0), "+v"(u1));
    const f32x4 v0 = (g0 + cg * nmu[0]) * (u0 + cu * nmu[0]), v1 = (g1 + cg * nmu[1]) * (u1 + cu * nmu[1]);
    const unsigned a0 = pk2(v0[0], v0[1]), a1 = pk2(v0[2], v0[3]), b0 = pk2(v1[0], v1[1]), b1 = pk2(v1[2], v1[3]);
    auto s0 = __builtin_amdgcn_permlane32_swap(a0, b0, false, false);
    auto s1 = __builtin_amdgcn_permlane32_swap(a1, b1, false, false);
    const unsigned z = opq(l16);
    bstore16<16>((u32x4){s0[0], s1[0], s0[1], s1[1]}, rhh, (z >> 9) * 144 * 1024 + (z & 511) + 512 * (j & 1), (T0 * 144 + 18 * Qd + 16 + (j >> 1)) * 1024);
    STAMP(ws, 4, 6);
    STAMP_FLUSH(ws, 4);
  }
}

constexpr int k5_E(int i) { int e = 36; if (K5_P + 2 * (i + 1) < e) e = K5_P + 2 * (i + 1); if (i >= 0 && i + 8 < e) e = i + 8; return e; }
static_assert(k5_E(6) <= 16 && k5_E(20) <= 32, "");

DI void p5(const Ctx& c) {
  char* ws = c.ws;
  const int w = c.w, r = c.r, xcd = c.xcd, lane = fresh_lane();
  const int sg = xcd >> 1, mt = 8 * (xcd & 1) + (r >> 2), nt = r & 3;
  const unsigned lds0 = c.lds0;
  const int t16 = lane & 15, g = lane >> 4;
  const int tile = 4 * mt + nt;
  const rsrc_t rpt = mk_rsrc(ws + WS_PT, 64 * 8 * 4 * PT_REC);
  const unsigned f4 = FL4 + (4096 + (r * 8 + xcd) * 8 + w) * 64;
  STAMP_DECL; STAMP(ws, 5, 0);
  if (w >= 4) {
    const int l = w - 4;
    const rsrc_t rwd = mk_rsrc(c.wd, F_ * D_ * 2), rhh = mk_rsrc(ws + WS_HH, 128 * 144 * 1024);
    const rsrc_t rwt = mk_rsrc(ws + WS_WT, 4 * 4608 * 32);
    unsigned mv[2][2];
#pragma unroll
    for (int sp = 0; sp < 2; ++sp)
#pragma unroll
      for (int i = 0; i < 2; ++i) mv[sp][i] = k5_mv(4 * (2 * l + i) + g, sp, nt, t16);
    const unsigned hs = ((8 * mt + 2 * l) * 144 + 36 * sg) * 1024, wsb = sg * 1152 * 1152, dl = lds0 + l * 2048;
    auto issue = [&](auto V) {
      constexpr int v = V.value, sp = v >= 32;
      const int u = k5_u(v, nt);
      constexpr unsigned so = k5_so(v);
      glds16(dl + so + 8192, mv[sp][0], rwd, wsb + u * 32 * 1152);
      glds16c(dl + so, lane * 16, rhh, hs + u * 1024);
      glds16(dl + so + 8192 + 1024, mv[sp][1], rwd, wsb + u * 32 * 1152);
      glds16c(dl + so + 1024, lane * 16, rhh, hs + (144 + u) * 1024);
      if ((v & 3) == l) glds16c(lds0 + so + 16384, lane * 16, rwt, (nt * 144 + 36 * sg + u) * 1024);
    };
    sfor<36>([&](auto I) {
      constexpr int i = I.value, y = k5_E(i - 1) - (i + 2 < 36 ? i + 2 : 36), yy = k5_E(i - 1) - K5_P;
      if constexpr (i == 0) wait_vm<2>();
      else if constexpr (i == 2) { wait_vm<4 * yy + yy / 4>(); set_flag(c, f4); }
      else if constexpr (i + 2 > K5_P) wait_vm<4 * y + y / 4>();
      bar();
      if constexpr (i == 0) STAMP(ws, 5, 1);
      if constexpr (i == 18) STAMP(ws, 5, 2);
      sfor<k5_E(i) - k5_E(i - 1)>([&](auto U) { issue(ic<k5_E(i - 1) + U.value>{}); });
    });
    bar();
    STAMP(ws, 5, 3);
    bar();
  } else {
    const int r2 = 4 * g + ((lane >> 2) & 3);
    unsigned wa[2][10];
#pragma unroll
    for (int i = 0; i < 8; ++i) wa[0][i] = lds0 + K5_BASE + 8192 + r2 * 256 + 32 * (i ^ (r2 & 7)) + 8 * (lane & 3);
    wa[0][8] = lds0 + K5_BASE + 16384 + r2 * 32 + 8 * (lane & 3);
    wa[0][9] = lds0 + K5_BASE + w * 2048 + lane * 16;
#pragma unroll
    for (int i = 0; i < 10; ++i) { wa[1][i] = wa[0][i] + 4 * K5_SLOT; PIN(wa[0][i]); PIN(wa[1][i]); }
    const unsigned fo = k5_fl(lane & 31, sg, mt);
    f32x4 acc[9][2];
    bf16x4 fr[3][3][2];
    bf16x8 hb[2][2];
    u32x2 pf;
    auto rd1 = [&](auto N, auto I) {
      constexpr int n = N.value, u = n / 3, i = I.value;
      if constexpr (u < 36) {
        constexpr int pos = (u + 7) & 7, hi = pos >> 2, cc = 3 * (n % 3) + i / 2;
        constexpr unsigned so = (pos & 3) * K5_SLOT;
        if constexpr (i >= 6) hb[u & 1][i - 6] = ldsr<bf16x8>(wa[hi][9] + so + (i - 6) * 1024);
        else fr[n % 3][i / 2][i & 1] = trh(wa[hi][cc] + so + (i & 1) * (cc < 8 ? 4096 : 512));
      }
    };
    sfor<36>([&](auto U) {
      constexpr int u = U.value;
      if constexpr (u == 0) wait_vm<2>();
      if constexpr (u == 4) { wait_vm<0>(); set_flag(c, f4); }
      if constexpr (u == 4 || u == 18) bload8c(pf, fo, fl_rsrc(c), FL4 + (u == 4 ? 1 : 2) * 2048 * 64);
      if constexpr (u == 7 || u == 21) {
        asm volatile("s_waitcnt vmcnt(0)" : "+v"(pf)::"memory");
        if (__builtin_expect(!tag_ok(c, pf), 0)) poll(c, fo, FL4 + (u == 7 ? 1 : 2) * 2048 * 64);
      }
      bar();
      if constexpr (u == 0) STAMP(ws, 5, 1);
      if constexpr (u == 18) STAMP(ws, 5, 2);
      if constexpr (u == 0) { rd1(ic<0>{}, ic<6>{}); rd1(ic<0>{}, ic<7>{}); sfor<6>([&](auto I) { rd1(ic<0>{}, I); }); sfor<6>([&](auto I) { rd1(ic<1>{}, I); }); }
      sfor<18>([&](auto M) {
        constexpr int mi = M.value, v = mi / 6, n = 3 * u + v, cc = mi / 2;
        if constexpr (u == 0) mfma16_z(acc[cc][mi & 1], cat(fr[n % 3][cc % 3][0], fr[n % 3][cc % 3][1]), hb[u & 1][mi & 1]);
        else mfma16_ip(acc[cc][mi & 1], cat(fr[n % 3][cc % 3][0], fr[n % 3][cc % 3][1]), hb[u & 1][mi & 1]);
        SBAR();
        if constexpr (mi == 6 || mi == 8) rd1(ic<n + 2>{}, ic<6 + (mi - 6) / 2>{});
        rd1(ic<n + 2>{}, ic<mi % 6>{});
        SBAR();
      });
    });
    asm volatile("s_nop 9" : "+v"(acc[7][0]), "+v"(acc[7][1]), "+v"(acc[8][0]), "+v"(acc[8][1]));
#pragma unroll
    for (int i = 0; i < 7; ++i) asm volatile("" : "+v"(acc[i][0]), "+v"(acc[i][1]));
    bar();
    STAMP(ws, 5, 3);
    if (w == sg) {
      sfor<2>([&](auto T) { sfor<9>([&](auto C) { ldsw<f32x4>(lds0 + (T.value * 9 + C.value) * 1024 + lane * 16, acc[C.value][T.value]); }); });
      lds_bar();
    } else {
      sfor<2>([&](auto T) {
        const unsigned pb = (((tile * 4 + w) * 4 + sg) * 2 + T.value) * PT_REC;
        sfor<4>([&](auto I) {
          constexpr int i = I.value;
          const f32x4 a = acc[2 * i][T.value], b = acc[2 * i + 1][T.value];
          bstore16<16>((u32x4){pk2(a[0], a[1]), pk2(a[2], a[3]), pk2(b[0], b[1]), pk2(b[2], b[3])}, rpt, lane * 16 + i * 1024, pb);
        });
        const f32x4 a = acc[8][T.value];
        bstore8<16>((u32x2){pk2(a[0], a[1]), pk2(a[2], a[3])}, rpt, lane * 8 + 4096, pb);
      });
      bar();
      wait_vm<0>();
      set_flag(c, FL5 + (tile * 4 + w) * 256 + sg * FS5);
    }
  }
  const int T = w >> 2, pi = w & 3, tt = 8 * mt + 2 * sg + T;
  const rsrc_t rhf = mk_rsrc(ws + WS_HF, 128 * 18 * 1024);
  u32x4 hv, pv[3];
  u32x2 hl, pl[3];
  unsigned rs2u;
  STAMP(ws, 5, 4);
  bload16c(hv, lane * 16, rhf, (tt * 18 + 4 * nt + pi) * 1024);
  bload8c(hl, lane * 16 + 8 * (nt & 1), rhf, (tt * 18 + 16 + (nt >> 1)) * 1024);
  bload4c(rs2u, t16 * 4, mk_rsrc(ws + WS_RS, 4 * 2048 * 4), (sg * 2048 + 16 * tt) * 4);
  poll(c, ((sg + 1 + min(lane, 2)) & 3) * FS5, FL5 + (tile * 4 + sg) * 256);
  STAMP(ws, 5, 5);
#pragma unroll
  for (int o = 0; o < 3; ++o) {
    const unsigned so = (((tile * 4 + sg) * 4 + ((sg + 1 + o) & 3)) * 2 + T) * PT_REC;
    bload16c(pv[o], lane * 16 + pi * 1024, rpt, so);
    bload8c(pl[o], pi == 0 ? lane * 8 + 4096 : OOB, rpt, so);
  }
  const unsigned orow = (16 * tt + t16) * 1152, own = lds0 + T * 9216 + lane * 16;
  const f32x4 o0 = ldsr<f32x4>(own + pi * 2048), o1 = ldsr<f32x4>(own + pi * 2048 + 1024), o2 = ldsr<f32x4>(own + 8192);
  asm volatile("s_waitcnt vmcnt(0)" : "+v"(hv), "+v"(hl), "+v"(rs2u), "+v"(pv[0]), "+v"(pv[1]), "+v"(pv[2]), "+v"(pl[0]), "+v"(pl[1]), "+v"(pl[2])::"memory");
  STAMP(ws, 5, 6);
  const float rs2 = __uint_as_float(rs2u);
  const rsrc_t rout = mk_rsrc(c.out, S_ * D_ * 2);
  auto fin = [&](const f32x4& ow, unsigned a0, unsigned a1, unsigned b0, unsigned b1, unsigned c0, unsigned c1, unsigned h0, unsigned h1, unsigned& d0, unsigned& d1) {
    const float v0 = ((ow[0] + bflo(b0)) + (bflo(a0) + bflo(c0))) * rs2 + bflo(h0);
    const float v1 = ((ow[1] + bfhi(b0)) + (bfhi(a0) + bfhi(c0))) * rs2 + bfhi(h0);
    const float v2 = ((ow[2] + bflo(b1)) + (bflo(a1) + bflo(c1))) * rs2 + bflo(h1);
    const float v3 = ((ow[3] + bfhi(b1)) + (bfhi(a1) + bfhi(c1))) * rs2 + bfhi(h1);
    d0 = pk2(v0, v1); d1 = pk2(v2, v3);
  };
  {
    unsigned a0, a1, b0, b1;
    fin(o0, pv[0][0], pv[0][1], pv[1][0], pv[1][1], pv[2][0], pv[2][1], hv[0], hv[1], a0, a1);
    fin(o1, pv[0][2], pv[0][3], pv[1][2], pv[1][3], pv[2][2], pv[2][3], hv[2], hv[3], b0, b1);
    auto s0 = __builtin_amdgcn_permlane16_swap(a0, b0, false, false);
    auto s1 = __builtin_amdgcn_permlane16_swap(a1, b1, false, false);
    bstore16<16>((u32x4){s0[0], s1[0], s0[1], s1[1]}, rout, orow + 256 * nt + 64 * pi + 32 * (g & 1) + 16 * (g >> 1), 0);
  }
  {
    unsigned e0, e1;
    fin(o2, pl[0][0], pl[0][1], pl[1][0], pl[1][1], pl[2][0], pl[2][1], hl[0], hl[1], e0, e1);
    bstore8<16>((u32x2){e0, e1}, rout, pi == 0 ? orow + 1024 + 32 * nt + 8 * g : OOB, 0);
  }
  STAMP(ws, 5, 7);
  STAMP_FLUSH(ws, 5);
}

extern "C" __global__ __launch_bounds__(512, 2) void k1_all(const void* x, const void* wqkv, const void* wo, const void* wg, const void* wu, const void* wd, char* ws, void* out) {
  Ctx c = {x, wqkv, wo, wg, wu, wd, ws, out, (int)rfl(threadIdx.x >> 6), (int)(blockIdx.x & 7), (int)(blockIdx.x >> 3), (unsigned)(size_t)smem, 0u, 0u};
  if (c.w == 7) {
    unsigned long long pc; unsigned back, size;
    asm volatile("s_getpc_b64 %0\n.Lk1pc%=:\n\ts_mov_b32 %1, .Lk1pc%=-k1_all\n\ts_mov_b32 %2, k9_end-k1_all" : "=s"(pc), "=s"(back), "=s"(size));
    const int lane = fresh_lane();
    warm(c.lds0 + LDS_SCR, lane < 24 ? (c.r * 24 + lane) * 128 : OOB, mk_rsrc((const void*)(pc - back), size), 0);
  }
  {
    const int mt = 4 * (c.xcd & 3) + (c.r & 3), nt = 8 * (c.xcd >> 2) + (c.r >> 2);
    if (c.w < 4) p1_body<0>(c, mt, nt); else p1_body<1>(c, mt, nt);
  }
  p2(c);
  if (c.r < 18) p3_gemm(c); else { cnt_wait(c, 1, 8); p3_side(c); }
  if (c.w < 4) k4_first<0>(c, fresh_lane());
  else { cnt_wait(c, 1, 8); k4_first<4>(c, fresh_lane()); }
  p4(c);
  p5(c);
  STAMP_OUT();
}

extern "C" __global__ void k9_end() {}

extern "C" void kernel_launch(void* const* d_in, const int* in_sizes, int n_in, void* d_out, int out_size, void* d_ws, size_t ws_size,
                              hipStream_t stream) {
  (void)hipFuncSetAttribute((const void*)k1_all, hipFuncAttributeMaxDynamicSharedMemorySize, LDS_ALL);
  k1_all<<<256, 512, LDS_ALL, stream>>>(d_in[0], d_in[1], d_in[2], d_in[4], d_in[5], d_in[6], (char*)d_ws, d_out);
}
```

```cpp
#include <hip/hip_runtime.h>
#include <stdint.h>

typedef short bf16x8 __attribute__((ext_vector_type(8)));
typedef short bf16x4 __attribute__((ext_vector_type(4)));
typedef float f32x2 __attribute__((ext_vector_type(2)));
typedef float f32x4 __attribute__((ext_vector_type(4)));
typedef float f32x16 __attribute__((ext_vector_type(16)));
typedef unsigned u32x2 __attribute__((ext_vector_type(2)));
typedef unsigned u32x4 __attribute__((ext_vector_type(4)));
typedef __bf16 bf2_t __attribute__((ext_vector_type(2)));
typedef __amdgpu_buffer_rsrc_t rsrc_t;
#define LAS __attribute__((address_space(3)))
#define GAS __attribute__((address_space(1)))
#define DI __device__ __forceinline__

constexpr int S_ = 2048, D_ = 576, F_ = 4608;
constexpr float QSCALE = 0.23570226039551584f * 1.4426950408889634f;
constexpr unsigned ONES2 = 0x3f803f80u;

constexpr size_t QREC = 1280, KVREC = 2560;
constexpr unsigned OOB = 0x7fffff00u;
constexpr unsigned FL1 = 128;
constexpr unsigned FL2 = FL1 + 256 * 64;
constexpr unsigned FL3 = FL2 + 2048 * 64;
constexpr unsigned FLS = FL3 + 2048 * 64;
constexpr unsigned FL4 = FLS + 128 * 64;
constexpr unsigned FL5 = FL4 + 3 * 2048 * 64;
constexpr unsigned FLW = FL5 + 1024 * 64;
constexpr unsigned FL_END = FLW + 128 * 64;
constexpr size_t WS_QF = 1u << 20;
constexpr size_t WS_KV = WS_QF + 32 * 64 * QREC;
constexpr size_t WS_OF = WS_KV + 32 * 64 * KVREC;
constexpr size_t WS_HF = WS_OF + 128 * 18 * 1024;
constexpr size_t WS_ST = WS_HF + 128 * 18 * 1024;
constexpr size_t WS_MX = WS_ST + 9 * 2048 * 8;
constexpr size_t WS_WB = WS_MX + 2048 * 4;
constexpr size_t WS_LG = WS_WB + 2048;
constexpr size_t WS_WT = WS_LG + 8 * 4 * 36864;
constexpr size_t WS_HH = WS_WT + 4 * 4608 * 32;
constexpr size_t PT_REC = 4608;
constexpr size_t WS_PT = WS_HH + (size_t)128 * 144 * 1024;
constexpr size_t WS_END = WS_PT + (size_t)64 * 8 * 4 * PT_REC;

DI unsigned rfl(unsigned v) { return __builtin_amdgcn_readfirstlane(v); }
DI rsrc_t mk_rsrc(const void* p, unsigned bytes) { return __builtin_amdgcn_make_buffer_rsrc((void*)p, (short)0, (int)bytes, 0x00020000); }
DI void glds16(unsigned lds, unsigned voff, rsrc_t r, unsigned soff) {
  asm volatile("s_mov_b32 m0, %0\n\ts_nop 0\n\tbuffer_load_dwordx4 %1, %2, %3 offen lds" ::"s"(lds), "v"(voff), "s"(r), "s"(soff) : "memory", "m0");
}
DI void glds16c(unsigned lds, unsigned voff, rsrc_t r, unsigned soff) {
  asm volatile("s_mov_b32 m0, %0\n\ts_nop 0\n\tbuffer_load_dwordx4 %1, %2, %3 offen sc1 lds" ::"s"(lds), "v"(voff), "s"(r), "s"(soff) : "memory", "m0");
}
template <class T> DI void bload16c(T& d, unsigned voff, rsrc_t r, unsigned soff) {
  asm volatile("buffer_load_dwordx4 %0, %1, %2, %3 offen sc1" : "=v"(d) : "v"(voff), "s"(r), "s"(soff) : "memory");
}
DI void bload8c(u32x2& d, unsigned voff, rsrc_t r, unsigned soff) {
  asm volatile("buffer_load_dwordx2 %0, %1, %2, %3 offen sc1" : "=v"(d) : "v"(voff), "s"(r), "s"(soff) : "memory");
}
DI void bload4c(unsigned& d, unsigned voff, rsrc_t r, unsigned soff) {
  asm volatile("buffer_load_dword %0, %1, %2, %3 offen sc1" : "=v"(d) : "v"(voff), "s"(r), "s"(soff) : "memory");
}
DI void bload16(bf16x8& d, unsigned voff, rsrc_t r, unsigned soff) {
  asm volatile("buffer_load_dwordx4 %0, %1, %2, %3 offen" : "=v"(d) : "v"(voff), "s"(r), "s"(soff) : "memory");
}
DI void warm(unsigned lds, unsigned voff, rsrc_t r, unsigned soff) {
  asm volatile("s_mov_b32 m0, %0\n\ts_nop 0\n\tbuffer_load_dword %1, %2, %3 offen lds" ::"s"(lds), "v"(voff), "s"(r), "s"(soff) : "memory", "m0");
}
template <int N> DI void wait_vm() { asm volatile("s_waitcnt vmcnt(%0)" ::"i"(N) : "memory"); }
#define PIN(x) asm volatile("" : "+v"(x))
#define SBAR() __builtin_amdgcn_sched_barrier(0)
DI void bar() { SBAR(); asm volatile("s_barrier" ::: "memory"); SBAR(); }
DI void lds_bar() { SBAR(); asm volatile("s_waitcnt lgkmcnt(0)\n\ts_barrier" ::: "memory"); SBAR(); }
DI bf16x8 trfrag(unsigned a0, unsigned a1) {
  bf16x4 lo = __builtin_amdgcn_ds_read_tr16_b64_v4i16((LAS bf16x4*)(size_t)a0);
  bf16x4 hi = __builtin_amdgcn_ds_read_tr16_b64_v4i16((LAS bf16x4*)(size_t)a1);
  return __builtin_shufflevector(lo, hi, 0, 1, 2, 3, 4, 5, 6, 7);
}
DI bf16x4 trh(unsigned a) { return __builtin_amdgcn_ds_read_tr16_b64_v4i16((LAS bf16x4*)(size_t)a); }
DI bf16x8 cat(bf16x4 lo, bf16x4 hi) { return __builtin_shufflevector(lo, hi, 0, 1, 2, 3, 4, 5, 6, 7); }
template <class T> DI T ldsr(unsigned a) { return *(LAS T*)(size_t)a; }
template <class T> DI void ldsw(unsigned a, T v) { *(LAS T*)(size_t)a = v; }
DI unsigned pk2(float a, float b) { f32x2 v = {a, b}; return __builtin_bit_cast(unsigned, __builtin_convertvector(v, bf2_t)); }
DI float bflo(unsigned u) { return __uint_as_float(u << 16); }
DI float bfhi(unsigned u) { return __uint_as_float(u & 0xffff0000u); }
DI f32x4 mfma16(bf16x8 a, bf16x8 b, f32x4 c) { return __builtin_amdgcn_mfma_f32_16x16x32_bf16(a, b, c, 0, 0, 0); }
DI void mfma16_ip(f32x4& c, bf16x8 a, bf16x8 b) { asm volatile("v_mfma_f32_16x16x32_bf16 %0, %1, %2, %0" : "+v"(c) : "v"(a), "v"(b)); }
DI void mfma16_z(f32x4& c, bf16x8 a, bf16x8 b) { asm volatile("v_mfma_f32_16x16x32_bf16 %0, %1, %2, 0" : "=&v"(c) : "v"(a), "v"(b)); }
DI float mul1(float a, float b) { float d; asm volatile("v_mul_f32 %0, %1, %2" : "=v"(d) : "v"(a), "v"(b)); return d; }
DI f32x16 mfma32(bf16x8 a, bf16x8 b, f32x16 c) { return __builtin_amdgcn_mfma_f32_32x32x16_bf16(a, b, c, 0, 0, 0); }
DI float xor32(float v) { auto r = __builtin_amdgcn_permlane32_swap(__float_as_uint(v), __float_as_uint(v), false, false); return __uint_as_float((threadIdx.x & 32) ? r[0] : r[1]); }
DI float sum_x32(float v) { auto r = __builtin_amdgcn_permlane32_swap(__float_as_uint(v), __float_as_uint(v), false, false); return __uint_as_float(r[0]) + __uint_as_float(r[1]); }
DI float max_x32(float v) { auto r = __builtin_amdgcn_permlane32_swap(__float_as_uint(v), __float_as_uint(v), false, false); return fmaxf(__uint_as_float(r[0]), __uint_as_float(r[1])); }
DI float sum_x16(float v) { auto r = __builtin_amdgcn_permlane16_swap(__float_as_uint(v), __float_as_uint(v), false, false); return __uint_as_float(r[0]) + __uint_as_float(r[1]); }
template <int AUX> DI void bstore16(u32x4 v, rsrc_t r, unsigned voff, unsigned soff) {
  if constexpr (AUX == 16) asm volatile("buffer_store_dwordx4 %0, %1, %2, %3 offen sc1\n\ts_nop 1" ::"v"(v), "v"(voff), "s"(r), "s"(soff) : "memory");
  else asm volatile("buffer_store_dwordx4 %0, %1, %2, %3 offen\n\ts_nop 1" ::"v"(v), "v"(voff), "s"(r), "s"(soff) : "memory");
}
template <int AUX> DI void bstore8(u32x2 v, rsrc_t r, unsigned voff, unsigned soff) { __builtin_amdgcn_raw_buffer_store_b64(v, r, voff, soff, AUX); }
template <int AUX> DI void bstore4(unsigned v, rsrc_t r, unsigned voff, unsigned soff) { __builtin_amdgcn_raw_buffer_store_b32(v, r, voff, soff, AUX); }

template <int... I> struct iseq {};
template <int N, int... I> struct mkseq : mkseq<N - 1, N - 1, I...> {};
template <int... I> struct mkseq<0, I...> { typedef iseq<I...> type; };
template <int V> struct ic { static constexpr int value = V; };
template <class Fn, int... I> DI void sfor_(Fn&& f, iseq<I...>) { (f(ic<I>{}), ...); }
template <int N, class Fn> DI void sfor(Fn&& f) { sfor_(f, typename mkseq<N>::type{}); }


struct Ctx {
  const void *x, *wqkv, *wo, *wg, *wu, *wd;
  char* ws; void* out;
  int w, xcd, r;
  unsigned lds0, t0, t1;
};
constexpr int LDS_ALL = 160 * 1024, LDS_SCR = LDS_ALL - 256;
DI int fresh_lane() { int l; asm volatile("v_mbcnt_lo_u32_b32 %0, -1, 0\n\tv_mbcnt_hi_u32_b32 %0, -1, %0" : "=v"(l)); __builtin_assume(l >= 0 && l < 64); return l; }
DI unsigned opq(unsigned v) { PIN(v); return v; }
constexpr int LDS_CNT = 147456;
DI void cnt_add(const Ctx& c, int i) {
  asm volatile("s_mov_b64 exec, 1\n\tds_add_u32 %0, %1\n\ts_mov_b64 exec, -1" ::"v"(c.lds0 + LDS_CNT + 4 * i), "v"(1u) : "memory");
}
DI unsigned cnt_get(const Ctx& c, int i) {
  unsigned v;
  asm volatile("ds_read_b32 %0, %1\n\ts_waitcnt lgkmcnt(0)" : "=v"(v) : "v"(c.lds0 + LDS_CNT + 4 * i) : "memory");
  return rfl(v);
}
DI void cnt_wait(const Ctx& c, int i, unsigned n) {
  if (__builtin_expect(cnt_get(c, i) < n, 0))
    for (int spin = 0; spin < (1 << 22) && cnt_get(c, i) < n; ++spin) __builtin_amdgcn_s_sleep(1);
}
DI rsrc_t fl_rsrc(const Ctx& c) { return mk_rsrc(c.ws, FL_END); }
DI void set_flag(const Ctx& c, unsigned off) {
  u32x2 t;
  asm volatile("v_mov_b32 %0, %1" : "=v"(t[0]) : "s"(c.t0));
  asm volatile("v_mov_b32 %0, %1" : "=v"(t[1]) : "s"(c.t1));
  asm volatile("s_mov_b64 exec, 1\n\tbuffer_store_dwordx2 %0, off, %1, %2 sc1\n\ts_mov_b64 exec, -1" ::"v"(t), "s"(fl_rsrc(c)), "s"(off) : "memory");
}
DI bool tag_ok(const Ctx& c, u32x2 v) { return __builtin_amdgcn_ballot_w64(v[0] != c.t0 || v[1] != c.t1) == 0; }
DI void poll(const Ctx& c, unsigned voff, unsigned soff) {
  for (int spin = 0; spin < (1 << 20); ++spin) {
    u32x2 v;
    asm volatile("buffer_load_dwordx2 %0, %1, %2, %3 offen sc1\n\ts_waitcnt vmcnt(0)" : "=&v"(v) : "v"(voff), "s"(fl_rsrc(c)), "s"(soff) : "memory");
    if (tag_ok(c, v)) break;
  }
}

DI bool poll_first(const Ctx& c, unsigned voff, unsigned soff, unsigned long long first) {
  unsigned long long m = 0;
  for (int spin = 0; spin < (1 << 20); ++spin) {
    u32x2 v;
    asm volatile("buffer_load_dwordx2 %0, %1, %2, %3 offen sc1\n\ts_waitcnt vmcnt(0)" : "=&v"(v) : "v"(voff), "s"(fl_rsrc(c)), "s"(soff) : "memory");
    m = __builtin_amdgcn_ballot_w64(v[0] != c.t0 || v[1] != c.t1);
    if ((m & first) == 0) break;
  }
  return m == 0;
}

constexpr int K1_R = 5, K1_INIT = 3, K1_NA = 2;
constexpr int K1_SLOT = 30720, K1_TBL = K1_SLOT * 5, K1_NUL = K1_TBL + 2048;
extern "C" __global__ void k9_end();

DI int mean_task(const Ctx& c) { return (31 - c.r) * 8 + c.xcd + 72 * (7 - c.w); }
template <int NN> DI void p1_body(Ctx& c, int mt, int nt, bf16x8 (&mv)[9]) {
  char* ws = c.ws;
  const int w = c.w, lane = fresh_lane();
  const unsigned lds0 = c.lds0;
  const rsrc_t rx = mk_rsrc(c.x, S_ * D_ * 2);
  const int t16 = lane & 15, g = lane >> 4, q4 = (lane >> 2) & 3, p = lane & 3, m = w & 3;
  constexpr int NT = NN ? 3 : 4;
  unsigned xv[2], wv[2];
#pragma unroll
  for (int i = 0; i < 2; ++i) {
    const int row = 8 * (2 * w + i) + (lane >> 3);
    xv[i] = (128 * mt + row) * 1152 + 16 * ((lane & 7) ^ ((row >> 1) & 7));
    const int li = 64 * (2 * w + i) + lane;
    const int r64 = (li * 4682) >> 16, cc = li - 14 * r64;
    const int rho = r64 & 31, k = (r64 & 32) + 8 * ((rho >> 2) & 3) + 4 * (rho >> 4) + (rho & 3);
    wv[i] = k * 3456 + ((216 * nt) & ~15) + 16 * cc;
  }
  const rsrc_t rw = mk_rsrc(c.wqkv, NN && w == 7 ? 0 : D_ * 3 * D_ * 2);
  auto issue1 = [&](int slab, int i) {
    const unsigned dst = lds0 + (slab % K1_R) * K1_SLOT + w * 2048 + (i >> 1) * 1024;
    if (i & 1) glds16(NN && w == 7 ? lds0 + K1_NUL : dst + 16384, wv[i >> 1], rw, slab * 64 * 3456);
    else glds16(dst, xv[i >> 1], rx, slab * 128);
  };
#pragma unroll
  for (int sl = 0; sl < K1_INIT; ++sl)
#pragma unroll
    for (int i = 0; i < 4; ++i) issue1(sl, i);
  u32x2 ev;
  bload8c(ev, 0u, fl_rsrc(c), 0);
  if (NN && w == 7) {
    unsigned long long pc; unsigned back, size;
    asm volatile("s_getpc_b64 %0\n.Lk1pc%=:\n\ts_mov_b32 %1, .Lk1pc%=-k1_all\n\ts_mov_b32 %2, k9_end-k1_all" : "=s"(pc), "=s"(back), "=s"(size));
    warm(LDS_SCR, lane < 24 ? (c.r * 24 + lane) * 128 : OOB, mk_rsrc((const void*)(pc - back), size), 0);
  }
  const unsigned wa = lds0 + 16384 + (4 * g + q4) * 224 + (nt & 1) * 8;
  unsigned ad[3][5];
  ad[0][0] = wa + 8 * p + 144 * NN;
  ad[0][1] = wa + 72 * (p < 3 ? p : 2) + 32;
  ad[0][2] = NN && m >= 2 ? ad[0][1] : ad[0][0] + 72 * (m >> 1) + 40 * (m & 1);
  ad[0][3] = lds0 + (32 * m + t16) * 128 + 16 * (g ^ ((t16 >> 1) & 7));
  ad[0][4] = ad[0][3] ^ 64;
#pragma unroll
  for (int i = 0; i < 5; ++i) { ad[1][i] = ad[0][i] + 2 * K1_SLOT; ad[2][i] = ad[0][i] + 4 * K1_SLOT; PIN(ad[0][i]); PIN(ad[1][i]); PIN(ad[2][i]); }
  const unsigned tb = lds0 + K1_TBL, cb = tb + 1024, dummy = cb + 512;
  unsigned e_cs = t16 == 0 ? cb + (w * 16 + 4 * g) * 4 : dummy, e_st = tb + (32 * m + t16) * 8, e_cc = cb + (64 * NN + 4 * g) * 4;
  unsigned e_s1 = g == 3 ? e_st : dummy, e_s2 = g == q4 ? e_st + 4 : dummy, e_vo[2], e_so[4], e_t2 = 0, e_one = 0;
  unsigned e_sc[2];
  float e_tsc = 0.f;
  {
    constexpr unsigned KV0 = WS_KV - WS_QF;
    const int tile32 = 4 * mt + m, key = 16 * (g >> 1) + t16;
    const unsigned lo_qk = (g & 1) * 512 + key * 16, lo_v = 1280 + key * 32 + (g & 1) * 16;
#pragma unroll
    for (int i = 0; i < 2 - NN; ++i) {
      const int pg = 3 * nt + 2 * NN + i, type = pg >> 4, rl = type ? KVREC : QREC;
      e_so[2 * i] = (type ? KV0 : 0) + (2 * (pg & 15) * 64 + tile32) * rl;
      e_so[2 * i + 1] = e_so[2 * i] + 64 * rl;
      e_vo[i] = type == 2 ? lo_v : lo_qk;
      e_sc[i] = type ? 0x3f800000u : __builtin_bit_cast(unsigned, QSCALE);
      asm volatile("" : "+s"(e_so[2 * i]), "+s"(e_so[2 * i + 1]), "+s"(e_sc[i]), "+v"(e_vo[i]));
    }
    if constexpr (NN) {
      const int pg = 3 * nt + g, type = pg >> 4, rl = type ? KVREC : QREC;
      e_vo[1] = g < 3 ? (type ? KV0 : 0) + (2 * (pg & 15) * 64 + tile32) * rl + (type == 2 ? 2304 : 1024) + t16 * 8 : OOB;
      e_t2 = e_vo[1] + 64 * rl;
      e_one = type == 1 ? 0u : 0x00003f80u;
      e_tsc = type ? 1.f : QSCALE;
      PIN(e_vo[1]); PIN(e_t2); PIN(e_one); PIN(e_tsc); PIN(e_s1); PIN(e_s2);
    }
    PIN(e_cs); PIN(e_st); PIN(e_cc);
  }
  f32x4 acc[4][2] = {}, cs = {};
  bf16x8 ones = __builtin_bit_cast(bf16x8, (u32x4){ONES2, ONES2, ONES2, ONES2});
  PIN(ones);
  bf16x8 fb[2][2];
  bf16x4 fa[2][NT + 1][2];
  constexpr int NR = 2 * NT + 4;
  auto rd1 = [&](auto KS, auto I) {
    constexpr int ks = KS.value, i = I.value, sp = ks & 1, sl = (ks >> 1) % K1_R, ti = (i - 2) >> 1;
    constexpr unsigned so = (sl & 1) * K1_SLOT;
    if constexpr (ks < 18 && i < NR) {
      if constexpr (i < 2) fb[sp][i] = ldsr<bf16x8>(ad[sl >> 1][3 + sp] + so + i * 2048);
      else {
        constexpr int ai = ti == NT ? 2 : (NN && ti == 2) ? 1 : 0;
        constexpr unsigned co = ai ? 0 : 72 * (ti >> 1) + 40 * (ti & 1);
        fa[sp][ti][i & 1] = trh(ad[sl >> 1][ai] + so + co + sp * 32 * 224 + (i & 1) * 16 * 224);
      }
    }
  };
  sfor<9>([&](auto SL) {
    constexpr int sl = SL.value;
    constexpr int iss = sl == 0 ? K1_INIT : (sl + K1_R - 1 < 9 ? sl + K1_R - 1 : 9);
    constexpr int need = sl < K1_NA ? sl : (sl + 1 < 9 ? sl + 1 : 8);
    constexpr int nw = 4 * (iss - need - 1) + (need < K1_INIT ? 1 : 0);
    if constexpr (sl == 5) asm volatile("s_waitcnt vmcnt(%1)" : "+v"(ev) : "i"(nw) : "memory");
    else wait_vm<nw>();
    bar();
    if constexpr (sl <= K1_NA) sfor<NR>([&](auto I) { rd1(ic<2 * sl>{}, I); });
    if constexpr (sl == 5) {
      const unsigned long long e = (((unsigned long long)rfl(ev[1]) << 32) | rfl(ev[0])) + 1ull;
      c.t0 = (unsigned)e; c.t1 = (unsigned)(e >> 32);
    }
    sfor<2>([&](auto SP) {
      constexpr int sp = SP.value, ks = 2 * sl + sp;
      bf16x8 tl;
      if constexpr (NN) tl = t16 >= 12 ? ones : cat(fa[sp][2][0], fa[sp][2][1]);
      sfor<9>([&](auto M) {
        constexpr int mi = M.value, ti = mi >> 1;
        if constexpr (mi == 8) cs = mfma16(cat(fa[sp][NT][0], fa[sp][NT][1]), ones, cs);
        else if constexpr (NN && ti == 3) acc[3][mi & 1] = mfma16(fb[sp][mi & 1], fb[sp][mi & 1], acc[3][mi & 1]);
        else if constexpr (NN && ti == 2) acc[2][mi & 1] = mfma16(tl, fb[sp][mi & 1], acc[2][mi & 1]);
        else acc[ti][mi & 1] = mfma16(cat(fa[sp][ti][0], fa[sp][ti][1]), fb[sp][mi & 1], acc[ti][mi & 1]);
        SBAR();
        if constexpr (sp == 0 || sl >= K1_NA) {
          if constexpr (mi < 3) { rd1(ic<ks + 1>{}, ic<2 * mi>{}); rd1(ic<ks + 1>{}, ic<2 * mi + 1>{}); }
          else rd1(ic<ks + 1>{}, ic<3 + mi>{});
        }
        if constexpr (sl == 0 && K1_R - K1_INIT != 1) {
          constexpr int no = 4 * (K1_R - K1_INIT);
          sfor<no>([&](auto O) { if constexpr (O.value * 18 / (no ? no : 1) == 9 * sp + mi) issue1(K1_INIT + O.value / 4, O.value & 3); });
        } else if constexpr (sl + K1_R - 1 < 9 && (mi == 2 + 2 * NN || mi == 6 + 2 * NN)) issue1(sl + K1_R - 1, 2 * sp + (mi >= 6));
        SBAR();
      });
    });
  });
  ldsw<f32x4>(e_cs, cs);
  if constexpr (NN) sfor<2>([&](auto T) {
    const f32x4 gr = acc[3][T.value];
    const float d01 = (p & 1) ? gr[1] : gr[0], d23 = (p & 1) ? gr[3] : gr[2];
    ldsw<float>(e_s1 + 128 * T.value, acc[2][T.value][0]);
    ldsw<float>(e_s2 + 128 * T.value, (p & 2) ? d23 : d01);
  });
  lds_bar();
  f32x2 sv[2];
  f32x4 cc[4];
  sfor<2>([&](auto T) { sv[T.value] = ldsr<f32x2>(e_st + 128 * T.value); });
  sfor<NT>([&](auto I) { cc[I.value] = ldsr<f32x4>(e_cc + 64 * I.value); });
  SBAR();
  float nmu[2], rs[2];
  sfor<2>([&](auto T) {
    nmu[T.value] = sv[T.value][0] * (-1.f / D_);
    rs[T.value] = __builtin_amdgcn_rsqf(fmaxf(sv[T.value][1] * (1.f / D_) - nmu[T.value] * nmu[T.value], 0.f) + 1e-5f);
  });
  const rsrc_t ro = mk_rsrc(ws + WS_QF, 32 * 64 * (QREC + KVREC));
  if (!NN && nt == 0) bstore4<16>(__float_as_uint(-((g & 1) ? nmu[1] : nmu[0])), mk_rsrc(ws + WS_MX + (128 * mt + 32 * m) * 4, 128), fresh_lane() * 4, 0);
  sfor<(NN ? 2 : 4)>([&](auto TI) {
    constexpr int ti = TI.value;
    const f32x4 v0 = (acc[ti][0] + cc[ti] * nmu[0]) * (rs[0] * __uint_as_float(e_sc[ti >> 1])), v1 = (acc[ti][1] + cc[ti] * nmu[1]) * (rs[1] * __uint_as_float(e_sc[ti >> 1]));
    auto s0 = __builtin_amdgcn_permlane32_swap(pk2(v0[0], v0[1]), pk2(v1[0], v1[1]), false, false);
    auto s1 = __builtin_amdgcn_permlane32_swap(pk2(v0[2], v0[3]), pk2(v1[2], v1[3]), false, false);
    bstore16<16>((u32x4){s0[0], s1[0], s0[1], s1[1]}, ro, e_vo[ti >> 1], e_so[ti]);
  });
  if constexpr (NN) sfor<2>([&](auto T) {
    const f32x4 v = (acc[2][T.value] + cc[2] * nmu[T.value]) * (rs[T.value] * e_tsc);
    bstore8<16>((u32x2){pk2(v[0], v[1]), e_one}, ro, e_vo[1], T.value * 128);
    bstore8<16>((u32x2){pk2(v[2], v[3]), e_one}, ro, e_t2, T.value * 128);
  });
  if (NN && mean_task(c) < 72) {
    const int z = fresh_lane(), ks = mean_task(c) >> 2, kg = mean_task(c) & 3, j = z & 7;
    const int hd = ks < 16 ? 2 * ks + (kg >> 1) : 16 * (ks - 16) + 4 * kg + (j >> 1);
    const int row = 18 * hd + (ks < 16 ? ((hd & 1) ? 2 : 0) + 8 * (kg & 1) + j : ((hd & 1) ? 0 : 16) + (j & 1));
    const unsigned vo = row * 1152 + (z >> 3) * 16;
    sfor<9>([&](auto K) { bload16(mv[K.value], vo, mk_rsrc(c.wo, D_ * D_ * 2), K.value * 128); });
    wait_vm<9>();
  } else wait_vm<0>();
  bar();
  if (w == 0) set_flag(c, FL1 + (mt * 16 + nt) * 64);
}

constexpr int P3_WO = 73728;
constexpr int K4_RING = 8192;
constexpr unsigned k4_so(int u) { return ((((u >> 1) + 2) & 3) * 2 + (u & 1)) * 8192; }
DI unsigned k4_wv(int lane, int w, int Qd, int j) {
  const int rho = 8 * (w & 3) + (lane >> 3);
  return rho * 9216 + 1152 * Qd + 256 * j + 32 * (((lane & 7) >> 1) ^ ((rho >> 1) & 3)) + 16 * (lane & 1);
}

template <int U0, int N> DI void k4_first(const Ctx& c, int lane) {
  const rsrc_t rg = mk_rsrc(c.wg, D_ * F_ * 2), ru = mk_rsrc(c.wu, D_ * F_ * 2);
  const unsigned wv4 = k4_wv(lane, c.w, c.xcd, c.r & 3), dst = c.lds0 + K4_RING + (c.w & 3) * 1024;
  sfor<N>([&](auto U) {
    glds16(dst + k4_so(U0 + U.value), wv4, rg, (U0 + U.value) * 32 * 9216);
    glds16(dst + 4096 + k4_so(U0 + U.value), wv4, ru, (U0 + U.value) * 32 * 9216);
  });
}
constexpr int P3_XA = K4_RING + k4_so(3), P3_XB = LDS_CNT + 256, P3_WB = P3_XB + 8192;
DI int p3_T(const Ctx& c, int w) { return 16 * c.xcd + 8 * (w >> 2) + 4 * (c.r >= 9) + (w & 3); }
DI void p2_early(const Ctx& c) {
  const int w = c.w, lane = fresh_lane(), row = lane >> 3, nt = c.r >= 9 ? c.r - 9 : c.r;
  const rsrc_t rx = mk_rsrc(c.x, c.r < 18 ? S_ * D_ * 2 : 0);
  const unsigned xo = row * 1152 + 16 * ((lane & 7) ^ (row >> 1)), so = 16 * p3_T(c, w) * 1152 + 128 * nt;
  const unsigned xd = c.lds0 + (w < 4 ? P3_XA + w * 1024 : P3_XB + (w - 4) * 2048);
  glds16(xd, xo, rx, so);
  glds16(xd + (w < 4 ? 4096 : 1024), xo ^ 64, rx, so + 8 * 1152);
  if (w >= 4) k4_first<0, 3>(c, lane);
}
DI void p2_means(const Ctx& c, unsigned flag) {
  const int lane = fresh_lane();
  u32x2 pf, pg;
  bload8c(pf, lane * 64, fl_rsrc(c), FLW);
  bload8c(pg, (64 + (lane & 7)) * 64, fl_rsrc(c), FLW);
  wait_vm<2>();
  set_flag(c, flag);
  asm volatile("s_waitcnt vmcnt(1)" : "+v"(pf), "+v"(pg)::"memory");
  if (__builtin_expect(!tag_ok(c, pf) || !tag_ok(c, pg), 0)) { poll(c, lane * 64, FLW); poll(c, (64 + (lane & 7)) * 64, FLW); }
  const rsrc_t rwb = mk_rsrc(c.ws + WS_WB, 1152);
  glds16c(c.lds0 + P3_WB, lane * 16, rwb, 0);
  glds16c(c.lds0 + P3_WB + 1024, lane * 16 + 1024, rwb, 0);
}

DI void p2(const Ctx& c, bf16x8 (&mv)[9]) {
  constexpr int WQ = 2, EX0 = 2;
  const int head = 4 * c.xcd + (c.r & 3), qb = c.r >> 2;
  char* ws = c.ws;
  const int w = c.w, lane = fresh_lane();
  const unsigned lds0 = c.lds0;
  const int q = lane & 31, h = lane >> 5, G = lane >> 4;
  const int qt = 8 * qb + w;
  const rsrc_t rq = mk_rsrc(ws + WS_QF, 32 * 64 * QREC), rkv = mk_rsrc(ws + WS_KV, 32 * 64 * KVREC);
  unsigned kb = lds0 + w * KVREC + lane * 16, ktb = lds0 + w * KVREC + 1024 + q * 8;
  unsigned vb[4];
#pragma unroll
  for (int i = 0; i < 4; ++i) { const int row = 8 * i + 4 * (G >> 1) + ((lane >> 2) & 3); vb[i] = lds0 + w * KVREC + ((G & 1) ? 2304 + row * 8 : 1280 + row * 32 + 8 * (lane & 3)); PIN(vb[i]); }
  unsigned mf[16], mk[8];
#pragma unroll
  for (int e = 0; e < 16; ++e) { mf[e] = ((e & 3) + 8 * (e >> 2) <= q - 4 * h) ? ~0u : 0u; PIN(mf[e]); }
#pragma unroll
  for (int e = 0; e < 8; ++e) { mk[e] = (mf[2 * e] & 0xffffu) | (mf[2 * e + 1] & 0xffff0000u); PIN(mk[e]); }
  const unsigned tt = 2 * qt + (q >> 4);
  unsigned om_o = (tt * 18 + (head >> 1)) * 1024 + ((q & 15) + 16 * (2 * (head & 1) + h)) * 16;
  unsigned ot_o = h ? OOB : (tt * 18 + 16 + (head >> 4)) * 1024 + ((q & 15) + 16 * ((head >> 2) & 3)) * 16 + (head & 3) * 4;
  unsigned l16 = lane * 16, qto = h ? OOB : 1024 + q * 8;
  const bool ga = w < 4 && c.r < 18;
  const rsrc_t rwo = mk_rsrc(c.wo, D_ * D_ * 2);
  unsigned wv, wvt;
  {
    const int rho = 8 * w + (lane >> 3), rg = (rho >> 2) & 3, rj = 4 * (rho >> 4) + (rho & 3), th = 4 * rg + (rj >> 1);
    const unsigned cbyte = 128 * (c.r >= 9 ? c.r - 9 : c.r) + 32 * (((lane & 7) >> 1) ^ ((rho >> 1) & 3)) + 16 * (lane & 1);
    wv = (20 * (rg >> 1) + 8 * (rg & 1) + rj) * 1152 + cbyte; wvt = (18 * th + ((th & 1) ? 0 : 16) + (rj & 1)) * 1152 + cbyte;
    PIN(wv); PIN(wvt);
  }
  auto wo = [&](auto K0, auto N) {
    if (ga) sfor<N.value>([&](auto I) {
      constexpr int k = K0.value + I.value;
      if constexpr (k < 16) glds16(lds0 + P3_WO + (w + 4 * k) * 1024, wv, rwo, k * 36 * 1152);
      else glds16(lds0 + P3_WO + (w + 4 * k) * 1024, wvt, rwo, (k - 16) * 288 * 1152);
    });
    SBAR();
  };
  bf16x4 neg = __builtin_bit_cast(bf16x4, (u32x2){h ? 0u : 0xbf80bf80u, h ? 0u : 0x0000bf80u});
  PIN(neg);
  PIN(kb); PIN(ktb); PIN(om_o); PIN(ot_o); PIN(l16); PIN(qto);
  if (qb == 0) {
#pragma unroll
    for (int i = 0; i < 3; ++i) {
      const unsigned o = min(w * 64 + lane + 512 * i, 1279) * 16, m = (o % KVREC - 1024 < 256) ? 0x0000f149u : 0u;
      ldsw<u32x4>(lds0 + o, (u32x4){0u, m, 0u, m});
    }
  }
  if (w >= 1 && w <= 3) warm(lds0 + LDS_SCR, (w * 64 + lane < 224 ? (w * 64 + lane - 64) * 128 : OOB), rkv, (head * 64 + 8 * qb) * KVREC);
  if (w == 4 || w == 5) warm(lds0 + LDS_SCR, (w * 64 + lane < 336 ? (w * 64 + lane - 256) * 128 : OOB), rq, (head * 64 + 8 * qb) * QREC);
  if (mean_task(c) < 72) {
    const bf16x8 ones = __builtin_bit_cast(bf16x8, (u32x4){ONES2, ONES2, ONES2, ONES2});
    f32x4 a = {};
    wait_vm<0>();
    sfor<9>([&](auto K) { PIN(mv[K.value]); a = mfma16(mv[K.value], ones, a); });
    const unsigned d0 = pk2(sum_x32(a[0]) * (1.f / D_), sum_x32(a[1]) * (1.f / D_)), d1 = pk2(sum_x32(a[2]) * (1.f / D_), sum_x32(a[3]) * (1.f / D_));
    bstore8<16>((u32x2){d0, d1}, mk_rsrc(ws + WS_WB, 1152), (lane & 47) == 0 ? lane >> 1 : OOB, mean_task(c) * 16);
  }
  if (w == 0) {
    const int ty = lane < 4 ? 1 : lane < 8 ? 2 : 0, pm = max(2 * qb + (ty ? (lane & 3) - 2 : (lane & 1)), 0);
    ldsw<unsigned>(lds0 + LDS_CNT + 4 * (lane & 7), 0u);
    poll(c, (pm * 16 + (((16 * ty + (head >> 1)) * 43) >> 7)) * 64, FL1);
  }
  lds_bar();
  bf16x8 qm; u32x2 qt2;
#pragma unroll
  for (int i = 0; i < 5; ++i) { const int pc = max(w + 8 * i, qb ? 0 : 20 + w); glds16c(lds0 + pc * 1024, l16, rkv, (head * 64 + 8 * qb - 8) * KVREC + pc * 1024); }
  bload16c(qm, l16, rq, (head * 64 + qt) * QREC);
  bload8c(qt2, qto, rq, (head * 64 + qt) * QREC);
  auto toff = [&](int i) { return (unsigned)(i * KVREC); };
  asm volatile("s_waitcnt vmcnt(0)" : "+v"(qm), "+v"(qt2)::"memory");
  if (mean_task(c) < 72) set_flag(c, FLW + mean_task(c) * 64);
  bar();
  p2_early(c);
  const bf16x4 qt4 = __builtin_bit_cast(bf16x4, qt2);
  bf16x8 km[9]; bf16x4 kt[9];
  constexpr int ord[9] = {0, 8, 1, 2, 3, 4, 5, 6, 7};
  sfor<9>([&](auto I) { constexpr int i = ord[I.value]; km[i] = ldsr<bf16x8>(kb + toff(i)); kt[i] = ldsr<bf16x4>(ktb + toff(i)); });
  SBAR();
  f32x16 s[9];
  float m4[4];
  auto qk = [&](auto I, auto J) {
    constexpr int i = I.value, j = J.value;
    f32x16 a = mfma32(km[i], qm, (f32x16){}), b;
    if constexpr (j < 9) b = mfma32(km[j], qm, (f32x16){});
    s[i] = __builtin_amdgcn_mfma_f32_32x32x8bf16_1k(kt[i], qt4, a, 0, 0, 0);
    if constexpr (j < 9) s[j] = __builtin_amdgcn_mfma_f32_32x32x8bf16_1k(kt[j], qt4, b, 0, 0, 0);
    SBAR();
  };
  auto mx1 = [&](auto I) {
    constexpr int i = I.value;
#pragma unroll
    for (int e = 0; e < 8; ++e) m4[e & 3] = fmaxf(fmaxf(m4[e & 3], s[i][2 * e]), s[i][2 * e + 1]);
  };
  bf16x4 vf[2][4];
  auto rdv = [&](int set, unsigned off) {
#pragma unroll
    for (int i = 0; i < 4; ++i) vf[set][i] = trh(vb[i] + off);
  };
  qk(ic<0>{}, ic<8>{}); wo(ic<0>{}, ic<WQ>{});
  qk(ic<1>{}, ic<2>{}); wo(ic<WQ>{}, ic<WQ>{});
  SBAR();
#pragma unroll
  for (int e = 0; e < 16; ++e) s[8][e] = __uint_as_float((__float_as_uint(s[8][e]) & mf[e]) | (__float_as_uint(s[0][e]) & ~mf[e]));
#pragma unroll
  for (int e = 0; e < 4; ++e) asm("v_max_f32 %0, %1, %2\n\tv_max3_f32 %0, %0, %3, %4" : "=&v"(m4[e]) : "v"(s[8][4 * e]), "v"(s[8][4 * e + 1]), "v"(s[8][4 * e + 2]), "v"(s[8][4 * e + 3]));
  SBAR();
  qk(ic<3>{}, ic<4>{}); wo(ic<2 * WQ>{}, ic<WQ>{});
  mx1(ic<1>{}); mx1(ic<2>{}); SBAR();
  qk(ic<5>{}, ic<6>{}); wo(ic<3 * WQ>{}, ic<WQ>{});
  mx1(ic<3>{}); mx1(ic<4>{}); SBAR();
  qk(ic<7>{}, ic<9>{}); wo(ic<4 * WQ>{}, ic<(18 - 4 * WQ < WQ ? 18 - 4 * WQ : WQ)>{});
  rdv(1, toff(1));
  mx1(ic<5>{}); mx1(ic<6>{}); SBAR();
  mx1(ic<7>{});
  float mx = max_x32(fmaxf(fmaxf(m4[0], m4[1]), fmaxf(m4[2], m4[3])));
  const float r1 = mx - __uint_as_float(__float_as_uint(mx) & 0xffff0000u), r2 = r1 - __uint_as_float(__float_as_uint(r1) & 0xffff0000u);
  const bf16x4 m3 = __builtin_bit_cast(bf16x4, (u32x2){__builtin_amdgcn_perm(__float_as_uint(r1), __float_as_uint(mx), 0x07060302u), __float_as_uint(r2) >> 16});
  s[1] = __builtin_amdgcn_mfma_f32_32x32x8bf16_1k(neg, m3, s[1], 0, 0, 0);
  s[2] = __builtin_amdgcn_mfma_f32_32x32x8bf16_1k(neg, m3, s[2], 0, 0, 0);
  SBAR();
  f32x16 o = {};
  u32x4 pp;
  sfor<16>([&](auto H) {
    constexpr int hh = H.value, i = 1 + hh / 2, ks = hh & 1;
    float pv[8];
    auto ex = [&](auto E0, auto N) { sfor<N.value>([&](auto E) { constexpr int e = E0.value + E.value; pv[e] = __builtin_amdgcn_exp2f(s[i][8 * ks + e]); PIN(pv[e]); }); SBAR(); };
    ex(ic<0>{}, ic<EX0>{});
    if constexpr (hh > 0 && hh < 15) o = mfma32(cat(vf[(1 + (hh - 1) / 2) & 1][2 * (ks ^ 1)], vf[(1 + (hh - 1) / 2) & 1][2 * (ks ^ 1) + 1]), __builtin_bit_cast(bf16x8, pp), o);
    if constexpr (hh == 15) {
      const u32x4 pd = {pp[0] & mk[0], pp[1] & mk[1], pp[2] & mk[2], pp[3] & mk[3]};
      o = mfma32(cat(vf[0][0], vf[0][1]), __builtin_bit_cast(bf16x8, pd), o);
      o = mfma32(cat(vf[1][0], vf[1][1]), __builtin_bit_cast(bf16x8, pp ^ pd), o);
    }
    SBAR();
    if constexpr (ks == 0) rdv((i + 1) & 1, toff(i < 8 ? i + 1 : 0));
    SBAR();
    if constexpr (5 * WQ + hh < 18) wo(ic<5 * WQ + hh>{}, ic<1>{});
    ex(ic<EX0>{}, ic<4>{});
    if constexpr (ks == 1 && i + 2 < 9) { s[i + 2] = __builtin_amdgcn_mfma_f32_32x32x8bf16_1k(neg, m3, s[i + 2], 0, 0, 0); SBAR(); }
    ex(ic<EX0 + 4>{}, ic<4 - EX0>{});
    pp = (u32x4){pk2(pv[0], pv[1]), pk2(pv[2], pv[3]), pk2(pv[4], pv[5]), pk2(pv[6], pv[7])};
    PIN(pp);
    SBAR();
  });
  {
    const u32x4 pd = {pp[0] & mk[4], pp[1] & mk[5], pp[2] & mk[6], pp[3] & mk[7]};
    o = mfma32(cat(vf[0][2], vf[0][3]), __builtin_bit_cast(bf16x8, pd), o);
    o = mfma32(cat(vf[1][2], vf[1][3]), __builtin_bit_cast(bf16x8, pp ^ pd), o);
  }
  auto lr = __builtin_amdgcn_permlane32_swap(__float_as_uint(o[10]), __float_as_uint(o[10]), false, false);
  const float inv = __builtin_amdgcn_rcpf(__uint_as_float(lr[0]));
  const rsrc_t ro = mk_rsrc(ws + WS_OF, 128 * 18 * 1024);
  const u32x4 om = {pk2(o[0] * inv, o[1] * inv), pk2(o[2] * inv, o[3] * inv), pk2(o[4] * inv, o[5] * inv), pk2(o[6] * inv, o[7] * inv)};
  bstore16<16>(om, ro, om_o, 0);
  bstore4<16>(pk2(o[8] * inv, o[9] * inv), ro, ot_o, 0);
  cnt_add(c, 1);
  if (w == 3 && c.r < 18) p2_means(c, FL2 + (qt * 32 + head) * 64);

  else { wait_vm<0>(); set_flag(c, FL2 + (qt * 32 + head) * 64); if (w >= 4) cnt_add(c, 4); }
}

DI void p3_warm(const Ctx& c) {
  const int lane = fresh_lane(), rw = (lane * 57) >> 9;
  const unsigned o = lane < 36 ? rw * 9216 + (lane - 9 * rw) * 128 : OOB, so = 4 * ((c.r - 18) * 8 + c.w) * 9216 + c.xcd * 1152;
  warm(c.lds0 + LDS_SCR, o, mk_rsrc(c.wg, D_ * F_ * 2), so);
  warm(c.lds0 + LDS_SCR, o, mk_rsrc(c.wu, D_ * F_ * 2), so);
}
DI void p3_side(const Ctx& c) {
  char* ws = c.ws;
  const int w = c.w, xcd = c.xcd, lane = fresh_lane();
  const int sb = c.r - 18, blk = sb * 8 + xcd;
  const rsrc_t rwt = mk_rsrc(ws + WS_WT, 4 * 4608 * 32), rlg = mk_rsrc(ws + WS_LG, 8 * 4 * 36864);
  u32x2 pf;
  bload8c(pf, lane * 64, mk_rsrc(c.ws, blk == 104 && w >= 4 ? FL_END : 0), FL1 + (w & 3) * 64 * 64);
  const int mat = sb >= 5, t0 = sb * 8 + w, m0 = t0 >= 72, r0 = 8 * (t0 - 72 * m0) + (lane >> 3), r1 = 8 * (t0 + 40) + (lane >> 3);
  const unsigned co = (9 * xcd + 8) * 128 + (lane & 7) * 16, lo = ((lane & 7) >> 1) * 36864 + (lane & 1) * 16;
  const u32x4 v0 = __builtin_amdgcn_raw_buffer_load_b128(mk_rsrc(m0 ? c.wu : c.wg, D_ * F_ * 2), r0 * 9216 + co, 0, 0);
  const u32x4 v1 = __builtin_amdgcn_raw_buffer_load_b128(mk_rsrc(c.wu, t0 < 32 ? D_ * F_ * 2 : 0), r1 * 9216 + co, 0, 0);
  const int drow = 64 * (blk - 40) + 8 * w + (lane >> 3);
  const u32x4 dv = __builtin_amdgcn_raw_buffer_load_b128(mk_rsrc(c.wd, F_ * D_ * 2), mat ? drow * 1152 + 1024 + (lane & 7) * 16 : OOB, 0, 0);
  bstore16<16>(v0, rlg, lo + r0 * 32 + m0 * 18432, xcd * 8 * 18432);
  bstore16<16>(v1, rlg, t0 < 32 ? lo + r1 * 32 : OOB, (xcd * 8 + 1) * 18432);
  const int pr = (drow % 576) >= 512 ? ((drow & ~24) | ((drow & 8) << 1) | ((drow & 16) >> 1)) : drow;
  bstore16<16>(dv, rwt, mat ? (((lane & 7) >> 1) * 4608 + pr) * 32 + (lane & 1) * 16 : OOB, 0);
  asm volatile("s_waitcnt vmcnt(0)" : "+v"(pf)::"memory");
  if (blk == 104 && w >= 4 && !tag_ok(c, pf)) poll(c, lane * 64, FL1 + (w - 4) * 64 * 64);
  bar();
  if (w == 0) {
    set_flag(c, FLS + blk * 64);
    if (blk == 104) set_flag(c, 0);
  }
}

DI void p3_gemm(const Ctx& c) {
  char* ws = c.ws;
  const int w = c.w, lane = fresh_lane();
  const unsigned lds0 = c.lds0;
  const int t16 = lane & 15, g = lane >> 4;
  const int nt = c.r >= 9 ? c.r - 9 : c.r;
  const int T = p3_T(c, w);
  const rsrc_t rof = mk_rsrc(ws + WS_OF, 128 * 18 * 1024);
  unsigned wa[4];
  {
    const int r2 = 4 * g + ((lane >> 2) & 3);
#pragma unroll
    for (int i = 0; i < 4; ++i) wa[i] = lds0 + P3_WO + r2 * 128 + 32 * (i ^ ((r2 >> 1) & 3)) + 8 * (lane & 3);
  }
  unsigned l16 = lane * 16, so = g == 0 ? t16 * 8 : OOB, fo = (lane & 31) * 64;
  unsigned xa = lds0 + (w < 4 ? P3_XA + w * 1024 + (t16 >> 3) * 4096 : P3_XB + (w - 4) * 2048 + (t16 >> 3) * 1024) + (t16 & 7) * 128 + 16 * ((g >> 1) ^ (t16 >> 1)) + 8 * (g & 1);
  PIN(l16); PIN(so); PIN(fo); PIN(xa);
  if (w < 4) { wait_vm<0>(); cnt_add(c, 0); }
  u32x2 pf;
  bload8c(pf, fo, fl_rsrc(c), FL2 + (T >> 1) * 32 * 64);
  cnt_wait(c, 0, 4);
  u32x2 xr[4];
  sfor<4>([&](auto I) { xr[I.value] = ldsr<u32x2>(xa ^ (32 * I.value)); });
  constexpr int AH = 5;
  bf16x8 of[18];
  bf16x4 fr[AH + 1][4][2];
  unsigned wb[4];
#pragma unroll
  for (int i = 0; i < 4; ++i) { wb[i] = wa[i] + 65536; PIN(wb[i]); }
  auto rd1 = [&](auto KS, auto I) {
    constexpr int ks = KS.value, i = I.value;
    if constexpr (ks < 18) fr[ks % (AH + 1)][i >> 1][i & 1] = trh((ks < 16 ? wa : wb)[i >> 1] + (ks & 15) * 4096 + (i & 1) * 2048);
  };
  bf16x8 mb[AH + 1];
  unsigned mba = lds0 + P3_WB + g * 16;
  PIN(mba);
  auto rdm = [&](auto KS) { if constexpr (KS.value < 18) mb[KS.value % (AH + 1)] = ldsr<bf16x8>(mba + KS.value * 64); };
  sfor<AH>([&](auto K) { sfor<8>([&](auto I) { rd1(K, I); }); rdm(K); });
  SBAR();
  asm volatile("s_waitcnt vmcnt(0)" : "+v"(pf)::"memory");
  const unsigned long long m0 = __builtin_amdgcn_ballot_w64(pf[0] != c.t0 || pf[1] != c.t1);
  const bool all = (m0 & 0xffffull) == 0 ? m0 == 0 : poll_first(c, fo, FL2 + (T >> 1) * 32 * 64, 0xffffull);
  unsigned mx;
  bload4c(mx, t16 * 4, mk_rsrc(ws + WS_MX, 2048 * 4), T * 64);
  sfor<8>([&](auto K) { bload16c(of[K.value], l16, rof, (T * 18 + K.value) * 1024); });
  f32x4 acc[4];
#pragma unroll
  for (int i = 0; i < 4; ++i) { acc[i] = (f32x4){bflo(xr[i][0]), bfhi(xr[i][0]), bflo(xr[i][1]), bfhi(xr[i][1])}; PIN(acc[i]); }
  if (!all) poll(c, (16 + (lane & 15)) * 64, FL2 + (T >> 1) * 32 * 64);
  sfor<10>([&](auto K) { bload16c(of[8 + K.value], l16, rof, (T * 18 + 8 + K.value) * 1024); });
  f32x4 am;
  sfor<18>([&](auto KS) {
    constexpr int ks = KS.value;
    asm volatile("s_waitcnt vmcnt(%1)" : "+v"(of[ks]) : "i"(17 - ks) : "memory");
    sfor<4>([&](auto C) {
      mfma16_ip(acc[C.value], cat(fr[ks % (AH + 1)][C.value][0], fr[ks % (AH + 1)][C.value][1]), of[ks]);
      SBAR();
      rd1(ic<ks + AH>{}, ic<2 * C.value>{}); rd1(ic<ks + AH>{}, ic<2 * C.value + 1>{});
      SBAR();
    });
    if constexpr (ks == 0) mfma16_z(am, mb[0], of[0]); else mfma16_ip(am, mb[ks % (AH + 1)], of[ks]);
    SBAR();
    rdm(ic<ks + AH>{});
    SBAR();
  });
  asm volatile("s_nop 9" : "+v"(acc[0]), "+v"(acc[1]), "+v"(acc[2]), "+v"(acc[3]), "+v"(am), "+v"(mx));
  const float mu = am[0] + __uint_as_float(mx);
  unsigned d[8];
#pragma unroll
  for (int i = 0; i < 4; ++i) { d[2 * i] = pk2(acc[i][0] - mu, acc[i][1] - mu); d[2 * i + 1] = pk2(acc[i][2] - mu, acc[i][3] - mu); }
  const rsrc_t rhf = mk_rsrc(ws + WS_HF, 128 * 18 * 1024);
  bstore16<16>((u32x4){d[0], d[1], d[2], d[3]}, rhf, l16, (T * 18 + 2 * nt) * 1024);
  bstore16<16>((u32x4){d[4], d[5], d[6], d[7]}, rhf, l16, (T * 18 + 2 * nt + 1) * 1024);
  float s2[2] = {0.f, 0.f};
#pragma unroll
  for (int i = 0; i < 8; ++i) {
    const bf2_t v = __builtin_bit_cast(bf2_t, d[i]);
    s2[i & 1] = __builtin_amdgcn_fdot2_f32_bf16(v, v, s2[i & 1], false);
  }
  bstore8<16>((u32x2){__float_as_uint(mu), __float_as_uint(sum_x32(sum_x16(s2[0] + s2[1])))}, mk_rsrc(ws + WS_ST, 9 * 2048 * 8), so, (T * 9 + nt) * 128);
  wait_vm<0>();
  set_flag(c, FL3 + (T * 16 + nt) * 64);
}

constexpr int K4_LG = P3_WO;
constexpr int WD0 = 10, WDN = 4;
struct K4Op { int kind, idx; };
struct K4Ops { int n; K4Op op[12]; };
constexpr K4Ops k4_ops(int S) {
  K4Ops o = {};
  auto add = [&](int k, int i) { o.op[o.n++] = {k, i}; };
  if (S == 3) add(9, 0);
  if (S == 0) for (int u = 4; u < 8; ++u) { add(1, u); add(2, u); }
  else for (int u = 2 * S + 6; u < 2 * S + 8 && u < 36; ++u) {
    if (S == 2) { add(0, 2 * u - 12); add(0, 2 * u - 11); }
    if (S > 2) add(0, u);
    if (u < 18) { add(1, u); add(2, u); }
  }
  if (S >= 10 && S < 15) add(3, S - 10);
  if (S == 12) add(7, 0);
  if (S == 14) add(8, 0);
  if (S >= WD0 && S < WD0 + WDN) add(4, S - WD0);
  return o;
}
constexpr int k4_nst(int S) { return (S == 8 || S == 9) ? 2 : 0; }
constexpr int k4_issued(int S) { int n = 8; for (int s = 0; s < S; ++s) n += k4_ops(s).n + k4_nst(s); return n; }
constexpr int k4_pos(int kind, int idx) {
  if (kind == 0 && idx < 8) return -1;
  if ((kind == 1 || kind == 2) && idx < 4) return 2 * idx + kind - 1;
  for (int S = 0;; ++S) for (int i = 0; i < k4_ops(S).n; ++i) if (k4_ops(S).op[i].kind == kind && k4_ops(S).op[i].idx == idx) return k4_issued(S) + k4_nst(S) + i;
}
constexpr int k4_need(int S) {
  if (S >= 16) return k4_issued(15) - 1;
  const int a = S < 9 ? k4_pos(2, 2 * S + 1) : -1, b = k4_pos(0, 2 * S + 2);
  return a > b ? a : b;
}
static_assert(k4_need(5) >= k4_pos(9, 0) && k4_need(12) >= k4_issued(10) - 1, "");
constexpr int K5_SLOT = 17408, K5_BASE = 0, K5_P = 4;
constexpr unsigned k5_so(int v) { return K5_BASE + ((v + 7) & 7) * K5_SLOT; }
DI int k5_u(int v, int nt) {
  if (v < 32) { const int q = ((v >> 2) + nt) & 3; return 18 * (q >> 1) + 8 * (q & 1) + 4 * ((v >> 1) & 1) + (v & 1) + 2 * (v >> 4); }
  const int q = (v + nt) & 3;
  return 18 * (q >> 1) + 16 + (q & 1);
}
DI unsigned k5_mv(int rho, int sp, int nt, int t16) {
  const int rr = sp ? ((rho & 7) | ((rho & 8) << 1) | ((rho & 16) >> 1)) : rho;
  return rr * 1152 + 256 * nt + 32 * ((t16 >> 1) ^ (rho & 7)) + 16 * (t16 & 1);
}
DI unsigned k5_fl(int i, int sg, int mt5) { return (((((mt5 >> 1) * 4 + ((i >> 2) & 3)) * 8 + 2 * sg + ((i >> 4) & 1)) * 8) + 4 * (mt5 & 1) + (i & 3)) * 64; }

DI void p4(const Ctx& c) {
  char* ws = c.ws;
  const int w = c.w, r = c.r, xcd = c.xcd, lane = fresh_lane();
  const int mt = r >> 2, Qd = xcd, j = r & 3;
  const unsigned lds0 = c.lds0;
  const int g = lane >> 4;
  const int T0 = 16 * mt + 2 * w;
  const rsrc_t rwm = mk_rsrc(w < 4 ? c.wg : c.wu, D_ * F_ * 2), rhf = mk_rsrc(ws + WS_HF, 128 * 18 * 1024), rwd = mk_rsrc(c.wd, F_ * D_ * 2);
  const rsrc_t rhh = mk_rsrc(ws + WS_HH, 128 * 144 * 1024);
  const unsigned f4 = FL4 + ((r * 8 + xcd) * 8 + w) * 64;
  const int sg = xcd >> 1, mt5 = 8 * (xcd & 1) + (r >> 2);
  const unsigned wv = opq(k4_wv(lane, w, Qd, j));
  const unsigned wdst = lds0 + K4_RING + w * 1024, l16 = opq(lane * 16);
  unsigned wa[4];
  {
    const int r2 = 4 * g + ((lane >> 2) & 3);
#pragma unroll
    for (int i = 0; i < 4; ++i) wa[i] = opq(lds0 + K4_RING + r2 * 128 + 32 * (i ^ ((r2 >> 1) & 3)) + 8 * (lane & 3));
  }
  const bool all = poll_first(c, (min(lane & 15, 8) + (lane & 16)) * 64, FL3 + T0 * 16 * 64, 0x30003ull);
  bf16x8 act[18][2];
  u32x2 pf;
  auto pf_off = [&] { const int z = opq(l16) >> 4; const unsigned a = opq(FL4 + k5_fl(z, sg, mt5)), b = opq(FLS + (40 + 18 * sg + min(z - 32, 17)) * 64); return z < 32 ? a : b; };
  auto doop = [&](auto KIND, auto IDX) {
    constexpr int kind = KIND.value, u = IDX.value;
    if constexpr (kind == 0) glds16(wdst + k4_so(u), wv, rwm, (u % 18) * 32 * 9216 + (u / 18) * 128);
    else if constexpr (kind <= 2) bload16c(act[u][kind - 1], l16, rhf, (T0 * 18 + 18 * (kind - 1) + u) * 1024);
    else if constexpr (kind == 3) {
      const int pc = min(w + 8 * u, 35);
      glds16c(lds0 + K4_LG + pc * 1024, l16, mk_rsrc(ws + WS_LG, 8 * 4 * 36864), (Qd * 4 + j) * 36864 + pc * 1024);
    }
    else if constexpr (kind == 4) warm(lds0 + LDS_SCR, opq(l16) < 1024 / WDN ? l16 * 8 : OOB, rwd, sg * 1327104 + min(r * 8 + w, 161) * 8192 + u * (8192 / WDN));
    else if constexpr (kind == 7) set_flag(c, f4);
    else if constexpr (kind == 8) bload8c(pf, pf_off(), fl_rsrc(c), 0);
    else bload8c(pf, min(opq(l16), 13u * 16) * 32, fl_rsrc(c), FLS + xcd * 64);
  };
  sfor<4>([&](auto U) { doop(ic<1>{}, U); doop(ic<2>{}, U); });
  if (w < 4) cnt_add(c, 4);
  if (!all) poll(c, (min(fresh_lane() & 15, 8) + (fresh_lane() & 16)) * 64, FL3 + T0 * 16 * 64);
  cnt_wait(c, 4, 8);
  f32x4 ag[4][2], au[4][2];
  bf16x4 fr[4][2];
  unsigned hd[2][2], lga;
  auto rd1 = [&](auto H, auto I) {
    constexpr int n = H.value >> 1, m = H.value & 1, i = I.value;
    if constexpr (n < 144) fr[H.value & 3][i] = trh(wa[n % 4] + k4_so(n / 4) + 4096 * m + 2048 * i);
    else if constexpr (n < 162) fr[H.value & 3][i] = trh(lga + (n - 144) * 1024 + m * 18432 + i * 512);
  };
  auto rd = [&](auto H) { rd1(H, ic<0>{}); rd1(H, ic<1>{}); };
  auto mm = [&](auto N, auto KS, f32x4& g0, f32x4& g1, f32x4& u0, f32x4& u1, auto&& hook) {
    constexpr int n = N.value, ks = KS.value;
    constexpr int h0 = n == 15 ? 999 : 2 * n + 3, h1 = (n == 14 || n == 15) ? 999 : 2 * n + 4;
    const bf16x8 fg = cat(fr[(2 * n) & 3][0], fr[(2 * n) & 3][1]);
    if constexpr (ks == 0) mfma16_z(g0, fg, act[ks][0]); else mfma16_ip(g0, fg, act[ks][0]);
    SBAR(); rd1(ic<h0>{}, ic<0>{}); SBAR(); hook(ic<0>{}); SBAR();
    if constexpr (ks == 0) mfma16_z(g1, fg, act[ks][1]); else mfma16_ip(g1, fg, act[ks][1]);
    SBAR(); rd1(ic<h0>{}, ic<1>{}); SBAR(); hook(ic<1>{}); SBAR();
    const bf16x8 fu = cat(fr[(2 * n + 1) & 3][0], fr[(2 * n + 1) & 3][1]);
    if constexpr (ks == 0) mfma16_z(u1, fu, act[ks][1]); else mfma16_ip(u1, fu, act[ks][1]);
    SBAR(); rd1(ic<h1>{}, ic<0>{}); SBAR(); hook(ic<2>{}); SBAR();
    if constexpr (ks == 0) mfma16_z(u0, fu, act[ks][0]); else mfma16_ip(u0, fu, act[ks][0]);
    SBAR(); rd1(ic<h1>{}, ic<1>{}); SBAR(); hook(ic<3>{}); SBAR();
  };
  f32x4 pr;
  auto epi = [&](auto G, auto SG) {
    constexpr int grp = G.value, cc = SG.value / 6, T = 1 - SG.value % 6 / 3, p = SG.value % 3;
    if constexpr (p < 2) { pr[2 * p] = mul1(ag[cc][T][2 * p], au[cc][T][2 * p]); pr[2 * p + 1] = mul1(ag[cc][T][2 * p + 1], au[cc][T][2 * p + 1]); }
    else {
      const unsigned d0 = pk2(pr[0], pr[1]), d1 = pk2(pr[2], pr[3]);
      if constexpr ((cc & 1) == 0) { hd[T][0] = d0; hd[T][1] = d1; PIN(hd[T][0]); PIN(hd[T][1]); }
      else bstore16<16>((u32x4){hd[T][0], hd[T][1], d0, d1}, rhh, l16, ((T0 + T) * 144 + 18 * Qd + 4 * j + 2 * grp + (cc >> 1)) * 1024);
    }
  };
  unsigned mv5;
  auto pf5 = [&](auto N) {
    constexpr int n = N.value, v = (n - 1) >> 1;
    if constexpr (n == 0) {
      const int vv = min(w, K5_P - 1), uu = k5_u(vv, j);
      glds16c(lds0 + K5_BASE + ((vv + 7) & 7) * K5_SLOT + 16384, l16, mk_rsrc(ws + WS_WT, 4 * 4608 * 32), (j * 144 + 36 * sg + uu) * 1024);
      const unsigned z = opq(l16);
      mv5 = k5_mv(4 * w + (z >> 8), 0, j, (z >> 4) & 15);
    } else if constexpr (n & 1) glds16(lds0 + k5_so(v) + 8192 + w * 1024, mv5, rwd, sg * 1152 * 1152 + k5_u(v, j) * 32 * 1152);
    else glds16c(lds0 + k5_so(v) + w * 1024, l16, rhh, ((8 * mt5 + w) * 144 + 36 * sg + k5_u(v, j)) * 1024);
  };
  sfor<18>([&](auto SS) {
    constexpr int S = SS.value;
    constexpr int nw = k4_issued(S) - k4_need(S) - 1;
    static_assert(nw >= 0 && nw < 64, "");
    if constexpr (S == 5) asm volatile("s_waitcnt vmcnt(%5)" : "+v"(act[2 * S][0]), "+v"(act[2 * S][1]), "+v"(act[2 * S + 1][0]), "+v"(act[2 * S + 1][1]), "+v"(pf) : "i"(nw) : "memory");
    else if constexpr (S < 9) asm volatile("s_waitcnt vmcnt(%4)" : "+v"(act[2 * S][0]), "+v"(act[2 * S][1]), "+v"(act[2 * S + 1][0]), "+v"(act[2 * S + 1][1]) : "i"(nw) : "memory");
    else if constexpr (S == 16) asm volatile("s_waitcnt vmcnt(%1)" : "+v"(pf) : "i"(nw) : "memory");
    else wait_vm<nw>();
    if constexpr (S == 16) { if (__builtin_expect(!tag_ok(c, pf), 0)) poll(c, pf_off(), 0); }
    if constexpr (S == 5) { if (__builtin_expect(!tag_ok(c, pf), 0)) poll(c, min(opq(l16), 13u * 16) * 32, FLS + xcd * 64); }
    if constexpr (S >= 2) bar();
    if constexpr (S == 0 || S == 2) { rd(ic<16 * S>{}); rd(ic<16 * S + 1>{}); rd(ic<16 * S + 2>{}); }
    sfor<8>([&](auto L) {
      constexpr int l = L.value, n = 8 * S + l, t = n / 4, cc = n % 4, ks = t % 18, no = k4_ops(S).n;
      mm(ic<n>{}, ic<ks>{}, ag[cc][0], ag[cc][1], au[cc][0], au[cc][1], [&](auto I) {
        constexpr int i = I.value;
        sfor<no>([&](auto O) {
          constexpr int nd = no ? no : 1, at = O.value * 32 / nd + 16 / nd;
          if constexpr (at == 4 * l + i) doop(ic<k4_ops(S).op[O.value].kind>{}, ic<k4_ops(S).op[O.value].idx>{});
        });
        if constexpr (S == 17 && l == 0 && i == 0) { const unsigned z = opq(l16); lga = lds0 + K4_LG + (z >> 6) * 32 + ((z >> 1) & 24); PIN(lga); }
        if constexpr (S == 17 && l < 5 && i == 1) pf5(L);
        if constexpr (ks == 17 && cc >= 1) epi(ic<t / 18>{}, ic<4 * (cc - 1) + i>{});
        if constexpr (t == 18 && cc < 3) epi(ic<0>{}, ic<12 + 4 * cc + i>{});
      });
    });
  });
  {
    f32x4 g0, g1, u0, u1;
    sfor<18>([&](auto K) {
      constexpr int ks = K.value, n = 144 + ks;
      mm(ic<n>{}, ic<ks>{}, g0, g1, u0, u1, [&](auto I) {
        if constexpr (ks < 3) epi(ic<1>{}, ic<12 + 4 * ks + I.value>{});
        if constexpr (ks >= 3 && ks <= 6 && I.value == 1) pf5(ic<2 + ks>{});
        if constexpr (ks == 10 && I.value == 1) { wait_vm<4>(); set_flag(c, f4 + 2048 * 64); }
      });
    });
    asm volatile("s_nop 9" : "+v"(g0), "+v"(g1), "+v"(u0), "+v"(u1));
    const f32x4 v0 = g0 * u0, v1 = g1 * u1;
    const unsigned a0 = pk2(v0[0], v0[1]), a1 = pk2(v0[2], v0[3]), b0 = pk2(v1[0], v1[1]), b1 = pk2(v1[2], v1[3]);
    auto s0 = __builtin_amdgcn_permlane32_swap(a0, b0, false, false);
    auto s1 = __builtin_amdgcn_permlane32_swap(a1, b1, false, false);
    const unsigned z = opq(l16);
    bstore16<16>((u32x4){s0[0], s1[0], s0[1], s1[1]}, rhh, (z >> 9) * 144 * 1024 + (z & 511) + 512 * (j & 1), (T0 * 144 + 18 * Qd + 16 + (j >> 1)) * 1024);
  }
}

constexpr int k5_E(int i) { int e = 36; if (K5_P + 2 * (i + 1) < e) e = K5_P + 2 * (i + 1); if (i >= 0 && i + 8 < e) e = i + 8; return e; }
static_assert(k5_E(6) <= 16 && k5_E(20) <= 32, "");

DI void p5(const Ctx& c) {
  char* ws = c.ws;
  const int w = c.w, r = c.r, xcd = c.xcd, lane = fresh_lane();
  const int sg = xcd >> 1, mt = 8 * (xcd & 1) + (r >> 2), nt = r & 3;
  const unsigned lds0 = c.lds0;
  const int t16 = lane & 15, g = lane >> 4;
  const int tile = 4 * mt + nt;
  const rsrc_t rpt = mk_rsrc(ws + WS_PT, 64 * 8 * 4 * PT_REC);
  const unsigned f4 = FL4 + (4096 + (r * 8 + xcd) * 8 + w) * 64;
  if (w >= 4) {
    const int l = w - 4;
    const rsrc_t rwd = mk_rsrc(c.wd, F_ * D_ * 2), rhh = mk_rsrc(ws + WS_HH, 128 * 144 * 1024);
    const rsrc_t rwt = mk_rsrc(ws + WS_WT, 4 * 4608 * 32);
    unsigned mv[2][2];
#pragma unroll
    for (int sp = 0; sp < 2; ++sp)
#pragma unroll
      for (int i = 0; i < 2; ++i) mv[sp][i] = k5_mv(4 * (2 * l + i) + g, sp, nt, t16);
    const unsigned hs = ((8 * mt + 2 * l) * 144 + 36 * sg) * 1024, wsb = sg * 1152 * 1152, dl = lds0 + l * 2048;
    auto issue = [&](auto V) {
      constexpr int v = V.value, sp = v >= 32;
      const int u = k5_u(v, nt);
      constexpr unsigned so = k5_so(v);
      glds16(dl + so + 8192, mv[sp][0], rwd, wsb + u * 32 * 1152);
      glds16c(dl + so, lane * 16, rhh, hs + u * 1024);
      glds16(dl + so + 8192 + 1024, mv[sp][1], rwd, wsb + u * 32 * 1152);
      glds16c(dl + so + 1024, lane * 16, rhh, hs + (144 + u) * 1024);
      if ((v & 3) == l) glds16c(lds0 + so + 16384, lane * 16, rwt, (nt * 144 + 36 * sg + u) * 1024);
    };
    sfor<36>([&](auto I) {
      constexpr int i = I.value, y = k5_E(i - 1) - (i + 2 < 36 ? i + 2 : 36), yy = k5_E(i - 1) - K5_P;
      if constexpr (i == 0) wait_vm<2>();
      else if constexpr (i == 2) { wait_vm<4 * yy + yy / 4>(); set_flag(c, f4); }
      else if constexpr (i + 2 > K5_P) wait_vm<4 * y + y / 4>();
      bar();
      sfor<k5_E(i) - k5_E(i - 1)>([&](auto U) { issue(ic<k5_E(i - 1) + U.value>{}); });
    });
    bar();
    bar();
  } else {
    const int r2 = 4 * g + ((lane >> 2) & 3);
    unsigned wa[2][10];
#pragma unroll
    for (int i = 0; i < 8; ++i) wa[0][i] = lds0 + K5_BASE + 8192 + r2 * 256 + 32 * (i ^ (r2 & 7)) + 8 * (lane & 3);
    wa[0][8] = lds0 + K5_BASE + 16384 + r2 * 32 + 8 * (lane & 3);
    wa[0][9] = lds0 + K5_BASE + w * 2048 + lane * 16;
#pragma unroll
    for (int i = 0; i < 10; ++i) { wa[1][i] = wa[0][i] + 4 * K5_SLOT; PIN(wa[0][i]); PIN(wa[1][i]); }
    const unsigned fo = k5_fl(lane & 31, sg, mt);
    f32x4 acc[9][2];
    bf16x4 fr[3][3][2];
    bf16x8 hb[2][2];
    u32x2 pf;
    auto rd1 = [&](auto N, auto I) {
      constexpr int n = N.value, u = n / 3, i = I.value;
      if constexpr (u < 36) {
        constexpr int pos = (u + 7) & 7, hi = pos >> 2, cc = 3 * (n % 3) + i / 2;
        constexpr unsigned so = (pos & 3) * K5_SLOT;
        if constexpr (i >= 6) hb[u & 1][i - 6] = ldsr<bf16x8>(wa[hi][9] + so + (i - 6) * 1024);
        else fr[n % 3][i / 2][i & 1] = trh(wa[hi][cc] + so + (i & 1) * (cc < 8 ? 4096 : 512));
      }
    };
    sfor<36>([&](auto U) {
      constexpr int u = U.value;
      if constexpr (u == 0) wait_vm<2>();
      if constexpr (u == 4) { wait_vm<0>(); set_flag(c, f4); }
      if constexpr (u == 4 || u == 18) bload8c(pf, fo, fl_rsrc(c), FL4 + (u == 4 ? 1 : 2) * 2048 * 64);
      if constexpr (u == 7 || u == 21) {
        asm volatile("s_waitcnt vmcnt(0)" : "+v"(pf)::"memory");
        if (__builtin_expect(!tag_ok(c, pf), 0)) poll(c, fo, FL4 + (u == 7 ? 1 : 2) * 2048 * 64);
      }
      bar();
      if constexpr (u == 0) { rd1(ic<0>{}, ic<6>{}); rd1(ic<0>{}, ic<7>{}); sfor<6>([&](auto I) { rd1(ic<0>{}, I); }); sfor<6>([&](auto I) { rd1(ic<1>{}, I); }); }
      sfor<18>([&](auto M) {
        constexpr int mi = M.value, v = mi / 6, n = 3 * u + v, cc = mi / 2;
        if constexpr (u == 0) mfma16_z(acc[cc][mi & 1], cat(fr[n % 3][cc % 3][0], fr[n % 3][cc % 3][1]), hb[u & 1][mi & 1]);
        else mfma16_ip(acc[cc][mi & 1], cat(fr[n % 3][cc % 3][0], fr[n % 3][cc % 3][1]), hb[u & 1][mi & 1]);
        SBAR();
        if constexpr (mi == 6 || mi == 8) rd1(ic<n + 2>{}, ic<6 + (mi - 6) / 2>{});
        rd1(ic<n + 2>{}, ic<mi % 6>{});
        SBAR();
      });
    });
    asm volatile("s_nop 9" : "+v"(acc[7][0]), "+v"(acc[7][1]), "+v"(acc[8][0]), "+v"(acc[8][1]));
#pragma unroll
    for (int i = 0; i < 7; ++i) asm volatile("" : "+v"(acc[i][0]), "+v"(acc[i][1]));
    bar();
    if (w == sg) {
      sfor<2>([&](auto T) { sfor<9>([&](auto C) { ldsw<f32x4>(lds0 + (T.value * 9 + C.value) * 1024 + lane * 16, acc[C.value][T.value]); }); });
      lds_bar();
    } else {
      sfor<2>([&](auto T) {
        const unsigned pb = (((tile * 4 + w) * 4 + sg) * 2 + T.value) * PT_REC;
        sfor<4>([&](auto I) {
          constexpr int i = I.value;
          const f32x4 a = acc[2 * i][T.value], b = acc[2 * i + 1][T.value];
          bstore16<16>((u32x4){pk2(a[0], a[1]), pk2(a[2], a[3]), pk2(b[0], b[1]), pk2(b[2], b[3])}, rpt, lane * 16 + i * 1024, pb);
        });
        const f32x4 a = acc[8][T.value];
        bstore8<16>((u32x2){pk2(a[0], a[1]), pk2(a[2], a[3])}, rpt, lane * 8 + 4096, pb);
      });
      bar();
      wait_vm<0>();
      set_flag(c, FL5 + (tile * 4 + w) * 256 + sg * 64);
    }
  }
  const int T = w >> 2, pi = w & 3, tt = 8 * mt + 2 * sg + T;
  const rsrc_t rhf = mk_rsrc(ws + WS_HF, 128 * 18 * 1024);
  u32x4 hv, pv[3];
  u32x2 hl, pl[3];
  u32x2 sp[3];
  bload16c(hv, lane * 16, rhf, (tt * 18 + 4 * nt + pi) * 1024);
  bload8c(hl, lane * 16 + 8 * (nt & 1), rhf, (tt * 18 + 16 + (nt >> 1)) * 1024);
  sfor<3>([&](auto I) { bload8c(sp[I.value], I.value < 2 || g == 0 ? lane * 8 + 512 * I.value : OOB, mk_rsrc(ws + WS_ST, 9 * 2048 * 8), tt * 1152); });
  poll(c, ((sg + 1 + min(lane, 2)) & 3) * 64, FL5 + (tile * 4 + sg) * 256);
  asm volatile("" : "+v"(sp[0]), "+v"(sp[1]), "+v"(sp[2]));
#pragma unroll
  for (int o = 0; o < 3; ++o) {
    const unsigned so = (((tile * 4 + sg) * 4 + ((sg + 1 + o) & 3)) * 2 + T) * PT_REC;
    bload16c(pv[o], lane * 16 + pi * 1024, rpt, so);
    bload8c(pl[o], pi == 0 ? lane * 8 + 4096 : OOB, rpt, so);
  }
  const float mu = __uint_as_float(sp[0][0]);
  float rs2 = __builtin_amdgcn_rcpf(sum_x32(sum_x16(__uint_as_float(sp[0][1]) + __uint_as_float(sp[1][1]) + __uint_as_float(sp[2][1]))) * (1.f / D_) + 1e-5f);
  PIN(rs2);
  const unsigned orow = (16 * tt + t16) * 1152, own = lds0 + T * 9216 + lane * 16;
  const f32x4 o0 = ldsr<f32x4>(own + pi * 2048), o1 = ldsr<f32x4>(own + pi * 2048 + 1024), o2 = ldsr<f32x4>(own + 8192);
  asm volatile("s_waitcnt vmcnt(0)" : "+v"(hv), "+v"(hl), "+v"(pv[0]), "+v"(pv[1]), "+v"(pv[2]), "+v"(pl[0]), "+v"(pl[1]), "+v"(pl[2])::"memory");
  const rsrc_t rout = mk_rsrc(c.out, S_ * D_ * 2);
  auto fin = [&](const f32x4& ow, unsigned a0, unsigned a1, unsigned b0, unsigned b1, unsigned c0, unsigned c1, unsigned h0, unsigned h1, unsigned& d0, unsigned& d1) {
    const float v0 = ((ow[0] + bflo(b0)) + (bflo(a0) + bflo(c0))) * rs2 + (bflo(h0) + mu);
    const float v1 = ((ow[1] + bfhi(b0)) + (bfhi(a0) + bfhi(c0))) * rs2 + (bfhi(h0) + mu);
    const float v2 = ((ow[2] + bflo(b1)) + (bflo(a1) + bflo(c1))) * rs2 + (bflo(h1) + mu);
    const float v3 = ((ow[3] + bfhi(b1)) + (bfhi(a1) + bfhi(c1))) * rs2 + (bfhi(h1) + mu);
    d0 = pk2(v0, v1); d1 = pk2(v2, v3);
  };
  {
    unsigned a0, a1, b0, b1;
    fin(o0, pv[0][0], pv[0][1], pv[1][0], pv[1][1], pv[2][0], pv[2][1], hv[0], hv[1], a0, a1);
    fin(o1, pv[0][2], pv[0][3], pv[1][2], pv[1][3], pv[2][2], pv[2][3], hv[2], hv[3], b0, b1);
    auto s0 = __builtin_amdgcn_permlane16_swap(a0, b0, false, false);
    auto s1 = __builtin_amdgcn_permlane16_swap(a1, b1, false, false);
    bstore16<0>((u32x4){s0[0], s1[0], s0[1], s1[1]}, rout, orow + 256 * nt + 64 * pi + 32 * (g & 1) + 16 * (g >> 1), 0);
  }
  {
    unsigned e0, e1;
    fin(o2, pl[0][0], pl[0][1], pl[1][0], pl[1][1], pl[2][0], pl[2][1], hl[0], hl[1], e0, e1);
    bstore8<0>((u32x2){e0, e1}, rout, pi == 0 ? orow + 1024 + 32 * nt + 8 * g : OOB, 0);
  }
}

extern "C" __global__ __launch_bounds__(512, 2) void k1_all(const void* x, const void* wqkv, const void* wo, const void* wg, const void* wu, const void* wd, char* ws, void* out) {
  Ctx c = {x, wqkv, wo, wg, wu, wd, ws, out, (int)rfl(threadIdx.x >> 6), (int)(blockIdx.x & 7), (int)(blockIdx.x >> 3), 0u, 0u, 0u};
  bf16x8 mv[9];
  {
    const int mt = 4 * (c.xcd & 3) + (c.r & 3), nt = 8 * (c.xcd >> 2) + (c.r >> 2);
    if (c.w < 4) p1_body<0>(c, mt, nt, mv); else p1_body<1>(c, mt, nt, mv);
  }
  p2(c, mv);
  if (c.r < 18) p3_gemm(c); else { cnt_wait(c, 4, 4); p3_warm(c); }
  if (c.w < 4) k4_first<3, 1>(c, fresh_lane());
  else { cnt_wait(c, 1, 8); k4_first<4, 4>(c, fresh_lane()); }
  if (c.r >= 18) p3_side(c);
  p4(c);
  p5(c);
}

extern "C" __global__ void k9_end() {}

extern "C" void kernel_launch(void* const* d_in, const int* in_sizes, int n_in, void* d_out, int out_size, void* d_ws, size_t ws_size,
                              hipStream_t stream) {
  (void)hipFuncSetAttribute((const void*)k1_all, hipFuncAttributeMaxDynamicSharedMemorySize, LDS_ALL);
  k1_all<<<256, 512, LDS_ALL, stream>>>(d_in[0], d_in[1], d_in[2], d_in[4], d_in[5], d_in[6], (char*)d_ws, d_out);
}
```
